```python
import torch

def _softmax(x, axis=-1):
    return torch.softmax(x, dim=axis)
def _gelu(x):
    return torch.nn.functional.gelu(x, approximate="tanh")
def _silu(x):
    return torch.nn.functional.silu(x)
def _sigmoid(x):
    return torch.sigmoid(x)
def _layernorm(x, axis=-1, eps=1e-5):
    m = x.mean(dim=axis, keepdim=True)
    v = x.var(dim=axis, keepdim=True, unbiased=False)
    return (x - m) / torch.sqrt(v + eps)
def _scatter_add(tgt, idx, src, axis=0):
    return tgt.clone().index_add_(axis, idx, src)
def _segment_sum(src, seg, n):
    out = torch.zeros((n,) + src.shape[1:], dtype=src.dtype)
    return out.index_add_(0, seg, src)
def _pad(x, pw):
    flat = tuple(v for pair in reversed(pw) for v in pair)
    return torch.nn.functional.pad(x, flat)

kernel_name = 'standalone__standard_2d_1d_3d_conv__vision'

def setup_inputs():

    _w = (lambda a: (lambda t: t.to(torch.bfloat16) if t.is_floating_point() else t)(torch.as_tensor(a)))
    H = W = 384
    C = 384
    K = 1
    return {
        "x": _w((lambda *s: torch.randn(*s, dtype=torch.bfloat16) * 0.05)(1, H, W, C)),
        "w": _w((lambda *s: torch.randn(*s, dtype=torch.bfloat16) * 0.05)(K, K, C, C)),
        "b": _w((lambda *s: torch.randn(*s, dtype=torch.bfloat16) * 0.05)(C)),
    }

def reference(x, w, b):
    B, H, W, Cin = x.shape
    K = w.shape[0]
    pl = (K - 1) // 2
    pr = K // 2
    xp = _pad(x, ((0, 0), (pl, pr), (pl, pr), (0, 0)))
    out = sum(
        torch.einsum('bhwc,cd->bhwd', xp[:, i:i+H, j:j+W, :], w[i, j])
        for i in range(K) for j in range(K)
    )
    return out + b[None, None, None, :]
```

```cpp
#include <hip/hip_runtime.h>
#include <stdint.h>

#define LAS __attribute__((address_space(3)))

__global__ __launch_bounds__(256, 1) void conv1x1_kernel(const uint16_t* __restrict__ x,
                                                         const uint16_t* __restrict__ w,
                                                         const uint16_t* __restrict__ bias,
                                                         uint16_t* __restrict__ out, unsigned long long* ts) {
  __shared__ __attribute__((aligned(1024))) unsigned char lds[160768];

  unsigned long long xa, wa, ba, oa, sa, rs;
  asm volatile(
      "s_load_dwordx2 s[16:17], %6, 0x8\n\t"
      "s_load_dwordx2 s[20:21], %6, 0x0\n\t"
      "s_load_dwordx2 s[36:37], %6, 0x10\n\t"
      "s_load_dwordx2 s[28:29], %6, 0x18\n\t"
      "s_load_dwordx2 s[24:25], %6, 0x20\n\t"
      "s_memrealtime s[52:53]\n\t"
      : "={s[20:21]}"(xa), "={s[16:17]}"(wa), "={s[36:37]}"(ba), "={s[28:29]}"(oa), "={s[24:25]}"(sa), "={s[52:53]}"(rs)
      : "s"(__builtin_amdgcn_kernarg_segment_ptr())
      : "memory");
  __builtin_amdgcn_sched_barrier(0);
  const unsigned tid  = threadIdx.x;
  unsigned lane = tid & 63;
  unsigned wv   = __builtin_amdgcn_readfirstlane(tid >> 6);
  const unsigned lds0 = (unsigned)(size_t)(LAS unsigned char*)lds;
  const unsigned blk  = blockIdx.x;
  const unsigned role = (blk >> 2) & 1u;
  unsigned pk = (blk & 3u) * 32u + (blk >> 3);
  const unsigned sst  = (role ^ (blk >> 4)) & 1u;
  const unsigned t0   = pk * 49152u + sst * 24576u;
  const unsigned p0   = lds0 + 3072u * wv;
  const unsigned lq = lane >> 4, ph = (lane >> 2) & 3, pl = lane & 3;
  const unsigned gs = 256u * lq + 64u * ((ph - lq) & 3) + 16u * (pl ^ ((4u - wv) & 3));
  const unsigned ga = gs + 3072u * (wv < 3u ? wv : 5u);
  const unsigned gb = gs + 3072u * (wv < 2u ? wv + 6u : wv + 1u);
  const unsigned pv = blk >> 6, ps = (blk & 63u) ^ (4u * (pv & 1u)), pgr = (ps >> 2) & 7u, pi = (ps & 3u) * 2u + (pv >> 1);
  const unsigned ppg = min(pgr * 8u, 52u) + (pi & (0x13777777u >> (4u * pgr)) & 7u);
  const unsigned pop = (ppg << 21) + ((ps & 3u) * 4u + pv) * 0x1c080u + 0x8000u;
  unsigned pfire = ((role ^ ppg) & 1u) * 2u;
  const unsigned vpub = lane == 0u ? 0u + (pv * 64u + ps) * 8u : 0x40000000u;
  const unsigned pfo = (blk >> 3) * 9216u;
  const unsigned pfv = tid * 16u;
  const unsigned ty = (0x10101010u >> (4u * ((blk >> 3) & 7))) & 15u;
  const unsigned tidx = (blk >> 3) * 4u + wv, tsel = tidx >> 6;
  const unsigned toff = (tidx & 63u) < 54u ? (tidx & 63u) << 21 : 113246204u;
  unsigned tres;
  unsigned long long pres;
  asm volatile(
      "s_add_u32 m0, s43, 0x27000\n\t"
      "s_add_u32 s51, s42, 0x1000\n\t"
      "s_mov_b32 s61, 0x7fc1a5e3\n\t"
      "s_mov_b32 s62, s61\n\t"
      "s_waitcnt lgkmcnt(0)\n\t"
      "s_cmp_lg_u32 s41, s43\n\t"
      "s_cbranch_scc1 .Lw0pi_%=\n\t"
      "s_cmp_eq_u32 s78, 0\n\t"
      "s_cselect_b64 s[96:97], s[20:21], s[28:29]\n\t"
      "s_load_dword s60, s[96:97], s76 offset:0x0\n\t"
      "s_load_dword s61, s[96:97], s76 offset:0x3300\n\t"
      ".Lw0pi_%=:\n\t"
      "buffer_load_dwordx4 v216, s[16:19], s42 offen lds\n\t"
      "buffer_load_dwordx4 v216, s[16:19], s51 offen lds\n\t"
      "s_add_u32 s51, s42, 0x2000\n\t"
      "buffer_load_dwordx4 v216, s[16:19], s51 offen lds\n\t"
      "s_mov_b32 m0, s41\n\t"
      "s_nop 0\n\t"
      "buffer_load_dwordx4 v208, s[20:23], s40 offen lds\n\t"
      "buffer_load_dwordx4 v208, s[20:23], s40 offen offset:1024 lds\n\t"
      "buffer_load_dwordx4 v208, s[20:23], s40 offen offset:2048 lds\n\t"
      "s_add_u32 m0, s41, 0x3000\n\t"
      "s_nop 0\n\t"
      "buffer_load_dwordx4 v209, s[20:23], s40 offen lds\n\t"
      "buffer_load_dwordx4 v209, s[20:23], s40 offen offset:1024 lds\n\t"
      "buffer_load_dwordx4 v209, s[20:23], s40 offen offset:2048 lds\n\t"
      "s_cmp_lg_u32 s41, s43\n\t"
      "s_cbranch_scc1 .Lw0pe_%=\n\t"
      "s_andn2_b32 s79, s52, 15\n\t"
      "v_mov_b32_e32 v241, s53\n\t"
      "s_waitcnt lgkmcnt(1)\n\t"
      "s_nop 7\n\t"
      "s_nop 7\n\t"
      "s_cmp_eq_u32 s61, s62\n\t"
      "s_cselect_b32 s77, s77, 1\n\t"
      "s_or_b32 s79, s79, s77\n\t"
      "v_mov_b32_e32 v240, s79\n\t"
      "buffer_store_dwordx2 v[240:241], v244, s[24:27], 0 offen sc1\n\t"
      ".Lw0pe_%=:\n\t"
      "s_cmp_eq_u32 s99, 0\n\t"
      "s_cselect_b64 s[96:97], s[20:21], s[28:29]\n\t"
      "s_load_dword s95, s[96:97], s98\n\t"
      : "={s95}"(tres), "={s[60:61]}"(pres), "+{s77}"(pfire), "+v"(lane), "+s"(wv), "+s"(pk)
      : "{s[20:21]}"(xa), "{s[16:17]}"(wa), "{s[36:37]}"(ba), "{s[28:29]}"(oa), "{s[24:25]}"(sa), "{s[52:53]}"(rs),
        "{s[22:23]}"(0x0002000006c00000ull), "{s[26:27]}"(0x0002000000000800ull), "{s[30:31]}"(0x0002000006c00000ull),
        "{s[18:19]}"(0x0002000000048000ull), "{s[38:39]}"(0x0002000000000300ull),
        "{s40}"(t0), "{s41}"(p0), "{s43}"(lds0), "{s94}"(ty),
        "{v208}"(ga), "{v209}"(gb),
        "{s42}"(pfo), "{s98}"(toff), "{s99}"(tsel), "{s76}"(pop), "{s78}"(ps >> 5),
        "{v216}"(pfv), "{v244}"(vpub)
      : "memory", "scc", "s51", "s96", "s97", "s62", "s79", "v240", "v241");
  __builtin_amdgcn_sched_barrier(0);
  const unsigned sp = lane < 18u ? lane : lane - 14u;
  const unsigned ob = (unsigned)(((sp < 12u ? 0x41ccd62ca418820ull >> (5u * sp) : 0x22a4c1eeull >> (5u * ((sp - 12u) & 7u)))) & 31u) * 6291456u + pk * 49152u;
  const unsigned pg = (ob + 24576u) >> 21;
  const unsigned ta = lane < 18u ? ob + 24576u * sst : 113246208u, t1 = lane < 32u ? ob + 24576u * (sst ^ 1u) : 113246208u;
  const unsigned tq = lane >= 9u ? 82u : 80u, tp = lane >= 14u ? 0u : tq;
  const unsigned mk = lane >= 4u && lane < 18u ? 1u << (pg < 52u ? (pg >> 3) * 4u : 28u) : 0u;
  const unsigned plm = (unsigned)__builtin_amdgcn_ballot_w64(((pg ^ role ^ sst) & 1u) != 0u), plb = wv < 2u ? plm : ~plm;
  const unsigned vgv = 0u + wv * 512u + lane * 8u;
  const unsigned vxr = lds0 + 159744u + lane * 16u, vxw = vxr + wv * 4u;
  const unsigned li = lane & 15, lg = lane >> 4, rl = li & 3, hi = li >> 2;
  const unsigned rb = 768u * li + 16u * (lg ^ ((4u - hi) & 3)) - 3072u * wv;
  unsigned cfa[4], csw[3];
#pragma unroll
  for (unsigned u = 0; u < 4; ++u) cfa[u] = rb + 64u * ((u - rl) & 3);
#pragma unroll
  for (unsigned j = 0; j < 3; ++j) {
    const unsigned e = 3u * wv + j;
    csw[j] = rb + 256u * (e >> 2) + 64u * (((e & 3) + (e >> 2) - rl) & 3);
  }
  const unsigned csr = 16u * lane;
  const unsigned tbase = p0 + (4u * lg + (li >> 2)) * 192u + 16u * (li & 3);
  unsigned woff[3];
#pragma unroll
  for (unsigned j = 0; j < 3; ++j) {
    const unsigned q = 64u * j + lane, rr = q / 12u;
    woff[j] = (8u * (rr >> 2) + (rr & 3)) * 768u + 192u * wv + 16u * (q % 12u);
  }
  const unsigned boff = 192u * wv + 16u * lg;
  asm volatile(
      "s_cmp_eq_u32 s94, 1\n\t"
      "s_cbranch_scc0 .Lc0_%=\n\t"
      "s_add_u32 m0, s41, 0x6000\n\t"
      "s_add_u32 s51, s40, 0x600000\n\t"
      "buffer_load_dwordx4 v208, s[20:23], s51 offen lds\n\t"
      "buffer_load_dwordx4 v208, s[20:23], s51 offen offset:1024 lds\n\t"
      "buffer_load_dwordx4 v208, s[20:23], s51 offen offset:2048 lds\n\t"
      "s_add_u32 m0, s41, 0x9000\n\t"
      "s_add_u32 s51, s40, 0x600000\n\t"
      "buffer_load_dwordx4 v209, s[20:23], s51 offen lds\n\t"
      "buffer_load_dwordx4 v209, s[20:23], s51 offen offset:1024 lds\n\t"
      "buffer_load_dwordx4 v209, s[20:23], s51 offen offset:2048 lds\n\t"
      ".Lc0_%=:\n\t"
      "buffer_load_dwordx4 v[226:229], v215, s[36:39], 0 offen offset:0\n\t"
      "buffer_load_dwordx4 v[230:233], v215, s[36:39], 0 offen offset:64\n\t"
      "buffer_load_dwordx4 v[234:237], v215, s[36:39], 0 offen offset:128\n\t"
      "s_mov_b32 s51, 0x0\n\t"
      "s_add_u32 m0, s41, 0x1e000\n\t"
      "s_nop 0\n\t"
      "buffer_load_dwordx4 v211, s[16:19], s51 offen lds\n\t"
      "s_add_u32 m0, s41, 0x1e400\n\t"
      "s_nop 0\n\t"
      "buffer_load_dwordx4 v212, s[16:19], s51 offen lds\n\t"
      "s_add_u32 m0, s41, 0x1e800\n\t"
      "s_nop 0\n\t"
      "buffer_load_dwordx4 v213, s[16:19], s51 offen lds\n\t"
      "s_mov_b32 s51, 0xc00\n\t"
      "s_add_u32 m0, s41, 0x21000\n\t"
      "s_nop 0\n\t"
      "buffer_load_dwordx4 v211, s[16:19], s51 offen lds\n\t"
      "s_add_u32 m0, s41, 0x21400\n\t"
      "s_nop 0\n\t"
      "buffer_load_dwordx4 v212, s[16:19], s51 offen lds\n\t"
      "s_add_u32 m0, s41, 0x21800\n\t"
      "s_nop 0\n\t"
      "buffer_load_dwordx4 v213, s[16:19], s51 offen lds\n\t"
      "s_mov_b32 s51, 0x6000\n\t"
      "s_add_u32 m0, s41, 0x24000\n\t"
      "s_nop 0\n\t"
      "buffer_load_dwordx4 v211, s[16:19], s51 offen lds\n\t"
      "s_add_u32 m0, s41, 0x24400\n\t"
      "s_nop 0\n\t"
      "buffer_load_dwordx4 v212, s[16:19], s51 offen lds\n\t"
      "s_add_u32 m0, s41, 0x24800\n\t"
      "s_nop 0\n\t"
      "buffer_load_dwordx4 v213, s[16:19], s51 offen lds\n\t"
      "s_mov_b32 s51, 0x6c00\n\t"
      "s_add_u32 m0, s41, 0x12000\n\t"
      "s_nop 0\n\t"
      "buffer_load_dwordx4 v211, s[16:19], s51 offen lds\n\t"
      "s_add_u32 m0, s41, 0x12400\n\t"
      "s_nop 0\n\t"
      "buffer_load_dwordx4 v212, s[16:19], s51 offen lds\n\t"
      "s_add_u32 m0, s41, 0x12800\n\t"
      "s_nop 0\n\t"
      "buffer_load_dwordx4 v213, s[16:19], s51 offen lds\n\t"
      "s_mov_b32 s51, 0xc000\n\t"
      "s_add_u32 m0, s41, 0x15000\n\t"
      "s_nop 0\n\t"
      "buffer_load_dwordx4 v211, s[16:19], s51 offen lds\n\t"
      "s_add_u32 m0, s41, 0x15400\n\t"
      "s_nop 0\n\t"
      "buffer_load_dwordx4 v212, s[16:19], s51 offen lds\n\t"
      "s_add_u32 m0, s41, 0x15800\n\t"
      "s_nop 0\n\t"
      "buffer_load_dwordx4 v213, s[16:19], s51 offen lds\n\t"
      "s_mov_b32 s51, 0xcc00\n\t"
      "s_add_u32 m0, s41, 0x18000\n\t"
      "s_nop 0\n\t"
      "buffer_load_dwordx4 v211, s[16:19], s51 offen lds\n\t"
      "s_add_u32 m0, s41, 0x18400\n\t"
      "s_nop 0\n\t"
      "buffer_load_dwordx4 v212, s[16:19], s51 offen lds\n\t"
      "s_add_u32 m0, s41, 0x18800\n\t"
      "s_nop 0\n\t"
      "buffer_load_dwordx4 v213, s[16:19], s51 offen lds\n\t"
      "s_mov_b32 s51, 0x12000\n\t"
      "s_add_u32 m0, s41, 0x1b000\n\t"
      "s_nop 0\n\t"
      "buffer_load_dwordx4 v211, s[16:19], s51 offen lds\n\t"
      "s_add_u32 m0, s41, 0x1b400\n\t"
      "s_nop 0\n\t"
      "buffer_load_dwordx4 v212, s[16:19], s51 offen lds\n\t"
      "s_add_u32 m0, s41, 0x1b800\n\t"
      "s_nop 0\n\t"
      "buffer_load_dwordx4 v213, s[16:19], s51 offen lds\n\t"
      "s_mov_b32 s44, 0\n\t"
      "s_mov_b32 s85, 3\n\t"
      "s_mov_b32 s49, 17\n\t"
      "s_mov_b32 s90, 0\n\t"
      "s_add_u32 s50, s41, 0x12000\n\t"
      "s_mov_b32 s45, s41\n\t"
      "s_add_u32 s46, s41, 0x6000\n\t"
      "s_add_u32 s47, s41, 0xc000\n\t"
      "v_add_u32_e32 v196, s41, v200\n\t"
      "v_add_u32_e32 v197, s41, v201\n\t"
      "v_add_u32_e32 v198, s41, v202\n\t"
      "v_add_u32_e32 v199, s41, v203\n\t"
      "v_add_u32_e32 v217, s41, v204\n\t"
      "v_add_u32_e32 v218, s41, v205\n\t"
      "v_add_u32_e32 v219, s41, v206\n\t"
      "v_add_u32_e32 v220, s41, v207\n\t"
      "s_waitcnt vmcnt(18)\n\t"
      "s_barrier\n\t"
      "v_add_u32_e32 v221, 0x1e000, v214\n\t"
      "ds_read_b64_tr_b16 a[0:1], v221 offset:0\n\t"
      "ds_read_b64_tr_b16 a[4:5], v221 offset:8\n\t"
      "ds_read_b64_tr_b16 a[8:9], v221 offset:64\n\t"
      "ds_read_b64_tr_b16 a[12:13], v221 offset:72\n\t"
      "ds_read_b64_tr_b16 a[16:17], v221 offset:128\n\t"
      "ds_read_b64_tr_b16 a[20:21], v221 offset:136\n\t"
      "ds_read_b128 v[140:143], v196 offset:0\n\t"
      "v_lshlrev_b32_e32 v8, 16, v226\n\t"
      "v_and_b32_e32 v9, 0xffff0000, v226\n\t"
      "v_lshlrev_b32_e32 v10, 16, v227\n\t"
      "v_and_b32_e32 v11, 0xffff0000, v227\n\t"
      "v_lshlrev_b32_e32 v12, 16, v228\n\t"
      "v_and_b32_e32 v13, 0xffff0000, v228\n\t"
      "v_lshlrev_b32_e32 v14, 16, v229\n\t"
      "v_and_b32_e32 v15, 0xffff0000, v229\n\t"
      "v_lshlrev_b32_e32 v16, 16, v230\n\t"
      "v_and_b32_e32 v17, 0xffff0000, v230\n\t"
      "v_lshlrev_b32_e32 v18, 16, v231\n\t"
      "v_and_b32_e32 v19, 0xffff0000, v231\n\t"
      "v_lshlrev_b32_e32 v20, 16, v232\n\t"
      "v_and_b32_e32 v21, 0xffff0000, v232\n\t"
      "v_lshlrev_b32_e32 v22, 16, v233\n\t"
      "v_and_b32_e32 v23, 0xffff0000, v233\n\t"
      "v_lshlrev_b32_e32 v24, 16, v234\n\t"
      "v_and_b32_e32 v25, 0xffff0000, v234\n\t"
      "v_lshlrev_b32_e32 v26, 16, v235\n\t"
      "v_and_b32_e32 v27, 0xffff0000, v235\n\t"
      "v_lshlrev_b32_e32 v28, 16, v236\n\t"
      "v_and_b32_e32 v29, 0xffff0000, v236\n\t"
      "v_lshlrev_b32_e32 v30, 16, v237\n\t"
      "v_and_b32_e32 v31, 0xffff0000, v237\n\t"
      "s_waitcnt vmcnt(15)\n\t"
      "v_add_u32_e32 v221, 0x21000, v214\n\t"
      "ds_read_b64_tr_b16 a[2:3], v221 offset:0\n\t"
      "ds_read_b64_tr_b16 a[6:7], v221 offset:8\n\t"
      "ds_read_b64_tr_b16 a[10:11], v221 offset:64\n\t"
      "ds_read_b64_tr_b16 a[14:15], v221 offset:72\n\t"
      "ds_read_b64_tr_b16 a[18:19], v221 offset:128\n\t"
      "ds_read_b64_tr_b16 a[22:23], v221 offset:136\n\t"
      "ds_read_b128 v[152:155], v196 offset:12288\n\t"
      "s_waitcnt lgkmcnt(8)\n\t"
      "s_mov_b32 s51, 0x12c00\n\t"
      "s_add_u32 m0, s41, 0x1e000\n\t"
      "s_nop 0\n\t"
      "buffer_load_dwordx4 v211, s[16:19], s51 offen lds\n\t"
      "s_add_u32 m0, s41, 0x1e400\n\t"
      "s_nop 0\n\t"
      "buffer_load_dwordx4 v212, s[16:19], s51 offen lds\n\t"
      "s_add_u32 m0, s41, 0x1e800\n\t"
      "s_nop 0\n\t"
      "buffer_load_dwordx4 v213, s[16:19], s51 offen lds\n\t"
      "s_waitcnt vmcnt(15)\n\t"
      "v_add_u32_e32 v221, 0x24000, v214\n\t"
      "ds_read_b64_tr_b16 a[24:25], v221 offset:0\n\t"
      "ds_read_b64_tr_b16 a[28:29], v221 offset:8\n\t"
      "ds_read_b64_tr_b16 a[32:33], v221 offset:64\n\t"
      "ds_read_b64_tr_b16 a[36:37], v221 offset:72\n\t"
      "ds_read_b64_tr_b16 a[40:41], v221 offset:128\n\t"
      "ds_read_b64_tr_b16 a[44:45], v221 offset:136\n\t"
      "ds_read_b128 v[144:147], v197 offset:0\n\t"
      "s_waitcnt lgkmcnt(8)\n\t"
      "v_mfma_f32_16x16x32_bf16 v[80:83], a[0:3], v[140:143], v[8:11]\n\t"
      "s_mov_b32 s51, 0x18000\n\t"
      "s_add_u32 m0, s41, 0x21000\n\t"
      "s_nop 0\n\t"
      "buffer_load_dwordx4 v211, s[16:19], s51 offen lds\n\t"
      "s_add_u32 m0, s41, 0x21400\n\t"
      "s_nop 0\n\t"
      "buffer_load_dwordx4 v212, s[16:19], s51 offen lds\n\t"
      "v_mfma_f32_16x16x32_bf16 v[84:87], a[4:7], v[140:143], v[12:15]\n\t"
      "s_add_u32 m0, s41, 0x21800\n\t"
      "s_nop 0\n\t"
      "buffer_load_dwordx4 v213, s[16:19], s51 offen lds\n\t"
      "v_mfma_f32_16x16x32_bf16 v[88:91], a[8:11], v[140:143], v[16:19]\n\t"
      "s_sub_u32 s63, s52, 800\n\t"
      "s_andn2_b32 s63, s63, 15\n\t"
      "s_waitcnt vmcnt(15)\n\t"
      "v_add_u32_e32 v221, 0x12000, v214\n\t"
      "ds_read_b64_tr_b16 a[26:27], v221 offset:0\n\t"
      "v_mfma_f32_16x16x32_bf16 v[92:95], a[12:15], v[140:143], v[20:23]\n\t"
      "ds_read_b64_tr_b16 a[30:31], v221 offset:8\n\t"
      "ds_read_b64_tr_b16 a[34:35], v221 offset:64\n\t"
      "v_mfma_f32_16x16x32_bf16 v[96:99], a[16:19], v[140:143], v[24:27]\n\t"
      "ds_read_b64_tr_b16 a[38:39], v221 offset:72\n\t"
      "ds_read_b64_tr_b16 a[42:43], v221 offset:128\n\t"
      "v_mfma_f32_16x16x32_bf16 v[100:103], a[20:23], v[140:143], v[28:31]\n\t"
      "ds_read_b64_tr_b16 a[46:47], v221 offset:136\n\t"
      "ds_read_b128 v[156:159], v197 offset:12288\n\t"
      "s_waitcnt lgkmcnt(8)\n\t"
      "v_mfma_f32_16x16x32_bf16 v[104:107], a[0:3], v[152:155], v[8:11]\n\t"
      "s_mov_b32 s51, 0x18c00\n\t"
      "s_add_u32 m0, s41, 0x24000\n\t"
      "s_nop 0\n\t"
      "buffer_load_dwordx4 v211, s[16:19], s51 offen lds\n\t"
      "v_mfma_f32_16x16x32_bf16 v[108:111], a[4:7], v[152:155], v[12:15]\n\t"
      "s_add_u32 m0, s41, 0x24400\n\t"
      "s_nop 0\n\t"
      "buffer_load_dwordx4 v212, s[16:19], s51 offen lds\n\t"
      "v_mfma_f32_16x16x32_bf16 v[112:115], a[8:11], v[152:155], v[16:19]\n\t"
      "s_add_u32 m0, s41, 0x24800\n\t"
      "s_nop 0\n\t"
      "buffer_load_dwordx4 v213, s[16:19], s51 offen lds\n\t"
      "s_waitcnt vmcnt(15)\n\t"
      "v_add_u32_e32 v221, 0x15000, v214\n\t"
      "ds_read_b64_tr_b16 a[48:49], v221 offset:0\n\t"
      "v_mfma_f32_16x16x32_bf16 v[116:119], a[12:15], v[152:155], v[20:23]\n\t"
      "ds_read_b64_tr_b16 a[52:53], v221 offset:8\n\t"
      "ds_read_b64_tr_b16 a[56:57], v221 offset:64\n\t"
      "v_mfma_f32_16x16x32_bf16 v[120:123], a[16:19], v[152:155], v[24:27]\n\t"
      "ds_read_b64_tr_b16 a[60:61], v221 offset:72\n\t"
      "ds_read_b64_tr_b16 a[64:65], v221 offset:128\n\t"
      "v_mfma_f32_16x16x32_bf16 v[132:135], a[20:23], v[152:155], v[28:31]\n\t"
      "ds_read_b64_tr_b16 a[68:69], v221 offset:136\n\t"
      "ds_read_b128 v[148:151], v198 offset:0\n\t"
      "s_waitcnt lgkmcnt(8)\n\t"
      "v_mfma_f32_16x16x32_bf16 v[80:83], a[24:27], v[144:147], v[80:83]\n\t"
      "s_mov_b32 s51, 0x1e000\n\t"
      "s_add_u32 m0, s41, 0x12000\n\t"
      "s_nop 0\n\t"
      "buffer_load_dwordx4 v211, s[16:19], s51 offen lds\n\t"
      "v_mfma_f32_16x16x32_bf16 v[84:87], a[28:31], v[144:147], v[84:87]\n\t"
      "s_add_u32 m0, s41, 0x12400\n\t"
      "s_nop 0\n\t"
      "buffer_load_dwordx4 v212, s[16:19], s51 offen lds\n\t"
      "v_mfma_f32_16x16x32_bf16 v[88:91], a[32:35], v[144:147], v[88:91]\n\t"
      "s_add_u32 m0, s41, 0x12800\n\t"
      "s_nop 0\n\t"
      "buffer_load_dwordx4 v213, s[16:19], s51 offen lds\n\t"
      "s_waitcnt vmcnt(15)\n\t"
      "v_add_u32_e32 v221, 0x18000, v214\n\t"
      "ds_read_b64_tr_b16 a[50:51], v221 offset:0\n\t"
      "v_mfma_f32_16x16x32_bf16 v[92:95], a[36:39], v[144:147], v[92:95]\n\t"
      "ds_read_b64_tr_b16 a[54:55], v221 offset:8\n\t"
      "ds_read_b64_tr_b16 a[58:59], v221 offset:64\n\t"
      "v_mfma_f32_16x16x32_bf16 v[96:99], a[40:43], v[144:147], v[96:99]\n\t"
      "ds_read_b64_tr_b16 a[62:63], v221 offset:72\n\t"
      "ds_read_b64_tr_b16 a[66:67], v221 offset:128\n\t"
      "v_mfma_f32_16x16x32_bf16 v[100:103], a[44:47], v[144:147], v[100:103]\n\t"
      "ds_read_b64_tr_b16 a[70:71], v221 offset:136\n\t"
      "ds_read_b128 v[160:163], v198 offset:12288\n\t"
      "s_waitcnt lgkmcnt(8)\n\t"
      "v_mfma_f32_16x16x32_bf16 v[104:107], a[24:27], v[156:159], v[104:107]\n\t"
      "s_mov_b32 s51, 0x1ec00\n\t"
      "s_add_u32 m0, s41, 0x15000\n\t"
      "s_nop 0\n\t"
      "buffer_load_dwordx4 v211, s[16:19], s51 offen lds\n\t"
      "v_mfma_f32_16x16x32_bf16 v[108:111], a[28:31], v[156:159], v[108:111]\n\t"
      "s_add_u32 m0, s41, 0x15400\n\t"
      "s_nop 0\n\t"
      "buffer_load_dwordx4 v212, s[16:19], s51 offen lds\n\t"
      "v_mfma_f32_16x16x32_bf16 v[112:115], a[32:35], v[156:159], v[112:115]\n\t"
      "s_add_u32 m0, s41, 0x15800\n\t"
      "s_nop 0\n\t"
      "buffer_load_dwordx4 v213, s[16:19], s51 offen lds\n\t"
      "s_waitcnt vmcnt(15)\n\t"
      "v_add_u32_e32 v221, 0x1b000, v214\n\t"
      "ds_read_b64_tr_b16 a[72:73], v221 offset:0\n\t"
      "v_mfma_f32_16x16x32_bf16 v[116:119], a[36:39], v[156:159], v[116:119]\n\t"
      "ds_read_b64_tr_b16 a[76:77], v221 offset:8\n\t"
      "ds_read_b64_tr_b16 a[80:81], v221 offset:64\n\t"
      "v_mfma_f32_16x16x32_bf16 v[120:123], a[40:43], v[156:159], v[120:123]\n\t"
      "ds_read_b64_tr_b16 a[84:85], v221 offset:72\n\t"
      "ds_read_b64_tr_b16 a[88:89], v221 offset:128\n\t"
      "v_mfma_f32_16x16x32_bf16 v[132:135], a[44:47], v[156:159], v[132:135]\n\t"
      "ds_read_b64_tr_b16 a[92:93], v221 offset:136\n\t"
      "ds_read_b128 v[140:143], v199 offset:0\n\t"
      "s_waitcnt lgkmcnt(8)\n\t"
      "v_mfma_f32_16x16x32_bf16 v[80:83], a[48:51], v[148:151], v[80:83]\n\t"
      "s_mov_b32 s51, 0x24000\n\t"
      "s_add_u32 m0, s41, 0x18000\n\t"
      "s_nop 0\n\t"
      "buffer_load_dwordx4 v211, s[16:19], s51 offen lds\n\t"
      "v_mfma_f32_16x16x32_bf16 v[84:87], a[52:55], v[148:151], v[84:87]\n\t"
      "s_add_u32 m0, s41, 0x18400\n\t"
      "s_nop 0\n\t"
      "buffer_load_dwordx4 v212, s[16:19], s51 offen lds\n\t"
      "v_mfma_f32_16x16x32_bf16 v[88:91], a[56:59], v[148:151], v[88:91]\n\t"
      "s_add_u32 m0, s41, 0x18800\n\t"
      "s_nop 0\n\t"
      "buffer_load_dwordx4 v213, s[16:19], s51 offen lds\n\t"
      "s_waitcnt vmcnt(15)\n\t"
      "v_add_u32_e32 v221, 0x1e000, v214\n\t"
      "ds_read_b64_tr_b16 a[74:75], v221 offset:0\n\t"
      "v_mfma_f32_16x16x32_bf16 v[92:95], a[60:63], v[148:151], v[92:95]\n\t"
      "ds_read_b64_tr_b16 a[78:79], v221 offset:8\n\t"
      "ds_read_b64_tr_b16 a[82:83], v221 offset:64\n\t"
      "v_mfma_f32_16x16x32_bf16 v[96:99], a[64:67], v[148:151], v[96:99]\n\t"
      "ds_read_b64_tr_b16 a[86:87], v221 offset:72\n\t"
      "ds_read_b64_tr_b16 a[90:91], v221 offset:128\n\t"
      "v_mfma_f32_16x16x32_bf16 v[100:103], a[68:71], v[148:151], v[100:103]\n\t"
      "ds_read_b64_tr_b16 a[94:95], v221 offset:136\n\t"
      "ds_read_b128 v[152:155], v199 offset:12288\n\t"
      "s_waitcnt lgkmcnt(8)\n\t"
      "v_mfma_f32_16x16x32_bf16 v[104:107], a[48:51], v[160:163], v[104:107]\n\t"
      "s_mov_b32 s51, 0x24c00\n\t"
      "s_add_u32 m0, s41, 0x1b000\n\t"
      "s_nop 0\n\t"
      "buffer_load_dwordx4 v211, s[16:19], s51 offen lds\n\t"
      "v_mfma_f32_16x16x32_bf16 v[108:111], a[52:55], v[160:163], v[108:111]\n\t"
      "s_add_u32 m0, s41, 0x1b400\n\t"
      "s_nop 0\n\t"
      "buffer_load_dwordx4 v212, s[16:19], s51 offen lds\n\t"
      "v_mfma_f32_16x16x32_bf16 v[112:115], a[56:59], v[160:163], v[112:115]\n\t"
      "s_add_u32 m0, s41, 0x1b800\n\t"
      "s_nop 0\n\t"
      "buffer_load_dwordx4 v213, s[16:19], s51 offen lds\n\t"
      "s_waitcnt vmcnt(15)\n\t"
      "v_add_u32_e32 v221, 0x21000, v214\n\t"
      "ds_read_b64_tr_b16 a[96:97], v221 offset:0\n\t"
      "v_mfma_f32_16x16x32_bf16 v[116:119], a[60:63], v[160:163], v[116:119]\n\t"
      "ds_read_b64_tr_b16 a[100:101], v221 offset:8\n\t"
      "ds_read_b64_tr_b16 a[104:105], v221 offset:64\n\t"
      "v_mfma_f32_16x16x32_bf16 v[120:123], a[64:67], v[160:163], v[120:123]\n\t"
      "ds_read_b64_tr_b16 a[108:109], v221 offset:72\n\t"
      "ds_read_b64_tr_b16 a[112:113], v221 offset:128\n\t"
      "v_mfma_f32_16x16x32_bf16 v[132:135], a[68:71], v[160:163], v[132:135]\n\t"
      "ds_read_b64_tr_b16 a[116:117], v221 offset:136\n\t"
      "ds_read_b128 v[144:147], v197 offset:256\n\t"
      "s_waitcnt lgkmcnt(8)\n\t"
      "v_mfma_f32_16x16x32_bf16 v[80:83], a[72:75], v[140:143], v[80:83]\n\t"
      "s_mov_b32 s51, 0x2a000\n\t"
      "s_add_u32 m0, s41, 0x1e000\n\t"
      "s_nop 0\n\t"
      "buffer_load_dwordx4 v211, s[16:19], s51 offen lds\n\t"
      "v_mfma_f32_16x16x32_bf16 v[84:87], a[76:79], v[140:143], v[84:87]\n\t"
      "s_add_u32 m0, s41, 0x1e400\n\t"
      "s_nop 0\n\t"
      "buffer_load_dwordx4 v212, s[16:19], s51 offen lds\n\t"
      "v_mfma_f32_16x16x32_bf16 v[88:91], a[80:83], v[140:143], v[88:91]\n\t"
      "s_add_u32 m0, s41, 0x1e800\n\t"
      "s_nop 0\n\t"
      "buffer_load_dwordx4 v213, s[16:19], s51 offen lds\n\t"
      "s_waitcnt vmcnt(15)\n\t"
      "v_add_u32_e32 v221, 0x24000, v214\n\t"
      "ds_read_b64_tr_b16 a[98:99], v221 offset:0\n\t"
      "v_mfma_f32_16x16x32_bf16 v[92:95], a[84:87], v[140:143], v[92:95]\n\t"
      "ds_read_b64_tr_b16 a[102:103], v221 offset:8\n\t"
      "ds_read_b64_tr_b16 a[106:107], v221 offset:64\n\t"
      "v_mfma_f32_16x16x32_bf16 v[96:99], a[88:91], v[140:143], v[96:99]\n\t"
      "ds_read_b64_tr_b16 a[110:111], v221 offset:72\n\t"
      "ds_read_b64_tr_b16 a[114:115], v221 offset:128\n\t"
      "v_mfma_f32_16x16x32_bf16 v[100:103], a[92:95], v[140:143], v[100:103]\n\t"
      "ds_read_b64_tr_b16 a[118:119], v221 offset:136\n\t"
      "ds_read_b128 v[156:159], v197 offset:12544\n\t"
      "s_waitcnt lgkmcnt(8)\n\t"
      "v_mfma_f32_16x16x32_bf16 v[104:107], a[72:75], v[152:155], v[104:107]\n\t"
      "s_mov_b32 s51, 0x2ac00\n\t"
      "s_add_u32 m0, s41, 0x21000\n\t"
      "s_nop 0\n\t"
      "buffer_load_dwordx4 v211, s[16:19], s51 offen lds\n\t"
      "v_mfma_f32_16x16x32_bf16 v[108:111], a[76:79], v[152:155], v[108:111]\n\t"
      "s_add_u32 m0, s41, 0x21400\n\t"
      "s_nop 0\n\t"
      "buffer_load_dwordx4 v212, s[16:19], s51 offen lds\n\t"
      "v_mfma_f32_16x16x32_bf16 v[112:115], a[80:83], v[152:155], v[112:115]\n\t"
      "s_add_u32 m0, s41, 0x21800\n\t"
      "s_nop 0\n\t"
      "buffer_load_dwordx4 v213, s[16:19], s51 offen lds\n\t"
      "s_waitcnt vmcnt(15)\n\t"
      "v_add_u32_e32 v221, 0x12000, v214\n\t"
      "ds_read_b64_tr_b16 a[120:121], v221 offset:0\n\t"
      "v_mfma_f32_16x16x32_bf16 v[116:119], a[84:87], v[152:155], v[116:119]\n\t"
      "ds_read_b64_tr_b16 a[124:125], v221 offset:8\n\t"
      "ds_read_b64_tr_b16 a[128:129], v221 offset:64\n\t"
      "v_mfma_f32_16x16x32_bf16 v[120:123], a[88:91], v[152:155], v[120:123]\n\t"
      "ds_read_b64_tr_b16 a[132:133], v221 offset:72\n\t"
      "ds_read_b64_tr_b16 a[136:137], v221 offset:128\n\t"
      "v_mfma_f32_16x16x32_bf16 v[132:135], a[92:95], v[152:155], v[132:135]\n\t"
      "ds_read_b64_tr_b16 a[140:141], v221 offset:136\n\t"
      "ds_read_b128 v[148:151], v198 offset:256\n\t"
      "s_waitcnt lgkmcnt(8)\n\t"
      "v_mfma_f32_16x16x32_bf16 v[80:83], a[96:99], v[144:147], v[80:83]\n\t"
      "s_mov_b32 s51, 0x30000\n\t"
      "s_add_u32 m0, s41, 0x24000\n\t"
      "s_nop 0\n\t"
      "buffer_load_dwordx4 v211, s[16:19], s51 offen lds\n\t"
      "v_mfma_f32_16x16x32_bf16 v[84:87], a[100:103], v[144:147], v[84:87]\n\t"
      "s_add_u32 m0, s41, 0x24400\n\t"
      "s_nop 0\n\t"
      "buffer_load_dwordx4 v212, s[16:19], s51 offen lds\n\t"
      "v_mfma_f32_16x16x32_bf16 v[88:91], a[104:107], v[144:147], v[88:91]\n\t"
      "s_add_u32 m0, s41, 0x24800\n\t"
      "s_nop 0\n\t"
      "buffer_load_dwordx4 v213, s[16:19], s51 offen lds\n\t"
      "s_waitcnt vmcnt(15)\n\t"
      "v_add_u32_e32 v221, 0x15000, v214\n\t"
      "ds_read_b64_tr_b16 a[122:123], v221 offset:0\n\t"
      "v_mfma_f32_16x16x32_bf16 v[92:95], a[108:111], v[144:147], v[92:95]\n\t"
      "ds_read_b64_tr_b16 a[126:127], v221 offset:8\n\t"
      "ds_read_b64_tr_b16 a[130:131], v221 offset:64\n\t"
      "v_mfma_f32_16x16x32_bf16 v[96:99], a[112:115], v[144:147], v[96:99]\n\t"
      "ds_read_b64_tr_b16 a[134:135], v221 offset:72\n\t"
      "ds_read_b64_tr_b16 a[138:139], v221 offset:128\n\t"
      "v_mfma_f32_16x16x32_bf16 v[100:103], a[116:119], v[144:147], v[100:103]\n\t"
      "ds_read_b64_tr_b16 a[142:143], v221 offset:136\n\t"
      "ds_read_b128 v[160:163], v198 offset:12544\n\t"
      "s_waitcnt lgkmcnt(8)\n\t"
      "v_mfma_f32_16x16x32_bf16 v[104:107], a[96:99], v[156:159], v[104:107]\n\t"
      "s_mov_b32 s51, 0x30c00\n\t"
      "s_add_u32 m0, s41, 0x12000\n\t"
      "s_nop 0\n\t"
      "buffer_load_dwordx4 v211, s[16:19], s51 offen lds\n\t"
      "v_mfma_f32_16x16x32_bf16 v[108:111], a[100:103], v[156:159], v[108:111]\n\t"
      "s_add_u32 m0, s41, 0x12400\n\t"
      "s_nop 0\n\t"
      "buffer_load_dwordx4 v212, s[16:19], s51 offen lds\n\t"
      "v_mfma_f32_16x16x32_bf16 v[112:115], a[104:107], v[156:159], v[112:115]\n\t"
      "s_add_u32 m0, s41, 0x12800\n\t"
      "s_nop 0\n\t"
      "buffer_load_dwordx4 v213, s[16:19], s51 offen lds\n\t"
      "s_waitcnt vmcnt(15)\n\t"
      "v_add_u32_e32 v221, 0x18000, v214\n\t"
      "ds_read_b64_tr_b16 a[144:145], v221 offset:0\n\t"
      "v_mfma_f32_16x16x32_bf16 v[116:119], a[108:111], v[156:159], v[116:119]\n\t"
      "ds_read_b64_tr_b16 a[148:149], v221 offset:8\n\t"
      "ds_read_b64_tr_b16 a[152:153], v221 offset:64\n\t"
      "v_mfma_f32_16x16x32_bf16 v[120:123], a[112:115], v[156:159], v[120:123]\n\t"
      "ds_read_b64_tr_b16 a[156:157], v221 offset:72\n\t"
      "ds_read_b64_tr_b16 a[160:161], v221 offset:128\n\t"
      "v_mfma_f32_16x16x32_bf16 v[132:135], a[116:119], v[156:159], v[132:135]\n\t"
      "ds_read_b64_tr_b16 a[164:165], v221 offset:136\n\t"
      "ds_read_b128 v[140:143], v199 offset:256\n\t"
      "s_waitcnt lgkmcnt(8)\n\t"
      "v_mfma_f32_16x16x32_bf16 v[80:83], a[120:123], v[148:151], v[80:83]\n\t"
      "s_mov_b32 s51, 0x36000\n\t"
      "s_add_u32 m0, s41, 0x15000\n\t"
      "s_nop 0\n\t"
      "buffer_load_dwordx4 v211, s[16:19], s51 offen lds\n\t"
      "v_mfma_f32_16x16x32_bf16 v[84:87], a[124:127], v[148:151], v[84:87]\n\t"
      "s_add_u32 m0, s41, 0x15400\n\t"
      "s_nop 0\n\t"
      "buffer_load_dwordx4 v212, s[16:19], s51 offen lds\n\t"
      "v_mfma_f32_16x16x32_bf16 v[88:91], a[128:131], v[148:151], v[88:91]\n\t"
      "s_add_u32 m0, s41, 0x15800\n\t"
      "s_nop 0\n\t"
      "buffer_load_dwordx4 v213, s[16:19], s51 offen lds\n\t"
      "s_waitcnt vmcnt(15)\n\t"
      "v_add_u32_e32 v221, 0x1b000, v214\n\t"
      "ds_read_b64_tr_b16 a[146:147], v221 offset:0\n\t"
      "v_mfma_f32_16x16x32_bf16 v[92:95], a[132:135], v[148:151], v[92:95]\n\t"
      "ds_read_b64_tr_b16 a[150:151], v221 offset:8\n\t"
      "ds_read_b64_tr_b16 a[154:155], v221 offset:64\n\t"
      "v_mfma_f32_16x16x32_bf16 v[96:99], a[136:139], v[148:151], v[96:99]\n\t"
      "ds_read_b64_tr_b16 a[158:159], v221 offset:72\n\t"
      "ds_read_b64_tr_b16 a[162:163], v221 offset:128\n\t"
      "v_mfma_f32_16x16x32_bf16 v[100:103], a[140:143], v[148:151], v[100:103]\n\t"
      "ds_read_b64_tr_b16 a[166:167], v221 offset:136\n\t"
      "ds_read_b128 v[152:155], v199 offset:12544\n\t"
      "s_waitcnt lgkmcnt(8)\n\t"
      "v_mfma_f32_16x16x32_bf16 v[104:107], a[120:123], v[160:163], v[104:107]\n\t"
      "s_mov_b32 s51, 0x36c00\n\t"
      "s_add_u32 m0, s41, 0x18000\n\t"
      "s_nop 0\n\t"
      "buffer_load_dwordx4 v211, s[16:19], s51 offen lds\n\t"
      "v_mfma_f32_16x16x32_bf16 v[108:111], a[124:127], v[160:163], v[108:111]\n\t"
      "s_add_u32 m0, s41, 0x18400\n\t"
      "s_nop 0\n\t"
      "buffer_load_dwordx4 v212, s[16:19], s51 offen lds\n\t"
      "v_mfma_f32_16x16x32_bf16 v[112:115], a[128:131], v[160:163], v[112:115]\n\t"
      "s_add_u32 m0, s41, 0x18800\n\t"
      "s_nop 0\n\t"
      "buffer_load_dwordx4 v213, s[16:19], s51 offen lds\n\t"
      "s_waitcnt vmcnt(15)\n\t"
      "v_add_u32_e32 v221, 0x1e000, v214\n\t"
      "ds_read_b64_tr_b16 a[168:169], v221 offset:0\n\t"
      "v_mfma_f32_16x16x32_bf16 v[116:119], a[132:135], v[160:163], v[116:119]\n\t"
      "ds_read_b64_tr_b16 a[172:173], v221 offset:8\n\t"
      "ds_read_b64_tr_b16 a[176:177], v221 offset:64\n\t"
      "v_mfma_f32_16x16x32_bf16 v[120:123], a[136:139], v[160:163], v[120:123]\n\t"
      "ds_read_b64_tr_b16 a[180:181], v221 offset:72\n\t"
      "ds_read_b64_tr_b16 a[184:185], v221 offset:128\n\t"
      "v_mfma_f32_16x16x32_bf16 v[132:135], a[140:143], v[160:163], v[132:135]\n\t"
      "ds_read_b64_tr_b16 a[188:189], v221 offset:136\n\t"
      "ds_read_b128 v[144:147], v196 offset:256\n\t"
      "s_waitcnt lgkmcnt(8)\n\t"
      "v_mfma_f32_16x16x32_bf16 v[80:83], a[144:147], v[140:143], v[80:83]\n\t"
      "s_mov_b32 s51, 0x3c000\n\t"
      "s_add_u32 m0, s41, 0x1b000\n\t"
      "s_nop 0\n\t"
      "buffer_load_dwordx4 v211, s[16:19], s51 offen lds\n\t"
      "v_mfma_f32_16x16x32_bf16 v[84:87], a[148:151], v[140:143], v[84:87]\n\t"
      "s_add_u32 m0, s41, 0x1b400\n\t"
      "s_nop 0\n\t"
      "buffer_load_dwordx4 v212, s[16:19], s51 offen lds\n\t"
      "v_mfma_f32_16x16x32_bf16 v[88:91], a[152:155], v[140:143], v[88:91]\n\t"
      "s_add_u32 m0, s41, 0x1b800\n\t"
      "s_nop 0\n\t"
      "buffer_load_dwordx4 v213, s[16:19], s51 offen lds\n\t"
      "s_waitcnt vmcnt(15)\n\t"
      "v_add_u32_e32 v221, 0x21000, v214\n\t"
      "ds_read_b64_tr_b16 a[170:171], v221 offset:0\n\t"
      "v_mfma_f32_16x16x32_bf16 v[92:95], a[156:159], v[140:143], v[92:95]\n\t"
      "ds_read_b64_tr_b16 a[174:175], v221 offset:8\n\t"
      "ds_read_b64_tr_b16 a[178:179], v221 offset:64\n\t"
      "v_mfma_f32_16x16x32_bf16 v[96:99], a[160:163], v[140:143], v[96:99]\n\t"
      "ds_read_b64_tr_b16 a[182:183], v221 offset:72\n\t"
      "ds_read_b64_tr_b16 a[186:187], v221 offset:128\n\t"
      "v_mfma_f32_16x16x32_bf16 v[100:103], a[164:167], v[140:143], v[100:103]\n\t"
      "ds_read_b64_tr_b16 a[190:191], v221 offset:136\n\t"
      "ds_read_b128 v[156:159], v196 offset:12544\n\t"
      "s_waitcnt lgkmcnt(8)\n\t"
      "v_mfma_f32_16x16x32_bf16 v[104:107], a[144:147], v[152:155], v[104:107]\n\t"
      "s_mov_b32 s51, 0x3cc00\n\t"
      "s_add_u32 m0, s41, 0x1e000\n\t"
      "s_nop 0\n\t"
      "buffer_load_dwordx4 v211, s[16:19], s51 offen lds\n\t"
      "v_mfma_f32_16x16x32_bf16 v[108:111], a[148:151], v[152:155], v[108:111]\n\t"
      "s_add_u32 m0, s41, 0x1e400\n\t"
      "s_nop 0\n\t"
      "buffer_load_dwordx4 v212, s[16:19], s51 offen lds\n\t"
      "v_mfma_f32_16x16x32_bf16 v[112:115], a[152:155], v[152:155], v[112:115]\n\t"
      "s_add_u32 m0, s41, 0x1e800\n\t"
      "s_nop 0\n\t"
      "buffer_load_dwordx4 v213, s[16:19], s51 offen lds\n\t"
      "s_waitcnt vmcnt(15)\n\t"
      "v_add_u32_e32 v221, 0x24000, v214\n\t"
      "ds_read_b64_tr_b16 a[192:193], v221 offset:0\n\t"
      "v_mfma_f32_16x16x32_bf16 v[116:119], a[156:159], v[152:155], v[116:119]\n\t"
      "ds_read_b64_tr_b16 a[196:197], v221 offset:8\n\t"
      "ds_read_b64_tr_b16 a[200:201], v221 offset:64\n\t"
      "v_mfma_f32_16x16x32_bf16 v[120:123], a[160:163], v[152:155], v[120:123]\n\t"
      "ds_read_b64_tr_b16 a[204:205], v221 offset:72\n\t"
      "ds_read_b64_tr_b16 a[208:209], v221 offset:128\n\t"
      "v_mfma_f32_16x16x32_bf16 v[132:135], a[164:167], v[152:155], v[132:135]\n\t"
      "ds_read_b64_tr_b16 a[212:213], v221 offset:136\n\t"
      "ds_read_b128 v[148:151], v198 offset:512\n\t"
      "s_waitcnt lgkmcnt(8)\n\t"
      "v_mfma_f32_16x16x32_bf16 v[80:83], a[168:171], v[144:147], v[80:83]\n\t"
      "s_mov_b32 s51, 0x42000\n\t"
      "s_add_u32 m0, s41, 0x21000\n\t"
      "s_nop 0\n\t"
      "buffer_load_dwordx4 v211, s[16:19], s51 offen lds\n\t"
      "v_mfma_f32_16x16x32_bf16 v[84:87], a[172:175], v[144:147], v[84:87]\n\t"
      "s_add_u32 m0, s41, 0x21400\n\t"
      "s_nop 0\n\t"
      "buffer_load_dwordx4 v212, s[16:19], s51 offen lds\n\t"
      "v_mfma_f32_16x16x32_bf16 v[88:91], a[176:179], v[144:147], v[88:91]\n\t"
      "s_add_u32 m0, s41, 0x21800\n\t"
      "s_nop 0\n\t"
      "buffer_load_dwordx4 v213, s[16:19], s51 offen lds\n\t"
      "s_waitcnt vmcnt(15)\n\t"
      "v_add_u32_e32 v221, 0x12000, v214\n\t"
      "ds_read_b64_tr_b16 a[194:195], v221 offset:0\n\t"
      "v_mfma_f32_16x16x32_bf16 v[92:95], a[180:183], v[144:147], v[92:95]\n\t"
      "ds_read_b64_tr_b16 a[198:199], v221 offset:8\n\t"
      "ds_read_b64_tr_b16 a[202:203], v221 offset:64\n\t"
      "v_mfma_f32_16x16x32_bf16 v[96:99], a[184:187], v[144:147], v[96:99]\n\t"
      "ds_read_b64_tr_b16 a[206:207], v221 offset:72\n\t"
      "ds_read_b64_tr_b16 a[210:211], v221 offset:128\n\t"
      "v_mfma_f32_16x16x32_bf16 v[100:103], a[188:191], v[144:147], v[100:103]\n\t"
      "ds_read_b64_tr_b16 a[214:215], v221 offset:136\n\t"
      "ds_read_b128 v[160:163], v198 offset:12800\n\t"
      "s_waitcnt lgkmcnt(8)\n\t"
      "v_mfma_f32_16x16x32_bf16 v[104:107], a[168:171], v[156:159], v[104:107]\n\t"
      "s_mov_b32 s51, 0x42c00\n\t"
      "s_add_u32 m0, s41, 0x24000\n\t"
      "s_nop 0\n\t"
      "buffer_load_dwordx4 v211, s[16:19], s51 offen lds\n\t"
      "v_mfma_f32_16x16x32_bf16 v[108:111], a[172:175], v[156:159], v[108:111]\n\t"
      "s_add_u32 m0, s41, 0x24400\n\t"
      "s_nop 0\n\t"
      "buffer_load_dwordx4 v212, s[16:19], s51 offen lds\n\t"
      "v_mfma_f32_16x16x32_bf16 v[112:115], a[176:179], v[156:159], v[112:115]\n\t"
      "s_add_u32 m0, s41, 0x24800\n\t"
      "s_nop 0\n\t"
      "buffer_load_dwordx4 v213, s[16:19], s51 offen lds\n\t"
      "s_waitcnt vmcnt(15)\n\t"
      "v_add_u32_e32 v221, 0x15000, v214\n\t"
      "ds_read_b64_tr_b16 a[216:217], v221 offset:0\n\t"
      "v_mfma_f32_16x16x32_bf16 v[116:119], a[180:183], v[156:159], v[116:119]\n\t"
      "ds_read_b64_tr_b16 a[220:221], v221 offset:8\n\t"
      "ds_read_b64_tr_b16 a[224:225], v221 offset:64\n\t"
      "v_mfma_f32_16x16x32_bf16 v[120:123], a[184:187], v[156:159], v[120:123]\n\t"
      "ds_read_b64_tr_b16 a[228:229], v221 offset:72\n\t"
      "ds_read_b64_tr_b16 a[232:233], v221 offset:128\n\t"
      "v_mfma_f32_16x16x32_bf16 v[132:135], a[188:191], v[156:159], v[132:135]\n\t"
      "ds_read_b64_tr_b16 a[236:237], v221 offset:136\n\t"
      "ds_read_b128 v[140:143], v199 offset:512\n\t"
      "s_waitcnt lgkmcnt(8)\n\t"
      "v_mfma_f32_16x16x32_bf16 v[80:83], a[192:195], v[148:151], v[80:83]\n\t"
      "s_cmp_eq_u32 s94, 0\n\t"
      "s_cbranch_scc0 .Lc1_%=\n\t"
      "s_add_u32 m0, s41, 0x6000\n\t"
      "s_add_u32 s51, s40, 0x600000\n\t"
      "buffer_load_dwordx4 v208, s[20:23], s51 offen lds\n\t"
      "buffer_load_dwordx4 v208, s[20:23], s51 offen offset:1024 lds\n\t"
      "buffer_load_dwordx4 v208, s[20:23], s51 offen offset:2048 lds\n\t"
      ".Lc1_%=:\n\t"
      "v_mfma_f32_16x16x32_bf16 v[84:87], a[196:199], v[148:151], v[84:87]\n\t"
      "v_mfma_f32_16x16x32_bf16 v[88:91], a[200:203], v[148:151], v[88:91]\n\t"
      "s_waitcnt vmcnt(15)\n\t"
      "s_cmp_eq_u32 s94, 1\n\t"
      "s_cbranch_scc0 .Lwpw19_1_%=\n\t"
      "s_waitcnt vmcnt(12)\n\t"
      ".Lwpw19_1_%=:\n\t"
      "v_add_u32_e32 v221, 0x18000, v214\n\t"
      "ds_read_b64_tr_b16 a[218:219], v221 offset:0\n\t"
      "v_mfma_f32_16x16x32_bf16 v[92:95], a[204:207], v[148:151], v[92:95]\n\t"
      "ds_read_b64_tr_b16 a[222:223], v221 offset:8\n\t"
      "ds_read_b64_tr_b16 a[226:227], v221 offset:64\n\t"
      "v_mfma_f32_16x16x32_bf16 v[96:99], a[208:211], v[148:151], v[96:99]\n\t"
      "ds_read_b64_tr_b16 a[230:231], v221 offset:72\n\t"
      "ds_read_b64_tr_b16 a[234:235], v221 offset:128\n\t"
      "v_mfma_f32_16x16x32_bf16 v[100:103], a[212:215], v[148:151], v[100:103]\n\t"
      "ds_read_b64_tr_b16 a[238:239], v221 offset:136\n\t"
      "ds_read_b128 v[152:155], v199 offset:12800\n\t"
      "s_waitcnt lgkmcnt(14)\n\t"
      "v_mfma_f32_16x16x32_bf16 v[104:107], a[192:195], v[160:163], v[104:107]\n\t"
      "s_cmp_eq_u32 s94, 0\n\t"
      "s_cbranch_scc0 .Lc2_%=\n\t"
      "s_add_u32 m0, s41, 0x9000\n\t"
      "s_add_u32 s51, s40, 0x600000\n\t"
      "buffer_load_dwordx4 v209, s[20:23], s51 offen lds\n\t"
      "buffer_load_dwordx4 v209, s[20:23], s51 offen offset:1024 lds\n\t"
      "buffer_load_dwordx4 v209, s[20:23], s51 offen offset:2048 lds\n\t"
      ".Lc2_%=:\n\t"
      "v_mfma_f32_16x16x32_bf16 v[108:111], a[196:199], v[160:163], v[108:111]\n\t"
      "v_mfma_f32_16x16x32_bf16 v[112:115], a[200:203], v[160:163], v[112:115]\n\t"
      "s_waitcnt vmcnt(15)\n\t"
      "s_cmp_eq_u32 s94, 1\n\t"
      "s_cbranch_scc0 .Lwpw20_1_%=\n\t"
      "s_waitcnt vmcnt(9)\n\t"
      ".Lwpw20_1_%=:\n\t"
      "v_add_u32_e32 v221, 0x1b000, v214\n\t"
      "ds_read_b64_tr_b16 a[240:241], v221 offset:0\n\t"
      "v_mfma_f32_16x16x32_bf16 v[116:119], a[204:207], v[160:163], v[116:119]\n\t"
      "ds_read_b64_tr_b16 a[244:245], v221 offset:8\n\t"
      "ds_read_b64_tr_b16 a[248:249], v221 offset:64\n\t"
      "v_mfma_f32_16x16x32_bf16 v[120:123], a[208:211], v[160:163], v[120:123]\n\t"
      "ds_read_b64_tr_b16 a[252:253], v221 offset:72\n\t"
      "ds_read_b64_tr_b16 v[164:165], v221 offset:128\n\t"
      "v_mfma_f32_16x16x32_bf16 v[132:135], a[212:215], v[160:163], v[132:135]\n\t"
      "ds_read_b64_tr_b16 v[168:169], v221 offset:136\n\t"
      "ds_read_b128 v[144:147], v196 offset:512\n\t"
      "s_waitcnt lgkmcnt(8)\n\t"
      "v_mfma_f32_16x16x32_bf16 v[80:83], a[216:219], v[140:143], v[80:83]\n\t"
      "s_cmp_eq_u32 s94, 0\n\t"
      "s_cbranch_scc0 .Lc3_%=\n\t"
      "s_add_u32 m0, s41, 0xc000\n\t"
      "s_add_u32 s51, s40, 0xc00000\n\t"
      "buffer_load_dwordx4 v208, s[20:23], s51 offen lds\n\t"
      "buffer_load_dwordx4 v208, s[20:23], s51 offen offset:1024 lds\n\t"
      "buffer_load_dwordx4 v208, s[20:23], s51 offen offset:2048 lds\n\t"
      ".Lc3_%=:\n\t"
      "v_mfma_f32_16x16x32_bf16 v[84:87], a[220:223], v[140:143], v[84:87]\n\t"
      "s_cmp_eq_u32 s94, 1\n\t"
      "s_cbranch_scc0 .Lc4_%=\n\t"
      "s_add_u32 m0, s41, 0xc000\n\t"
      "s_add_u32 s51, s40, 0xc00000\n\t"
      "buffer_load_dwordx4 v208, s[20:23], s51 offen lds\n\t"
      "buffer_load_dwordx4 v208, s[20:23], s51 offen offset:1024 lds\n\t"
      "buffer_load_dwordx4 v208, s[20:23], s51 offen offset:2048 lds\n\t"
      ".Lc4_%=:\n\t"
      "v_mfma_f32_16x16x32_bf16 v[88:91], a[224:227], v[140:143], v[88:91]\n\t"
      "s_waitcnt vmcnt(15)\n\t"
      "s_cmp_eq_u32 s94, 1\n\t"
      "s_cbranch_scc0 .Lwpw21_1_%=\n\t"
      "s_waitcnt vmcnt(9)\n\t"
      ".Lwpw21_1_%=:\n\t"
      "v_add_u32_e32 v221, 0x1e000, v214\n\t"
      "ds_read_b64_tr_b16 a[242:243], v221 offset:0\n\t"
      "v_mfma_f32_16x16x32_bf16 v[92:95], a[228:231], v[140:143], v[92:95]\n\t"
      "ds_read_b64_tr_b16 a[246:247], v221 offset:8\n\t"
      "ds_read_b64_tr_b16 a[250:251], v221 offset:64\n\t"
      "v_mfma_f32_16x16x32_bf16 v[96:99], a[232:235], v[140:143], v[96:99]\n\t"
      "ds_read_b64_tr_b16 a[254:255], v221 offset:72\n\t"
      "ds_read_b64_tr_b16 v[166:167], v221 offset:128\n\t"
      "v_mfma_f32_16x16x32_bf16 v[100:103], a[236:239], v[140:143], v[100:103]\n\t"
      "ds_read_b64_tr_b16 v[170:171], v221 offset:136\n\t"
      "ds_read_b128 v[156:159], v196 offset:12800\n\t"
      "s_waitcnt lgkmcnt(14)\n\t"
      "v_mfma_f32_16x16x32_bf16 v[104:107], a[216:219], v[152:155], v[104:107]\n\t"
      "s_cmp_eq_u32 s94, 0\n\t"
      "s_cbranch_scc0 .Lc5_%=\n\t"
      "s_add_u32 m0, s41, 0xf000\n\t"
      "s_add_u32 s51, s40, 0xc00000\n\t"
      "buffer_load_dwordx4 v209, s[20:23], s51 offen lds\n\t"
      "buffer_load_dwordx4 v209, s[20:23], s51 offen offset:1024 lds\n\t"
      "buffer_load_dwordx4 v209, s[20:23], s51 offen offset:2048 lds\n\t"
      ".Lc5_%=:\n\t"
      "v_mfma_f32_16x16x32_bf16 v[108:111], a[220:223], v[152:155], v[108:111]\n\t"
      "s_cmp_eq_u32 s94, 1\n\t"
      "s_cbranch_scc0 .Lc6_%=\n\t"
      "s_add_u32 m0, s41, 0xf000\n\t"
      "s_add_u32 s51, s40, 0xc00000\n\t"
      "buffer_load_dwordx4 v209, s[20:23], s51 offen lds\n\t"
      "buffer_load_dwordx4 v209, s[20:23], s51 offen offset:1024 lds\n\t"
      "buffer_load_dwordx4 v209, s[20:23], s51 offen offset:2048 lds\n\t"
      ".Lc6_%=:\n\t"
      "v_mfma_f32_16x16x32_bf16 v[112:115], a[224:227], v[152:155], v[112:115]\n\t"
      "s_waitcnt vmcnt(15)\n\t"
      "s_cmp_eq_u32 s94, 1\n\t"
      "s_cbranch_scc0 .Lwpw22_1_%=\n\t"
      "s_waitcnt vmcnt(9)\n\t"
      ".Lwpw22_1_%=:\n\t"
      "v_add_u32_e32 v221, 0x21000, v214\n\t"
      "ds_read_b64_tr_b16 v[172:173], v221 offset:0\n\t"
      "v_mfma_f32_16x16x32_bf16 v[116:119], a[228:231], v[152:155], v[116:119]\n\t"
      "ds_read_b64_tr_b16 v[176:177], v221 offset:8\n\t"
      "ds_read_b64_tr_b16 v[180:181], v221 offset:64\n\t"
      "ds_read_b64_tr_b16 v[184:185], v221 offset:72\n\t"
      "v_mfma_f32_16x16x32_bf16 v[120:123], a[232:235], v[152:155], v[120:123]\n\t"
      "ds_read_b64_tr_b16 v[188:189], v221 offset:128\n\t"
      "ds_read_b64_tr_b16 v[192:193], v221 offset:136\n\t"
      "v_mfma_f32_16x16x32_bf16 v[132:135], a[236:239], v[152:155], v[132:135]\n\t"
      "ds_read_b128 v[148:151], v197 offset:512\n\t"
      "ds_read_b128 v[160:163], v197 offset:12800\n\t"
      "s_waitcnt lgkmcnt(9)\n\t"
      "v_mfma_f32_16x16x32_bf16 v[80:83], a[240:243], v[144:147], v[80:83]\n\t"
      "buffer_load_dwordx2 v[240:241], v245, s[24:27], 0 offen sc1\n\t"
      "v_mfma_f32_16x16x32_bf16 v[84:87], a[244:247], v[144:147], v[84:87]\n\t"
      "v_mfma_f32_16x16x32_bf16 v[88:91], a[248:251], v[144:147], v[88:91]\n\t"
      "s_waitcnt vmcnt(13)\n\t"
      "s_cmp_eq_u32 s94, 1\n\t"
      "s_cbranch_scc0 .Lwpw23_1_%=\n\t"
      "s_waitcnt vmcnt(7)\n\t"
      ".Lwpw23_1_%=:\n\t"
      "v_add_u32_e32 v221, 0x24000, v214\n\t"
      "ds_read_b64_tr_b16 v[174:175], v221 offset:0\n\t"
      "v_mfma_f32_16x16x32_bf16 v[92:95], a[252:255], v[144:147], v[92:95]\n\t"
      "ds_read_b64_tr_b16 v[178:179], v221 offset:8\n\t"
      "ds_read_b64_tr_b16 v[182:183], v221 offset:64\n\t"
      "v_mfma_f32_16x16x32_bf16 v[96:99], v[164:167], v[144:147], v[96:99]\n\t"
      "ds_read_b64_tr_b16 v[186:187], v221 offset:72\n\t"
      "ds_read_b64_tr_b16 v[190:191], v221 offset:128\n\t"
      "v_mfma_f32_16x16x32_bf16 v[100:103], v[168:171], v[144:147], v[100:103]\n\t"
      "ds_read_b64_tr_b16 v[194:195], v221 offset:136\n\t"
      "s_memrealtime s[80:81]\n\t"
      "v_add_u32_e32 v196, s46, v200\n\t"
      "v_add_u32_e32 v197, s46, v201\n\t"
      "v_add_u32_e32 v198, s46, v202\n\t"
      "v_add_u32_e32 v199, s46, v203\n\t"
      "s_waitcnt vmcnt(63)\n\t"
      "s_cmp_eq_u32 s94, 0\n\t"
      "s_cbranch_scc0 .Lwpx2_0_%=\n\t"
      "s_waitcnt vmcnt(10)\n\t"
      ".Lwpx2_0_%=:\n\t"
      "s_waitcnt lgkmcnt(6)\n\t"
      "s_barrier\n\t"
      "v_mfma_f32_16x16x32_bf16 v[104:107], a[240:243], v[156:159], v[104:107]\n\t"
      "ds_read_b128 v[32:35], v196 offset:0\n\t"
      "v_mfma_f32_16x16x32_bf16 v[108:111], a[244:247], v[156:159], v[108:111]\n\t"
      "ds_read_b128 v[36:39], v197 offset:0\n\t"
      "v_mfma_f32_16x16x32_bf16 v[112:115], a[248:251], v[156:159], v[112:115]\n\t"
      "ds_read_b128 v[40:43], v198 offset:0\n\t"
      "v_mfma_f32_16x16x32_bf16 v[116:119], a[252:255], v[156:159], v[116:119]\n\t"
      "ds_read_b128 v[44:47], v199 offset:0\n\t"
      "v_mfma_f32_16x16x32_bf16 v[120:123], v[164:167], v[156:159], v[120:123]\n\t"
      "ds_read_b128 v[48:51], v197 offset:256\n\t"
      "v_mfma_f32_16x16x32_bf16 v[132:135], v[168:171], v[156:159], v[132:135]\n\t"
      "s_waitcnt lgkmcnt(5)\n\t"
      "s_mov_b32 s82, s80\n\t"
      "s_mov_b32 s83, 0\n\t"
      "s_mov_b32 s86, 80\n\t"
      "s_mov_b32 s84, 92\n\t"
      "v_mfma_f32_16x16x32_bf16 v[80:83], v[172:175], v[148:151], v[80:83]\n\t"
      "ds_read_b128 v[52:55], v198 offset:256\n\t"
      "v_mfma_f32_16x16x32_bf16 v[84:87], v[176:179], v[148:151], v[84:87]\n\t"
      "ds_read_b128 v[56:59], v199 offset:256\n\t"
      "v_mfma_f32_16x16x32_bf16 v[88:91], v[180:183], v[148:151], v[88:91]\n\t"
      "ds_read_b128 v[60:63], v196 offset:256\n\t"
      "v_mfma_f32_16x16x32_bf16 v[92:95], v[184:187], v[148:151], v[92:95]\n\t"
      "ds_read_b128 v[64:67], v198 offset:512\n\t"
      "v_mfma_f32_16x16x32_bf16 v[96:99], v[188:191], v[148:151], v[96:99]\n\t"
      "ds_read_b128 v[68:71], v199 offset:512\n\t"
      "v_mfma_f32_16x16x32_bf16 v[100:103], v[192:195], v[148:151], v[100:103]\n\t"
      "ds_read_b128 v[72:75], v196 offset:512\n\t"
      "v_mfma_f32_16x16x32_bf16 v[104:107], v[172:175], v[160:163], v[104:107]\n\t"
      "ds_read_b128 v[76:79], v197 offset:512\n\t"
      "v_mfma_f32_16x16x32_bf16 v[108:111], v[176:179], v[160:163], v[108:111]\n\t"
      "v_mfma_f32_16x16x32_bf16 v[112:115], v[180:183], v[160:163], v[112:115]\n\t"
      "v_mfma_f32_16x16x32_bf16 v[116:119], v[184:187], v[160:163], v[116:119]\n\t"
      "v_mfma_f32_16x16x32_bf16 v[120:123], v[188:191], v[160:163], v[120:123]\n\t"
      "v_mfma_f32_16x16x32_bf16 v[132:135], v[192:195], v[160:163], v[132:135]\n\t"
      "s_waitcnt lgkmcnt(8)\n\t"
      "v_mfma_f32_16x16x32_bf16 v[128:131], a[0:3], v[32:35], v[8:11]\n\t"
      "v_cvt_pk_bf16_f32 v80, v80, v81\n\t"
      "v_cvt_pk_bf16_f32 v81, v82, v83\n\t"
      "v_mfma_f32_16x16x32_bf16 v[128:131], a[24:27], v[36:39], v[128:131]\n\t"
      "v_cvt_pk_bf16_f32 v136, v120, v121\n\t"
      "v_cvt_pk_bf16_f32 v137, v122, v123\n\t"
      "v_mfma_f32_16x16x32_bf16 v[128:131], a[48:51], v[40:43], v[128:131]\n\t"
      "v_cvt_pk_bf16_f32 v138, v132, v133\n\t"
      "v_mfma_f32_16x16x32_bf16 v[128:131], a[72:75], v[44:47], v[128:131]\n\t"
      "v_cvt_pk_bf16_f32 v139, v134, v135\n\t"
      "s_waitcnt lgkmcnt(4)\n\t"
      "v_mfma_f32_16x16x32_bf16 v[128:131], a[96:99], v[48:51], v[128:131]\n\t"
      "ds_write_b128 v219, v[136:139] offset:12288\n\t"
      "v_mfma_f32_16x16x32_bf16 v[128:131], a[120:123], v[52:55], v[128:131]\n\t"
      "v_cvt_pk_bf16_f32 v82, v84, v85\n\t"
      "v_cvt_pk_bf16_f32 v83, v86, v87\n\t"
      "v_mfma_f32_16x16x32_bf16 v[128:131], a[144:147], v[56:59], v[128:131]\n\t"
      "v_cvt_pk_bf16_f32 v88, v88, v89\n\t"
      "v_cvt_pk_bf16_f32 v89, v90, v91\n\t"
      "v_mfma_f32_16x16x32_bf16 v[128:131], a[168:171], v[60:63], v[128:131]\n\t"
      "ds_write_b128 v217, v[80:83] offset:0\n\t"
      "v_cvt_pk_bf16_f32 v90, v92, v93\n\t"
      "s_waitcnt lgkmcnt(2)\n\t"
      "v_mfma_f32_16x16x32_bf16 v[128:131], a[192:195], v[64:67], v[128:131]\n\t"
      "v_cvt_pk_bf16_f32 v91, v94, v95\n\t"
      "v_cvt_pk_bf16_f32 v96, v96, v97\n\t"
      "v_mfma_f32_16x16x32_bf16 v[128:131], a[216:219], v[68:71], v[128:131]\n\t"
      "ds_write_b128 v218, v[88:91] offset:0\n\t"
      "v_cvt_pk_bf16_f32 v97, v98, v99\n\t"
      "v_mfma_f32_16x16x32_bf16 v[128:131], a[240:243], v[72:75], v[128:131]\n\t"
      "v_cvt_pk_bf16_f32 v98, v100, v101\n\t"
      "v_cvt_pk_bf16_f32 v99, v102, v103\n\t"
      "v_mfma_f32_16x16x32_bf16 v[128:131], v[172:175], v[76:79], v[128:131]\n\t"
      "s_cmp_gt_i32 s83, 1\n\t"
      "s_cbranch_scc1 .LslpP2_%=\n\t"
      ".LretP2_%=:\n\t"
      "v_cvt_pk_bf16_f32 v104, v104, v105\n\t"
      "v_cvt_pk_bf16_f32 v105, v106, v107\n\t"
      "v_mfma_f32_16x16x32_bf16 v[132:135], a[4:7], v[32:35], v[12:15]\n\t"
      "ds_write_b128 v219, v[96:99] offset:0\n\t"
      "v_cvt_pk_bf16_f32 v106, v108, v109\n\t"
      "v_mfma_f32_16x16x32_bf16 v[132:135], a[28:31], v[36:39], v[132:135]\n\t"
      "v_cvt_pk_bf16_f32 v107, v110, v111\n\t"
      "v_cvt_pk_bf16_f32 v112, v112, v113\n\t"
      "v_mfma_f32_16x16x32_bf16 v[132:135], a[52:55], v[40:43], v[132:135]\n\t"
      "v_cvt_pk_bf16_f32 v136, v128, v129\n\t"
      "v_mfma_f32_16x16x32_bf16 v[132:135], a[76:79], v[44:47], v[132:135]\n\t"
      "v_cvt_pk_bf16_f32 v137, v130, v131\n\t"
      "v_mfma_f32_16x16x32_bf16 v[132:135], a[100:103], v[48:51], v[132:135]\n\t"
      "ds_write_b128 v217, v[104:107] offset:12288\n\t"
      "v_cvt_pk_bf16_f32 v113, v114, v115\n\t"
      "s_add_u32 s48, s47, 0x6000\n\t"
      "v_mfma_f32_16x16x32_bf16 v[132:135], a[124:127], v[52:55], v[132:135]\n\t"
      "v_cvt_pk_bf16_f32 v114, v116, v117\n\t"
      "v_cvt_pk_bf16_f32 v115, v118, v119\n\t"
      "s_cmp_eq_u32 s48, s50\n\t"
      "s_cselect_b32 s48, s41, s48\n\t"
      "v_mfma_f32_16x16x32_bf16 v[132:135], a[148:151], v[56:59], v[132:135]\n\t"
      "ds_write_b128 v218, v[112:115] offset:12288\n\t"
      "v_mfma_f32_16x16x32_bf16 v[132:135], a[172:175], v[60:63], v[132:135]\n\t"
      "v_add_u32_e32 v217, s46, v204\n\t"
      "v_mfma_f32_16x16x32_bf16 v[132:135], a[196:199], v[64:67], v[132:135]\n\t"
      "v_add_u32_e32 v218, s46, v205\n\t"
      "v_mfma_f32_16x16x32_bf16 v[132:135], a[220:223], v[68:71], v[132:135]\n\t"
      "v_readlane_b32 s73, v248, 3\n\t"
      "v_add_u32_e32 v219, s46, v206\n\t"
      "v_mfma_f32_16x16x32_bf16 v[132:135], a[244:247], v[72:75], v[132:135]\n\t"
      "v_readlane_b32 s72, v248, 0\n\t"
      "v_mfma_f32_16x16x32_bf16 v[132:135], v[176:179], v[76:79], v[132:135]\n\t"
      "s_waitcnt lgkmcnt(0)\n\t"
      "s_barrier\n\t"
      "v_mfma_f32_16x16x32_bf16 v[128:131], a[8:11], v[32:35], v[16:19]\n\t"
      "ds_read_b128 v[140:143], v220 offset:0\n\t"
      "v_mfma_f32_16x16x32_bf16 v[128:131], a[32:35], v[36:39], v[128:131]\n\t"
      "ds_read_b128 v[144:147], v220 offset:1024\n\t"
      "s_memrealtime s[80:81]\n\t"
      "v_mfma_f32_16x16x32_bf16 v[128:131], a[56:59], v[40:43], v[128:131]\n\t"
      "v_cvt_pk_bf16_f32 v138, v132, v133\n\t"
      "s_mov_b32 m0, s45\n\t"
      "v_mfma_f32_16x16x32_bf16 v[128:131], a[80:83], v[44:47], v[128:131]\n\t"
      "v_cvt_pk_bf16_f32 v139, v134, v135\n\t"
      "v_mfma_f32_16x16x32_bf16 v[128:131], a[104:107], v[48:51], v[128:131]\n\t"
      "ds_write_b128 v217, v[136:139] offset:0\n\t"
      "v_mfma_f32_16x16x32_bf16 v[128:131], a[128:131], v[52:55], v[128:131]\n\t"
      "s_waitcnt lgkmcnt(2)\n\t"
      "buffer_store_dwordx4 v[140:143], v208, s[28:31], s72 offen sc1\n\t"
      "v_mfma_f32_16x16x32_bf16 v[128:131], a[152:155], v[56:59], v[128:131]\n\t"
      "ds_read_b128 v[148:151], v220 offset:2048\n\t"
      "v_mfma_f32_16x16x32_bf16 v[128:131], a[176:179], v[60:63], v[128:131]\n\t"
      "ds_read_b128 v[152:155], v220 offset:12288\n\t"
      "v_mfma_f32_16x16x32_bf16 v[128:131], a[200:203], v[64:67], v[128:131]\n\t"
      "ds_read_b128 v[156:159], v220 offset:13312\n\t"
      "v_mfma_f32_16x16x32_bf16 v[128:131], a[224:227], v[68:71], v[128:131]\n\t"
      "s_waitcnt lgkmcnt(5)\n\t"
      "buffer_load_dwordx4 v208, s[20:23], s73 offen lds\n\t"
      "v_mfma_f32_16x16x32_bf16 v[128:131], a[248:251], v[72:75], v[128:131]\n\t"
      "ds_read_b128 v[160:163], v220 offset:14336\n\t"
      "v_mfma_f32_16x16x32_bf16 v[128:131], v[180:183], v[76:79], v[128:131]\n\t"
      "v_mfma_f32_16x16x32_bf16 v[132:135], a[12:15], v[32:35], v[20:23]\n\t"
      "v_mfma_f32_16x16x32_bf16 v[132:135], a[36:39], v[36:39], v[132:135]\n\t"
      "s_waitcnt lgkmcnt(5)\n\t"
      "buffer_store_dwordx4 v[144:147], v208, s[28:31], s72 offen offset:1024 sc1\n\t"
      "v_mfma_f32_16x16x32_bf16 v[132:135], a[60:63], v[40:43], v[132:135]\n\t"
      "v_cvt_pk_bf16_f32 v136, v128, v129\n\t"
      "v_mfma_f32_16x16x32_bf16 v[132:135], a[84:87], v[44:47], v[132:135]\n\t"
      "v_cvt_pk_bf16_f32 v137, v130, v131\n\t"
      "v_mfma_f32_16x16x32_bf16 v[132:135], a[108:111], v[48:51], v[132:135]\n\t"
      "v_mfma_f32_16x16x32_bf16 v[132:135], a[132:135], v[52:55], v[132:135]\n\t"
      "s_waitcnt lgkmcnt(5)\n\t"
      "buffer_load_dwordx4 v208, s[20:23], s73 offen offset:1024 lds\n\t"
      "v_mfma_f32_16x16x32_bf16 v[132:135], a[156:159], v[56:59], v[132:135]\n\t"
      "v_mfma_f32_16x16x32_bf16 v[132:135], a[180:183], v[60:63], v[132:135]\n\t"
      "v_mfma_f32_16x16x32_bf16 v[132:135], a[204:207], v[64:67], v[132:135]\n\t"
      "v_mfma_f32_16x16x32_bf16 v[132:135], a[228:231], v[68:71], v[132:135]\n\t"
      "s_waitcnt lgkmcnt(3)\n\t"
      "buffer_store_dwordx4 v[148:151], v208, s[28:31], s72 offen offset:2048 sc1\n\t"
      "v_mfma_f32_16x16x32_bf16 v[132:135], a[252:255], v[72:75], v[132:135]\n\t"
      "v_readlane_b32 s72, v249, 0\n\t"
      "v_mfma_f32_16x16x32_bf16 v[132:135], v[184:187], v[76:79], v[132:135]\n\t"
      "s_waitcnt vmcnt(12)\n\t"
      "s_barrier\n\t"
      "v_mfma_f32_16x16x32_bf16 v[128:131], a[16:19], v[32:35], v[24:27]\n\t"
      "ds_read_b128 v[80:83], v196 offset:12288\n\t"
      "v_mfma_f32_16x16x32_bf16 v[128:131], a[40:43], v[36:39], v[128:131]\n\t"
      "s_waitcnt lgkmcnt(4)\n\t"
      "buffer_load_dwordx4 v208, s[20:23], s73 offen offset:2048 lds\n\t"
      "v_mfma_f32_16x16x32_bf16 v[128:131], a[64:67], v[40:43], v[128:131]\n\t"
      "ds_read_b128 v[84:87], v197 offset:12288\n\t"
      "v_cvt_pk_bf16_f32 v138, v132, v133\n\t"
      "v_readlane_b32 s73, v249, 3\n\t"
      "s_add_u32 m0, s45, 0x3000\n\t"
      "v_mfma_f32_16x16x32_bf16 v[128:131], a[88:91], v[44:47], v[128:131]\n\t"
      "ds_read_b128 v[88:91], v198 offset:12288\n\t"
      "v_cvt_pk_bf16_f32 v139, v134, v135\n\t"
      "v_mfma_f32_16x16x32_bf16 v[128:131], a[112:115], v[48:51], v[128:131]\n\t"
      "ds_write_b128 v218, v[136:139] offset:0\n\t"
      "v_mfma_f32_16x16x32_bf16 v[128:131], a[136:139], v[52:55], v[128:131]\n\t"
      "s_waitcnt lgkmcnt(6)\n\t"
      "buffer_store_dwordx4 v[152:155], v209, s[28:31], s72 offen sc1\n\t"
      "s_waitcnt vmcnt(7)\n\t"
      "v_subrev_u32_e32 v242, s63, v240\n\t"
      "v_xor_b32_e32 v241, s53, v241\n\t"
      "v_mfma_f32_16x16x32_bf16 v[128:131], a[160:163], v[56:59], v[128:131]\n\t"
      "ds_read_b128 v[92:95], v199 offset:12288\n\t"
      "v_lshrrev_b32_e32 v240, 13, v242\n\t"
      "v_or_b32_e32 v240, v240, v241\n\t"
      "v_mfma_f32_16x16x32_bf16 v[128:131], a[184:187], v[60:63], v[128:131]\n\t"
      "ds_read_b128 v[96:99], v197 offset:12544\n\t"
      "v_mfma_f32_16x16x32_bf16 v[128:131], a[208:211], v[64:67], v[128:131]\n\t"
      "v_cmp_ne_u32_e32 vcc, 0, v240\n\t"
      "v_and_b32_e32 v242, 15, v242\n\t"
      "v_mfma_f32_16x16x32_bf16 v[128:131], a[232:235], v[68:71], v[128:131]\n\t"
      "s_waitcnt lgkmcnt(8)\n\t"
      "buffer_load_dwordx4 v209, s[20:23], s73 offen lds\n\t"
      "v_mfma_f32_16x16x32_bf16 v[128:131], v[164:167], v[72:75], v[128:131]\n\t"
      "ds_read_b128 v[100:103], v198 offset:12544\n\t"
      "s_cbranch_vccnz .Lgs_%=\n\t"
      ".Lgr_%=:\n\t"
      "ds_write_b32 v246, v242\n\t"
      "v_mfma_f32_16x16x32_bf16 v[128:131], v[188:191], v[76:79], v[128:131]\n\t"
      "ds_read_b128 v[104:107], v199 offset:12544\n\t"
      "v_mfma_f32_16x16x32_bf16 v[132:135], a[20:23], v[32:35], v[28:31]\n\t"
      "ds_read_b128 v[108:111], v196 offset:12544\n\t"
      "v_mfma_f32_16x16x32_bf16 v[132:135], a[44:47], v[36:39], v[132:135]\n\t"
      "s_waitcnt lgkmcnt(11)\n\t"
      "buffer_store_dwordx4 v[156:159], v209, s[28:31], s72 offen offset:1024 sc1\n\t"
      "v_mfma_f32_16x16x32_bf16 v[132:135], a[68:71], v[40:43], v[132:135]\n\t"
      "ds_read_b128 v[112:115], v198 offset:12800\n\t"
      "v_cvt_pk_bf16_f32 v136, v128, v129\n\t"
      "v_add_u32_e32 v220, s46, v207\n\t"
      "v_mfma_f32_16x16x32_bf16 v[132:135], a[92:95], v[44:47], v[132:135]\n\t"
      "ds_read_b128 v[116:119], v199 offset:12800\n\t"
      "v_cvt_pk_bf16_f32 v137, v130, v131\n\t"
      "s_sub_u32 s84, s84, 4\n\t"
      "s_max_i32 s84, s84, s86\n\t"
      "v_mfma_f32_16x16x32_bf16 v[132:135], a[116:119], v[48:51], v[132:135]\n\t"
      "s_sub_u32 s83, s82, s80\n\t"
      "v_mfma_f32_16x16x32_bf16 v[132:135], a[140:143], v[52:55], v[132:135]\n\t"
      "s_waitcnt lgkmcnt(13)\n\t"
      "buffer_load_dwordx4 v209, s[20:23], s73 offen offset:1024 lds\n\t"
      "s_max_i32 s83, s83, 0\n\t"
      "s_add_u32 s82, s80, s83\n\t"
      "v_mfma_f32_16x16x32_bf16 v[132:135], a[164:167], v[56:59], v[132:135]\n\t"
      "ds_read_b128 v[120:123], v196 offset:12800\n\t"
      "v_mfma_f32_16x16x32_bf16 v[132:135], a[188:191], v[60:63], v[132:135]\n\t"
      "ds_read_b128 v[124:127], v197 offset:12800\n\t"
      "v_mfma_f32_16x16x32_bf16 v[132:135], a[212:215], v[64:67], v[132:135]\n\t"
      "s_add_u32 s44, s44, 1\n\t"
      "v_mfma_f32_16x16x32_bf16 v[132:135], a[236:239], v[68:71], v[132:135]\n\t"
      "s_waitcnt lgkmcnt(14)\n\t"
      "buffer_store_dwordx4 v[160:163], v209, s[28:31], s72 offen offset:2048 sc1\n\t"
      "s_add_u32 s82, s82, s84\n\t"
      "v_mfma_f32_16x16x32_bf16 v[132:135], v[168:171], v[72:75], v[132:135]\n\t"
      "s_add_u32 s85, s85, 1\n\t"
      "v_mfma_f32_16x16x32_bf16 v[132:135], v[192:195], v[76:79], v[132:135]\n\t"
      "s_waitcnt lgkmcnt(9)\n\t"
      "v_mfma_f32_16x16x32_bf16 v[128:131], a[0:3], v[80:83], v[8:11]\n\t"
      "v_mfma_f32_16x16x32_bf16 v[128:131], a[24:27], v[84:87], v[128:131]\n\t"
      "v_readlane_b32 s72, v248, s44\n\t"
      "v_mfma_f32_16x16x32_bf16 v[128:131], a[48:51], v[88:91], v[128:131]\n\t"
      "v_cvt_pk_bf16_f32 v138, v132, v133\n\t"
      "v_mfma_f32_16x16x32_bf16 v[128:131], a[72:75], v[92:95], v[128:131]\n\t"
      "v_cvt_pk_bf16_f32 v139, v134, v135\n\t"
      "s_waitcnt lgkmcnt(4)\n\t"
      "v_mfma_f32_16x16x32_bf16 v[128:131], a[96:99], v[96:99], v[128:131]\n\t"
      "ds_write_b128 v219, v[136:139] offset:0\n\t"
      "v_mfma_f32_16x16x32_bf16 v[128:131], a[120:123], v[100:103], v[128:131]\n\t"
      "v_mfma_f32_16x16x32_bf16 v[128:131], a[144:147], v[104:107], v[128:131]\n\t"
      "buffer_load_dwordx4 v209, s[20:23], s73 offen offset:2048 lds\n\t"
      "v_mfma_f32_16x16x32_bf16 v[128:131], a[168:171], v[108:111], v[128:131]\n\t"
      "v_add_u32_e32 v196, s47, v200\n\t"
      "s_waitcnt lgkmcnt(1)\n\t"
      "v_mfma_f32_16x16x32_bf16 v[128:131], a[192:195], v[112:115], v[128:131]\n\t"
      "v_add_u32_e32 v197, s47, v201\n\t"
      "v_mfma_f32_16x16x32_bf16 v[128:131], a[216:219], v[116:119], v[128:131]\n\t"
      "v_add_u32_e32 v198, s47, v202\n\t"
      "v_mfma_f32_16x16x32_bf16 v[128:131], a[240:243], v[120:123], v[128:131]\n\t"
      "v_add_u32_e32 v199, s47, v203\n\t"
      "v_mfma_f32_16x16x32_bf16 v[128:131], v[172:175], v[124:127], v[128:131]\n\t"
      "s_cmp_gt_i32 s83, 1\n\t"
      "s_cbranch_scc1 .LslpP3_%=\n\t"
      ".LretP3_%=:\n\t"
      "s_waitcnt vmcnt(16) lgkmcnt(0)\n\t"
      "s_barrier\n\t"
      "v_mfma_f32_16x16x32_bf16 v[132:135], a[4:7], v[80:83], v[12:15]\n\t"
      "ds_read_b128 v[140:143], v220 offset:0\n\t"
      "v_mfma_f32_16x16x32_bf16 v[132:135], a[28:31], v[84:87], v[132:135]\n\t"
      "ds_read_b128 v[240:243], v247 offset:0\n\t"
      "v_mfma_f32_16x16x32_bf16 v[132:135], a[52:55], v[88:91], v[132:135]\n\t"
      "v_cvt_pk_bf16_f32 v136, v128, v129\n\t"
      "v_mfma_f32_16x16x32_bf16 v[132:135], a[76:79], v[92:95], v[132:135]\n\t"
      "ds_read_b128 v[32:35], v196 offset:0\n\t"
      "v_cvt_pk_bf16_f32 v137, v130, v131\n\t"
      "v_mfma_f32_16x16x32_bf16 v[132:135], a[100:103], v[96:99], v[132:135]\n\t"
      "v_mfma_f32_16x16x32_bf16 v[132:135], a[124:127], v[100:103], v[132:135]\n\t"
      "v_mfma_f32_16x16x32_bf16 v[132:135], a[148:151], v[104:107], v[132:135]\n\t"
      "ds_read_b128 v[36:39], v197 offset:0\n\t"
      "v_mfma_f32_16x16x32_bf16 v[132:135], a[172:175], v[108:111], v[132:135]\n\t"
      "v_mfma_f32_16x16x32_bf16 v[132:135], a[196:199], v[112:115], v[132:135]\n\t"
      "s_waitcnt lgkmcnt(2)\n\t"
      "v_add3_u32 v240, v240, v241, v242\n\t"
      "v_mfma_f32_16x16x32_bf16 v[132:135], a[220:223], v[116:119], v[132:135]\n\t"
      "ds_read_b128 v[40:43], v198 offset:0\n\t"
      "v_mfma_f32_16x16x32_bf16 v[132:135], a[244:247], v[120:123], v[132:135]\n\t"
      "v_add_u32_e32 v240, v240, v243\n\t"
      "v_mfma_f32_16x16x32_bf16 v[132:135], v[176:179], v[124:127], v[132:135]\n\t"
      "s_waitcnt lgkmcnt(4)\n\t"
      "buffer_store_dwordx4 v[140:143], v208, s[28:31], s72 offen sc1\n\t"
      "v_mfma_f32_16x16x32_bf16 v[128:131], a[8:11], v[80:83], v[16:19]\n\t"
      "ds_read_b128 v[44:47], v199 offset:0\n\t"
      "v_mfma_f32_16x16x32_bf16 v[128:131], a[32:35], v[84:87], v[128:131]\n\t"
      "v_add_u32_dpp v241, v240, v240 quad_perm:[1,0,3,2] row_mask:0xf bank_mask:0xf\n\t"
      "s_memrealtime s[80:81]\n\t"
      "v_mfma_f32_16x16x32_bf16 v[128:131], a[56:59], v[88:91], v[128:131]\n\t"
      "v_cvt_pk_bf16_f32 v138, v132, v133\n\t"
      "v_mfma_f32_16x16x32_bf16 v[128:131], a[80:83], v[92:95], v[128:131]\n\t"
      "v_cvt_pk_bf16_f32 v139, v134, v135\n\t"
      "v_mfma_f32_16x16x32_bf16 v[128:131], a[104:107], v[96:99], v[128:131]\n\t"
      "ds_write_b128 v217, v[136:139] offset:12288\n\t"
      "v_add_u32_dpp v240, v241, v241 quad_perm:[2,3,0,1] row_mask:0xf bank_mask:0xf\n\t"
      "v_mfma_f32_16x16x32_bf16 v[128:131], a[128:131], v[100:103], v[128:131]\n\t"
      "s_mov_b32 m0, s46\n\t"
      "v_mfma_f32_16x16x32_bf16 v[128:131], a[152:155], v[104:107], v[128:131]\n\t"
      "v_cmp_le_u32_e64 s[76:77], 18, v240\n\t"
      "v_cmp_ge_u32_e64 s[78:79], 14, v240\n\t"
      "v_mfma_f32_16x16x32_bf16 v[128:131], a[176:179], v[108:111], v[128:131]\n\t"
      "ds_read_b128 v[48:51], v197 offset:256\n\t"
      "v_mfma_f32_16x16x32_bf16 v[128:131], a[200:203], v[112:115], v[128:131]\n\t"
      "v_cmp_lt_u32_e32 vcc, 63, v240\n\t"
      "s_mov_b32 s52, s76\n\t"
      "v_mfma_f32_16x16x32_bf16 v[128:131], a[224:227], v[116:119], v[128:131]\n\t"
      "ds_read_b128 v[52:55], v198 offset:256\n\t"
      "v_mfma_f32_16x16x32_bf16 v[128:131], a[248:251], v[120:123], v[128:131]\n\t"
      "s_and_b32 s65, s76, s77\n\t"
      "s_and_b32 s78, s78, s79\n\t"
      "v_mfma_f32_16x16x32_bf16 v[128:131], v[180:183], v[124:127], v[128:131]\n\t"
      "v_mfma_f32_16x16x32_bf16 v[132:135], a[12:15], v[80:83], v[20:23]\n\t"
      "ds_read_b128 v[56:59], v199 offset:256\n\t"
      "s_or_b32 s65, s65, s78\n\t"
      "s_cmp_lg_u64 vcc, 0\n\t"
      "s_cselect_b32 s65, 0, s65\n\t"
      "s_cselect_b32 s64, 0xfffc0000, 0\n\t"
      "s_cselect_b32 s49, 31, 17\n\t"
      "v_mfma_f32_16x16x32_bf16 v[132:135], a[36:39], v[84:87], v[132:135]\n\t"
      "v_and_b32_e32 v240, s52, v252\n\t"
      "v_and_b32_e32 v241, s65, v252\n\t"
      "v_mfma_f32_16x16x32_bf16 v[132:135], a[60:63], v[88:91], v[132:135]\n\t"
      "v_cvt_pk_bf16_f32 v136, v128, v129\n\t"
      "v_mfma_f32_16x16x32_bf16 v[132:135], a[84:87], v[92:95], v[132:135]\n\t"
      "v_cvt_pk_bf16_f32 v137, v130, v131\n\t"
      "ds_read_b128 v[144:147], v220 offset:1024\n\t"
      "v_mfma_f32_16x16x32_bf16 v[132:135], a[108:111], v[96:99], v[132:135]\n\t"
      "v_cmp_ne_u32_e64 s[76:77], 0, v240\n\t"
      "v_cmp_ne_u32_e64 s[78:79], 0, v241\n\t"
      "v_mfma_f32_16x16x32_bf16 v[132:135], a[132:135], v[100:103], v[132:135]\n\t"
      "v_mov_b32_e32 v243, 76\n\t"
      "v_mfma_f32_16x16x32_bf16 v[132:135], a[156:159], v[104:107], v[132:135]\n\t"
      "ds_read_b128 v[60:63], v196 offset:256\n\t"
      "v_min_u32_e32 v242, 78, v250\n\t"
      "v_mfma_f32_16x16x32_bf16 v[132:135], a[180:183], v[108:111], v[132:135]\n\t"
      "s_xor_b32 s75, s75, s76\n\t"
      "s_xor_b32 s74, s74, s76\n\t"
      "v_mfma_f32_16x16x32_bf16 v[132:135], a[204:207], v[112:115], v[132:135]\n\t"
      "s_and_b32 s76, s75, s78\n\t"
      "s_or_b32 s76, s76, s64\n\t"
      "v_cndmask_b32_e64 v249, v248, v251, s[76:77]\n\t"
      "v_mfma_f32_16x16x32_bf16 v[132:135], a[228:231], v[116:119], v[132:135]\n\t"
      "ds_read_b128 v[64:67], v198 offset:512\n\t"
      "v_mfma_f32_16x16x32_bf16 v[132:135], a[252:255], v[120:123], v[132:135]\n\t"
      "s_and_b32 s76, s74, s78\n\t"
      "s_or_b32 s76, s76, s64\n\t"
      "v_cndmask_b32_e64 v248, v248, v251, s[76:77]\n\t"
      "v_mfma_f32_16x16x32_bf16 v[132:135], v[184:187], v[124:127], v[132:135]\n\t"
      "s_waitcnt lgkmcnt(2)\n\t"
      "buffer_store_dwordx4 v[144:147], v208, s[28:31], s72 offen offset:1024 sc1\n\t"
      "v_mfma_f32_16x16x32_bf16 v[128:131], a[16:19], v[80:83], v[24:27]\n\t"
      "ds_read_b128 v[68:71], v199 offset:512\n\t"
      "v_mfma_f32_16x16x32_bf16 v[128:131], a[40:43], v[84:87], v[128:131]\n\t"
      "s_lshr_b32 s74, s78, 3\n\t"
      "s_or_b32 s76, s74, s78\n\t"
      "v_mfma_f32_16x16x32_bf16 v[128:131], a[64:67], v[88:91], v[128:131]\n\t"
      "v_cvt_pk_bf16_f32 v138, v132, v133\n\t"
      "v_readlane_b32 s73, v248, s85\n\t"
      "v_mfma_f32_16x16x32_bf16 v[128:131], a[88:91], v[92:95], v[128:131]\n\t"
      "v_cvt_pk_bf16_f32 v139, v134, v135\n\t"
      "v_mfma_f32_16x16x32_bf16 v[128:131], a[112:115], v[96:99], v[128:131]\n\t"
      "ds_write_b128 v218, v[136:139] offset:12288\n\t"
      "v_mfma_f32_16x16x32_bf16 v[128:131], a[136:139], v[100:103], v[128:131]\n\t"
      "v_cndmask_b32_e64 v250, v250, v242, s[76:77]\n\t"
      "s_and_b32 s78, s74, s78\n\t"
      "v_mfma_f32_16x16x32_bf16 v[128:131], a[160:163], v[104:107], v[128:131]\n\t"
      "s_and_b32 s78, s78, 0x3fff\n\t"
      "v_cndmask_b32_e64 v250, v250, v243, s[78:79]\n\t"
      "v_mfma_f32_16x16x32_bf16 v[128:131], a[184:187], v[108:111], v[128:131]\n\t"
      "ds_read_b128 v[148:151], v220 offset:2048\n\t"
      "v_mfma_f32_16x16x32_bf16 v[128:131], a[208:211], v[112:115], v[128:131]\n\t"
      "buffer_load_dwordx4 v208, s[20:23], s73 offen lds\n\t"
      "v_mfma_f32_16x16x32_bf16 v[128:131], a[232:235], v[116:119], v[128:131]\n\t"
      "ds_read_b128 v[72:75], v196 offset:512\n\t"
      "v_mfma_f32_16x16x32_bf16 v[128:131], v[164:167], v[120:123], v[128:131]\n\t"
      "v_readlane_b32 s86, v250, s44\n\t"
      "v_mfma_f32_16x16x32_bf16 v[128:131], v[188:191], v[124:127], v[128:131]\n\t"
      "v_mfma_f32_16x16x32_bf16 v[132:135], a[20:23], v[80:83], v[28:31]\n\t"
      "ds_read_b128 v[76:79], v197 offset:512\n\t"
      "v_mfma_f32_16x16x32_bf16 v[132:135], a[44:47], v[84:87], v[132:135]\n\t"
      "buffer_load_dwordx4 v208, s[20:23], s73 offen offset:1024 lds\n\t"
      "v_mfma_f32_16x16x32_bf16 v[132:135], a[68:71], v[88:91], v[132:135]\n\t"
      "v_cvt_pk_bf16_f32 v136, v128, v129\n\t"
      "v_mfma_f32_16x16x32_bf16 v[132:135], a[92:95], v[92:95], v[132:135]\n\t"
      "v_cvt_pk_bf16_f32 v137, v130, v131\n\t"
      "s_sub_u32 s84, s84, 4\n\t"
      "s_max_i32 s84, s84, s86\n\t"
      "v_mfma_f32_16x16x32_bf16 v[132:135], a[116:119], v[96:99], v[132:135]\n\t"
      "s_sub_u32 s83, s82, s80\n\t"
      "v_mfma_f32_16x16x32_bf16 v[132:135], a[140:143], v[100:103], v[132:135]\n\t"
      "s_max_i32 s83, s83, 0\n\t"
      "s_add_u32 s82, s80, s83\n\t"
      "v_mfma_f32_16x16x32_bf16 v[132:135], a[164:167], v[104:107], v[132:135]\n\t"
      "s_mov_b32 s45, s46\n\t"
      "v_mfma_f32_16x16x32_bf16 v[132:135], a[188:191], v[108:111], v[132:135]\n\t"
      "s_mov_b32 s46, s47\n\t"
      "v_mfma_f32_16x16x32_bf16 v[132:135], a[212:215], v[112:115], v[132:135]\n\t"
      "s_mov_b32 s47, s48\n\t"
      "v_mfma_f32_16x16x32_bf16 v[132:135], a[236:239], v[116:119], v[132:135]\n\t"
      "s_add_u32 s82, s82, s84\n\t"
      "v_mfma_f32_16x16x32_bf16 v[132:135], v[168:171], v[120:123], v[132:135]\n\t"
      "s_cmp_lg_u32 s44, s49\n\t"
      "v_mfma_f32_16x16x32_bf16 v[132:135], v[192:195], v[124:127], v[132:135]\n\t"
      "s_waitcnt lgkmcnt(2)\n\t"
      "buffer_store_dwordx4 v[148:151], v208, s[28:31], s72 offen offset:2048 sc1\n\t"
      ".p2align 6\n\t"
      ".Lloop_%=:\n\t"
      "s_waitcnt lgkmcnt(12)\n\t"
      "v_mfma_f32_16x16x32_bf16 v[128:131], a[0:3], v[32:35], v[8:11]\n\t"
      "v_mfma_f32_16x16x32_bf16 v[128:131], a[24:27], v[36:39], v[128:131]\n\t"
      "v_readlane_b32 s72, v249, s44\n\t"
      "v_mfma_f32_16x16x32_bf16 v[128:131], a[48:51], v[40:43], v[128:131]\n\t"
      "v_cvt_pk_bf16_f32 v138, v132, v133\n\t"
      "v_mfma_f32_16x16x32_bf16 v[128:131], a[72:75], v[44:47], v[128:131]\n\t"
      "v_cvt_pk_bf16_f32 v139, v134, v135\n\t"
      "s_waitcnt lgkmcnt(6)\n\t"
      "v_mfma_f32_16x16x32_bf16 v[128:131], a[96:99], v[48:51], v[128:131]\n\t"
      "ds_write_b128 v219, v[136:139] offset:12288\n\t"
      "v_mfma_f32_16x16x32_bf16 v[128:131], a[120:123], v[52:55], v[128:131]\n\t"
      "v_mfma_f32_16x16x32_bf16 v[128:131], a[144:147], v[56:59], v[128:131]\n\t"
      "buffer_load_dwordx4 v208, s[20:23], s73 offen offset:2048 lds\n\t"
      "v_mfma_f32_16x16x32_bf16 v[128:131], a[168:171], v[60:63], v[128:131]\n\t"
      "s_waitcnt lgkmcnt(1)\n\t"
      "v_mfma_f32_16x16x32_bf16 v[128:131], a[192:195], v[64:67], v[128:131]\n\t"
      "v_add_u32_e32 v217, s46, v204\n\t"
      "v_mfma_f32_16x16x32_bf16 v[128:131], a[216:219], v[68:71], v[128:131]\n\t"
      "v_add_u32_e32 v218, s46, v205\n\t"
      "v_mfma_f32_16x16x32_bf16 v[128:131], a[240:243], v[72:75], v[128:131]\n\t"
      "v_add_u32_e32 v219, s46, v206\n\t"
      "v_mfma_f32_16x16x32_bf16 v[128:131], v[172:175], v[76:79], v[128:131]\n\t"
      "s_cmp_gt_i32 s83, 1\n\t"
      "s_cbranch_scc1 .LslpA_%=\n\t"
      ".LretA_%=:\n\t"
      "s_waitcnt vmcnt(18) lgkmcnt(0)\n\t"
      "s_barrier\n\t"
      "v_mfma_f32_16x16x32_bf16 v[132:135], a[4:7], v[32:35], v[12:15]\n\t"
      "ds_read_b128 v[140:143], v220 offset:12288\n\t"
      "v_mfma_f32_16x16x32_bf16 v[132:135], a[28:31], v[36:39], v[132:135]\n\t"
      "v_mfma_f32_16x16x32_bf16 v[132:135], a[52:55], v[40:43], v[132:135]\n\t"
      "v_cvt_pk_bf16_f32 v136, v128, v129\n\t"
      "v_mfma_f32_16x16x32_bf16 v[132:135], a[76:79], v[44:47], v[132:135]\n\t"
      "ds_read_b128 v[80:83], v196 offset:12288\n\t"
      "v_cvt_pk_bf16_f32 v137, v130, v131\n\t"
      "v_mfma_f32_16x16x32_bf16 v[132:135], a[100:103], v[48:51], v[132:135]\n\t"
      "s_add_u32 s48, s47, 0x6000\n\t"
      "v_mfma_f32_16x16x32_bf16 v[132:135], a[124:127], v[52:55], v[132:135]\n\t"
      "s_cmp_eq_u32 s48, s50\n\t"
      "s_cselect_b32 s48, s41, s48\n\t"
      "v_mfma_f32_16x16x32_bf16 v[132:135], a[148:151], v[56:59], v[132:135]\n\t"
      "ds_read_b128 v[84:87], v197 offset:12288\n\t"
      "v_mfma_f32_16x16x32_bf16 v[132:135], a[172:175], v[60:63], v[132:135]\n\t"
      "v_readlane_b32 s73, v249, s85\n\t"
      "v_mfma_f32_16x16x32_bf16 v[132:135], a[196:199], v[64:67], v[132:135]\n\t"
      "v_mfma_f32_16x16x32_bf16 v[132:135], a[220:223], v[68:71], v[132:135]\n\t"
      "ds_read_b128 v[88:91], v198 offset:12288\n\t"
      "v_mfma_f32_16x16x32_bf16 v[132:135], a[244:247], v[72:75], v[132:135]\n\t"
      "v_mfma_f32_16x16x32_bf16 v[132:135], v[176:179], v[76:79], v[132:135]\n\t"
      "s_waitcnt lgkmcnt(3)\n\t"
      "buffer_store_dwordx4 v[140:143], v209, s[28:31], s72 offen sc1\n\t"
      "v_mfma_f32_16x16x32_bf16 v[128:131], a[8:11], v[32:35], v[16:19]\n\t"
      "ds_read_b128 v[92:95], v199 offset:12288\n\t"
      "v_mfma_f32_16x16x32_bf16 v[128:131], a[32:35], v[36:39], v[128:131]\n\t"
      "s_memrealtime s[80:81]\n\t"
      "v_mfma_f32_16x16x32_bf16 v[128:131], a[56:59], v[40:43], v[128:131]\n\t"
      "v_cvt_pk_bf16_f32 v138, v132, v133\n\t"
      "v_mfma_f32_16x16x32_bf16 v[128:131], a[80:83], v[44:47], v[128:131]\n\t"
      "v_cvt_pk_bf16_f32 v139, v134, v135\n\t"
      "v_mfma_f32_16x16x32_bf16 v[128:131], a[104:107], v[48:51], v[128:131]\n\t"
      "ds_write_b128 v217, v[136:139] offset:0\n\t"
      "v_mfma_f32_16x16x32_bf16 v[128:131], a[128:131], v[52:55], v[128:131]\n\t"
      "s_add_u32 m0, s45, 0x3000\n\t"
      "v_mfma_f32_16x16x32_bf16 v[128:131], a[152:155], v[56:59], v[128:131]\n\t"
      "v_mfma_f32_16x16x32_bf16 v[128:131], a[176:179], v[60:63], v[128:131]\n\t"
      "ds_read_b128 v[96:99], v197 offset:12544\n\t"
      "v_mfma_f32_16x16x32_bf16 v[128:131], a[200:203], v[64:67], v[128:131]\n\t"
      "v_mfma_f32_16x16x32_bf16 v[128:131], a[224:227], v[68:71], v[128:131]\n\t"
      "ds_read_b128 v[100:103], v198 offset:12544\n\t"
      "v_mfma_f32_16x16x32_bf16 v[128:131], a[248:251], v[72:75], v[128:131]\n\t"
      "v_mfma_f32_16x16x32_bf16 v[128:131], v[180:183], v[76:79], v[128:131]\n\t"
      "buffer_load_dwordx4 v209, s[20:23], s73 offen lds\n\t"
      "v_mfma_f32_16x16x32_bf16 v[132:135], a[12:15], v[32:35], v[20:23]\n\t"
      "ds_read_b128 v[104:107], v199 offset:12544\n\t"
      "v_mfma_f32_16x16x32_bf16 v[132:135], a[36:39], v[36:39], v[132:135]\n\t"
      "v_mfma_f32_16x16x32_bf16 v[132:135], a[60:63], v[40:43], v[132:135]\n\t"
      "v_cvt_pk_bf16_f32 v136, v128, v129\n\t"
      "v_mfma_f32_16x16x32_bf16 v[132:135], a[84:87], v[44:47], v[132:135]\n\t"
      "v_cvt_pk_bf16_f32 v137, v130, v131\n\t"
      "ds_read_b128 v[144:147], v220 offset:13312\n\t"
      "v_mfma_f32_16x16x32_bf16 v[132:135], a[108:111], v[48:51], v[132:135]\n\t"
      "v_mfma_f32_16x16x32_bf16 v[132:135], a[132:135], v[52:55], v[132:135]\n\t"
      "v_mfma_f32_16x16x32_bf16 v[132:135], a[156:159], v[56:59], v[132:135]\n\t"
      "ds_read_b128 v[108:111], v196 offset:12544\n\t"
      "v_mfma_f32_16x16x32_bf16 v[132:135], a[180:183], v[60:63], v[132:135]\n\t"
      "v_mfma_f32_16x16x32_bf16 v[132:135], a[204:207], v[64:67], v[132:135]\n\t"
      "v_mfma_f32_16x16x32_bf16 v[132:135], a[228:231], v[68:71], v[132:135]\n\t"
      "ds_read_b128 v[112:115], v198 offset:12800\n\t"
      "v_mfma_f32_16x16x32_bf16 v[132:135], a[252:255], v[72:75], v[132:135]\n\t"
      "v_mfma_f32_16x16x32_bf16 v[132:135], v[184:187], v[76:79], v[132:135]\n\t"
      "s_waitcnt lgkmcnt(2)\n\t"
      "buffer_store_dwordx4 v[144:147], v209, s[28:31], s72 offen offset:1024 sc1\n\t"
      "v_mfma_f32_16x16x32_bf16 v[128:131], a[16:19], v[32:35], v[24:27]\n\t"
      "ds_read_b128 v[116:119], v199 offset:12800\n\t"
      "v_mfma_f32_16x16x32_bf16 v[128:131], a[40:43], v[36:39], v[128:131]\n\t"
      "v_mfma_f32_16x16x32_bf16 v[128:131], a[64:67], v[40:43], v[128:131]\n\t"
      "v_cvt_pk_bf16_f32 v138, v132, v133\n\t"
      "v_mfma_f32_16x16x32_bf16 v[128:131], a[88:91], v[44:47], v[128:131]\n\t"
      "v_cvt_pk_bf16_f32 v139, v134, v135\n\t"
      "v_mfma_f32_16x16x32_bf16 v[128:131], a[112:115], v[48:51], v[128:131]\n\t"
      "ds_write_b128 v218, v[136:139] offset:0\n\t"
      "v_mfma_f32_16x16x32_bf16 v[128:131], a[136:139], v[52:55], v[128:131]\n\t"
      "v_mfma_f32_16x16x32_bf16 v[128:131], a[160:163], v[56:59], v[128:131]\n\t"
      "v_mfma_f32_16x16x32_bf16 v[128:131], a[184:187], v[60:63], v[128:131]\n\t"
      "ds_read_b128 v[148:151], v220 offset:14336\n\t"
      "v_mfma_f32_16x16x32_bf16 v[128:131], a[208:211], v[64:67], v[128:131]\n\t"
      "v_mfma_f32_16x16x32_bf16 v[128:131], a[232:235], v[68:71], v[128:131]\n\t"
      "ds_read_b128 v[120:123], v196 offset:12800\n\t"
      "v_mfma_f32_16x16x32_bf16 v[128:131], v[164:167], v[72:75], v[128:131]\n\t"
      "v_mfma_f32_16x16x32_bf16 v[128:131], v[188:191], v[76:79], v[128:131]\n\t"
      "buffer_load_dwordx4 v209, s[20:23], s73 offen offset:1024 lds\n\t"
      "v_mfma_f32_16x16x32_bf16 v[132:135], a[20:23], v[32:35], v[28:31]\n\t"
      "ds_read_b128 v[124:127], v197 offset:12800\n\t"
      "v_mfma_f32_16x16x32_bf16 v[132:135], a[44:47], v[36:39], v[132:135]\n\t"
      "v_mfma_f32_16x16x32_bf16 v[132:135], a[68:71], v[40:43], v[132:135]\n\t"
      "v_cvt_pk_bf16_f32 v136, v128, v129\n\t"
      "v_add_u32_e32 v220, s46, v207\n\t"
      "v_mfma_f32_16x16x32_bf16 v[132:135], a[92:95], v[44:47], v[132:135]\n\t"
      "v_cvt_pk_bf16_f32 v137, v130, v131\n\t"
      "s_sub_u32 s84, s84, 4\n\t"
      "s_max_i32 s84, s84, s86\n\t"
      "v_mfma_f32_16x16x32_bf16 v[132:135], a[116:119], v[48:51], v[132:135]\n\t"
      "s_sub_u32 s83, s82, s80\n\t"
      "v_mfma_f32_16x16x32_bf16 v[132:135], a[140:143], v[52:55], v[132:135]\n\t"
      "s_max_i32 s83, s83, 0\n\t"
      "s_add_u32 s82, s80, s83\n\t"
      "v_mfma_f32_16x16x32_bf16 v[132:135], a[164:167], v[56:59], v[132:135]\n\t"
      "v_mfma_f32_16x16x32_bf16 v[132:135], a[188:191], v[60:63], v[132:135]\n\t"
      "v_mfma_f32_16x16x32_bf16 v[132:135], a[212:215], v[64:67], v[132:135]\n\t"
      "s_add_u32 s44, s44, 1\n\t"
      "v_mfma_f32_16x16x32_bf16 v[132:135], a[236:239], v[68:71], v[132:135]\n\t"
      "s_add_u32 s82, s82, s84\n\t"
      "v_mfma_f32_16x16x32_bf16 v[132:135], v[168:171], v[72:75], v[132:135]\n\t"
      "s_add_u32 s85, s85, 1\n\t"
      "v_mfma_f32_16x16x32_bf16 v[132:135], v[192:195], v[76:79], v[132:135]\n\t"
      "s_waitcnt lgkmcnt(2)\n\t"
      "buffer_store_dwordx4 v[148:151], v209, s[28:31], s72 offen offset:2048 sc1\n\t"
      "s_waitcnt lgkmcnt(12)\n\t"
      "v_mfma_f32_16x16x32_bf16 v[128:131], a[0:3], v[80:83], v[8:11]\n\t"
      "v_mfma_f32_16x16x32_bf16 v[128:131], a[24:27], v[84:87], v[128:131]\n\t"
      "v_readlane_b32 s72, v248, s44\n\t"
      "v_mfma_f32_16x16x32_bf16 v[128:131], a[48:51], v[88:91], v[128:131]\n\t"
      "v_cvt_pk_bf16_f32 v138, v132, v133\n\t"
      "v_mfma_f32_16x16x32_bf16 v[128:131], a[72:75], v[92:95], v[128:131]\n\t"
      "v_cvt_pk_bf16_f32 v139, v134, v135\n\t"
      "s_waitcnt lgkmcnt(6)\n\t"
      "v_mfma_f32_16x16x32_bf16 v[128:131], a[96:99], v[96:99], v[128:131]\n\t"
      "ds_write_b128 v219, v[136:139] offset:0\n\t"
      "v_mfma_f32_16x16x32_bf16 v[128:131], a[120:123], v[100:103], v[128:131]\n\t"
      "v_mfma_f32_16x16x32_bf16 v[128:131], a[144:147], v[104:107], v[128:131]\n\t"
      "buffer_load_dwordx4 v209, s[20:23], s73 offen offset:2048 lds\n\t"
      "v_mfma_f32_16x16x32_bf16 v[128:131], a[168:171], v[108:111], v[128:131]\n\t"
      "v_add_u32_e32 v196, s47, v200\n\t"
      "s_waitcnt lgkmcnt(1)\n\t"
      "v_mfma_f32_16x16x32_bf16 v[128:131], a[192:195], v[112:115], v[128:131]\n\t"
      "v_add_u32_e32 v197, s47, v201\n\t"
      "v_mfma_f32_16x16x32_bf16 v[128:131], a[216:219], v[116:119], v[128:131]\n\t"
      "v_add_u32_e32 v198, s47, v202\n\t"
      "v_mfma_f32_16x16x32_bf16 v[128:131], a[240:243], v[120:123], v[128:131]\n\t"
      "v_add_u32_e32 v199, s47, v203\n\t"
      "v_mfma_f32_16x16x32_bf16 v[128:131], v[172:175], v[124:127], v[128:131]\n\t"
      "s_cmp_gt_i32 s83, 1\n\t"
      "s_cbranch_scc1 .LslpB_%=\n\t"
      ".LretB_%=:\n\t"
      "s_waitcnt vmcnt(18) lgkmcnt(0)\n\t"
      "s_barrier\n\t"
      "v_mfma_f32_16x16x32_bf16 v[132:135], a[4:7], v[80:83], v[12:15]\n\t"
      "ds_read_b128 v[140:143], v220 offset:0\n\t"
      "v_mfma_f32_16x16x32_bf16 v[132:135], a[28:31], v[84:87], v[132:135]\n\t"
      "v_mfma_f32_16x16x32_bf16 v[132:135], a[52:55], v[88:91], v[132:135]\n\t"
      "v_cvt_pk_bf16_f32 v136, v128, v129\n\t"
      "v_mfma_f32_16x16x32_bf16 v[132:135], a[76:79], v[92:95], v[132:135]\n\t"
      "ds_read_b128 v[32:35], v196 offset:0\n\t"
      "v_cvt_pk_bf16_f32 v137, v130, v131\n\t"
      "v_mfma_f32_16x16x32_bf16 v[132:135], a[100:103], v[96:99], v[132:135]\n\t"
      "v_mfma_f32_16x16x32_bf16 v[132:135], a[124:127], v[100:103], v[132:135]\n\t"
      "v_mfma_f32_16x16x32_bf16 v[132:135], a[148:151], v[104:107], v[132:135]\n\t"
      "ds_read_b128 v[36:39], v197 offset:0\n\t"
      "v_mfma_f32_16x16x32_bf16 v[132:135], a[172:175], v[108:111], v[132:135]\n\t"
      "v_readlane_b32 s73, v248, s85\n\t"
      "v_mfma_f32_16x16x32_bf16 v[132:135], a[196:199], v[112:115], v[132:135]\n\t"
      "v_mfma_f32_16x16x32_bf16 v[132:135], a[220:223], v[116:119], v[132:135]\n\t"
      "ds_read_b128 v[40:43], v198 offset:0\n\t"
      "v_mfma_f32_16x16x32_bf16 v[132:135], a[244:247], v[120:123], v[132:135]\n\t"
      "v_mfma_f32_16x16x32_bf16 v[132:135], v[176:179], v[124:127], v[132:135]\n\t"
      "s_waitcnt lgkmcnt(3)\n\t"
      "buffer_store_dwordx4 v[140:143], v208, s[28:31], s72 offen sc1\n\t"
      "v_mfma_f32_16x16x32_bf16 v[128:131], a[8:11], v[80:83], v[16:19]\n\t"
      "ds_read_b128 v[44:47], v199 offset:0\n\t"
      "v_mfma_f32_16x16x32_bf16 v[128:131], a[32:35], v[84:87], v[128:131]\n\t"
      "s_memrealtime s[80:81]\n\t"
      "v_mfma_f32_16x16x32_bf16 v[128:131], a[56:59], v[88:91], v[128:131]\n\t"
      "v_cvt_pk_bf16_f32 v138, v132, v133\n\t"
      "v_mfma_f32_16x16x32_bf16 v[128:131], a[80:83], v[92:95], v[128:131]\n\t"
      "v_cvt_pk_bf16_f32 v139, v134, v135\n\t"
      "v_mfma_f32_16x16x32_bf16 v[128:131], a[104:107], v[96:99], v[128:131]\n\t"
      "ds_write_b128 v217, v[136:139] offset:12288\n\t"
      "v_mfma_f32_16x16x32_bf16 v[128:131], a[128:131], v[100:103], v[128:131]\n\t"
      "s_mov_b32 m0, s46\n\t"
      "v_mfma_f32_16x16x32_bf16 v[128:131], a[152:155], v[104:107], v[128:131]\n\t"
      "v_mfma_f32_16x16x32_bf16 v[128:131], a[176:179], v[108:111], v[128:131]\n\t"
      "ds_read_b128 v[48:51], v197 offset:256\n\t"
      "v_mfma_f32_16x16x32_bf16 v[128:131], a[200:203], v[112:115], v[128:131]\n\t"
      "v_mfma_f32_16x16x32_bf16 v[128:131], a[224:227], v[116:119], v[128:131]\n\t"
      "ds_read_b128 v[52:55], v198 offset:256\n\t"
      "v_mfma_f32_16x16x32_bf16 v[128:131], a[248:251], v[120:123], v[128:131]\n\t"
      "v_mfma_f32_16x16x32_bf16 v[128:131], v[180:183], v[124:127], v[128:131]\n\t"
      "buffer_load_dwordx4 v208, s[20:23], s73 offen lds\n\t"
      "v_mfma_f32_16x16x32_bf16 v[132:135], a[12:15], v[80:83], v[20:23]\n\t"
      "ds_read_b128 v[56:59], v199 offset:256\n\t"
      "v_mfma_f32_16x16x32_bf16 v[132:135], a[36:39], v[84:87], v[132:135]\n\t"
      "v_mfma_f32_16x16x32_bf16 v[132:135], a[60:63], v[88:91], v[132:135]\n\t"
      "v_cvt_pk_bf16_f32 v136, v128, v129\n\t"
      "v_mfma_f32_16x16x32_bf16 v[132:135], a[84:87], v[92:95], v[132:135]\n\t"
      "v_cvt_pk_bf16_f32 v137, v130, v131\n\t"
      "ds_read_b128 v[144:147], v220 offset:1024\n\t"
      "v_mfma_f32_16x16x32_bf16 v[132:135], a[108:111], v[96:99], v[132:135]\n\t"
      "v_mfma_f32_16x16x32_bf16 v[132:135], a[132:135], v[100:103], v[132:135]\n\t"
      "v_mfma_f32_16x16x32_bf16 v[132:135], a[156:159], v[104:107], v[132:135]\n\t"
      "ds_read_b128 v[60:63], v196 offset:256\n\t"
      "v_mfma_f32_16x16x32_bf16 v[132:135], a[180:183], v[108:111], v[132:135]\n\t"
      "v_mfma_f32_16x16x32_bf16 v[132:135], a[204:207], v[112:115], v[132:135]\n\t"
      "v_mfma_f32_16x16x32_bf16 v[132:135], a[228:231], v[116:119], v[132:135]\n\t"
      "ds_read_b128 v[64:67], v198 offset:512\n\t"
      "v_mfma_f32_16x16x32_bf16 v[132:135], a[252:255], v[120:123], v[132:135]\n\t"
      "v_mfma_f32_16x16x32_bf16 v[132:135], v[184:187], v[124:127], v[132:135]\n\t"
      "s_waitcnt lgkmcnt(2)\n\t"
      "buffer_store_dwordx4 v[144:147], v208, s[28:31], s72 offen offset:1024 sc1\n\t"
      "v_mfma_f32_16x16x32_bf16 v[128:131], a[16:19], v[80:83], v[24:27]\n\t"
      "ds_read_b128 v[68:71], v199 offset:512\n\t"
      "v_mfma_f32_16x16x32_bf16 v[128:131], a[40:43], v[84:87], v[128:131]\n\t"
      "v_readlane_b32 s86, v250, s44\n\t"
      "v_mfma_f32_16x16x32_bf16 v[128:131], a[64:67], v[88:91], v[128:131]\n\t"
      "v_cvt_pk_bf16_f32 v138, v132, v133\n\t"
      "v_mfma_f32_16x16x32_bf16 v[128:131], a[88:91], v[92:95], v[128:131]\n\t"
      "v_cvt_pk_bf16_f32 v139, v134, v135\n\t"
      "v_mfma_f32_16x16x32_bf16 v[128:131], a[112:115], v[96:99], v[128:131]\n\t"
      "ds_write_b128 v218, v[136:139] offset:12288\n\t"
      "v_mfma_f32_16x16x32_bf16 v[128:131], a[136:139], v[100:103], v[128:131]\n\t"
      "v_mfma_f32_16x16x32_bf16 v[128:131], a[160:163], v[104:107], v[128:131]\n\t"
      "v_mfma_f32_16x16x32_bf16 v[128:131], a[184:187], v[108:111], v[128:131]\n\t"
      "ds_read_b128 v[148:151], v220 offset:2048\n\t"
      "v_mfma_f32_16x16x32_bf16 v[128:131], a[208:211], v[112:115], v[128:131]\n\t"
      "v_mfma_f32_16x16x32_bf16 v[128:131], a[232:235], v[116:119], v[128:131]\n\t"
      "ds_read_b128 v[72:75], v196 offset:512\n\t"
      "v_mfma_f32_16x16x32_bf16 v[128:131], v[164:167], v[120:123], v[128:131]\n\t"
      "v_mfma_f32_16x16x32_bf16 v[128:131], v[188:191], v[124:127], v[128:131]\n\t"
      "buffer_load_dwordx4 v208, s[20:23], s73 offen offset:1024 lds\n\t"
      "v_mfma_f32_16x16x32_bf16 v[132:135], a[20:23], v[80:83], v[28:31]\n\t"
      "ds_read_b128 v[76:79], v197 offset:512\n\t"
      "v_mfma_f32_16x16x32_bf16 v[132:135], a[44:47], v[84:87], v[132:135]\n\t"
      "v_mfma_f32_16x16x32_bf16 v[132:135], a[68:71], v[88:91], v[132:135]\n\t"
      "v_cvt_pk_bf16_f32 v136, v128, v129\n\t"
      "v_mfma_f32_16x16x32_bf16 v[132:135], a[92:95], v[92:95], v[132:135]\n\t"
      "v_cvt_pk_bf16_f32 v137, v130, v131\n\t"
      "s_sub_u32 s84, s84, 4\n\t"
      "s_max_i32 s84, s84, s86\n\t"
      "v_mfma_f32_16x16x32_bf16 v[132:135], a[116:119], v[96:99], v[132:135]\n\t"
      "s_sub_u32 s83, s82, s80\n\t"
      "v_mfma_f32_16x16x32_bf16 v[132:135], a[140:143], v[100:103], v[132:135]\n\t"
      "s_max_i32 s83, s83, 0\n\t"
      "s_add_u32 s82, s80, s83\n\t"
      "v_mfma_f32_16x16x32_bf16 v[132:135], a[164:167], v[104:107], v[132:135]\n\t"
      "s_mov_b32 s45, s46\n\t"
      "v_mfma_f32_16x16x32_bf16 v[132:135], a[188:191], v[108:111], v[132:135]\n\t"
      "s_mov_b32 s46, s47\n\t"
      "v_mfma_f32_16x16x32_bf16 v[132:135], a[212:215], v[112:115], v[132:135]\n\t"
      "s_mov_b32 s47, s48\n\t"
      "v_mfma_f32_16x16x32_bf16 v[132:135], a[236:239], v[116:119], v[132:135]\n\t"
      "s_add_u32 s82, s82, s84\n\t"
      "v_mfma_f32_16x16x32_bf16 v[132:135], v[168:171], v[120:123], v[132:135]\n\t"
      "s_cmp_lg_u32 s44, s49\n\t"
      "v_mfma_f32_16x16x32_bf16 v[132:135], v[192:195], v[124:127], v[132:135]\n\t"
      "s_waitcnt lgkmcnt(2)\n\t"
      "buffer_store_dwordx4 v[148:151], v208, s[28:31], s72 offen offset:2048 sc1\n\t"
      "s_cbranch_scc1 .Lloop_%=\n\t"
      "v_readlane_b32 s72, v249, s44\n\t"
      "s_nop 6\n\t"
      "v_cvt_pk_bf16_f32 v138, v132, v133\n\t"
      "v_cvt_pk_bf16_f32 v139, v134, v135\n\t"
      "ds_write_b128 v219, v[136:139] offset:12288\n\t"
      "s_waitcnt lgkmcnt(0)\n\t"
      "s_barrier\n\t"
      "ds_read_b128 v[32:35], v220 offset:12288\n\t"
      "ds_read_b128 v[36:39], v220 offset:13312\n\t"
      "ds_read_b128 v[40:43], v220 offset:14336\n\t"
      "s_waitcnt lgkmcnt(2)\n\t"
      "buffer_store_dwordx4 v[32:35], v209, s[28:31], s72 offen sc1\n\t"
      "s_waitcnt lgkmcnt(1)\n\t"
      "buffer_store_dwordx4 v[36:39], v209, s[28:31], s72 offen offset:1024 sc1\n\t"
      "s_waitcnt lgkmcnt(0)\n\t"
      "buffer_store_dwordx4 v[40:43], v209, s[28:31], s72 offen offset:2048 sc1\n\t"
      "s_branch .Lend_%=\n\t"
      ".LslpP2_%=:\n\t"
      "s_sleep 1\n\t"
      "s_sub_u32 s83, s83, 3\n\t"
      "s_cmp_gt_i32 s83, 1\n\t"
      "s_cbranch_scc1 .LslpP2_%=\n\t"
      "s_branch .LretP2_%=\n\t"
      ".LslpP3_%=:\n\t"
      "s_sleep 1\n\t"
      "s_sub_u32 s83, s83, 3\n\t"
      "s_cmp_gt_i32 s83, 1\n\t"
      "s_cbranch_scc1 .LslpP3_%=\n\t"
      "s_branch .LretP3_%=\n\t"
      ".LslpA_%=:\n\t"
      "s_sleep 1\n\t"
      "s_sub_u32 s83, s83, 3\n\t"
      "s_cmp_gt_i32 s83, 1\n\t"
      "s_cbranch_scc1 .LslpA_%=\n\t"
      "s_branch .LretA_%=\n\t"
      ".LslpB_%=:\n\t"
      "s_sleep 1\n\t"
      "s_sub_u32 s83, s83, 3\n\t"
      "s_cmp_gt_i32 s83, 1\n\t"
      "s_cbranch_scc1 .LslpB_%=\n\t"
      "s_branch .LretB_%=\n\t"
      ".Lgs_%=:\n\t"
      "s_sleep 8\n\t"
      "buffer_load_dwordx2 v[240:241], v245, s[24:27], 0 offen sc1\n\t"
      "s_memrealtime s[76:77]\n\t"
      "s_waitcnt vmcnt(0) lgkmcnt(0)\n\t"
      "v_subrev_u32_e32 v242, s63, v240\n\t"
      "v_xor_b32_e32 v241, s53, v241\n\t"
      "v_lshrrev_b32_e32 v240, 13, v242\n\t"
      "v_or_b32_e32 v240, v240, v241\n\t"
      "v_cmp_ne_u32_e32 vcc, 0, v240\n\t"
      "v_and_b32_e32 v242, 15, v242\n\t"
      "s_cbranch_vccz .Lgr_%=\n\t"
      "s_sub_u32 s76, s76, s52\n\t"
      "s_cmp_lt_u32 s76, 2000\n\t"
      "s_cbranch_scc1 .Lgs_%=\n\t"
      "v_mov_b32_e32 v242, 64\n\t"
      "s_branch .Lgr_%=\n\t"
      ".Lend_%=:\n\t"
      :
      : "{s[20:21]}"(xa), "{s[16:17]}"(wa), "{s[36:37]}"(ba), "{s[28:29]}"(oa), "{s[24:25]}"(sa), "{s[52:53]}"(rs),
        "{s[22:23]}"(0x0002000006c00000ull), "{s[26:27]}"(0x0002000000000800ull), "{s[30:31]}"(0x0002000006c00000ull),
        "{s[18:19]}"(0x0002000000048000ull), "{s[38:39]}"(0x0002000000000300ull),
        "{s40}"(t0), "{s41}"(p0), "{s43}"(lds0), "{s94}"(ty),
        "{v208}"(ga), "{v209}"(gb),
        "{v211}"(woff[0]), "{v212}"(woff[1]), "{v213}"(woff[2]), "{v215}"(boff),
        "{s95}"(tres), "{s[60:61]}"(pres),
        "{v248}"(ta), "{v249}"(ta), "{v250}"(tp), "{v251}"(t1), "{v252}"(mk), "{s74}"(plm), "{s75}"(plb),
        "{v245}"(vgv), "{v246}"(vxw), "{v247}"(vxr),
        "{v200}"(cfa[0]), "{v201}"(cfa[1]), "{v202}"(cfa[2]), "{v203}"(cfa[3]),
        "{v204}"(csw[0]), "{v205}"(csw[1]), "{v206}"(csw[2]), "{v207}"(csr),
        "{v214}"(tbase)
      : "memory", "scc", "vcc",
      "s44", "s45", "s46", "s47", "s48", "s49", "s50", "s51", "s54", "s55", "s56", "s57", "s58", "s59", "s62", "s63", "s64", "s65", "s66", "s67",
      "s68", "s69", "s70", "s71", "s72", "s73", "s79", "s80", "s81", "s82", "s83", "s84", "s85", "s86", "s87", "s88", "s89", "s90", "s91", "s92",
      "s93", "s96", "s97", "s76", "s78", "s77", "s98", "s99", "s42",
      "v1", "v2", "v3", "v4", "v5", "v6", "v7", "v8", "v9", "v10", "v11", "v12", "v13", "v14", "v15", "v16", "v17", "v18", "v19", "v20", "v21", "v22",
      "v23", "v24", "v25", "v26", "v27", "v28", "v29", "v30", "v31", "v32", "v33", "v34", "v35", "v36", "v37", "v38", "v39", "v40", "v41", "v42",
      "v43", "v44", "v45", "v46", "v47", "v48", "v49", "v50", "v51", "v52", "v53", "v54", "v55", "v56", "v57", "v58", "v59", "v60", "v61", "v62",
      "v63", "v64", "v65", "v66", "v67", "v68", "v69", "v70", "v71", "v72", "v73", "v74", "v75", "v76", "v77", "v78", "v79", "v80", "v81", "v82",
      "v83", "v84", "v85", "v86", "v87", "v88", "v89", "v90", "v91", "v92", "v93", "v94", "v95", "v96", "v97", "v98", "v99", "v100", "v101", "v102",
      "v103", "v104", "v105", "v106", "v107", "v108", "v109", "v110", "v111", "v112", "v113", "v114", "v115", "v116", "v117", "v118", "v119", "v120",
      "v121", "v122", "v123", "v124", "v125", "v126", "v127", "v128", "v129", "v130", "v131", "v132", "v133", "v134", "v135", "v136", "v137", "v138",
      "v139", "v140", "v141", "v142", "v143", "v144", "v145", "v146", "v147", "v148", "v149", "v150", "v151", "v152", "v153", "v154", "v155", "v156",
      "v157", "v158", "v159", "v160", "v161", "v162", "v163", "v164", "v165", "v166", "v167", "v168", "v169", "v170", "v171", "v172", "v173", "v174",
      "v175", "v176", "v177", "v178", "v179", "v180", "v181", "v182", "v183", "v184", "v185", "v186", "v187", "v188", "v189", "v190", "v191", "v192",
      "v193", "v194", "v195", "v196", "v197", "v198", "v199", "v210", "v217", "v218", "v219", "v220", "v221", "v222", "v223", "v224", "v225", "v226",
      "v227", "v228", "v229", "v230", "v231", "v232", "v233", "v234", "v235", "v236", "v237", "v238", "v239", "v240", "v241", "v242", "v243", "v253",
      "v254", "v255", "v244", "v216",
      "a0", "a1", "a2", "a3", "a4", "a5", "a6", "a7", "a8", "a9", "a10", "a11", "a12", "a13", "a14", "a15", "a16", "a17", "a18", "a19", "a20", "a21",
      "a22", "a23", "a24", "a25", "a26", "a27", "a28", "a29", "a30", "a31", "a32", "a33", "a34", "a35", "a36", "a37", "a38", "a39", "a40", "a41",
      "a42", "a43", "a44", "a45", "a46", "a47", "a48", "a49", "a50", "a51", "a52", "a53", "a54", "a55", "a56", "a57", "a58", "a59", "a60", "a61",
      "a62", "a63", "a64", "a65", "a66", "a67", "a68", "a69", "a70", "a71", "a72", "a73", "a74", "a75", "a76", "a77", "a78", "a79", "a80", "a81",
      "a82", "a83", "a84", "a85", "a86", "a87", "a88", "a89", "a90", "a91", "a92", "a93", "a94", "a95", "a96", "a97", "a98", "a99", "a100", "a101",
      "a102", "a103", "a104", "a105", "a106", "a107", "a108", "a109", "a110", "a111", "a112", "a113", "a114", "a115", "a116", "a117", "a118", "a119",
      "a120", "a121", "a122", "a123", "a124", "a125", "a126", "a127", "a128", "a129", "a130", "a131", "a132", "a133", "a134", "a135", "a136", "a137",
      "a138", "a139", "a140", "a141", "a142", "a143", "a144", "a145", "a146", "a147", "a148", "a149", "a150", "a151", "a152", "a153", "a154", "a155",
      "a156", "a157", "a158", "a159", "a160", "a161", "a162", "a163", "a164", "a165", "a166", "a167", "a168", "a169", "a170", "a171", "a172", "a173",
      "a174", "a175", "a176", "a177", "a178", "a179", "a180", "a181", "a182", "a183", "a184", "a185", "a186", "a187", "a188", "a189", "a190", "a191",
      "a192", "a193", "a194", "a195", "a196", "a197", "a198", "a199", "a200", "a201", "a202", "a203", "a204", "a205", "a206", "a207", "a208", "a209",
      "a210", "a211", "a212", "a213", "a214", "a215", "a216", "a217", "a218", "a219", "a220", "a221", "a222", "a223", "a224", "a225", "a226", "a227",
      "a228", "a229", "a230", "a231", "a232", "a233", "a234", "a235", "a236", "a237", "a238", "a239", "a240", "a241", "a242", "a243", "a244", "a245",
      "a246", "a247", "a248", "a249", "a250", "a251", "a252", "a253", "a254", "a255");
}

__global__ __launch_bounds__(64) void probek(const char* p, unsigned step) {
  unsigned t = (unsigned)__builtin_amdgcn_s_memrealtime();
  unsigned sum = 0;
  if (threadIdx.x == 0) {
    for (int i = 0; i < 64; ++i) {
      const char* a = p + (size_t)i * step + (i & 1) * 8192;
      const char* a0 = a + 65536 + 4096;
      unsigned v, v0; unsigned long long t0, t1;
      asm volatile("global_load_dword %3, %5, off\n\ts_waitcnt vmcnt(0)\n\ts_memtime %0\n\ts_waitcnt lgkmcnt(0)\n\tglobal_load_dword %2, %4, off\n\ts_waitcnt vmcnt(0)\n\ts_memtime %1\n\ts_waitcnt lgkmcnt(0)"
                   : "=&s"(t0), "=&s"(t1), "=&v"(v), "=&v"(v0) : "v"(a), "v"(a0) : "memory");
      unsigned d = (unsigned)(t1 - t0);
      sum += d > 2000u ? 2000u : d;
    }
  }
  sum = __builtin_amdgcn_readfirstlane(sum);
  int T = 20000 + (int)(sum / 64) * 10;
  while ((int)((unsigned)__builtin_amdgcn_s_memrealtime() - t) < T) {}
}
extern "C" void kernel_launch(void* const* d_in, const int* in_sizes, int n_in,
                              void* d_out, int out_size, void* d_ws, size_t ws_size,
                              hipStream_t stream) {
  const uint16_t* x = (const uint16_t*)d_in[0];
  const uint16_t* w = (const uint16_t*)d_in[1];
  const uint16_t* b = (const uint16_t*)d_in[2];
  uint16_t* out = (uint16_t*)d_out;
  probek<<<1, 64, 0, stream>>>((const char*)d_ws + 8ull * 1048576 + 1280, 1638400u);
  conv1x1_kernel<<<256, 256, 0, stream>>>(x, w, b, out, (unsigned long long*)d_ws);
}
```

```cpp
#include <hip/hip_runtime.h>
#include <stdint.h>

#define LAS __attribute__((address_space(3)))
typedef __attribute__((ext_vector_type(16))) unsigned u32x16;
typedef __attribute__((ext_vector_type(4))) unsigned u32x4;
typedef unsigned long long u64;

__global__ __launch_bounds__(256, 1) void conv1x1_kernel(const void* w, u64 cw, const void* x, u64 cx, void* ws, u64 cs, void* out, u64 co,
                                                         const void* bias, u64 cb) {
  __shared__ __attribute__((aligned(1024))) unsigned char lds[160768];

  u32x16 d16;
  u32x4 db;
  u64 rs;
  asm volatile(
      "s_load_dwordx16 s[16:31], %3, 0x0\n\t"
      "s_load_dwordx4 s[36:39], %3, 0x40\n\t"
      "s_memrealtime s[52:53]\n\t"
      : "={s[16:31]}"(d16), "={s[36:39]}"(db), "={s[52:53]}"(rs)
      : "s"(__builtin_amdgcn_kernarg_segment_ptr())
      : "memory");
  __builtin_amdgcn_sched_barrier(0);
  unsigned tid  = threadIdx.x;
  unsigned lane = tid & 63;
  unsigned wv   = __builtin_amdgcn_readfirstlane(tid >> 6);
  const unsigned lds0 = (unsigned)(size_t)(LAS unsigned char*)lds;
  unsigned blk  = blockIdx.x;
  const unsigned p0   = lds0 + 3072u * wv;
  const unsigned lq = lane >> 4, ph = (lane >> 2) & 3, pl = lane & 3;
  const unsigned gs = 256u * lq + 64u * ((ph - lq) & 3) + 16u * (pl ^ ((4u - wv) & 3));
  const unsigned ga = gs + 3072u * (wv < 3u ? wv : 5u);
  const unsigned gb = gs + 3072u * (wv < 2u ? wv + 6u : wv + 1u);
  const unsigned boff = 192u * wv + 16u * lq;
  unsigned tc0, tc1;
  asm volatile(
      "s_waitcnt lgkmcnt(0)\n\t"
      "global_load_ubyte v4, v0, s[20:21]\n\t"
      "global_load_ubyte v5, v0, s[16:17]\n\t"
      : "={v4}"(tc0), "={v5}"(tc1)
      : "{s[16:31]}"(d16), "{s[36:39]}"(db), "{s[52:53]}"(rs), "{v0}"(tid), "{v208}"(ga), "{v209}"(gb), "{v215}"(boff), "{s41}"(p0)
      : "memory");
  asm volatile("" : "+s"(blk));
  __builtin_amdgcn_sched_barrier(0);
  const unsigned role = (blk >> 2) & 1u;
  unsigned pk = (blk & 3u) * 32u + (blk >> 3);
  const unsigned sst  = (role ^ (blk >> 4)) & 1u;
  const unsigned t0   = pk * 49152u + sst * 24576u;
  const unsigned pv = blk >> 6, ps = (blk & 63u) ^ (4u * (pv & 1u)), pgr = (ps >> 2) & 7u, pi = (ps & 3u) * 2u + (pv >> 1);
  const unsigned ppg = min(pgr * 8u, 52u) + (pi & (0x13777777u >> (4u * pgr)) & 7u);
  const unsigned pop = (ppg << 21) + ((ps & 3u) * 4u + pv) * 0x1c080u + 0x8000u;
  unsigned pfire = ((role ^ ppg) & 1u) * 2u, psel = ps >> 5;
  const unsigned vpub = lane == 0u ? 0u + (pv * 64u + ps) * 8u : 0x40000000u;
  const unsigned pfo = (blk >> 4) * 18432u;
  const unsigned pfv = tid * 16u;
  const unsigned ty = (blk >> 3) & 1u;
  const unsigned tidx = (blk >> 3) * 4u + wv, tsel = tidx >> 6;
  const unsigned toff = (tidx & 63u) < 54u ? (tidx & 63u) << 21 : 113246204u;
  unsigned tres, pre1;
  unsigned cpo = ((blk >> 4) * 256u + tid) * 8u;
  u64 pres;
  u32x4 br0, br1, br2;
  asm volatile(
      "s_add_u32 m0, s43, 0x27000\n\t"
      "s_cmp_eq_u32 s41, s43\n\t"
      "s_cbranch_scc0 .Lc0_%=\n\t"
      "s_cmp_eq_u32 s78, 0\n\t"
      "s_cselect_b64 s[96:97], s[20:21], s[28:29]\n\t"
      "s_load_dword s69, s[96:97], s76 offset:0x0\n\t"
      "s_load_dwordx2 s[60:61], s[96:97], s76 offset:0x3300\n\t"
      ".Lc0_%=:\n\t"
      "s_cmp_eq_u32 s94, 0\n\t"
      "s_cbranch_scc1 .Lha_%=\n\t"
      "s_getpc_b64 s[92:93]\n\t"
      ".Lconv_cp:\n\t"
      "v_min_u32_e32 v6, .Lconv_end-.Lconv_cp-4, v6\n\t"
      "global_load_dword v6, v6, s[92:93]\n\t"
      "buffer_load_dwordx4 v216, s[16:19], s42 offen lds\n\t"
      "s_add_u32 s51, s42, 0x1000\n\t"
      "buffer_load_dwordx4 v216, s[16:19], s51 offen lds\n\t"
      "s_add_u32 s51, s42, 0x2000\n\t"
      "buffer_load_dwordx4 v216, s[16:19], s51 offen lds\n\t"
      "s_add_u32 s51, s42, 0x3000\n\t"
      "buffer_load_dwordx4 v216, s[16:19], s51 offen lds\n\t"
      "s_add_u32 s51, s42, 0x4000\n\t"
      "buffer_load_dwordx4 v216, s[16:19], s51 offen lds\n\t"
      "buffer_load_dwordx4 v[226:229], v215, s[36:39], 0 offen offset:0\n\t"
      "buffer_load_dwordx4 v[230:233], v215, s[36:39], 0 offen offset:64\n\t"
      "buffer_load_dwordx4 v[234:237], v215, s[36:39], 0 offen offset:128\n\t"
      "s_branch .Lhb_%=\n\t"
      ".Lha_%=:\n\t"
      "buffer_load_dwordx4 v[226:229], v215, s[36:39], 0 offen offset:0\n\t"
      "buffer_load_dwordx4 v[230:233], v215, s[36:39], 0 offen offset:64\n\t"
      "buffer_load_dwordx4 v[234:237], v215, s[36:39], 0 offen offset:128\n\t"
      "s_mov_b32 m0, s41\n\t"
      "s_nop 0\n\t"
      "buffer_load_dwordx4 v208, s[20:23], s40 offen lds\n\t"
      "buffer_load_dwordx4 v208, s[20:23], s40 offen offset:1024 lds\n\t"
      "buffer_load_dwordx4 v208, s[20:23], s40 offen offset:2048 lds\n\t"
      "s_add_u32 m0, s41, 0x3000\n\t"
      "s_nop 0\n\t"
      "buffer_load_dwordx4 v209, s[20:23], s40 offen lds\n\t"
      "buffer_load_dwordx4 v209, s[20:23], s40 offen offset:1024 lds\n\t"
      "buffer_load_dwordx4 v209, s[20:23], s40 offen offset:2048 lds\n\t"
      ".Lhb_%=:\n\t"
      "s_cmp_eq_u32 s41, s43\n\t"
      "s_cbranch_scc1 .Lev0_%=\n\t"
      "s_cmp_eq_u32 s99, 0\n\t"
      "s_cselect_b64 s[96:97], s[20:21], s[28:29]\n\t"
      "s_load_dword s95, s[96:97], s98\n\t"
      "s_cmp_eq_u32 s94, 0\n\t"
      "s_cbranch_scc1 .Lev1_%=\n\t"
      "s_sleep 12\n\t"
      "s_branch .Lev1_%=\n\t"
      ".Lev0_%=:\n\t"
      "s_andn2_b32 s79, s52, 15\n\t"
      "v_mov_b32_e32 v241, s53\n\t"
      "s_waitcnt lgkmcnt(2)\n\t"
      "s_nop 7\n\t"
      "s_getreg_b32 s78, hwreg(HW_REG_IB_STS, 8, 4)\n\t"
      "s_cmp_eq_u32 s78, 1\n\t"
      "s_cselect_b32 s51, 2, 0\n\t"
      "s_xor_b32 s77, s77, s51\n\t"
      "s_cmp_lg_u32 s78, 0\n\t"
      "s_cselect_b32 s77, s77, 1\n\t"
      "s_or_b32 s79, s79, s77\n\t"
      "v_mov_b32_e32 v240, s79\n\t"
      "buffer_store_dwordx2 v[240:241], v244, s[24:27], 0 offen sc1\n\t"
      "s_cmp_eq_u32 s99, 0\n\t"
      "s_cselect_b64 s[96:97], s[20:21], s[28:29]\n\t"
      "s_load_dword s95, s[96:97], s98\n\t"
      ".Lev1_%=:\n\t"
      : "={s95}"(tres), "={s[60:61]}"(pres), "={s69}"(pre1), "={v[226:229]}"(br0), "={v[230:233]}"(br1), "={v[234:237]}"(br2),
        "+v"(lane), "+{s77}"(pfire), "+{s78}"(psel), "+{v6}"(cpo)
      : "{s[16:31]}"(d16), "{s[36:39]}"(db), "{s40}"(t0), "{s41}"(p0), "{s43}"(lds0), "{s94}"(ty),
        "{v208}"(ga), "{v209}"(gb), "{v215}"(boff), "{v216}"(pfv),
        "{s42}"(pfo), "{s98}"(toff), "{s99}"(tsel), "{s76}"(pop), "{s[52:53]}"(rs), "{v244}"(vpub)
      : "memory", "scc", "s51", "s92", "s93", "s96", "s97", "s79", "v240", "v241");
  asm volatile("" : "+s"(wv), "+s"(pk));
  __builtin_amdgcn_sched_barrier(0);
  const unsigned vcell = lane < 2u ? 2048u + pk * 8u + 4u * (role ^ lane) : 0x40000000u;
  const unsigned ob = (unsigned)(((lane < 12u ? 0x41ccd62ca418820ull >> (5u * lane) : 0x22a4c1eeull >> (5u * ((lane - 12u) & 7u)))) & 31u) * 6291456u + pk * 49152u;
  const unsigned pg = (ob + 24576u) >> 21;
  const unsigned ta = lane < 18u ? ob + 24576u * sst : 113246208u, t1 = ob + 24576u * (sst ^ 1u);
  const unsigned tq = lane >= 9u ? 82u : 80u, tp = lane >= 14u ? 0u : tq;
  const unsigned mk = lane >= 4u + ty && lane < 18u ? 1u << (pg < 52u ? (pg >> 3) * 4u : 28u) : 0u;
  const unsigned plm = (unsigned)__builtin_amdgcn_ballot_w64(((pg ^ role ^ sst) & 1u) != 0u), plb = wv < 2u ? plm : ~plm;
  const unsigned vgv = 0u + wv * 512u + lane * 8u;
  const unsigned vxr = lds0 + 159744u + lane * 16u, vxw = vxr + wv * 4u;
  const unsigned li = lane & 15, lg = lane >> 4, rl = li & 3, hi = li >> 2;
  const unsigned rb = 768u * li + 16u * (lg ^ ((4u - hi) & 3)) - 3072u * wv;
  unsigned cfa[4], csw[3];
#pragma unroll
  for (unsigned u = 0; u < 4; ++u) cfa[u] = rb + 64u * ((u - rl) & 3);
#pragma unroll
  for (unsigned j = 0; j < 3; ++j) {
    const unsigned e = 3u * wv + j;
    csw[j] = rb + 256u * (e >> 2) + 64u * (((e & 3) + (e >> 2) - rl) & 3);
  }
  const unsigned csr = 16u * lane;
  const unsigned tbase = p0 + (4u * lg + (li >> 2)) * 192u + 16u * (li & 3);
  unsigned woff[3];
#pragma unroll
  for (unsigned j = 0; j < 3; ++j) {
    const unsigned q = 64u * j + lane, rr = q / 12u;
    woff[j] = (8u * (rr >> 2) + (rr & 3)) * 768u + 192u * wv + 16u * (q % 12u) - 1024u * j;
  }
  asm volatile(
      "s_cmp_eq_u32 s94, 0\n\t"
      "s_cbranch_scc0 .Lc1_%=\n\t"
      "s_add_u32 m0, s41, 0x1e000\n\t"
      "s_mov_b32 s51, 0x0\n\t"
      "buffer_load_dwordx4 v211, s[16:19], s51 offen lds\n\t"
      "buffer_load_dwordx4 v212, s[16:19], s51 offen offset:1024 lds\n\t"
      "buffer_load_dwordx4 v213, s[16:19], s51 offen offset:2048 lds\n\t"
      ".Lc1_%=:\n\t"
      "s_add_u32 s49, s89, 5\n\t"
      "s_mov_b32 s62, 0\n\t"
      "s_mov_b32 s70, 0\n\t"
      "s_add_u32 s50, s41, 0x12000\n\t"
      "s_add_u32 s51, s41, 0x12000\n\t"
      "s_sub_u32 s63, s52, 800\n\t"
      "s_andn2_b32 s63, s63, 15\n\t"
      "v_add_u32_e32 v196, s41, v200\n\t"
      "v_add_u32_e32 v197, s41, v201\n\t"
      "v_add_u32_e32 v198, s41, v202\n\t"
      "v_add_u32_e32 v199, s41, v203\n\t"
      "v_add_u32_e32 v217, s51, v204\n\t"
      "v_add_u32_e32 v218, s51, v205\n\t"
      "v_add_u32_e32 v219, s51, v206\n\t"
      "v_add_u32_e32 v220, s51, v207\n\t"
      "v_add_u32_e32 v238, s51, v207\n\t"
      "s_cmp_eq_u32 s94, 0\n\t"
      "s_cbranch_scc0 .Lpb_%=\n\t"
      "s_add_u32 m0, s41, 0x21000\n\t"
      "s_mov_b32 s51, 0xc00\n\t"
      "buffer_load_dwordx4 v211, s[16:19], s51 offen lds\n\t"
      "buffer_load_dwordx4 v212, s[16:19], s51 offen offset:1024 lds\n\t"
      "buffer_load_dwordx4 v213, s[16:19], s51 offen offset:2048 lds\n\t"
      "s_add_u32 m0, s41, 0x24000\n\t"
      "s_mov_b32 s51, 0x6000\n\t"
      "buffer_load_dwordx4 v211, s[16:19], s51 offen lds\n\t"
      "buffer_load_dwordx4 v212, s[16:19], s51 offen offset:1024 lds\n\t"
      "buffer_load_dwordx4 v213, s[16:19], s51 offen offset:2048 lds\n\t"
      "s_waitcnt vmcnt(15)\n\t"
      "v_lshlrev_b32_e32 v8, 16, v226\n\t"
      "v_and_b32_e32 v9, 0xffff0000, v226\n\t"
      "v_lshlrev_b32_e32 v10, 16, v227\n\t"
      "v_and_b32_e32 v11, 0xffff0000, v227\n\t"
      "v_lshlrev_b32_e32 v12, 16, v228\n\t"
      "v_and_b32_e32 v13, 0xffff0000, v228\n\t"
      "v_lshlrev_b32_e32 v14, 16, v229\n\t"
      "v_and_b32_e32 v15, 0xffff0000, v229\n\t"
      "v_lshlrev_b32_e32 v16, 16, v230\n\t"
      "v_and_b32_e32 v17, 0xffff0000, v230\n\t"
      "v_lshlrev_b32_e32 v18, 16, v231\n\t"
      "v_and_b32_e32 v19, 0xffff0000, v231\n\t"
      "v_lshlrev_b32_e32 v20, 16, v232\n\t"
      "v_and_b32_e32 v21, 0xffff0000, v232\n\t"
      "v_lshlrev_b32_e32 v22, 16, v233\n\t"
      "v_and_b32_e32 v23, 0xffff0000, v233\n\t"
      "v_lshlrev_b32_e32 v24, 16, v234\n\t"
      "v_and_b32_e32 v25, 0xffff0000, v234\n\t"
      "v_lshlrev_b32_e32 v26, 16, v235\n\t"
      "v_and_b32_e32 v27, 0xffff0000, v235\n\t"
      "v_lshlrev_b32_e32 v28, 16, v236\n\t"
      "v_and_b32_e32 v29, 0xffff0000, v236\n\t"
      "v_lshlrev_b32_e32 v30, 16, v237\n\t"
      "v_and_b32_e32 v31, 0xffff0000, v237\n\t"
      "s_add_u32 m0, s41, 0x12000\n\t"
      "s_mov_b32 s51, 0x6c00\n\t"
      "buffer_load_dwordx4 v211, s[16:19], s51 offen lds\n\t"
      "buffer_load_dwordx4 v212, s[16:19], s51 offen offset:1024 lds\n\t"
      "buffer_load_dwordx4 v213, s[16:19], s51 offen offset:2048 lds\n\t"
      "s_add_u32 m0, s41, 0x15000\n\t"
      "s_mov_b32 s51, 0xc000\n\t"
      "buffer_load_dwordx4 v211, s[16:19], s51 offen lds\n\t"
      "buffer_load_dwordx4 v212, s[16:19], s51 offen offset:1024 lds\n\t"
      "buffer_load_dwordx4 v213, s[16:19], s51 offen offset:2048 lds\n\t"
      "s_add_u32 m0, s41, 0x18000\n\t"
      "s_mov_b32 s51, 0xcc00\n\t"
      "buffer_load_dwordx4 v211, s[16:19], s51 offen lds\n\t"
      "buffer_load_dwordx4 v212, s[16:19], s51 offen offset:1024 lds\n\t"
      "buffer_load_dwordx4 v213, s[16:19], s51 offen offset:2048 lds\n\t"
      "s_add_u32 m0, s41, 0x1b000\n\t"
      "s_mov_b32 s51, 0x12000\n\t"
      "buffer_load_dwordx4 v211, s[16:19], s51 offen lds\n\t"
      "buffer_load_dwordx4 v212, s[16:19], s51 offen offset:1024 lds\n\t"
      "buffer_load_dwordx4 v213, s[16:19], s51 offen offset:2048 lds\n\t"
      "s_mov_b32 s44, 0\n\t"
      "s_mov_b32 s85, 3\n\t"
      "s_mov_b32 s45, s41\n\t"
      "s_add_u32 s46, s41, 0x6000\n\t"
      "s_add_u32 s47, s41, 0xc000\n\t"
      "s_waitcnt vmcnt(18)\n\t"
      "s_barrier\n\t"
      "ds_read_b64_tr_b16 a[0:1], v214 offset:49152\n\t"
      "ds_read_b64_tr_b16 a[4:5], v214 offset:49160\n\t"
      "ds_read_b64_tr_b16 a[8:9], v214 offset:49216\n\t"
      "ds_read_b64_tr_b16 a[12:13], v214 offset:49224\n\t"
      "ds_read_b64_tr_b16 a[16:17], v214 offset:49280\n\t"
      "ds_read_b64_tr_b16 a[20:21], v214 offset:49288\n\t"
      "ds_read_b128 v[140:143], v196 offset:0\n\t"
      "s_waitcnt vmcnt(15)\n\t"
      "ds_read_b64_tr_b16 a[2:3], v221 offset:0\n\t"
      "ds_read_b64_tr_b16 a[6:7], v221 offset:8\n\t"
      "ds_read_b64_tr_b16 a[10:11], v221 offset:64\n\t"
      "ds_read_b64_tr_b16 a[14:15], v221 offset:72\n\t"
      "ds_read_b64_tr_b16 a[18:19], v221 offset:128\n\t"
      "ds_read_b64_tr_b16 a[22:23], v221 offset:136\n\t"
      "ds_read_b128 v[152:155], v196 offset:12288\n\t"
      "s_waitcnt lgkmcnt(8)\n\t"
      "s_add_u32 m0, s41, 0x1e000\n\t"
      "s_mov_b32 s51, 0x12c00\n\t"
      "buffer_load_dwordx4 v211, s[16:19], s51 offen lds\n\t"
      "buffer_load_dwordx4 v212, s[16:19], s51 offen offset:1024 lds\n\t"
      "buffer_load_dwordx4 v213, s[16:19], s51 offen offset:2048 lds\n\t"
      "s_waitcnt vmcnt(15)\n\t"
      "ds_read_b64_tr_b16 a[24:25], v221 offset:12288\n\t"
      "ds_read_b64_tr_b16 a[28:29], v221 offset:12296\n\t"
      "ds_read_b64_tr_b16 a[32:33], v221 offset:12352\n\t"
      "ds_read_b64_tr_b16 a[36:37], v221 offset:12360\n\t"
      "ds_read_b64_tr_b16 a[40:41], v221 offset:12416\n\t"
      "ds_read_b64_tr_b16 a[44:45], v221 offset:12424\n\t"
      "ds_read_b128 v[144:147], v197 offset:0\n\t"
      "s_waitcnt lgkmcnt(8)\n\t"
      "v_mfma_f32_16x16x32_bf16 v[80:83], a[0:3], v[140:143], v[8:11]\n\t"
      "s_add_u32 m0, s41, 0x21000\n\t"
      "s_mov_b32 s51, 0x18000\n\t"
      "buffer_load_dwordx4 v211, s[16:19], s51 offen lds\n\t"
      "v_mfma_f32_16x16x32_bf16 v[84:87], a[4:7], v[140:143], v[12:15]\n\t"
      "buffer_load_dwordx4 v212, s[16:19], s51 offen offset:1024 lds\n\t"
      "v_mfma_f32_16x16x32_bf16 v[88:91], a[8:11], v[140:143], v[16:19]\n\t"
      "buffer_load_dwordx4 v213, s[16:19], s51 offen offset:2048 lds\n\t"
      "s_waitcnt vmcnt(15)\n\t"
      "ds_read_b64_tr_b16 a[26:27], v214 offset:0\n\t"
      "v_mfma_f32_16x16x32_bf16 v[92:95], a[12:15], v[140:143], v[20:23]\n\t"
      "ds_read_b64_tr_b16 a[30:31], v214 offset:8\n\t"
      "ds_read_b64_tr_b16 a[34:35], v214 offset:64\n\t"
      "v_mfma_f32_16x16x32_bf16 v[96:99], a[16:19], v[140:143], v[24:27]\n\t"
      "ds_read_b64_tr_b16 a[38:39], v214 offset:72\n\t"
      "ds_read_b64_tr_b16 a[42:43], v214 offset:128\n\t"
      "v_mfma_f32_16x16x32_bf16 v[100:103], a[20:23], v[140:143], v[28:31]\n\t"
      "ds_read_b64_tr_b16 a[46:47], v214 offset:136\n\t"
      "ds_read_b128 v[156:159], v197 offset:12288\n\t"
      "s_waitcnt lgkmcnt(8)\n\t"
      "v_mfma_f32_16x16x32_bf16 v[104:107], a[0:3], v[152:155], v[8:11]\n\t"
      "s_add_u32 m0, s41, 0x24000\n\t"
      "s_mov_b32 s51, 0x18c00\n\t"
      "buffer_load_dwordx4 v211, s[16:19], s51 offen lds\n\t"
      "v_mfma_f32_16x16x32_bf16 v[108:111], a[4:7], v[152:155], v[12:15]\n\t"
      "buffer_load_dwordx4 v212, s[16:19], s51 offen offset:1024 lds\n\t"
      "v_mfma_f32_16x16x32_bf16 v[112:115], a[8:11], v[152:155], v[16:19]\n\t"
      "buffer_load_dwordx4 v213, s[16:19], s51 offen offset:2048 lds\n\t"
      "s_waitcnt vmcnt(15)\n\t"
      "ds_read_b64_tr_b16 a[48:49], v214 offset:12288\n\t"
      "v_mfma_f32_16x16x32_bf16 v[116:119], a[12:15], v[152:155], v[20:23]\n\t"
      "ds_read_b64_tr_b16 a[52:53], v214 offset:12296\n\t"
      "ds_read_b64_tr_b16 a[56:57], v214 offset:12352\n\t"
      "v_mfma_f32_16x16x32_bf16 v[120:123], a[16:19], v[152:155], v[24:27]\n\t"
      "ds_read_b64_tr_b16 a[60:61], v214 offset:12360\n\t"
      "ds_read_b64_tr_b16 a[64:65], v214 offset:12416\n\t"
      "v_mfma_f32_16x16x32_bf16 v[132:135], a[20:23], v[152:155], v[28:31]\n\t"
      "ds_read_b64_tr_b16 a[68:69], v214 offset:12424\n\t"
      "ds_read_b128 v[148:151], v198 offset:0\n\t"
      "s_waitcnt lgkmcnt(8)\n\t"
      "v_mfma_f32_16x16x32_bf16 v[80:83], a[24:27], v[144:147], v[80:83]\n\t"
      "s_add_u32 m0, s41, 0x12000\n\t"
      "s_mov_b32 s51, 0x1e000\n\t"
      "buffer_load_dwordx4 v211, s[16:19], s51 offen lds\n\t"
      "v_mfma_f32_16x16x32_bf16 v[84:87], a[28:31], v[144:147], v[84:87]\n\t"
      "buffer_load_dwordx4 v212, s[16:19], s51 offen offset:1024 lds\n\t"
      "v_mfma_f32_16x16x32_bf16 v[88:91], a[32:35], v[144:147], v[88:91]\n\t"
      "buffer_load_dwordx4 v213, s[16:19], s51 offen offset:2048 lds\n\t"
      "s_waitcnt vmcnt(15)\n\t"
      "ds_read_b64_tr_b16 a[50:51], v214 offset:24576\n\t"
      "v_mfma_f32_16x16x32_bf16 v[92:95], a[36:39], v[144:147], v[92:95]\n\t"
      "ds_read_b64_tr_b16 a[54:55], v214 offset:24584\n\t"
      "ds_read_b64_tr_b16 a[58:59], v214 offset:24640\n\t"
      "v_mfma_f32_16x16x32_bf16 v[96:99], a[40:43], v[144:147], v[96:99]\n\t"
      "ds_read_b64_tr_b16 a[62:63], v214 offset:24648\n\t"
      "ds_read_b64_tr_b16 a[66:67], v214 offset:24704\n\t"
      "v_mfma_f32_16x16x32_bf16 v[100:103], a[44:47], v[144:147], v[100:103]\n\t"
      "ds_read_b64_tr_b16 a[70:71], v214 offset:24712\n\t"
      "ds_read_b128 v[160:163], v198 offset:12288\n\t"
      "s_waitcnt lgkmcnt(8)\n\t"
      "v_mfma_f32_16x16x32_bf16 v[104:107], a[24:27], v[156:159], v[104:107]\n\t"
      "s_add_u32 m0, s41, 0x15000\n\t"
      "s_mov_b32 s51, 0x1ec00\n\t"
      "buffer_load_dwordx4 v211, s[16:19], s51 offen lds\n\t"
      "v_mfma_f32_16x16x32_bf16 v[108:111], a[28:31], v[156:159], v[108:111]\n\t"
      "buffer_load_dwordx4 v212, s[16:19], s51 offen offset:1024 lds\n\t"
      "v_mfma_f32_16x16x32_bf16 v[112:115], a[32:35], v[156:159], v[112:115]\n\t"
      "buffer_load_dwordx4 v213, s[16:19], s51 offen offset:2048 lds\n\t"
      "s_waitcnt vmcnt(15)\n\t"
      "ds_read_b64_tr_b16 a[72:73], v214 offset:36864\n\t"
      "v_mfma_f32_16x16x32_bf16 v[116:119], a[36:39], v[156:159], v[116:119]\n\t"
      "ds_read_b64_tr_b16 a[76:77], v214 offset:36872\n\t"
      "ds_read_b64_tr_b16 a[80:81], v214 offset:36928\n\t"
      "v_mfma_f32_16x16x32_bf16 v[120:123], a[40:43], v[156:159], v[120:123]\n\t"
      "ds_read_b64_tr_b16 a[84:85], v214 offset:36936\n\t"
      "ds_read_b64_tr_b16 a[88:89], v214 offset:36992\n\t"
      "v_mfma_f32_16x16x32_bf16 v[132:135], a[44:47], v[156:159], v[132:135]\n\t"
      "ds_read_b64_tr_b16 a[92:93], v214 offset:37000\n\t"
      "ds_read_b128 v[140:143], v199 offset:0\n\t"
      "s_waitcnt lgkmcnt(8)\n\t"
      "v_mfma_f32_16x16x32_bf16 v[80:83], a[48:51], v[148:151], v[80:83]\n\t"
      "s_add_u32 m0, s41, 0x18000\n\t"
      "s_mov_b32 s51, 0x24000\n\t"
      "buffer_load_dwordx4 v211, s[16:19], s51 offen lds\n\t"
      "v_mfma_f32_16x16x32_bf16 v[84:87], a[52:55], v[148:151], v[84:87]\n\t"
      "buffer_load_dwordx4 v212, s[16:19], s51 offen offset:1024 lds\n\t"
      "v_mfma_f32_16x16x32_bf16 v[88:91], a[56:59], v[148:151], v[88:91]\n\t"
      "buffer_load_dwordx4 v213, s[16:19], s51 offen offset:2048 lds\n\t"
      "s_waitcnt vmcnt(15)\n\t"
      "ds_read_b64_tr_b16 a[74:75], v214 offset:49152\n\t"
      "v_mfma_f32_16x16x32_bf16 v[92:95], a[60:63], v[148:151], v[92:95]\n\t"
      "ds_read_b64_tr_b16 a[78:79], v214 offset:49160\n\t"
      "ds_read_b64_tr_b16 a[82:83], v214 offset:49216\n\t"
      "v_mfma_f32_16x16x32_bf16 v[96:99], a[64:67], v[148:151], v[96:99]\n\t"
      "ds_read_b64_tr_b16 a[86:87], v214 offset:49224\n\t"
      "ds_read_b64_tr_b16 a[90:91], v214 offset:49280\n\t"
      "v_mfma_f32_16x16x32_bf16 v[100:103], a[68:71], v[148:151], v[100:103]\n\t"
      "ds_read_b64_tr_b16 a[94:95], v214 offset:49288\n\t"
      "ds_read_b128 v[152:155], v199 offset:12288\n\t"
      "s_waitcnt lgkmcnt(8)\n\t"
      "v_mfma_f32_16x16x32_bf16 v[104:107], a[48:51], v[160:163], v[104:107]\n\t"
      "s_add_u32 m0, s41, 0x1b000\n\t"
      "s_mov_b32 s51, 0x24c00\n\t"
      "buffer_load_dwordx4 v211, s[16:19], s51 offen lds\n\t"
      "v_mfma_f32_16x16x32_bf16 v[108:111], a[52:55], v[160:163], v[108:111]\n\t"
      "buffer_load_dwordx4 v212, s[16:19], s51 offen offset:1024 lds\n\t"
      "v_mfma_f32_16x16x32_bf16 v[112:115], a[56:59], v[160:163], v[112:115]\n\t"
      "buffer_load_dwordx4 v213, s[16:19], s51 offen offset:2048 lds\n\t"
      "s_waitcnt vmcnt(15)\n\t"
      "ds_read_b64_tr_b16 a[96:97], v221 offset:0\n\t"
      "v_mfma_f32_16x16x32_bf16 v[116:119], a[60:63], v[160:163], v[116:119]\n\t"
      "ds_read_b64_tr_b16 a[100:101], v221 offset:8\n\t"
      "ds_read_b64_tr_b16 a[104:105], v221 offset:64\n\t"
      "v_mfma_f32_16x16x32_bf16 v[120:123], a[64:67], v[160:163], v[120:123]\n\t"
      "ds_read_b64_tr_b16 a[108:109], v221 offset:72\n\t"
      "ds_read_b64_tr_b16 a[112:113], v221 offset:128\n\t"
      "v_mfma_f32_16x16x32_bf16 v[132:135], a[68:71], v[160:163], v[132:135]\n\t"
      "ds_read_b64_tr_b16 a[116:117], v221 offset:136\n\t"
      "ds_read_b128 v[144:147], v197 offset:256\n\t"
      "s_waitcnt lgkmcnt(8)\n\t"
      "v_mfma_f32_16x16x32_bf16 v[80:83], a[72:75], v[140:143], v[80:83]\n\t"
      "s_add_u32 m0, s41, 0x1e000\n\t"
      "s_mov_b32 s51, 0x2a000\n\t"
      "buffer_load_dwordx4 v211, s[16:19], s51 offen lds\n\t"
      "v_mfma_f32_16x16x32_bf16 v[84:87], a[76:79], v[140:143], v[84:87]\n\t"
      "buffer_load_dwordx4 v212, s[16:19], s51 offen offset:1024 lds\n\t"
      "v_mfma_f32_16x16x32_bf16 v[88:91], a[80:83], v[140:143], v[88:91]\n\t"
      "buffer_load_dwordx4 v213, s[16:19], s51 offen offset:2048 lds\n\t"
      "s_waitcnt vmcnt(15)\n\t"
      "ds_read_b64_tr_b16 a[98:99], v221 offset:12288\n\t"
      "v_mfma_f32_16x16x32_bf16 v[92:95], a[84:87], v[140:143], v[92:95]\n\t"
      "ds_read_b64_tr_b16 a[102:103], v221 offset:12296\n\t"
      "ds_read_b64_tr_b16 a[106:107], v221 offset:12352\n\t"
      "v_mfma_f32_16x16x32_bf16 v[96:99], a[88:91], v[140:143], v[96:99]\n\t"
      "ds_read_b64_tr_b16 a[110:111], v221 offset:12360\n\t"
      "ds_read_b64_tr_b16 a[114:115], v221 offset:12416\n\t"
      "v_mfma_f32_16x16x32_bf16 v[100:103], a[92:95], v[140:143], v[100:103]\n\t"
      "ds_read_b64_tr_b16 a[118:119], v221 offset:12424\n\t"
      "ds_read_b128 v[156:159], v197 offset:12544\n\t"
      "s_waitcnt lgkmcnt(8)\n\t"
      "v_mfma_f32_16x16x32_bf16 v[104:107], a[72:75], v[152:155], v[104:107]\n\t"
      "s_add_u32 m0, s41, 0x21000\n\t"
      "s_mov_b32 s51, 0x2ac00\n\t"
      "buffer_load_dwordx4 v211, s[16:19], s51 offen lds\n\t"
      "v_mfma_f32_16x16x32_bf16 v[108:111], a[76:79], v[152:155], v[108:111]\n\t"
      "buffer_load_dwordx4 v212, s[16:19], s51 offen offset:1024 lds\n\t"
      "v_mfma_f32_16x16x32_bf16 v[112:115], a[80:83], v[152:155], v[112:115]\n\t"
      "buffer_load_dwordx4 v213, s[16:19], s51 offen offset:2048 lds\n\t"
      "s_waitcnt vmcnt(15)\n\t"
      "ds_read_b64_tr_b16 a[120:121], v214 offset:0\n\t"
      "v_mfma_f32_16x16x32_bf16 v[116:119], a[84:87], v[152:155], v[116:119]\n\t"
      "ds_read_b64_tr_b16 a[124:125], v214 offset:8\n\t"
      "ds_read_b64_tr_b16 a[128:129], v214 offset:64\n\t"
      "v_mfma_f32_16x16x32_bf16 v[120:123], a[88:91], v[152:155], v[120:123]\n\t"
      "ds_read_b64_tr_b16 a[132:133], v214 offset:72\n\t"
      "ds_read_b64_tr_b16 a[136:137], v214 offset:128\n\t"
      "v_mfma_f32_16x16x32_bf16 v[132:135], a[92:95], v[152:155], v[132:135]\n\t"
      "ds_read_b64_tr_b16 a[140:141], v214 offset:136\n\t"
      "ds_read_b128 v[148:151], v198 offset:256\n\t"
      "s_waitcnt lgkmcnt(8)\n\t"
      "v_mfma_f32_16x16x32_bf16 v[80:83], a[96:99], v[144:147], v[80:83]\n\t"
      "s_add_u32 m0, s41, 0x24000\n\t"
      "s_mov_b32 s51, 0x30000\n\t"
      "buffer_load_dwordx4 v211, s[16:19], s51 offen lds\n\t"
      "v_mfma_f32_16x16x32_bf16 v[84:87], a[100:103], v[144:147], v[84:87]\n\t"
      "buffer_load_dwordx4 v212, s[16:19], s51 offen offset:1024 lds\n\t"
      "v_mfma_f32_16x16x32_bf16 v[88:91], a[104:107], v[144:147], v[88:91]\n\t"
      "buffer_load_dwordx4 v213, s[16:19], s51 offen offset:2048 lds\n\t"
      "s_waitcnt vmcnt(15)\n\t"
      "ds_read_b64_tr_b16 a[122:123], v214 offset:12288\n\t"
      "v_mfma_f32_16x16x32_bf16 v[92:95], a[108:111], v[144:147], v[92:95]\n\t"
      "ds_read_b64_tr_b16 a[126:127], v214 offset:12296\n\t"
      "ds_read_b64_tr_b16 a[130:131], v214 offset:12352\n\t"
      "v_mfma_f32_16x16x32_bf16 v[96:99], a[112:115], v[144:147], v[96:99]\n\t"
      "ds_read_b64_tr_b16 a[134:135], v214 offset:12360\n\t"
      "ds_read_b64_tr_b16 a[138:139], v214 offset:12416\n\t"
      "v_mfma_f32_16x16x32_bf16 v[100:103], a[116:119], v[144:147], v[100:103]\n\t"
      "ds_read_b64_tr_b16 a[142:143], v214 offset:12424\n\t"
      "ds_read_b128 v[160:163], v198 offset:12544\n\t"
      "s_waitcnt lgkmcnt(8)\n\t"
      "v_mfma_f32_16x16x32_bf16 v[104:107], a[96:99], v[156:159], v[104:107]\n\t"
      "s_add_u32 m0, s41, 0x12000\n\t"
      "s_mov_b32 s51, 0x30c00\n\t"
      "buffer_load_dwordx4 v211, s[16:19], s51 offen lds\n\t"
      "v_mfma_f32_16x16x32_bf16 v[108:111], a[100:103], v[156:159], v[108:111]\n\t"
      "buffer_load_dwordx4 v212, s[16:19], s51 offen offset:1024 lds\n\t"
      "v_mfma_f32_16x16x32_bf16 v[112:115], a[104:107], v[156:159], v[112:115]\n\t"
      "buffer_load_dwordx4 v213, s[16:19], s51 offen offset:2048 lds\n\t"
      "s_waitcnt vmcnt(15)\n\t"
      "ds_read_b64_tr_b16 a[144:145], v214 offset:24576\n\t"
      "v_mfma_f32_16x16x32_bf16 v[116:119], a[108:111], v[156:159], v[116:119]\n\t"
      "ds_read_b64_tr_b16 a[148:149], v214 offset:24584\n\t"
      "ds_read_b64_tr_b16 a[152:153], v214 offset:24640\n\t"
      "v_mfma_f32_16x16x32_bf16 v[120:123], a[112:115], v[156:159], v[120:123]\n\t"
      "ds_read_b64_tr_b16 a[156:157], v214 offset:24648\n\t"
      "ds_read_b64_tr_b16 a[160:161], v214 offset:24704\n\t"
      "v_mfma_f32_16x16x32_bf16 v[132:135], a[116:119], v[156:159], v[132:135]\n\t"
      "ds_read_b64_tr_b16 a[164:165], v214 offset:24712\n\t"
      "ds_read_b128 v[140:143], v199 offset:256\n\t"
      "s_waitcnt lgkmcnt(8)\n\t"
      "v_mfma_f32_16x16x32_bf16 v[80:83], a[120:123], v[148:151], v[80:83]\n\t"
      "s_add_u32 m0, s41, 0x15000\n\t"
      "s_mov_b32 s51, 0x36000\n\t"
      "buffer_load_dwordx4 v211, s[16:19], s51 offen lds\n\t"
      "v_mfma_f32_16x16x32_bf16 v[84:87], a[124:127], v[148:151], v[84:87]\n\t"
      "buffer_load_dwordx4 v212, s[16:19], s51 offen offset:1024 lds\n\t"
      "v_mfma_f32_16x16x32_bf16 v[88:91], a[128:131], v[148:151], v[88:91]\n\t"
      "buffer_load_dwordx4 v213, s[16:19], s51 offen offset:2048 lds\n\t"
      "s_waitcnt vmcnt(15)\n\t"
      "ds_read_b64_tr_b16 a[146:147], v214 offset:36864\n\t"
      "v_mfma_f32_16x16x32_bf16 v[92:95], a[132:135], v[148:151], v[92:95]\n\t"
      "ds_read_b64_tr_b16 a[150:151], v214 offset:36872\n\t"
      "ds_read_b64_tr_b16 a[154:155], v214 offset:36928\n\t"
      "v_mfma_f32_16x16x32_bf16 v[96:99], a[136:139], v[148:151], v[96:99]\n\t"
      "ds_read_b64_tr_b16 a[158:159], v214 offset:36936\n\t"
      "ds_read_b64_tr_b16 a[162:163], v214 offset:36992\n\t"
      "v_mfma_f32_16x16x32_bf16 v[100:103], a[140:143], v[148:151], v[100:103]\n\t"
      "ds_read_b64_tr_b16 a[166:167], v214 offset:37000\n\t"
      "ds_read_b128 v[152:155], v199 offset:12544\n\t"
      "s_waitcnt lgkmcnt(8)\n\t"
      "v_mfma_f32_16x16x32_bf16 v[104:107], a[120:123], v[160:163], v[104:107]\n\t"
      "s_add_u32 m0, s41, 0x18000\n\t"
      "s_mov_b32 s51, 0x36c00\n\t"
      "buffer_load_dwordx4 v211, s[16:19], s51 offen lds\n\t"
      "v_mfma_f32_16x16x32_bf16 v[108:111], a[124:127], v[160:163], v[108:111]\n\t"
      "buffer_load_dwordx4 v212, s[16:19], s51 offen offset:1024 lds\n\t"
      "v_mfma_f32_16x16x32_bf16 v[112:115], a[128:131], v[160:163], v[112:115]\n\t"
      "buffer_load_dwordx4 v213, s[16:19], s51 offen offset:2048 lds\n\t"
      "s_waitcnt vmcnt(15)\n\t"
      "ds_read_b64_tr_b16 a[168:169], v214 offset:49152\n\t"
      "v_mfma_f32_16x16x32_bf16 v[116:119], a[132:135], v[160:163], v[116:119]\n\t"
      "ds_read_b64_tr_b16 a[172:173], v214 offset:49160\n\t"
      "ds_read_b64_tr_b16 a[176:177], v214 offset:49216\n\t"
      "v_mfma_f32_16x16x32_bf16 v[120:123], a[136:139], v[160:163], v[120:123]\n\t"
      "ds_read_b64_tr_b16 a[180:181], v214 offset:49224\n\t"
      "ds_read_b64_tr_b16 a[184:185], v214 offset:49280\n\t"
      "v_mfma_f32_16x16x32_bf16 v[132:135], a[140:143], v[160:163], v[132:135]\n\t"
      "ds_read_b64_tr_b16 a[188:189], v214 offset:49288\n\t"
      "ds_read_b128 v[144:147], v196 offset:256\n\t"
      "s_waitcnt lgkmcnt(8)\n\t"
      "v_mfma_f32_16x16x32_bf16 v[80:83], a[144:147], v[140:143], v[80:83]\n\t"
      "s_add_u32 m0, s41, 0x1b000\n\t"
      "s_mov_b32 s51, 0x3c000\n\t"
      "buffer_load_dwordx4 v211, s[16:19], s51 offen lds\n\t"
      "v_mfma_f32_16x16x32_bf16 v[84:87], a[148:151], v[140:143], v[84:87]\n\t"
      "buffer_load_dwordx4 v212, s[16:19], s51 offen offset:1024 lds\n\t"
      "v_mfma_f32_16x16x32_bf16 v[88:91], a[152:155], v[140:143], v[88:91]\n\t"
      "buffer_load_dwordx4 v213, s[16:19], s51 offen offset:2048 lds\n\t"
      "s_waitcnt vmcnt(15)\n\t"
      "ds_read_b64_tr_b16 a[170:171], v221 offset:0\n\t"
      "v_mfma_f32_16x16x32_bf16 v[92:95], a[156:159], v[140:143], v[92:95]\n\t"
      "ds_read_b64_tr_b16 a[174:175], v221 offset:8\n\t"
      "ds_read_b64_tr_b16 a[178:179], v221 offset:64\n\t"
      "v_mfma_f32_16x16x32_bf16 v[96:99], a[160:163], v[140:143], v[96:99]\n\t"
      "ds_read_b64_tr_b16 a[182:183], v221 offset:72\n\t"
      "ds_read_b64_tr_b16 a[186:187], v221 offset:128\n\t"
      "v_mfma_f32_16x16x32_bf16 v[100:103], a[164:167], v[140:143], v[100:103]\n\t"
      "ds_read_b64_tr_b16 a[190:191], v221 offset:136\n\t"
      "ds_read_b128 v[156:159], v196 offset:12544\n\t"
      "s_waitcnt lgkmcnt(8)\n\t"
      "v_mfma_f32_16x16x32_bf16 v[104:107], a[144:147], v[152:155], v[104:107]\n\t"
      "s_add_u32 m0, s41, 0x1e000\n\t"
      "s_mov_b32 s51, 0x3cc00\n\t"
      "buffer_load_dwordx4 v211, s[16:19], s51 offen lds\n\t"
      "v_mfma_f32_16x16x32_bf16 v[108:111], a[148:151], v[152:155], v[108:111]\n\t"
      "buffer_load_dwordx4 v212, s[16:19], s51 offen offset:1024 lds\n\t"
      "v_mfma_f32_16x16x32_bf16 v[112:115], a[152:155], v[152:155], v[112:115]\n\t"
      "buffer_load_dwordx4 v213, s[16:19], s51 offen offset:2048 lds\n\t"
      "s_waitcnt vmcnt(15)\n\t"
      "ds_read_b64_tr_b16 a[192:193], v221 offset:12288\n\t"
      "v_mfma_f32_16x16x32_bf16 v[116:119], a[156:159], v[152:155], v[116:119]\n\t"
      "ds_read_b64_tr_b16 a[196:197], v221 offset:12296\n\t"
      "ds_read_b64_tr_b16 a[200:201], v221 offset:12352\n\t"
      "v_mfma_f32_16x16x32_bf16 v[120:123], a[160:163], v[152:155], v[120:123]\n\t"
      "ds_read_b64_tr_b16 a[204:205], v221 offset:12360\n\t"
      "ds_read_b64_tr_b16 a[208:209], v221 offset:12416\n\t"
      "v_mfma_f32_16x16x32_bf16 v[132:135], a[164:167], v[152:155], v[132:135]\n\t"
      "ds_read_b64_tr_b16 a[212:213], v221 offset:12424\n\t"
      "ds_read_b128 v[148:151], v198 offset:512\n\t"
      "s_waitcnt lgkmcnt(8)\n\t"
      "v_mfma_f32_16x16x32_bf16 v[80:83], a[168:171], v[144:147], v[80:83]\n\t"
      "s_add_u32 m0, s41, 0x21000\n\t"
      "s_mov_b32 s51, 0x42000\n\t"
      "buffer_load_dwordx4 v211, s[16:19], s51 offen lds\n\t"
      "v_mfma_f32_16x16x32_bf16 v[84:87], a[172:175], v[144:147], v[84:87]\n\t"
      "buffer_load_dwordx4 v212, s[16:19], s51 offen offset:1024 lds\n\t"
      "v_mfma_f32_16x16x32_bf16 v[88:91], a[176:179], v[144:147], v[88:91]\n\t"
      "buffer_load_dwordx4 v213, s[16:19], s51 offen offset:2048 lds\n\t"
      "s_waitcnt vmcnt(15)\n\t"
      "ds_read_b64_tr_b16 a[194:195], v214 offset:0\n\t"
      "v_mfma_f32_16x16x32_bf16 v[92:95], a[180:183], v[144:147], v[92:95]\n\t"
      "ds_read_b64_tr_b16 a[198:199], v214 offset:8\n\t"
      "ds_read_b64_tr_b16 a[202:203], v214 offset:64\n\t"
      "v_mfma_f32_16x16x32_bf16 v[96:99], a[184:187], v[144:147], v[96:99]\n\t"
      "ds_read_b64_tr_b16 a[206:207], v214 offset:72\n\t"
      "ds_read_b64_tr_b16 a[210:211], v214 offset:128\n\t"
      "v_mfma_f32_16x16x32_bf16 v[100:103], a[188:191], v[144:147], v[100:103]\n\t"
      "ds_read_b64_tr_b16 a[214:215], v214 offset:136\n\t"
      "ds_read_b128 v[160:163], v198 offset:12800\n\t"
      "s_waitcnt lgkmcnt(8)\n\t"
      "v_mfma_f32_16x16x32_bf16 v[104:107], a[168:171], v[156:159], v[104:107]\n\t"
      "s_add_u32 m0, s41, 0x24000\n\t"
      "s_mov_b32 s51, 0x42c00\n\t"
      "buffer_load_dwordx4 v211, s[16:19], s51 offen lds\n\t"
      "v_mfma_f32_16x16x32_bf16 v[108:111], a[172:175], v[156:159], v[108:111]\n\t"
      "buffer_load_dwordx4 v212, s[16:19], s51 offen offset:1024 lds\n\t"
      "v_mfma_f32_16x16x32_bf16 v[112:115], a[176:179], v[156:159], v[112:115]\n\t"
      "buffer_load_dwordx4 v213, s[16:19], s51 offen offset:2048 lds\n\t"
      "s_waitcnt vmcnt(15)\n\t"
      "ds_read_b64_tr_b16 a[216:217], v214 offset:12288\n\t"
      "v_mfma_f32_16x16x32_bf16 v[116:119], a[180:183], v[156:159], v[116:119]\n\t"
      "ds_read_b64_tr_b16 a[220:221], v214 offset:12296\n\t"
      "ds_read_b64_tr_b16 a[224:225], v214 offset:12352\n\t"
      "v_mfma_f32_16x16x32_bf16 v[120:123], a[184:187], v[156:159], v[120:123]\n\t"
      "ds_read_b64_tr_b16 a[228:229], v214 offset:12360\n\t"
      "ds_read_b64_tr_b16 a[232:233], v214 offset:12416\n\t"
      "v_mfma_f32_16x16x32_bf16 v[132:135], a[188:191], v[156:159], v[132:135]\n\t"
      "ds_read_b64_tr_b16 a[236:237], v214 offset:12424\n\t"
      "ds_read_b128 v[140:143], v199 offset:512\n\t"
      "s_waitcnt lgkmcnt(8)\n\t"
      "v_mfma_f32_16x16x32_bf16 v[80:83], a[192:195], v[148:151], v[80:83]\n\t"
      "s_add_u32 m0, s41, 0x6000\n\t"
      "s_add_u32 s51, s40, 0x600000\n\t"
      "buffer_load_dwordx4 v208, s[20:23], s51 offen lds\n\t"
      "v_mfma_f32_16x16x32_bf16 v[84:87], a[196:199], v[148:151], v[84:87]\n\t"
      "buffer_load_dwordx4 v208, s[20:23], s51 offen offset:1024 lds\n\t"
      "v_mfma_f32_16x16x32_bf16 v[88:91], a[200:203], v[148:151], v[88:91]\n\t"
      "buffer_load_dwordx4 v208, s[20:23], s51 offen offset:2048 lds\n\t"
      "s_waitcnt vmcnt(15)\n\t"
      "ds_read_b64_tr_b16 a[218:219], v214 offset:24576\n\t"
      "v_mfma_f32_16x16x32_bf16 v[92:95], a[204:207], v[148:151], v[92:95]\n\t"
      "ds_read_b64_tr_b16 a[222:223], v214 offset:24584\n\t"
      "ds_read_b64_tr_b16 a[226:227], v214 offset:24640\n\t"
      "v_mfma_f32_16x16x32_bf16 v[96:99], a[208:211], v[148:151], v[96:99]\n\t"
      "ds_read_b64_tr_b16 a[230:231], v214 offset:24648\n\t"
      "ds_read_b64_tr_b16 a[234:235], v214 offset:24704\n\t"
      "v_mfma_f32_16x16x32_bf16 v[100:103], a[212:215], v[148:151], v[100:103]\n\t"
      "ds_read_b64_tr_b16 a[238:239], v214 offset:24712\n\t"
      "ds_read_b128 v[152:155], v199 offset:12800\n\t"
      "s_waitcnt lgkmcnt(14)\n\t"
      "v_mfma_f32_16x16x32_bf16 v[104:107], a[192:195], v[160:163], v[104:107]\n\t"
      "s_add_u32 m0, s41, 0x9000\n\t"
      "s_add_u32 s51, s40, 0x600000\n\t"
      "buffer_load_dwordx4 v209, s[20:23], s51 offen lds\n\t"
      "v_mfma_f32_16x16x32_bf16 v[108:111], a[196:199], v[160:163], v[108:111]\n\t"
      "buffer_load_dwordx4 v209, s[20:23], s51 offen offset:1024 lds\n\t"
      "v_mfma_f32_16x16x32_bf16 v[112:115], a[200:203], v[160:163], v[112:115]\n\t"
      "buffer_load_dwordx4 v209, s[20:23], s51 offen offset:2048 lds\n\t"
      "s_waitcnt vmcnt(15)\n\t"
      "ds_read_b64_tr_b16 a[240:241], v214 offset:36864\n\t"
      "v_mfma_f32_16x16x32_bf16 v[116:119], a[204:207], v[160:163], v[116:119]\n\t"
      "ds_read_b64_tr_b16 a[244:245], v214 offset:36872\n\t"
      "ds_read_b64_tr_b16 a[248:249], v214 offset:36928\n\t"
      "v_mfma_f32_16x16x32_bf16 v[120:123], a[208:211], v[160:163], v[120:123]\n\t"
      "ds_read_b64_tr_b16 a[252:253], v214 offset:36936\n\t"
      "ds_read_b64_tr_b16 v[164:165], v214 offset:36992\n\t"
      "v_mfma_f32_16x16x32_bf16 v[132:135], a[212:215], v[160:163], v[132:135]\n\t"
      "ds_read_b64_tr_b16 v[168:169], v214 offset:37000\n\t"
      "ds_read_b128 v[144:147], v196 offset:512\n\t"
      "s_waitcnt lgkmcnt(8)\n\t"
      "v_mfma_f32_16x16x32_bf16 v[80:83], a[216:219], v[140:143], v[80:83]\n\t"
      "s_add_u32 m0, s41, 0xc000\n\t"
      "s_add_u32 s51, s40, 0xc00000\n\t"
      "buffer_load_dwordx4 v208, s[20:23], s51 offen lds\n\t"
      "v_mfma_f32_16x16x32_bf16 v[84:87], a[220:223], v[140:143], v[84:87]\n\t"
      "buffer_load_dwordx4 v208, s[20:23], s51 offen offset:1024 lds\n\t"
      "v_mfma_f32_16x16x32_bf16 v[88:91], a[224:227], v[140:143], v[88:91]\n\t"
      "buffer_load_dwordx4 v208, s[20:23], s51 offen offset:2048 lds\n\t"
      "s_waitcnt vmcnt(15)\n\t"
      "ds_read_b64_tr_b16 a[242:243], v214 offset:49152\n\t"
      "v_mfma_f32_16x16x32_bf16 v[92:95], a[228:231], v[140:143], v[92:95]\n\t"
      "ds_read_b64_tr_b16 a[246:247], v214 offset:49160\n\t"
      "ds_read_b64_tr_b16 a[250:251], v214 offset:49216\n\t"
      "v_mfma_f32_16x16x32_bf16 v[96:99], a[232:235], v[140:143], v[96:99]\n\t"
      "ds_read_b64_tr_b16 a[254:255], v214 offset:49224\n\t"
      "ds_read_b64_tr_b16 v[166:167], v214 offset:49280\n\t"
      "v_mfma_f32_16x16x32_bf16 v[100:103], a[236:239], v[140:143], v[100:103]\n\t"
      "ds_read_b64_tr_b16 v[170:171], v214 offset:49288\n\t"
      "ds_read_b128 v[156:159], v196 offset:12800\n\t"
      "s_waitcnt lgkmcnt(14)\n\t"
      "v_mfma_f32_16x16x32_bf16 v[104:107], a[216:219], v[152:155], v[104:107]\n\t"
      "s_add_u32 m0, s41, 0xf000\n\t"
      "s_add_u32 s51, s40, 0xc00000\n\t"
      "buffer_load_dwordx4 v209, s[20:23], s51 offen lds\n\t"
      "v_mfma_f32_16x16x32_bf16 v[108:111], a[220:223], v[152:155], v[108:111]\n\t"
      "buffer_load_dwordx4 v209, s[20:23], s51 offen offset:1024 lds\n\t"
      "v_mfma_f32_16x16x32_bf16 v[112:115], a[224:227], v[152:155], v[112:115]\n\t"
      "buffer_load_dwordx4 v209, s[20:23], s51 offen offset:2048 lds\n\t"
      "s_waitcnt vmcnt(15)\n\t"
      "ds_read_b64_tr_b16 v[172:173], v221 offset:0\n\t"
      "v_mfma_f32_16x16x32_bf16 v[116:119], a[228:231], v[152:155], v[116:119]\n\t"
      "ds_read_b64_tr_b16 v[176:177], v221 offset:8\n\t"
      "ds_read_b64_tr_b16 v[180:181], v221 offset:64\n\t"
      "ds_read_b64_tr_b16 v[184:185], v221 offset:72\n\t"
      "v_mfma_f32_16x16x32_bf16 v[120:123], a[232:235], v[152:155], v[120:123]\n\t"
      "ds_read_b64_tr_b16 v[188:189], v221 offset:128\n\t"
      "ds_read_b64_tr_b16 v[192:193], v221 offset:136\n\t"
      "v_mfma_f32_16x16x32_bf16 v[132:135], a[236:239], v[152:155], v[132:135]\n\t"
      "ds_read_b128 v[148:151], v197 offset:512\n\t"
      "ds_read_b128 v[160:163], v197 offset:12800\n\t"
      "s_waitcnt lgkmcnt(9)\n\t"
      "v_mfma_f32_16x16x32_bf16 v[80:83], a[240:243], v[144:147], v[80:83]\n\t"
      "buffer_load_dwordx2 v[240:241], v245, s[24:27], 0 offen sc1\n\t"
      "v_mfma_f32_16x16x32_bf16 v[84:87], a[244:247], v[144:147], v[84:87]\n\t"
      "s_cmp_eq_u32 s41, s43\n\t"
      "s_cbranch_scc0 .Lc2_%=\n\t"
      "buffer_load_dword v254, v253, s[24:27], 0 offen sc1\n\t"
      ".Lc2_%=:\n\t"
      "v_mfma_f32_16x16x32_bf16 v[88:91], a[248:251], v[144:147], v[88:91]\n\t"
      "s_waitcnt vmcnt(13)\n\t"
      "ds_read_b64_tr_b16 v[174:175], v221 offset:12288\n\t"
      "v_mfma_f32_16x16x32_bf16 v[92:95], a[252:255], v[144:147], v[92:95]\n\t"
      "ds_read_b64_tr_b16 v[178:179], v221 offset:12296\n\t"
      "ds_read_b64_tr_b16 v[182:183], v221 offset:12352\n\t"
      "v_mfma_f32_16x16x32_bf16 v[96:99], v[164:167], v[144:147], v[96:99]\n\t"
      "ds_read_b64_tr_b16 v[186:187], v221 offset:12360\n\t"
      "ds_read_b64_tr_b16 v[190:191], v221 offset:12416\n\t"
      "v_mfma_f32_16x16x32_bf16 v[100:103], v[168:171], v[144:147], v[100:103]\n\t"
      "ds_read_b64_tr_b16 v[194:195], v221 offset:12424\n\t"
      "s_memrealtime s[80:81]\n\t"
      "v_add_u32_e32 v196, s46, v200\n\t"
      "v_add_u32_e32 v197, s46, v201\n\t"
      "v_add_u32_e32 v198, s46, v202\n\t"
      "v_add_u32_e32 v199, s46, v203\n\t"
      "s_waitcnt vmcnt(10)\n\t"
      "s_waitcnt lgkmcnt(6)\n\t"
      "s_barrier\n\t"
      "v_mfma_f32_16x16x32_bf16 v[104:107], a[240:243], v[156:159], v[104:107]\n\t"
      "ds_read_b128 v[32:35], v196 offset:0\n\t"
      "v_mfma_f32_16x16x32_bf16 v[108:111], a[244:247], v[156:159], v[108:111]\n\t"
      "ds_read_b128 v[36:39], v197 offset:0\n\t"
      "v_mfma_f32_16x16x32_bf16 v[112:115], a[248:251], v[156:159], v[112:115]\n\t"
      "ds_read_b128 v[40:43], v198 offset:0\n\t"
      "v_mfma_f32_16x16x32_bf16 v[116:119], a[252:255], v[156:159], v[116:119]\n\t"
      "ds_read_b128 v[44:47], v199 offset:0\n\t"
      "v_mfma_f32_16x16x32_bf16 v[120:123], v[164:167], v[156:159], v[120:123]\n\t"
      "ds_read_b128 v[48:51], v197 offset:256\n\t"
      "v_mfma_f32_16x16x32_bf16 v[132:135], v[168:171], v[156:159], v[132:135]\n\t"
      "s_waitcnt lgkmcnt(5)\n\t"
      "s_mov_b32 s86, 80\n\t"
      "s_mov_b32 s84, 92\n\t"
      "v_mfma_f32_16x16x32_bf16 v[80:83], v[172:175], v[148:151], v[80:83]\n\t"
      "ds_read_b128 v[52:55], v198 offset:256\n\t"
      "v_mfma_f32_16x16x32_bf16 v[84:87], v[176:179], v[148:151], v[84:87]\n\t"
      "ds_read_b128 v[56:59], v199 offset:256\n\t"
      "v_mfma_f32_16x16x32_bf16 v[88:91], v[180:183], v[148:151], v[88:91]\n\t"
      "ds_read_b128 v[60:63], v196 offset:256\n\t"
      "v_mfma_f32_16x16x32_bf16 v[92:95], v[184:187], v[148:151], v[92:95]\n\t"
      "ds_read_b128 v[64:67], v198 offset:512\n\t"
      "v_mfma_f32_16x16x32_bf16 v[96:99], v[188:191], v[148:151], v[96:99]\n\t"
      "ds_read_b128 v[68:71], v199 offset:512\n\t"
      "v_mfma_f32_16x16x32_bf16 v[100:103], v[192:195], v[148:151], v[100:103]\n\t"
      "ds_read_b128 v[72:75], v196 offset:512\n\t"
      "v_mfma_f32_16x16x32_bf16 v[104:107], v[172:175], v[160:163], v[104:107]\n\t"
      "ds_read_b128 v[76:79], v197 offset:512\n\t"
      "v_mfma_f32_16x16x32_bf16 v[108:111], v[176:179], v[160:163], v[108:111]\n\t"
      "v_mfma_f32_16x16x32_bf16 v[112:115], v[180:183], v[160:163], v[112:115]\n\t"
      "v_mfma_f32_16x16x32_bf16 v[116:119], v[184:187], v[160:163], v[116:119]\n\t"
      "v_mfma_f32_16x16x32_bf16 v[120:123], v[188:191], v[160:163], v[120:123]\n\t"
      "v_mfma_f32_16x16x32_bf16 v[132:135], v[192:195], v[160:163], v[132:135]\n\t"
      ".Lp2_%=:\n\t"
      "s_waitcnt lgkmcnt(8)\n\t"
      "v_mfma_f32_16x16x32_bf16 v[128:131], a[0:3], v[32:35], v[8:11]\n\t"
      "v_cvt_pk_bf16_f32 v80, v80, v81\n\t"
      "v_cvt_pk_bf16_f32 v81, v82, v83\n\t"
      "v_mfma_f32_16x16x32_bf16 v[128:131], a[24:27], v[36:39], v[128:131]\n\t"
      "v_cvt_pk_bf16_f32 v136, v120, v121\n\t"
      "v_cvt_pk_bf16_f32 v137, v122, v123\n\t"
      "v_mfma_f32_16x16x32_bf16 v[128:131], a[48:51], v[40:43], v[128:131]\n\t"
      "v_cvt_pk_bf16_f32 v138, v132, v133\n\t"
      "v_mfma_f32_16x16x32_bf16 v[128:131], a[72:75], v[44:47], v[128:131]\n\t"
      "v_cvt_pk_bf16_f32 v139, v134, v135\n\t"
      "s_waitcnt lgkmcnt(4)\n\t"
      "v_mfma_f32_16x16x32_bf16 v[128:131], a[96:99], v[48:51], v[128:131]\n\t"
      "ds_write_b128 v219, v[136:139] offset:12288\n\t"
      "v_mfma_f32_16x16x32_bf16 v[128:131], a[120:123], v[52:55], v[128:131]\n\t"
      "v_cvt_pk_bf16_f32 v82, v84, v85\n\t"
      "v_cvt_pk_bf16_f32 v83, v86, v87\n\t"
      "v_mfma_f32_16x16x32_bf16 v[128:131], a[144:147], v[56:59], v[128:131]\n\t"
      "v_cvt_pk_bf16_f32 v88, v88, v89\n\t"
      "v_cvt_pk_bf16_f32 v89, v90, v91\n\t"
      "v_mfma_f32_16x16x32_bf16 v[128:131], a[168:171], v[60:63], v[128:131]\n\t"
      "ds_write_b128 v217, v[80:83] offset:0\n\t"
      "v_cvt_pk_bf16_f32 v90, v92, v93\n\t"
      "s_waitcnt lgkmcnt(2)\n\t"
      "v_mfma_f32_16x16x32_bf16 v[128:131], a[192:195], v[64:67], v[128:131]\n\t"
      "v_cvt_pk_bf16_f32 v91, v94, v95\n\t"
      "v_cvt_pk_bf16_f32 v96, v96, v97\n\t"
      "v_mfma_f32_16x16x32_bf16 v[128:131], a[216:219], v[68:71], v[128:131]\n\t"
      "ds_write_b128 v218, v[88:91] offset:0\n\t"
      "v_cvt_pk_bf16_f32 v97, v98, v99\n\t"
      "v_mfma_f32_16x16x32_bf16 v[128:131], a[240:243], v[72:75], v[128:131]\n\t"
      "v_cvt_pk_bf16_f32 v98, v100, v101\n\t"
      "v_cvt_pk_bf16_f32 v99, v102, v103\n\t"
      "v_mfma_f32_16x16x32_bf16 v[128:131], v[172:175], v[76:79], v[128:131]\n\t"
      "v_cvt_pk_bf16_f32 v104, v104, v105\n\t"
      "v_cvt_pk_bf16_f32 v105, v106, v107\n\t"
      "v_mfma_f32_16x16x32_bf16 v[132:135], a[4:7], v[32:35], v[12:15]\n\t"
      "ds_write_b128 v219, v[96:99] offset:0\n\t"
      "v_cvt_pk_bf16_f32 v106, v108, v109\n\t"
      "v_mfma_f32_16x16x32_bf16 v[132:135], a[28:31], v[36:39], v[132:135]\n\t"
      "v_cvt_pk_bf16_f32 v107, v110, v111\n\t"
      "v_cvt_pk_bf16_f32 v112, v112, v113\n\t"
      "v_mfma_f32_16x16x32_bf16 v[132:135], a[52:55], v[40:43], v[132:135]\n\t"
      "v_cvt_pk_bf16_f32 v136, v128, v129\n\t"
      "v_mfma_f32_16x16x32_bf16 v[132:135], a[76:79], v[44:47], v[132:135]\n\t"
      "v_cvt_pk_bf16_f32 v137, v130, v131\n\t"
      "v_mfma_f32_16x16x32_bf16 v[132:135], a[100:103], v[48:51], v[132:135]\n\t"
      "ds_write_b128 v217, v[104:107] offset:12288\n\t"
      "v_cvt_pk_bf16_f32 v113, v114, v115\n\t"
      "s_add_u32 s48, s47, 0x6000\n\t"
      "v_mfma_f32_16x16x32_bf16 v[132:135], a[124:127], v[52:55], v[132:135]\n\t"
      "v_cvt_pk_bf16_f32 v114, v116, v117\n\t"
      "v_cvt_pk_bf16_f32 v115, v118, v119\n\t"
      "s_cmp_eq_u32 s48, s50\n\t"
      "s_cselect_b32 s48, s41, s48\n\t"
      "v_mfma_f32_16x16x32_bf16 v[132:135], a[148:151], v[56:59], v[132:135]\n\t"
      "ds_write_b128 v218, v[112:115] offset:12288\n\t"
      "v_mfma_f32_16x16x32_bf16 v[132:135], a[172:175], v[60:63], v[132:135]\n\t"
      "v_add_u32_e32 v217, s46, v204\n\t"
      "v_mfma_f32_16x16x32_bf16 v[132:135], a[196:199], v[64:67], v[132:135]\n\t"
      "v_add_u32_e32 v218, s46, v205\n\t"
      "v_mfma_f32_16x16x32_bf16 v[132:135], a[220:223], v[68:71], v[132:135]\n\t"
      "v_readlane_b32 s73, v248, s85\n\t"
      "v_add_u32_e32 v219, s46, v206\n\t"
      "v_mfma_f32_16x16x32_bf16 v[132:135], a[244:247], v[72:75], v[132:135]\n\t"
      "v_readlane_b32 s72, v248, s44\n\t"
      "s_mov_b32 m0, s45\n\t"
      "v_mfma_f32_16x16x32_bf16 v[132:135], v[176:179], v[76:79], v[132:135]\n\t"
      "s_waitcnt lgkmcnt(0)\n\t"
      "s_barrier\n\t"
      "v_mfma_f32_16x16x32_bf16 v[128:131], a[8:11], v[32:35], v[16:19]\n\t"
      "ds_read_b128 v[140:143], v220 offset:0\n\t"
      "s_mov_b32 s82, s80\n\t"
      "v_mfma_f32_16x16x32_bf16 v[128:131], a[32:35], v[36:39], v[128:131]\n\t"
      "ds_read_b128 v[144:147], v220 offset:1024\n\t"
      "s_memrealtime s[80:81]\n\t"
      "v_mfma_f32_16x16x32_bf16 v[128:131], a[56:59], v[40:43], v[128:131]\n\t"
      "v_cvt_pk_bf16_f32 v138, v132, v133\n\t"
      "v_mfma_f32_16x16x32_bf16 v[128:131], a[80:83], v[44:47], v[128:131]\n\t"
      "v_cvt_pk_bf16_f32 v139, v134, v135\n\t"
      "v_mfma_f32_16x16x32_bf16 v[128:131], a[104:107], v[48:51], v[128:131]\n\t"
      "ds_write_b128 v217, v[136:139] offset:0\n\t"
      "v_mfma_f32_16x16x32_bf16 v[128:131], a[128:131], v[52:55], v[128:131]\n\t"
      "buffer_load_dwordx4 v208, s[20:23], s73 offen lds\n\t"
      "v_mfma_f32_16x16x32_bf16 v[128:131], a[152:155], v[56:59], v[128:131]\n\t"
      "ds_read_b128 v[148:151], v220 offset:2048\n\t"
      "v_mfma_f32_16x16x32_bf16 v[128:131], a[176:179], v[60:63], v[128:131]\n\t"
      "ds_read_b128 v[152:155], v220 offset:12288\n\t"
      "v_mfma_f32_16x16x32_bf16 v[128:131], a[200:203], v[64:67], v[128:131]\n\t"
      "ds_read_b128 v[156:159], v220 offset:13312\n\t"
      "v_mfma_f32_16x16x32_bf16 v[128:131], a[224:227], v[68:71], v[128:131]\n\t"
      "s_waitcnt lgkmcnt(5)\n\t"
      "buffer_store_dwordx4 v[140:143], v208, s[28:31], s72 offen sc1\n\t"
      "v_mfma_f32_16x16x32_bf16 v[128:131], a[248:251], v[72:75], v[128:131]\n\t"
      "ds_read_b128 v[160:163], v220 offset:14336\n\t"
      "v_mfma_f32_16x16x32_bf16 v[128:131], v[180:183], v[76:79], v[128:131]\n\t"
      "v_mfma_f32_16x16x32_bf16 v[132:135], a[12:15], v[32:35], v[20:23]\n\t"
      "v_mfma_f32_16x16x32_bf16 v[132:135], a[36:39], v[36:39], v[132:135]\n\t"
      "buffer_load_dwordx4 v208, s[20:23], s73 offen offset:1024 lds\n\t"
      "v_mfma_f32_16x16x32_bf16 v[132:135], a[60:63], v[40:43], v[132:135]\n\t"
      "v_cvt_pk_bf16_f32 v136, v128, v129\n\t"
      "v_mfma_f32_16x16x32_bf16 v[132:135], a[84:87], v[44:47], v[132:135]\n\t"
      "v_cvt_pk_bf16_f32 v137, v130, v131\n\t"
      "v_mfma_f32_16x16x32_bf16 v[132:135], a[108:111], v[48:51], v[132:135]\n\t"
      "v_mfma_f32_16x16x32_bf16 v[132:135], a[132:135], v[52:55], v[132:135]\n\t"
      "s_waitcnt lgkmcnt(5)\n\t"
      "buffer_store_dwordx4 v[144:147], v208, s[28:31], s72 offen offset:1024 sc1\n\t"
      "v_mfma_f32_16x16x32_bf16 v[132:135], a[156:159], v[56:59], v[132:135]\n\t"
      "v_mfma_f32_16x16x32_bf16 v[132:135], a[180:183], v[60:63], v[132:135]\n\t"
      "v_mfma_f32_16x16x32_bf16 v[132:135], a[204:207], v[64:67], v[132:135]\n\t"
      "v_mfma_f32_16x16x32_bf16 v[132:135], a[228:231], v[68:71], v[132:135]\n\t"
      "buffer_load_dwordx4 v208, s[20:23], s73 offen offset:2048 lds\n\t"
      "v_mfma_f32_16x16x32_bf16 v[132:135], a[252:255], v[72:75], v[132:135]\n\t"
      "v_readlane_b32 s73, v249, s85\n\t"
      "s_add_u32 m0, s45, 0x3000\n\t"
      "v_mfma_f32_16x16x32_bf16 v[132:135], v[184:187], v[76:79], v[132:135]\n\t"
      "s_waitcnt vmcnt(12)\n\t"
      "s_barrier\n\t"
      "v_mfma_f32_16x16x32_bf16 v[128:131], a[16:19], v[32:35], v[24:27]\n\t"
      "ds_read_b128 v[80:83], v196 offset:12288\n\t"
      "v_mfma_f32_16x16x32_bf16 v[128:131], a[40:43], v[36:39], v[128:131]\n\t"
      "s_waitcnt lgkmcnt(4)\n\t"
      "buffer_store_dwordx4 v[148:151], v208, s[28:31], s72 offen offset:2048 sc1\n\t"
      "v_mfma_f32_16x16x32_bf16 v[128:131], a[64:67], v[40:43], v[128:131]\n\t"
      "ds_read_b128 v[84:87], v197 offset:12288\n\t"
      "v_cvt_pk_bf16_f32 v138, v132, v133\n\t"
      "v_readlane_b32 s72, v249, s44\n\t"
      "v_mfma_f32_16x16x32_bf16 v[128:131], a[88:91], v[44:47], v[128:131]\n\t"
      "ds_read_b128 v[88:91], v198 offset:12288\n\t"
      "v_cvt_pk_bf16_f32 v139, v134, v135\n\t"
      "v_mfma_f32_16x16x32_bf16 v[128:131], a[112:115], v[48:51], v[128:131]\n\t"
      "ds_write_b128 v218, v[136:139] offset:0\n\t"
      "v_mfma_f32_16x16x32_bf16 v[128:131], a[136:139], v[52:55], v[128:131]\n\t"
      "buffer_load_dwordx4 v209, s[20:23], s73 offen lds\n\t"
      "s_waitcnt vmcnt(13)\n\t"
      "s_cmp_eq_u32 s94, 0\n\t"
      "s_cbranch_scc0 .Lwgvw_0_%=\n\t"
      "s_waitcnt vmcnt(7)\n\t"
      ".Lwgvw_0_%=:\n\t"
      "v_subrev_u32_e32 v242, s63, v240\n\t"
      "v_xor_b32_e32 v241, s53, v241\n\t"
      "v_mfma_f32_16x16x32_bf16 v[128:131], a[160:163], v[56:59], v[128:131]\n\t"
      "ds_read_b128 v[92:95], v199 offset:12288\n\t"
      "v_lshrrev_b32_e32 v240, 13, v242\n\t"
      "v_or_b32_e32 v240, v240, v241\n\t"
      "v_mfma_f32_16x16x32_bf16 v[128:131], a[184:187], v[60:63], v[128:131]\n\t"
      "ds_read_b128 v[96:99], v197 offset:12544\n\t"
      "v_mfma_f32_16x16x32_bf16 v[128:131], a[208:211], v[64:67], v[128:131]\n\t"
      "v_cmp_ne_u32_e32 vcc, 0, v240\n\t"
      "v_and_b32_e32 v242, 15, v242\n\t"
      "v_mfma_f32_16x16x32_bf16 v[128:131], a[232:235], v[68:71], v[128:131]\n\t"
      "s_waitcnt lgkmcnt(8)\n\t"
      "buffer_store_dwordx4 v[152:155], v209, s[28:31], s72 offen sc1\n\t"
      "v_mfma_f32_16x16x32_bf16 v[128:131], v[164:167], v[72:75], v[128:131]\n\t"
      "ds_read_b128 v[100:103], v198 offset:12544\n\t"
      "s_cbranch_vccnz .Lgs_%=\n\t"
      ".Lgr_%=:\n\t"
      "ds_write_b32 v246, v242\n\t"
      "v_mfma_f32_16x16x32_bf16 v[128:131], v[188:191], v[76:79], v[128:131]\n\t"
      "ds_read_b128 v[104:107], v199 offset:12544\n\t"
      "v_mfma_f32_16x16x32_bf16 v[132:135], a[20:23], v[32:35], v[28:31]\n\t"
      "ds_read_b128 v[108:111], v196 offset:12544\n\t"
      "v_mfma_f32_16x16x32_bf16 v[132:135], a[44:47], v[36:39], v[132:135]\n\t"
      "buffer_load_dwordx4 v209, s[20:23], s73 offen offset:1024 lds\n\t"
      "v_mfma_f32_16x16x32_bf16 v[132:135], a[68:71], v[40:43], v[132:135]\n\t"
      "ds_read_b128 v[112:115], v198 offset:12800\n\t"
      "v_cvt_pk_bf16_f32 v136, v128, v129\n\t"
      "v_add_u32_e32 v220, s46, v207\n\t"
      "v_mfma_f32_16x16x32_bf16 v[132:135], a[92:95], v[44:47], v[132:135]\n\t"
      "ds_read_b128 v[116:119], v199 offset:12800\n\t"
      "v_cvt_pk_bf16_f32 v137, v130, v131\n\t"
      "s_sub_u32 s84, s84, 4\n\t"
      "s_max_i32 s84, s84, s86\n\t"
      "v_mfma_f32_16x16x32_bf16 v[132:135], a[116:119], v[48:51], v[132:135]\n\t"
      "s_sub_u32 s83, s82, s80\n\t"
      "v_mfma_f32_16x16x32_bf16 v[132:135], a[140:143], v[52:55], v[132:135]\n\t"
      "s_waitcnt lgkmcnt(13)\n\t"
      "buffer_store_dwordx4 v[156:159], v209, s[28:31], s72 offen offset:1024 sc1\n\t"
      "s_max_i32 s83, s83, 0\n\t"
      "s_add_u32 s82, s80, s83\n\t"
      "v_mfma_f32_16x16x32_bf16 v[132:135], a[164:167], v[56:59], v[132:135]\n\t"
      "ds_read_b128 v[120:123], v196 offset:12800\n\t"
      "v_mfma_f32_16x16x32_bf16 v[132:135], a[188:191], v[60:63], v[132:135]\n\t"
      "ds_read_b128 v[124:127], v197 offset:12800\n\t"
      "v_mfma_f32_16x16x32_bf16 v[132:135], a[212:215], v[64:67], v[132:135]\n\t"
      "v_mfma_f32_16x16x32_bf16 v[132:135], a[236:239], v[68:71], v[132:135]\n\t"
      "s_waitcnt lgkmcnt(14)\n\t"
      "buffer_store_dwordx4 v[160:163], v209, s[28:31], s72 offen offset:2048 sc1\n\t"
      "s_add_u32 s82, s82, s84\n\t"
      "v_mfma_f32_16x16x32_bf16 v[132:135], v[168:171], v[72:75], v[132:135]\n\t"
      "s_add_u32 s44, s44, 1\n\t"
      "s_add_u32 s85, s85, 1\n\t"
      "v_mfma_f32_16x16x32_bf16 v[132:135], v[192:195], v[76:79], v[132:135]\n\t"
      "s_waitcnt lgkmcnt(9)\n\t"
      "v_mfma_f32_16x16x32_bf16 v[128:131], a[0:3], v[80:83], v[8:11]\n\t"
      "v_mfma_f32_16x16x32_bf16 v[128:131], a[24:27], v[84:87], v[128:131]\n\t"
      "v_readlane_b32 s72, v248, s44\n\t"
      "v_mfma_f32_16x16x32_bf16 v[128:131], a[48:51], v[88:91], v[128:131]\n\t"
      "v_cvt_pk_bf16_f32 v138, v132, v133\n\t"
      "v_mfma_f32_16x16x32_bf16 v[128:131], a[72:75], v[92:95], v[128:131]\n\t"
      "v_cvt_pk_bf16_f32 v139, v134, v135\n\t"
      "s_waitcnt lgkmcnt(4)\n\t"
      "v_mfma_f32_16x16x32_bf16 v[128:131], a[96:99], v[96:99], v[128:131]\n\t"
      "ds_write_b128 v219, v[136:139] offset:0\n\t"
      "v_mfma_f32_16x16x32_bf16 v[128:131], a[120:123], v[100:103], v[128:131]\n\t"
      "v_mfma_f32_16x16x32_bf16 v[128:131], a[144:147], v[104:107], v[128:131]\n\t"
      "buffer_load_dwordx4 v209, s[20:23], s73 offen offset:2048 lds\n\t"
      "v_mfma_f32_16x16x32_bf16 v[128:131], a[168:171], v[108:111], v[128:131]\n\t"
      "v_add_u32_e32 v196, s47, v200\n\t"
      "s_waitcnt lgkmcnt(1)\n\t"
      "v_mfma_f32_16x16x32_bf16 v[128:131], a[192:195], v[112:115], v[128:131]\n\t"
      "v_add_u32_e32 v197, s47, v201\n\t"
      "v_mfma_f32_16x16x32_bf16 v[128:131], a[216:219], v[116:119], v[128:131]\n\t"
      "v_add_u32_e32 v198, s47, v202\n\t"
      "v_mfma_f32_16x16x32_bf16 v[128:131], a[240:243], v[120:123], v[128:131]\n\t"
      "v_add_u32_e32 v199, s47, v203\n\t"
      "v_mfma_f32_16x16x32_bf16 v[128:131], v[172:175], v[124:127], v[128:131]\n\t"
      "s_cmp_gt_i32 s83, 1\n\t"
      "s_cbranch_scc1 .LslpP3_%=\n\t"
      ".LretP3_%=:\n\t"
      "s_cmp_eq_u32 s94, 1\n\t"
      "s_cbranch_scc0 .LwP3barv_1_%=\n\t"
      "s_waitcnt vmcnt(15)\n\t"
      ".LwP3barv_1_%=:\n\t"
      "s_waitcnt lgkmcnt(0)\n\t"
      "s_barrier\n\t"
      "v_mfma_f32_16x16x32_bf16 v[132:135], a[4:7], v[80:83], v[12:15]\n\t"
      "ds_read_b128 v[140:143], v220 offset:0\n\t"
      "v_mfma_f32_16x16x32_bf16 v[132:135], a[28:31], v[84:87], v[132:135]\n\t"
      "ds_read_b128 v[240:243], v247 offset:0\n\t"
      "v_mfma_f32_16x16x32_bf16 v[132:135], a[52:55], v[88:91], v[132:135]\n\t"
      "v_cvt_pk_bf16_f32 v136, v128, v129\n\t"
      "v_mfma_f32_16x16x32_bf16 v[132:135], a[76:79], v[92:95], v[132:135]\n\t"
      "ds_read_b128 v[32:35], v196 offset:0\n\t"
      "v_cvt_pk_bf16_f32 v137, v130, v131\n\t"
      "v_mfma_f32_16x16x32_bf16 v[132:135], a[100:103], v[96:99], v[132:135]\n\t"
      "v_mfma_f32_16x16x32_bf16 v[132:135], a[124:127], v[100:103], v[132:135]\n\t"
      "v_mfma_f32_16x16x32_bf16 v[132:135], a[148:151], v[104:107], v[132:135]\n\t"
      "ds_read_b128 v[36:39], v197 offset:0\n\t"
      "v_mfma_f32_16x16x32_bf16 v[132:135], a[172:175], v[108:111], v[132:135]\n\t"
      "v_mfma_f32_16x16x32_bf16 v[132:135], a[196:199], v[112:115], v[132:135]\n\t"
      "s_waitcnt lgkmcnt(2)\n\t"
      "v_add3_u32 v240, v240, v241, v242\n\t"
      "v_mfma_f32_16x16x32_bf16 v[132:135], a[220:223], v[116:119], v[132:135]\n\t"
      "ds_read_b128 v[40:43], v198 offset:0\n\t"
      "v_mfma_f32_16x16x32_bf16 v[132:135], a[244:247], v[120:123], v[132:135]\n\t"
      "v_add_u32_e32 v240, v240, v243\n\t"
      "v_mfma_f32_16x16x32_bf16 v[132:135], v[176:179], v[124:127], v[132:135]\n\t"
      "buffer_store_dwordx4 v[140:143], v208, s[28:31], s72 offen sc1\n\t"
      "v_mfma_f32_16x16x32_bf16 v[128:131], a[8:11], v[80:83], v[16:19]\n\t"
      "ds_read_b128 v[44:47], v199 offset:0\n\t"
      "v_mfma_f32_16x16x32_bf16 v[128:131], a[32:35], v[84:87], v[128:131]\n\t"
      "v_add_u32_dpp v241, v240, v240 quad_perm:[1,0,3,2] row_mask:0xf bank_mask:0xf\n\t"
      "s_memrealtime s[80:81]\n\t"
      "v_mfma_f32_16x16x32_bf16 v[128:131], a[56:59], v[88:91], v[128:131]\n\t"
      "v_cvt_pk_bf16_f32 v138, v132, v133\n\t"
      "v_mfma_f32_16x16x32_bf16 v[128:131], a[80:83], v[92:95], v[128:131]\n\t"
      "v_cvt_pk_bf16_f32 v139, v134, v135\n\t"
      "v_mfma_f32_16x16x32_bf16 v[128:131], a[104:107], v[96:99], v[128:131]\n\t"
      "ds_write_b128 v217, v[136:139] offset:12288\n\t"
      "v_add_u32_dpp v240, v241, v241 quad_perm:[2,3,0,1] row_mask:0xf bank_mask:0xf\n\t"
      "v_mfma_f32_16x16x32_bf16 v[128:131], a[128:131], v[100:103], v[128:131]\n\t"
      "s_mov_b32 m0, s46\n\t"
      "v_mfma_f32_16x16x32_bf16 v[128:131], a[152:155], v[104:107], v[128:131]\n\t"
      "v_cmp_le_u32_e64 s[76:77], 18, v240\n\t"
      "v_cmp_ge_u32_e64 s[78:79], 14, v240\n\t"
      "v_mfma_f32_16x16x32_bf16 v[128:131], a[176:179], v[108:111], v[128:131]\n\t"
      "ds_read_b128 v[48:51], v197 offset:256\n\t"
      "v_mfma_f32_16x16x32_bf16 v[128:131], a[200:203], v[112:115], v[128:131]\n\t"
      "v_cmp_lt_u32_e32 vcc, 63, v240\n\t"
      "s_and_b32 s65, s76, s77\n\t"
      "v_mfma_f32_16x16x32_bf16 v[128:131], a[224:227], v[116:119], v[128:131]\n\t"
      "ds_read_b128 v[52:55], v198 offset:256\n\t"
      "v_mfma_f32_16x16x32_bf16 v[128:131], a[248:251], v[120:123], v[128:131]\n\t"
      "s_and_b32 s78, s78, s79\n\t"
      "s_and_b32 s67, s65, 0x11111111\n\t"
      "v_mfma_f32_16x16x32_bf16 v[128:131], v[180:183], v[124:127], v[128:131]\n\t"
      "v_mfma_f32_16x16x32_bf16 v[132:135], a[12:15], v[80:83], v[20:23]\n\t"
      "ds_read_b128 v[56:59], v199 offset:256\n\t"
      "s_or_b32 s65, s65, s78\n\t"
      "s_cmp_lg_u64 vcc, 0\n\t"
      "s_cselect_b32 s65, 0, s65\n\t"
      "s_cselect_b32 s67, 0, s67\n\t"
      "v_mfma_f32_16x16x32_bf16 v[132:135], a[36:39], v[84:87], v[132:135]\n\t"
      "v_and_b32_e32 v240, s67, v252\n\t"
      "v_and_b32_e32 v241, s65, v252\n\t"
      "v_mfma_f32_16x16x32_bf16 v[132:135], a[60:63], v[88:91], v[132:135]\n\t"
      "v_cvt_pk_bf16_f32 v136, v128, v129\n\t"
      "v_mfma_f32_16x16x32_bf16 v[132:135], a[84:87], v[92:95], v[132:135]\n\t"
      "v_cvt_pk_bf16_f32 v137, v130, v131\n\t"
      "ds_read_b128 v[144:147], v220 offset:1024\n\t"
      "v_mfma_f32_16x16x32_bf16 v[132:135], a[108:111], v[96:99], v[132:135]\n\t"
      "v_cmp_ne_u32_e64 s[76:77], 0, v240\n\t"
      "v_cmp_ne_u32_e64 s[78:79], 0, v241\n\t"
      "v_mfma_f32_16x16x32_bf16 v[132:135], a[132:135], v[100:103], v[132:135]\n\t"
      "v_mov_b32_e32 v243, 76\n\t"
      "s_and_b32 s64, s65, 0x22222222\n\t"
      "v_mfma_f32_16x16x32_bf16 v[132:135], a[156:159], v[104:107], v[132:135]\n\t"
      "ds_read_b128 v[60:63], v196 offset:256\n\t"
      "v_min_u32_e32 v242, 78, v250\n\t"
      "v_mfma_f32_16x16x32_bf16 v[132:135], a[180:183], v[108:111], v[132:135]\n\t"
      "s_xor_b32 s75, s75, s76\n\t"
      "s_xor_b32 s74, s74, s76\n\t"
      "v_mfma_f32_16x16x32_bf16 v[132:135], a[204:207], v[112:115], v[132:135]\n\t"
      "s_and_b32 s76, s75, s78\n\t"
      "v_cndmask_b32_e64 v249, v248, v251, s[76:77]\n\t"
      "v_mfma_f32_16x16x32_bf16 v[132:135], a[228:231], v[116:119], v[132:135]\n\t"
      "ds_read_b128 v[64:67], v198 offset:512\n\t"
      "v_mfma_f32_16x16x32_bf16 v[132:135], a[252:255], v[120:123], v[132:135]\n\t"
      "s_and_b32 s76, s74, s78\n\t"
      "v_cndmask_b32_e64 v248, v248, v251, s[76:77]\n\t"
      "v_mfma_f32_16x16x32_bf16 v[132:135], v[184:187], v[124:127], v[132:135]\n\t"
      "s_waitcnt lgkmcnt(2)\n\t"
      "buffer_store_dwordx4 v[144:147], v208, s[28:31], s72 offen offset:1024 sc1\n\t"
      "v_mfma_f32_16x16x32_bf16 v[128:131], a[16:19], v[80:83], v[24:27]\n\t"
      "ds_read_b128 v[68:71], v199 offset:512\n\t"
      "v_mfma_f32_16x16x32_bf16 v[128:131], a[40:43], v[84:87], v[128:131]\n\t"
      "s_lshr_b32 s74, s78, 3\n\t"
      "s_or_b32 s76, s74, s78\n\t"
      "v_mfma_f32_16x16x32_bf16 v[128:131], a[64:67], v[88:91], v[128:131]\n\t"
      "v_cvt_pk_bf16_f32 v138, v132, v133\n\t"
      "v_readlane_b32 s73, v248, s85\n\t"
      "v_mfma_f32_16x16x32_bf16 v[128:131], a[88:91], v[92:95], v[128:131]\n\t"
      "v_cvt_pk_bf16_f32 v139, v134, v135\n\t"
      "v_mfma_f32_16x16x32_bf16 v[128:131], a[112:115], v[96:99], v[128:131]\n\t"
      "ds_write_b128 v218, v[136:139] offset:12288\n\t"
      "v_mfma_f32_16x16x32_bf16 v[128:131], a[136:139], v[100:103], v[128:131]\n\t"
      "v_cndmask_b32_e64 v250, v250, v242, s[76:77]\n\t"
      "s_and_b32 s78, s74, s78\n\t"
      "v_mfma_f32_16x16x32_bf16 v[128:131], a[160:163], v[104:107], v[128:131]\n\t"
      "s_and_b32 s78, s78, 0x3fff\n\t"
      "v_cndmask_b32_e64 v250, v250, v243, s[78:79]\n\t"
      "v_mfma_f32_16x16x32_bf16 v[128:131], a[184:187], v[108:111], v[128:131]\n\t"
      "ds_read_b128 v[148:151], v220 offset:2048\n\t"
      "v_mfma_f32_16x16x32_bf16 v[128:131], a[208:211], v[112:115], v[128:131]\n\t"
      "buffer_load_dwordx4 v208, s[20:23], s73 offen lds\n\t"
      "v_mfma_f32_16x16x32_bf16 v[128:131], a[232:235], v[116:119], v[128:131]\n\t"
      "ds_read_b128 v[72:75], v196 offset:512\n\t"
      "s_or_b32 s64, s64, s67\n\t"
      "v_mfma_f32_16x16x32_bf16 v[128:131], v[164:167], v[120:123], v[128:131]\n\t"
      "v_readlane_b32 s86, v250, s44\n\t"
      "v_mfma_f32_16x16x32_bf16 v[128:131], v[188:191], v[124:127], v[128:131]\n\t"
      "v_mfma_f32_16x16x32_bf16 v[132:135], a[20:23], v[80:83], v[28:31]\n\t"
      "ds_read_b128 v[76:79], v197 offset:512\n\t"
      "v_mfma_f32_16x16x32_bf16 v[132:135], a[44:47], v[84:87], v[132:135]\n\t"
      "buffer_load_dwordx4 v208, s[20:23], s73 offen offset:1024 lds\n\t"
      "v_mfma_f32_16x16x32_bf16 v[132:135], a[68:71], v[88:91], v[132:135]\n\t"
      "v_cvt_pk_bf16_f32 v136, v128, v129\n\t"
      "v_mfma_f32_16x16x32_bf16 v[132:135], a[92:95], v[92:95], v[132:135]\n\t"
      "v_cvt_pk_bf16_f32 v137, v130, v131\n\t"
      "s_sub_u32 s84, s84, 4\n\t"
      "s_max_i32 s84, s84, s86\n\t"
      "v_mfma_f32_16x16x32_bf16 v[132:135], a[116:119], v[96:99], v[132:135]\n\t"
      "s_sub_u32 s83, s82, s80\n\t"
      "v_mfma_f32_16x16x32_bf16 v[132:135], a[140:143], v[100:103], v[132:135]\n\t"
      "s_max_i32 s83, s83, 0\n\t"
      "s_add_u32 s82, s80, s83\n\t"
      "v_mfma_f32_16x16x32_bf16 v[132:135], a[164:167], v[104:107], v[132:135]\n\t"
      "s_mov_b32 s45, s46\n\t"
      "v_mfma_f32_16x16x32_bf16 v[132:135], a[188:191], v[108:111], v[132:135]\n\t"
      "s_mov_b32 s46, s47\n\t"
      "v_mfma_f32_16x16x32_bf16 v[132:135], a[212:215], v[112:115], v[132:135]\n\t"
      "s_mov_b32 s47, s48\n\t"
      "v_mfma_f32_16x16x32_bf16 v[132:135], a[236:239], v[116:119], v[132:135]\n\t"
      "s_add_u32 s82, s82, s84\n\t"
      "v_mfma_f32_16x16x32_bf16 v[132:135], v[168:171], v[120:123], v[132:135]\n\t"
      "s_cmp_lg_u32 s44, s49\n\t"
      "v_mfma_f32_16x16x32_bf16 v[132:135], v[192:195], v[124:127], v[132:135]\n\t"
      "s_waitcnt lgkmcnt(2)\n\t"
      "buffer_store_dwordx4 v[148:151], v208, s[28:31], s72 offen offset:2048 sc1\n\t"
      ".p2align 6\n\t"
      ".Lloop_%=:\n\t"
      "s_waitcnt lgkmcnt(12)\n\t"
      "v_mfma_f32_16x16x32_bf16 v[128:131], a[0:3], v[32:35], v[8:11]\n\t"
      "v_mfma_f32_16x16x32_bf16 v[128:131], a[24:27], v[36:39], v[128:131]\n\t"
      "v_readlane_b32 s72, v249, s44\n\t"
      "v_mfma_f32_16x16x32_bf16 v[128:131], a[48:51], v[40:43], v[128:131]\n\t"
      "v_cvt_pk_bf16_f32 v138, v132, v133\n\t"
      "v_mfma_f32_16x16x32_bf16 v[128:131], a[72:75], v[44:47], v[128:131]\n\t"
      "v_cvt_pk_bf16_f32 v139, v134, v135\n\t"
      "s_waitcnt lgkmcnt(6)\n\t"
      "v_mfma_f32_16x16x32_bf16 v[128:131], a[96:99], v[48:51], v[128:131]\n\t"
      "ds_write_b128 v219, v[136:139] offset:12288\n\t"
      "v_mfma_f32_16x16x32_bf16 v[128:131], a[120:123], v[52:55], v[128:131]\n\t"
      "v_mfma_f32_16x16x32_bf16 v[128:131], a[144:147], v[56:59], v[128:131]\n\t"
      "buffer_load_dwordx4 v208, s[20:23], s73 offen offset:2048 lds\n\t"
      "v_mfma_f32_16x16x32_bf16 v[128:131], a[168:171], v[60:63], v[128:131]\n\t"
      "s_waitcnt lgkmcnt(1)\n\t"
      "v_mfma_f32_16x16x32_bf16 v[128:131], a[192:195], v[64:67], v[128:131]\n\t"
      "v_add_u32_e32 v217, s46, v204\n\t"
      "v_mfma_f32_16x16x32_bf16 v[128:131], a[216:219], v[68:71], v[128:131]\n\t"
      "v_add_u32_e32 v218, s46, v205\n\t"
      "v_mfma_f32_16x16x32_bf16 v[128:131], a[240:243], v[72:75], v[128:131]\n\t"
      "v_add_u32_e32 v219, s46, v206\n\t"
      "v_mfma_f32_16x16x32_bf16 v[128:131], v[172:175], v[76:79], v[128:131]\n\t"
      "s_cmp_gt_i32 s83, 1\n\t"
      "s_cbranch_scc1 .LslpA_%=\n\t"
      ".LretA_%=:\n\t"
      "s_waitcnt vmcnt(18) lgkmcnt(0)\n\t"
      "s_barrier\n\t"
      "v_mfma_f32_16x16x32_bf16 v[132:135], a[4:7], v[32:35], v[12:15]\n\t"
      "ds_read_b128 v[140:143], v220 offset:12288\n\t"
      "v_mfma_f32_16x16x32_bf16 v[132:135], a[28:31], v[36:39], v[132:135]\n\t"
      "v_mfma_f32_16x16x32_bf16 v[132:135], a[52:55], v[40:43], v[132:135]\n\t"
      "v_cvt_pk_bf16_f32 v136, v128, v129\n\t"
      "v_mfma_f32_16x16x32_bf16 v[132:135], a[76:79], v[44:47], v[132:135]\n\t"
      "ds_read_b128 v[80:83], v196 offset:12288\n\t"
      "v_cvt_pk_bf16_f32 v137, v130, v131\n\t"
      "v_mfma_f32_16x16x32_bf16 v[132:135], a[100:103], v[48:51], v[132:135]\n\t"
      "s_add_u32 s48, s47, 0x6000\n\t"
      "v_mfma_f32_16x16x32_bf16 v[132:135], a[124:127], v[52:55], v[132:135]\n\t"
      "s_cmp_eq_u32 s48, s50\n\t"
      "s_cselect_b32 s48, s41, s48\n\t"
      "v_mfma_f32_16x16x32_bf16 v[132:135], a[148:151], v[56:59], v[132:135]\n\t"
      "ds_read_b128 v[84:87], v197 offset:12288\n\t"
      "v_mfma_f32_16x16x32_bf16 v[132:135], a[172:175], v[60:63], v[132:135]\n\t"
      "v_readlane_b32 s73, v249, s85\n\t"
      "v_mfma_f32_16x16x32_bf16 v[132:135], a[196:199], v[64:67], v[132:135]\n\t"
      "v_mfma_f32_16x16x32_bf16 v[132:135], a[220:223], v[68:71], v[132:135]\n\t"
      "ds_read_b128 v[88:91], v198 offset:12288\n\t"
      "v_mfma_f32_16x16x32_bf16 v[132:135], a[244:247], v[72:75], v[132:135]\n\t"
      "v_mfma_f32_16x16x32_bf16 v[132:135], v[176:179], v[76:79], v[132:135]\n\t"
      "s_waitcnt lgkmcnt(3)\n\t"
      "buffer_store_dwordx4 v[140:143], v209, s[28:31], s72 offen sc1\n\t"
      "v_mfma_f32_16x16x32_bf16 v[128:131], a[8:11], v[32:35], v[16:19]\n\t"
      "ds_read_b128 v[92:95], v199 offset:12288\n\t"
      "v_mfma_f32_16x16x32_bf16 v[128:131], a[32:35], v[36:39], v[128:131]\n\t"
      "s_memrealtime s[80:81]\n\t"
      "v_mfma_f32_16x16x32_bf16 v[128:131], a[56:59], v[40:43], v[128:131]\n\t"
      "v_cvt_pk_bf16_f32 v138, v132, v133\n\t"
      "v_mfma_f32_16x16x32_bf16 v[128:131], a[80:83], v[44:47], v[128:131]\n\t"
      "v_cvt_pk_bf16_f32 v139, v134, v135\n\t"
      "v_mfma_f32_16x16x32_bf16 v[128:131], a[104:107], v[48:51], v[128:131]\n\t"
      "ds_write_b128 v217, v[136:139] offset:0\n\t"
      "v_mfma_f32_16x16x32_bf16 v[128:131], a[128:131], v[52:55], v[128:131]\n\t"
      "s_add_u32 m0, s45, 0x3000\n\t"
      "v_mfma_f32_16x16x32_bf16 v[128:131], a[152:155], v[56:59], v[128:131]\n\t"
      "v_mfma_f32_16x16x32_bf16 v[128:131], a[176:179], v[60:63], v[128:131]\n\t"
      "ds_read_b128 v[96:99], v197 offset:12544\n\t"
      "v_mfma_f32_16x16x32_bf16 v[128:131], a[200:203], v[64:67], v[128:131]\n\t"
      "v_mfma_f32_16x16x32_bf16 v[128:131], a[224:227], v[68:71], v[128:131]\n\t"
      "ds_read_b128 v[100:103], v198 offset:12544\n\t"
      "v_mfma_f32_16x16x32_bf16 v[128:131], a[248:251], v[72:75], v[128:131]\n\t"
      "v_mfma_f32_16x16x32_bf16 v[128:131], v[180:183], v[76:79], v[128:131]\n\t"
      "buffer_load_dwordx4 v209, s[20:23], s73 offen lds\n\t"
      "v_mfma_f32_16x16x32_bf16 v[132:135], a[12:15], v[32:35], v[20:23]\n\t"
      "ds_read_b128 v[104:107], v199 offset:12544\n\t"
      "v_mfma_f32_16x16x32_bf16 v[132:135], a[36:39], v[36:39], v[132:135]\n\t"
      "v_mfma_f32_16x16x32_bf16 v[132:135], a[60:63], v[40:43], v[132:135]\n\t"
      "v_cvt_pk_bf16_f32 v136, v128, v129\n\t"
      "v_mfma_f32_16x16x32_bf16 v[132:135], a[84:87], v[44:47], v[132:135]\n\t"
      "v_cvt_pk_bf16_f32 v137, v130, v131\n\t"
      "ds_read_b128 v[144:147], v220 offset:13312\n\t"
      "v_mfma_f32_16x16x32_bf16 v[132:135], a[108:111], v[48:51], v[132:135]\n\t"
      "v_mfma_f32_16x16x32_bf16 v[132:135], a[132:135], v[52:55], v[132:135]\n\t"
      "v_mfma_f32_16x16x32_bf16 v[132:135], a[156:159], v[56:59], v[132:135]\n\t"
      "ds_read_b128 v[108:111], v196 offset:12544\n\t"
      "v_mfma_f32_16x16x32_bf16 v[132:135], a[180:183], v[60:63], v[132:135]\n\t"
      "v_mfma_f32_16x16x32_bf16 v[132:135], a[204:207], v[64:67], v[132:135]\n\t"
      "v_mfma_f32_16x16x32_bf16 v[132:135], a[228:231], v[68:71], v[132:135]\n\t"
      "ds_read_b128 v[112:115], v198 offset:12800\n\t"
      "v_mfma_f32_16x16x32_bf16 v[132:135], a[252:255], v[72:75], v[132:135]\n\t"
      "v_mfma_f32_16x16x32_bf16 v[132:135], v[184:187], v[76:79], v[132:135]\n\t"
      "s_waitcnt lgkmcnt(2)\n\t"
      "buffer_store_dwordx4 v[144:147], v209, s[28:31], s72 offen offset:1024 sc1\n\t"
      "v_mfma_f32_16x16x32_bf16 v[128:131], a[16:19], v[32:35], v[24:27]\n\t"
      "ds_read_b128 v[116:119], v199 offset:12800\n\t"
      "v_mfma_f32_16x16x32_bf16 v[128:131], a[40:43], v[36:39], v[128:131]\n\t"
      "v_mfma_f32_16x16x32_bf16 v[128:131], a[64:67], v[40:43], v[128:131]\n\t"
      "v_cvt_pk_bf16_f32 v138, v132, v133\n\t"
      "v_mfma_f32_16x16x32_bf16 v[128:131], a[88:91], v[44:47], v[128:131]\n\t"
      "v_cvt_pk_bf16_f32 v139, v134, v135\n\t"
      "v_mfma_f32_16x16x32_bf16 v[128:131], a[112:115], v[48:51], v[128:131]\n\t"
      "ds_write_b128 v218, v[136:139] offset:0\n\t"
      "v_mfma_f32_16x16x32_bf16 v[128:131], a[136:139], v[52:55], v[128:131]\n\t"
      "v_mfma_f32_16x16x32_bf16 v[128:131], a[160:163], v[56:59], v[128:131]\n\t"
      "v_mfma_f32_16x16x32_bf16 v[128:131], a[184:187], v[60:63], v[128:131]\n\t"
      "ds_read_b128 v[148:151], v220 offset:14336\n\t"
      "v_mfma_f32_16x16x32_bf16 v[128:131], a[208:211], v[64:67], v[128:131]\n\t"
      "v_mfma_f32_16x16x32_bf16 v[128:131], a[232:235], v[68:71], v[128:131]\n\t"
      "ds_read_b128 v[120:123], v196 offset:12800\n\t"
      "v_mfma_f32_16x16x32_bf16 v[128:131], v[164:167], v[72:75], v[128:131]\n\t"
      "v_mfma_f32_16x16x32_bf16 v[128:131], v[188:191], v[76:79], v[128:131]\n\t"
      "buffer_load_dwordx4 v209, s[20:23], s73 offen offset:1024 lds\n\t"
      "v_mfma_f32_16x16x32_bf16 v[132:135], a[20:23], v[32:35], v[28:31]\n\t"
      "ds_read_b128 v[124:127], v197 offset:12800\n\t"
      "v_mfma_f32_16x16x32_bf16 v[132:135], a[44:47], v[36:39], v[132:135]\n\t"
      "v_mfma_f32_16x16x32_bf16 v[132:135], a[68:71], v[40:43], v[132:135]\n\t"
      "v_cvt_pk_bf16_f32 v136, v128, v129\n\t"
      "v_add_u32_e32 v220, s46, v207\n\t"
      "v_mfma_f32_16x16x32_bf16 v[132:135], a[92:95], v[44:47], v[132:135]\n\t"
      "v_cvt_pk_bf16_f32 v137, v130, v131\n\t"
      "s_sub_u32 s84, s84, 4\n\t"
      "s_max_i32 s84, s84, s86\n\t"
      "v_mfma_f32_16x16x32_bf16 v[132:135], a[116:119], v[48:51], v[132:135]\n\t"
      "s_sub_u32 s83, s82, s80\n\t"
      "v_mfma_f32_16x16x32_bf16 v[132:135], a[140:143], v[52:55], v[132:135]\n\t"
      "s_max_i32 s83, s83, 0\n\t"
      "s_add_u32 s82, s80, s83\n\t"
      "v_mfma_f32_16x16x32_bf16 v[132:135], a[164:167], v[56:59], v[132:135]\n\t"
      "v_mfma_f32_16x16x32_bf16 v[132:135], a[188:191], v[60:63], v[132:135]\n\t"
      "v_mfma_f32_16x16x32_bf16 v[132:135], a[212:215], v[64:67], v[132:135]\n\t"
      "s_add_u32 s44, s44, 1\n\t"
      "v_mfma_f32_16x16x32_bf16 v[132:135], a[236:239], v[68:71], v[132:135]\n\t"
      "s_add_u32 s82, s82, s84\n\t"
      "v_mfma_f32_16x16x32_bf16 v[132:135], v[168:171], v[72:75], v[132:135]\n\t"
      "s_add_u32 s85, s85, 1\n\t"
      "v_mfma_f32_16x16x32_bf16 v[132:135], v[192:195], v[76:79], v[132:135]\n\t"
      "s_waitcnt lgkmcnt(2)\n\t"
      "buffer_store_dwordx4 v[148:151], v209, s[28:31], s72 offen offset:2048 sc1\n\t"
      "s_waitcnt lgkmcnt(12)\n\t"
      "v_mfma_f32_16x16x32_bf16 v[128:131], a[0:3], v[80:83], v[8:11]\n\t"
      "v_mfma_f32_16x16x32_bf16 v[128:131], a[24:27], v[84:87], v[128:131]\n\t"
      "v_readlane_b32 s72, v248, s44\n\t"
      "v_mfma_f32_16x16x32_bf16 v[128:131], a[48:51], v[88:91], v[128:131]\n\t"
      "v_cvt_pk_bf16_f32 v138, v132, v133\n\t"
      "v_mfma_f32_16x16x32_bf16 v[128:131], a[72:75], v[92:95], v[128:131]\n\t"
      "v_cvt_pk_bf16_f32 v139, v134, v135\n\t"
      "s_waitcnt lgkmcnt(6)\n\t"
      "v_mfma_f32_16x16x32_bf16 v[128:131], a[96:99], v[96:99], v[128:131]\n\t"
      "ds_write_b128 v219, v[136:139] offset:0\n\t"
      "v_mfma_f32_16x16x32_bf16 v[128:131], a[120:123], v[100:103], v[128:131]\n\t"
      "v_mfma_f32_16x16x32_bf16 v[128:131], a[144:147], v[104:107], v[128:131]\n\t"
      "buffer_load_dwordx4 v209, s[20:23], s73 offen offset:2048 lds\n\t"
      "v_mfma_f32_16x16x32_bf16 v[128:131], a[168:171], v[108:111], v[128:131]\n\t"
      "v_add_u32_e32 v196, s47, v200\n\t"
      "s_waitcnt lgkmcnt(1)\n\t"
      "v_mfma_f32_16x16x32_bf16 v[128:131], a[192:195], v[112:115], v[128:131]\n\t"
      "v_add_u32_e32 v197, s47, v201\n\t"
      "v_mfma_f32_16x16x32_bf16 v[128:131], a[216:219], v[116:119], v[128:131]\n\t"
      "v_add_u32_e32 v198, s47, v202\n\t"
      "v_mfma_f32_16x16x32_bf16 v[128:131], a[240:243], v[120:123], v[128:131]\n\t"
      "v_add_u32_e32 v199, s47, v203\n\t"
      "v_mfma_f32_16x16x32_bf16 v[128:131], v[172:175], v[124:127], v[128:131]\n\t"
      "s_cmp_gt_i32 s83, 1\n\t"
      "s_cbranch_scc1 .LslpB_%=\n\t"
      ".LretB_%=:\n\t"
      "s_waitcnt vmcnt(18) lgkmcnt(0)\n\t"
      "s_barrier\n\t"
      "v_mfma_f32_16x16x32_bf16 v[132:135], a[4:7], v[80:83], v[12:15]\n\t"
      "ds_read_b128 v[140:143], v220 offset:0\n\t"
      "v_mfma_f32_16x16x32_bf16 v[132:135], a[28:31], v[84:87], v[132:135]\n\t"
      "v_mfma_f32_16x16x32_bf16 v[132:135], a[52:55], v[88:91], v[132:135]\n\t"
      "v_cvt_pk_bf16_f32 v136, v128, v129\n\t"
      "v_mfma_f32_16x16x32_bf16 v[132:135], a[76:79], v[92:95], v[132:135]\n\t"
      "ds_read_b128 v[32:35], v196 offset:0\n\t"
      "v_cvt_pk_bf16_f32 v137, v130, v131\n\t"
      "v_mfma_f32_16x16x32_bf16 v[132:135], a[100:103], v[96:99], v[132:135]\n\t"
      "v_mfma_f32_16x16x32_bf16 v[132:135], a[124:127], v[100:103], v[132:135]\n\t"
      "v_mfma_f32_16x16x32_bf16 v[132:135], a[148:151], v[104:107], v[132:135]\n\t"
      "ds_read_b128 v[36:39], v197 offset:0\n\t"
      "v_mfma_f32_16x16x32_bf16 v[132:135], a[172:175], v[108:111], v[132:135]\n\t"
      "v_readlane_b32 s73, v248, s85\n\t"
      "v_mfma_f32_16x16x32_bf16 v[132:135], a[196:199], v[112:115], v[132:135]\n\t"
      "v_mfma_f32_16x16x32_bf16 v[132:135], a[220:223], v[116:119], v[132:135]\n\t"
      "ds_read_b128 v[40:43], v198 offset:0\n\t"
      "v_mfma_f32_16x16x32_bf16 v[132:135], a[244:247], v[120:123], v[132:135]\n\t"
      "v_mfma_f32_16x16x32_bf16 v[132:135], v[176:179], v[124:127], v[132:135]\n\t"
      "s_waitcnt lgkmcnt(3)\n\t"
      "buffer_store_dwordx4 v[140:143], v208, s[28:31], s72 offen sc1\n\t"
      "v_mfma_f32_16x16x32_bf16 v[128:131], a[8:11], v[80:83], v[16:19]\n\t"
      "ds_read_b128 v[44:47], v199 offset:0\n\t"
      "v_mfma_f32_16x16x32_bf16 v[128:131], a[32:35], v[84:87], v[128:131]\n\t"
      "s_memrealtime s[80:81]\n\t"
      "v_mfma_f32_16x16x32_bf16 v[128:131], a[56:59], v[88:91], v[128:131]\n\t"
      "v_cvt_pk_bf16_f32 v138, v132, v133\n\t"
      "v_mfma_f32_16x16x32_bf16 v[128:131], a[80:83], v[92:95], v[128:131]\n\t"
      "v_cvt_pk_bf16_f32 v139, v134, v135\n\t"
      "v_mfma_f32_16x16x32_bf16 v[128:131], a[104:107], v[96:99], v[128:131]\n\t"
      "ds_write_b128 v217, v[136:139] offset:12288\n\t"
      "v_mfma_f32_16x16x32_bf16 v[128:131], a[128:131], v[100:103], v[128:131]\n\t"
      "s_mov_b32 m0, s46\n\t"
      "v_mfma_f32_16x16x32_bf16 v[128:131], a[152:155], v[104:107], v[128:131]\n\t"
      "v_mfma_f32_16x16x32_bf16 v[128:131], a[176:179], v[108:111], v[128:131]\n\t"
      "ds_read_b128 v[48:51], v197 offset:256\n\t"
      "v_mfma_f32_16x16x32_bf16 v[128:131], a[200:203], v[112:115], v[128:131]\n\t"
      "v_mfma_f32_16x16x32_bf16 v[128:131], a[224:227], v[116:119], v[128:131]\n\t"
      "ds_read_b128 v[52:55], v198 offset:256\n\t"
      "v_mfma_f32_16x16x32_bf16 v[128:131], a[248:251], v[120:123], v[128:131]\n\t"
      "v_mfma_f32_16x16x32_bf16 v[128:131], v[180:183], v[124:127], v[128:131]\n\t"
      "buffer_load_dwordx4 v208, s[20:23], s73 offen lds\n\t"
      "v_mfma_f32_16x16x32_bf16 v[132:135], a[12:15], v[80:83], v[20:23]\n\t"
      "ds_read_b128 v[56:59], v199 offset:256\n\t"
      "v_mfma_f32_16x16x32_bf16 v[132:135], a[36:39], v[84:87], v[132:135]\n\t"
      "v_mfma_f32_16x16x32_bf16 v[132:135], a[60:63], v[88:91], v[132:135]\n\t"
      "v_cvt_pk_bf16_f32 v136, v128, v129\n\t"
      "v_mfma_f32_16x16x32_bf16 v[132:135], a[84:87], v[92:95], v[132:135]\n\t"
      "v_cvt_pk_bf16_f32 v137, v130, v131\n\t"
      "ds_read_b128 v[144:147], v220 offset:1024\n\t"
      "v_mfma_f32_16x16x32_bf16 v[132:135], a[108:111], v[96:99], v[132:135]\n\t"
      "v_mfma_f32_16x16x32_bf16 v[132:135], a[132:135], v[100:103], v[132:135]\n\t"
      "v_mfma_f32_16x16x32_bf16 v[132:135], a[156:159], v[104:107], v[132:135]\n\t"
      "ds_read_b128 v[60:63], v196 offset:256\n\t"
      "v_mfma_f32_16x16x32_bf16 v[132:135], a[180:183], v[108:111], v[132:135]\n\t"
      "v_mfma_f32_16x16x32_bf16 v[132:135], a[204:207], v[112:115], v[132:135]\n\t"
      "v_mfma_f32_16x16x32_bf16 v[132:135], a[228:231], v[116:119], v[132:135]\n\t"
      "ds_read_b128 v[64:67], v198 offset:512\n\t"
      "v_mfma_f32_16x16x32_bf16 v[132:135], a[252:255], v[120:123], v[132:135]\n\t"
      "v_mfma_f32_16x16x32_bf16 v[132:135], v[184:187], v[124:127], v[132:135]\n\t"
      "s_waitcnt lgkmcnt(2)\n\t"
      "buffer_store_dwordx4 v[144:147], v208, s[28:31], s72 offen offset:1024 sc1\n\t"
      "v_mfma_f32_16x16x32_bf16 v[128:131], a[16:19], v[80:83], v[24:27]\n\t"
      "ds_read_b128 v[68:71], v199 offset:512\n\t"
      "v_mfma_f32_16x16x32_bf16 v[128:131], a[40:43], v[84:87], v[128:131]\n\t"
      "v_readlane_b32 s86, v250, s44\n\t"
      "v_mfma_f32_16x16x32_bf16 v[128:131], a[64:67], v[88:91], v[128:131]\n\t"
      "v_cvt_pk_bf16_f32 v138, v132, v133\n\t"
      "v_mfma_f32_16x16x32_bf16 v[128:131], a[88:91], v[92:95], v[128:131]\n\t"
      "v_cvt_pk_bf16_f32 v139, v134, v135\n\t"
      "v_mfma_f32_16x16x32_bf16 v[128:131], a[112:115], v[96:99], v[128:131]\n\t"
      "ds_write_b128 v218, v[136:139] offset:12288\n\t"
      "v_mfma_f32_16x16x32_bf16 v[128:131], a[136:139], v[100:103], v[128:131]\n\t"
      "v_mfma_f32_16x16x32_bf16 v[128:131], a[160:163], v[104:107], v[128:131]\n\t"
      "v_mfma_f32_16x16x32_bf16 v[128:131], a[184:187], v[108:111], v[128:131]\n\t"
      "ds_read_b128 v[148:151], v220 offset:2048\n\t"
      "v_mfma_f32_16x16x32_bf16 v[128:131], a[208:211], v[112:115], v[128:131]\n\t"
      "v_mfma_f32_16x16x32_bf16 v[128:131], a[232:235], v[116:119], v[128:131]\n\t"
      "ds_read_b128 v[72:75], v196 offset:512\n\t"
      "v_mfma_f32_16x16x32_bf16 v[128:131], v[164:167], v[120:123], v[128:131]\n\t"
      "v_mfma_f32_16x16x32_bf16 v[128:131], v[188:191], v[124:127], v[128:131]\n\t"
      "buffer_load_dwordx4 v208, s[20:23], s73 offen offset:1024 lds\n\t"
      "v_mfma_f32_16x16x32_bf16 v[132:135], a[20:23], v[80:83], v[28:31]\n\t"
      "ds_read_b128 v[76:79], v197 offset:512\n\t"
      "v_mfma_f32_16x16x32_bf16 v[132:135], a[44:47], v[84:87], v[132:135]\n\t"
      "v_mfma_f32_16x16x32_bf16 v[132:135], a[68:71], v[88:91], v[132:135]\n\t"
      "v_cvt_pk_bf16_f32 v136, v128, v129\n\t"
      "v_mfma_f32_16x16x32_bf16 v[132:135], a[92:95], v[92:95], v[132:135]\n\t"
      "v_cvt_pk_bf16_f32 v137, v130, v131\n\t"
      "s_sub_u32 s84, s84, 4\n\t"
      "s_max_i32 s84, s84, s86\n\t"
      "v_mfma_f32_16x16x32_bf16 v[132:135], a[116:119], v[96:99], v[132:135]\n\t"
      "s_sub_u32 s83, s82, s80\n\t"
      "v_mfma_f32_16x16x32_bf16 v[132:135], a[140:143], v[100:103], v[132:135]\n\t"
      "s_max_i32 s83, s83, 0\n\t"
      "s_add_u32 s82, s80, s83\n\t"
      "v_mfma_f32_16x16x32_bf16 v[132:135], a[164:167], v[104:107], v[132:135]\n\t"
      "s_mov_b32 s45, s46\n\t"
      "v_mfma_f32_16x16x32_bf16 v[132:135], a[188:191], v[108:111], v[132:135]\n\t"
      "s_mov_b32 s46, s47\n\t"
      "v_mfma_f32_16x16x32_bf16 v[132:135], a[212:215], v[112:115], v[132:135]\n\t"
      "s_mov_b32 s47, s48\n\t"
      "v_mfma_f32_16x16x32_bf16 v[132:135], a[236:239], v[116:119], v[132:135]\n\t"
      "s_add_u32 s82, s82, s84\n\t"
      "v_mfma_f32_16x16x32_bf16 v[132:135], v[168:171], v[120:123], v[132:135]\n\t"
      "s_cmp_lg_u32 s44, s49\n\t"
      "v_mfma_f32_16x16x32_bf16 v[132:135], v[192:195], v[124:127], v[132:135]\n\t"
      "s_waitcnt lgkmcnt(2)\n\t"
      "buffer_store_dwordx4 v[148:151], v208, s[28:31], s72 offen offset:2048 sc1\n\t"
      "s_cbranch_scc1 .Lloop_%=\n\t"
      "s_cmp_lg_u32 s44, 17\n\t"
      "s_cbranch_scc1 .Lstop_%=\n\t"
      "v_readlane_b32 s72, v249, s44\n\t"
      "v_readlane_b32 s76, v254, 1\n\t"
      "v_readlane_b32 s78, v255, 1\n\t"
      "s_nop 1\n\t"
      "v_cvt_pk_bf16_f32 v138, v132, v133\n\t"
      "v_cvt_pk_bf16_f32 v139, v134, v135\n\t"
      "ds_write_b128 v219, v[136:139] offset:12288\n\t"
      "s_cmp_eq_u32 s41, s43\n\t"
      "s_cbranch_scc0 .Lc4_%=\n\t"
      "s_andn2_b32 s76, 4, s76\n\t"
      "s_or_b32 s76, s76, s64\n\t"
      "s_xor_b32 s76, s76, s78\n\t"
      "v_mov_b32_e32 v240, s76\n\t"
      "ds_write_b32 v247, v240\n\t"
      ".Lc4_%=:\n\t"
      "s_waitcnt lgkmcnt(0)\n\t"
      "s_barrier\n\t"
      "ds_read_b128 v[32:35], v220 offset:12288\n\t"
      "ds_read_b128 v[36:39], v220 offset:13312\n\t"
      "ds_read_b128 v[40:43], v220 offset:14336\n\t"
      "ds_read_b32 v241, v247\n\t"
      "s_waitcnt lgkmcnt(3)\n\t"
      "buffer_store_dwordx4 v[32:35], v209, s[28:31], s72 offen sc1\n\t"
      "s_waitcnt lgkmcnt(2)\n\t"
      "buffer_store_dwordx4 v[36:39], v209, s[28:31], s72 offen offset:1024 sc1\n\t"
      "s_waitcnt lgkmcnt(1)\n\t"
      "buffer_store_dwordx4 v[40:43], v209, s[28:31], s72 offen offset:2048 sc1\n\t"
      "s_waitcnt lgkmcnt(0)\n\t"
      "v_cmp_ne_u32_e32 vcc, 0, v241\n\t"
      "s_cmp_lg_u32 s62, 0\n\t"
      "s_cbranch_scc1 .Lfin_%=\n\t"
      "s_cbranch_vccnz .Lsec_%=\n\t"
      ".Lfin_%=:\n\t"
      "s_endpgm\n\t"
      ".Lsec_%=:\n\t"
      "s_mov_b32 s62, 1\n\t"
      "v_lshl_add_u32 v240, v251, 1, s66\n\t"
      "v_sub_u32_e32 v248, v240, v248\n\t"
      "v_sub_u32_e32 v249, v240, v249\n\t"
      "v_cmp_eq_u32_e32 vcc, 0, v252\n\t"
      "v_mov_b32_e32 v240, 0x6c00000\n\t"
      "s_nop 1\n\t"
      "v_cndmask_b32_e32 v248, v248, v240, vcc\n\t"
      "v_cndmask_b32_e32 v249, v249, v240, vcc\n\t"
      "s_add_u32 s44, s94, 3\n\t"
      "s_add_u32 s85, s44, 1\n\t"
      "s_mov_b32 s83, 0\n\t"
      "s_waitcnt vmcnt(0) lgkmcnt(0)\n\t"
      "s_barrier\n\t"
      "v_readlane_b32 s73, v248, s85\n\t"
      "s_mov_b32 m0, s46\n\t"
      "s_nop 4\n\t"
      "buffer_load_dwordx4 v208, s[20:23], s73 offen lds\n\t"
      "buffer_load_dwordx4 v208, s[20:23], s73 offen offset:1024 lds\n\t"
      "buffer_load_dwordx4 v208, s[20:23], s73 offen offset:2048 lds\n\t"
      "v_readlane_b32 s73, v249, s85\n\t"
      "s_add_u32 m0, s46, 0x3000\n\t"
      "s_nop 4\n\t"
      "buffer_load_dwordx4 v209, s[20:23], s73 offen lds\n\t"
      "buffer_load_dwordx4 v209, s[20:23], s73 offen offset:1024 lds\n\t"
      "buffer_load_dwordx4 v209, s[20:23], s73 offen offset:2048 lds\n\t"
      "s_add_u32 s85, s85, 1\n\t"
      "s_nop 4\n\t"
      "v_readlane_b32 s73, v248, s85\n\t"
      "s_mov_b32 m0, s47\n\t"
      "s_nop 4\n\t"
      "buffer_load_dwordx4 v208, s[20:23], s73 offen lds\n\t"
      "buffer_load_dwordx4 v208, s[20:23], s73 offen offset:1024 lds\n\t"
      "buffer_load_dwordx4 v208, s[20:23], s73 offen offset:2048 lds\n\t"
      "v_readlane_b32 s73, v249, s85\n\t"
      "s_add_u32 m0, s47, 0x3000\n\t"
      "s_nop 4\n\t"
      "buffer_load_dwordx4 v209, s[20:23], s73 offen lds\n\t"
      "buffer_load_dwordx4 v209, s[20:23], s73 offen offset:1024 lds\n\t"
      "buffer_load_dwordx4 v209, s[20:23], s73 offen offset:2048 lds\n\t"
      "s_add_u32 s85, s85, 1\n\t"
      "s_nop 4\n\t"
      "v_readlane_b32 s73, v248, s85\n\t"
      "s_mov_b32 m0, s45\n\t"
      "s_nop 4\n\t"
      "buffer_load_dwordx4 v208, s[20:23], s73 offen lds\n\t"
      "buffer_load_dwordx4 v208, s[20:23], s73 offen offset:1024 lds\n\t"
      "buffer_load_dwordx4 v208, s[20:23], s73 offen offset:2048 lds\n\t"
      "s_waitcnt vmcnt(0)\n\t"
      "s_barrier\n\t"
      "ds_read_b128 v[32:35], v196 offset:0\n\t"
      "ds_read_b128 v[36:39], v197 offset:0\n\t"
      "ds_read_b128 v[40:43], v198 offset:0\n\t"
      "ds_read_b128 v[44:47], v199 offset:0\n\t"
      "ds_read_b128 v[48:51], v197 offset:256\n\t"
      "ds_read_b128 v[52:55], v198 offset:256\n\t"
      "ds_read_b128 v[56:59], v199 offset:256\n\t"
      "ds_read_b128 v[60:63], v196 offset:256\n\t"
      "ds_read_b128 v[64:67], v198 offset:512\n\t"
      "ds_read_b128 v[68:71], v199 offset:512\n\t"
      "ds_read_b128 v[72:75], v196 offset:512\n\t"
      "ds_read_b128 v[76:79], v197 offset:512\n\t"
      "s_waitcnt lgkmcnt(0)\n\t"
      "s_branch .Lloop_%=\n\t"
      ".Lstop_%=:\n\t"
      "s_mov_b32 s70, 0\n\t"
      "s_add_u32 s49, s89, 10\n\t"
      "s_cmp_lg_u32 s44, s49\n\t"
      "s_cselect_b32 s49, s49, 17\n\t"
      "s_cbranch_scc0 .Lstop2_%=\n\t"
      "s_cmp_eq_u32 s94, 0\n\t"
      "s_cbranch_scc1 .Llt0_%=\n\t"
      "ds_read_b128 v[226:229], v238 offset:24576\n\t"
      "ds_read_b128 v[230:233], v238 offset:25600\n\t"
      "ds_read_b128 v[234:237], v238 offset:26624\n\t"
      "v_readlane_b32 s68, v248, 0\n\t"
      "s_nop 4\n\t"
      "s_waitcnt lgkmcnt(2)\n\t"
      "buffer_store_dwordx4 v[226:229], v208, s[28:31], s68 offen sc1\n\t"
      "s_waitcnt lgkmcnt(1)\n\t"
      "buffer_store_dwordx4 v[230:233], v208, s[28:31], s68 offen offset:1024 sc1\n\t"
      "s_waitcnt lgkmcnt(0)\n\t"
      "buffer_store_dwordx4 v[234:237], v208, s[28:31], s68 offen offset:2048 sc1\n\t"
      ".Llt0_%=:\n\t"
      "v_readlane_b32 s76, v254, 0\n\t"
      "s_cmp_lg_u32 s41, s43\n\t"
      "s_cbranch_scc1 .Lloop_%=\n\t"
      "s_andn2_b32 s76, 4, s76\n\t"
      "s_or_b32 s76, s76, s64\n\t"
      "v_mov_b32_e32 v240, s76\n\t"
      "s_mov_b64 exec, 1\n\t"
      "buffer_store_dword v240, v253, s[24:27], 0 offen sc1\n\t"
      "s_mov_b64 exec, -1\n\t"
      "s_branch .Lloop_%=\n\t"
      ".Lstop2_%=:\n\t"
      "s_cmp_eq_u32 s94, 0\n\t"
      "s_cbranch_scc1 .Llt1_%=\n\t"
      "ds_read_b128 v[226:229], v238 offset:36864\n\t"
      "ds_read_b128 v[230:233], v238 offset:37888\n\t"
      "ds_read_b128 v[234:237], v238 offset:38912\n\t"
      "v_readlane_b32 s68, v249, 0\n\t"
      "s_nop 4\n\t"
      "s_waitcnt lgkmcnt(2)\n\t"
      "buffer_store_dwordx4 v[226:229], v209, s[28:31], s68 offen sc1\n\t"
      "s_waitcnt lgkmcnt(1)\n\t"
      "buffer_store_dwordx4 v[230:233], v209, s[28:31], s68 offen offset:1024 sc1\n\t"
      "s_waitcnt lgkmcnt(0)\n\t"
      "buffer_store_dwordx4 v[234:237], v209, s[28:31], s68 offen offset:2048 sc1\n\t"
      ".Llt1_%=:\n\t"
      "s_cmp_lg_u32 s41, s43\n\t"
      "s_cbranch_scc1 .Lloop_%=\n\t"
      "buffer_load_dword v255, v253, s[24:27], 0 offen sc1\n\t"
      "s_branch .Lloop_%=\n\t"
      ".LslpP3_%=:\n\t"
      "s_sleep 1\n\t"
      "s_sub_u32 s83, s83, 3\n\t"
      "s_cmp_gt_i32 s83, 1\n\t"
      "s_cbranch_scc1 .LslpP3_%=\n\t"
      "s_branch .LretP3_%=\n\t"
      ".LslpA_%=:\n\t"
      "s_sleep 1\n\t"
      "s_sub_u32 s83, s83, 3\n\t"
      "s_cmp_gt_i32 s83, 1\n\t"
      "s_cbranch_scc1 .LslpA_%=\n\t"
      "s_branch .LretA_%=\n\t"
      ".LslpB_%=:\n\t"
      "s_sleep 1\n\t"
      "s_sub_u32 s83, s83, 3\n\t"
      "s_cmp_gt_i32 s83, 1\n\t"
      "s_cbranch_scc1 .LslpB_%=\n\t"
      "s_branch .LretB_%=\n\t"
      ".Lgs_%=:\n\t"
      "s_sleep 8\n\t"
      "buffer_load_dwordx2 v[240:241], v245, s[24:27], 0 offen sc1\n\t"
      "s_memrealtime s[76:77]\n\t"
      "s_waitcnt vmcnt(0) lgkmcnt(0)\n\t"
      "v_subrev_u32_e32 v242, s63, v240\n\t"
      "v_xor_b32_e32 v241, s53, v241\n\t"
      "v_lshrrev_b32_e32 v240, 13, v242\n\t"
      "v_or_b32_e32 v240, v240, v241\n\t"
      "v_cmp_ne_u32_e32 vcc, 0, v240\n\t"
      "v_and_b32_e32 v242, 15, v242\n\t"
      "s_cbranch_vccz .Lgr_%=\n\t"
      "s_sub_u32 s76, s76, s52\n\t"
      "s_cmp_lt_u32 s76, 2000\n\t"
      "s_cbranch_scc1 .Lgs_%=\n\t"
      "v_mov_b32_e32 v242, 64\n\t"
      "s_branch .Lgr_%=\n\t"
      ".Lpb_%=:\n\t"
      "s_mov_b32 m0, s41\n\t"
      "s_nop 0\n\t"
      "buffer_load_dwordx4 v208, s[20:23], s40 offen lds\n\t"
      "buffer_load_dwordx4 v208, s[20:23], s40 offen offset:1024 lds\n\t"
      "buffer_load_dwordx4 v208, s[20:23], s40 offen offset:2048 lds\n\t"
      "s_add_u32 m0, s41, 0x3000\n\t"
      "s_nop 0\n\t"
      "buffer_load_dwordx4 v209, s[20:23], s40 offen lds\n\t"
      "buffer_load_dwordx4 v209, s[20:23], s40 offen offset:1024 lds\n\t"
      "buffer_load_dwordx4 v209, s[20:23], s40 offen offset:2048 lds\n\t"
      "s_add_u32 m0, s41, 0x6000\n\t"
      "s_add_u32 s51, s40, 0x600000\n\t"
      "buffer_load_dwordx4 v208, s[20:23], s51 offen lds\n\t"
      "buffer_load_dwordx4 v208, s[20:23], s51 offen offset:1024 lds\n\t"
      "buffer_load_dwordx4 v208, s[20:23], s51 offen offset:2048 lds\n\t"
      "s_add_u32 m0, s41, 0x9000\n\t"
      "s_add_u32 s51, s40, 0x600000\n\t"
      "buffer_load_dwordx4 v209, s[20:23], s51 offen lds\n\t"
      "buffer_load_dwordx4 v209, s[20:23], s51 offen offset:1024 lds\n\t"
      "buffer_load_dwordx4 v209, s[20:23], s51 offen offset:2048 lds\n\t"
      "s_waitcnt vmcnt(12)\n\t"
      "v_lshlrev_b32_e32 v8, 16, v226\n\t"
      "v_and_b32_e32 v9, 0xffff0000, v226\n\t"
      "v_lshlrev_b32_e32 v10, 16, v227\n\t"
      "v_and_b32_e32 v11, 0xffff0000, v227\n\t"
      "v_lshlrev_b32_e32 v12, 16, v228\n\t"
      "v_and_b32_e32 v13, 0xffff0000, v228\n\t"
      "v_lshlrev_b32_e32 v14, 16, v229\n\t"
      "v_and_b32_e32 v15, 0xffff0000, v229\n\t"
      "v_lshlrev_b32_e32 v16, 16, v230\n\t"
      "v_and_b32_e32 v17, 0xffff0000, v230\n\t"
      "v_lshlrev_b32_e32 v18, 16, v231\n\t"
      "v_and_b32_e32 v19, 0xffff0000, v231\n\t"
      "v_lshlrev_b32_e32 v20, 16, v232\n\t"
      "v_and_b32_e32 v21, 0xffff0000, v232\n\t"
      "v_lshlrev_b32_e32 v22, 16, v233\n\t"
      "v_and_b32_e32 v23, 0xffff0000, v233\n\t"
      "v_lshlrev_b32_e32 v24, 16, v234\n\t"
      "v_and_b32_e32 v25, 0xffff0000, v234\n\t"
      "v_lshlrev_b32_e32 v26, 16, v235\n\t"
      "v_and_b32_e32 v27, 0xffff0000, v235\n\t"
      "v_lshlrev_b32_e32 v28, 16, v236\n\t"
      "v_and_b32_e32 v29, 0xffff0000, v236\n\t"
      "v_lshlrev_b32_e32 v30, 16, v237\n\t"
      "v_and_b32_e32 v31, 0xffff0000, v237\n\t"
      "s_add_u32 m0, s41, 0x1e000\n\t"
      "s_mov_b32 s51, 0x0\n\t"
      "buffer_load_dwordx4 v211, s[16:19], s51 offen lds\n\t"
      "buffer_load_dwordx4 v212, s[16:19], s51 offen offset:1024 lds\n\t"
      "buffer_load_dwordx4 v213, s[16:19], s51 offen offset:2048 lds\n\t"
      "s_add_u32 m0, s41, 0x21000\n\t"
      "s_mov_b32 s51, 0xc00\n\t"
      "buffer_load_dwordx4 v211, s[16:19], s51 offen lds\n\t"
      "buffer_load_dwordx4 v212, s[16:19], s51 offen offset:1024 lds\n\t"
      "buffer_load_dwordx4 v213, s[16:19], s51 offen offset:2048 lds\n\t"
      "s_add_u32 m0, s41, 0x24000\n\t"
      "s_mov_b32 s51, 0x6000\n\t"
      "buffer_load_dwordx4 v211, s[16:19], s51 offen lds\n\t"
      "buffer_load_dwordx4 v212, s[16:19], s51 offen offset:1024 lds\n\t"
      "buffer_load_dwordx4 v213, s[16:19], s51 offen offset:2048 lds\n\t"
      "s_add_u32 m0, s41, 0x12000\n\t"
      "s_mov_b32 s51, 0x6c00\n\t"
      "buffer_load_dwordx4 v211, s[16:19], s51 offen lds\n\t"
      "buffer_load_dwordx4 v212, s[16:19], s51 offen offset:1024 lds\n\t"
      "buffer_load_dwordx4 v213, s[16:19], s51 offen offset:2048 lds\n\t"
      "s_mov_b32 s44, 1\n\t"
      "s_mov_b32 s85, 4\n\t"
      "s_add_u32 s45, s41, 0x6000\n\t"
      "s_add_u32 s46, s41, 0xc000\n\t"
      "s_mov_b32 s47, s41\n\t"
      "s_waitcnt vmcnt(9)\n\t"
      "s_barrier\n\t"
      "ds_read_b64_tr_b16 a[0:1], v214 offset:49152\n\t"
      "ds_read_b64_tr_b16 a[4:5], v214 offset:49160\n\t"
      "ds_read_b64_tr_b16 a[8:9], v214 offset:49216\n\t"
      "ds_read_b64_tr_b16 a[12:13], v214 offset:49224\n\t"
      "ds_read_b64_tr_b16 a[16:17], v214 offset:49280\n\t"
      "ds_read_b64_tr_b16 a[20:21], v214 offset:49288\n\t"
      "ds_read_b128 v[156:159], v196 offset:0\n\t"
      "ds_read_b128 v[160:163], v196 offset:12288\n\t"
      "ds_read_b128 v[128:131], v196 offset:24576\n\t"
      "s_add_u32 m0, s41, 0x15000\n\t"
      "s_mov_b32 s51, 0xc000\n\t"
      "buffer_load_dwordx4 v211, s[16:19], s51 offen lds\n\t"
      "buffer_load_dwordx4 v212, s[16:19], s51 offen offset:1024 lds\n\t"
      "buffer_load_dwordx4 v213, s[16:19], s51 offen offset:2048 lds\n\t"
      "s_waitcnt vmcnt(9)\n\t"
      "ds_read_b64_tr_b16 a[2:3], v221 offset:0\n\t"
      "ds_read_b64_tr_b16 a[6:7], v221 offset:8\n\t"
      "ds_read_b64_tr_b16 a[10:11], v221 offset:64\n\t"
      "ds_read_b64_tr_b16 a[14:15], v221 offset:72\n\t"
      "ds_read_b64_tr_b16 a[18:19], v221 offset:128\n\t"
      "ds_read_b64_tr_b16 a[22:23], v221 offset:136\n\t"
      "s_add_u32 m0, s41, 0x18000\n\t"
      "s_mov_b32 s51, 0xcc00\n\t"
      "buffer_load_dwordx4 v211, s[16:19], s51 offen lds\n\t"
      "buffer_load_dwordx4 v212, s[16:19], s51 offen offset:1024 lds\n\t"
      "buffer_load_dwordx4 v213, s[16:19], s51 offen offset:2048 lds\n\t"
      "s_waitcnt lgkmcnt(0)\n\t"
      "v_mfma_f32_16x16x32_bf16 v[32:35], a[0:3], v[156:159], v[8:11]\n\t"
      "s_add_u32 m0, s41, 0x1b000\n\t"
      "s_mov_b32 s51, 0x12000\n\t"
      "buffer_load_dwordx4 v211, s[16:19], s51 offen lds\n\t"
      "v_mfma_f32_16x16x32_bf16 v[36:39], a[4:7], v[156:159], v[12:15]\n\t"
      "s_waitcnt vmcnt(10)\n\t"
      "ds_read_b64_tr_b16 a[24:25], v221 offset:12288\n\t"
      "v_mfma_f32_16x16x32_bf16 v[40:43], a[8:11], v[156:159], v[16:19]\n\t"
      "ds_read_b64_tr_b16 a[28:29], v221 offset:12296\n\t"
      "buffer_load_dwordx4 v212, s[16:19], s51 offen offset:1024 lds\n\t"
      "v_mfma_f32_16x16x32_bf16 v[44:47], a[12:15], v[156:159], v[20:23]\n\t"
      "ds_read_b64_tr_b16 a[32:33], v221 offset:12352\n\t"
      "v_mfma_f32_16x16x32_bf16 v[48:51], a[16:19], v[156:159], v[24:27]\n\t"
      "ds_read_b64_tr_b16 a[36:37], v221 offset:12360\n\t"
      "buffer_load_dwordx4 v213, s[16:19], s51 offen offset:2048 lds\n\t"
      "ds_read_b128 v[136:139], v196 offset:36864\n\t"
      "v_mfma_f32_16x16x32_bf16 v[52:55], a[20:23], v[156:159], v[28:31]\n\t"
      "ds_read_b64_tr_b16 a[40:41], v221 offset:12416\n\t"
      "v_mfma_f32_16x16x32_bf16 v[56:59], a[0:3], v[160:163], v[8:11]\n\t"
      "ds_read_b64_tr_b16 a[44:45], v221 offset:12424\n\t"
      "v_mfma_f32_16x16x32_bf16 v[60:63], a[4:7], v[160:163], v[12:15]\n\t"
      "v_mfma_f32_16x16x32_bf16 v[64:67], a[8:11], v[160:163], v[16:19]\n\t"
      "s_waitcnt vmcnt(9)\n\t"
      "ds_read_b64_tr_b16 a[26:27], v214 offset:0\n\t"
      "v_mfma_f32_16x16x32_bf16 v[68:71], a[12:15], v[160:163], v[20:23]\n\t"
      "ds_read_b64_tr_b16 a[30:31], v214 offset:8\n\t"
      "v_mfma_f32_16x16x32_bf16 v[72:75], a[16:19], v[160:163], v[24:27]\n\t"
      "ds_read_b64_tr_b16 a[34:35], v214 offset:64\n\t"
      "ds_read_b128 v[140:143], v197 offset:0\n\t"
      "v_mfma_f32_16x16x32_bf16 v[76:79], a[20:23], v[160:163], v[28:31]\n\t"
      "ds_read_b64_tr_b16 a[38:39], v214 offset:72\n\t"
      "v_mfma_f32_16x16x32_bf16 v[80:83], a[0:3], v[128:131], v[8:11]\n\t"
      "ds_read_b64_tr_b16 a[42:43], v214 offset:128\n\t"
      "s_add_u32 m0, s41, 0x1e000\n\t"
      "s_mov_b32 s51, 0x12c00\n\t"
      "buffer_load_dwordx4 v211, s[16:19], s51 offen lds\n\t"
      "v_mfma_f32_16x16x32_bf16 v[84:87], a[4:7], v[128:131], v[12:15]\n\t"
      "ds_read_b64_tr_b16 a[46:47], v214 offset:136\n\t"
      "v_mfma_f32_16x16x32_bf16 v[88:91], a[8:11], v[128:131], v[16:19]\n\t"
      "v_mfma_f32_16x16x32_bf16 v[92:95], a[12:15], v[128:131], v[20:23]\n\t"
      "buffer_load_dwordx4 v212, s[16:19], s51 offen offset:1024 lds\n\t"
      "v_mfma_f32_16x16x32_bf16 v[96:99], a[16:19], v[128:131], v[24:27]\n\t"
      "ds_read_b128 v[144:147], v197 offset:12288\n\t"
      "v_mfma_f32_16x16x32_bf16 v[100:103], a[20:23], v[128:131], v[28:31]\n\t"
      "s_waitcnt lgkmcnt(10)\n\t"
      "v_mfma_f32_16x16x32_bf16 v[104:107], a[0:3], v[136:139], v[8:11]\n\t"
      "buffer_load_dwordx4 v213, s[16:19], s51 offen offset:2048 lds\n\t"
      "v_mfma_f32_16x16x32_bf16 v[108:111], a[4:7], v[136:139], v[12:15]\n\t"
      "v_mfma_f32_16x16x32_bf16 v[112:115], a[8:11], v[136:139], v[16:19]\n\t"
      "s_waitcnt vmcnt(9)\n\t"
      "ds_read_b64_tr_b16 a[48:49], v214 offset:12288\n\t"
      "v_mfma_f32_16x16x32_bf16 v[116:119], a[12:15], v[136:139], v[20:23]\n\t"
      "ds_read_b64_tr_b16 a[52:53], v214 offset:12296\n\t"
      "s_add_u32 m0, s41, 0x21000\n\t"
      "s_mov_b32 s51, 0x18000\n\t"
      "buffer_load_dwordx4 v211, s[16:19], s51 offen lds\n\t"
      "v_mfma_f32_16x16x32_bf16 v[120:123], a[16:19], v[136:139], v[24:27]\n\t"
      "ds_read_b64_tr_b16 a[56:57], v214 offset:12352\n\t"
      "ds_read_b128 v[148:151], v197 offset:24576\n\t"
      "v_mfma_f32_16x16x32_bf16 v[132:135], a[20:23], v[136:139], v[28:31]\n\t"
      "ds_read_b64_tr_b16 a[60:61], v214 offset:12360\n\t"
      "s_waitcnt lgkmcnt(6)\n\t"
      "v_mfma_f32_16x16x32_bf16 v[32:35], a[24:27], v[140:143], v[32:35]\n\t"
      "ds_read_b64_tr_b16 a[64:65], v214 offset:12416\n\t"
      "buffer_load_dwordx4 v212, s[16:19], s51 offen offset:1024 lds\n\t"
      "v_mfma_f32_16x16x32_bf16 v[36:39], a[28:31], v[140:143], v[36:39]\n\t"
      "ds_read_b64_tr_b16 a[68:69], v214 offset:12424\n\t"
      "v_mfma_f32_16x16x32_bf16 v[40:43], a[32:35], v[140:143], v[40:43]\n\t"
      "v_mfma_f32_16x16x32_bf16 v[44:47], a[36:39], v[140:143], v[44:47]\n\t"
      "buffer_load_dwordx4 v213, s[16:19], s51 offen offset:2048 lds\n\t"
      "v_mfma_f32_16x16x32_bf16 v[48:51], a[40:43], v[140:143], v[48:51]\n\t"
      "ds_read_b128 v[152:155], v197 offset:36864\n\t"
      "v_mfma_f32_16x16x32_bf16 v[52:55], a[44:47], v[140:143], v[52:55]\n\t"
      "s_waitcnt lgkmcnt(8)\n\t"
      "v_mfma_f32_16x16x32_bf16 v[56:59], a[24:27], v[144:147], v[56:59]\n\t"
      "s_add_u32 m0, s41, 0x24000\n\t"
      "s_mov_b32 s51, 0x18c00\n\t"
      "buffer_load_dwordx4 v211, s[16:19], s51 offen lds\n\t"
      "v_mfma_f32_16x16x32_bf16 v[60:63], a[28:31], v[144:147], v[60:63]\n\t"
      "v_mfma_f32_16x16x32_bf16 v[64:67], a[32:35], v[144:147], v[64:67]\n\t"
      "s_waitcnt vmcnt(10)\n\t"
      "ds_read_b64_tr_b16 a[50:51], v214 offset:24576\n\t"
      "v_mfma_f32_16x16x32_bf16 v[68:71], a[36:39], v[144:147], v[68:71]\n\t"
      "ds_read_b64_tr_b16 a[54:55], v214 offset:24584\n\t"
      "buffer_load_dwordx4 v212, s[16:19], s51 offen offset:1024 lds\n\t"
      "v_mfma_f32_16x16x32_bf16 v[72:75], a[40:43], v[144:147], v[72:75]\n\t"
      "ds_read_b64_tr_b16 a[58:59], v214 offset:24640\n\t"
      "ds_read_b128 v[156:159], v198 offset:0\n\t"
      "v_mfma_f32_16x16x32_bf16 v[76:79], a[44:47], v[144:147], v[76:79]\n\t"
      "ds_read_b64_tr_b16 a[62:63], v214 offset:24648\n\t"
      "s_waitcnt lgkmcnt(9)\n\t"
      "v_mfma_f32_16x16x32_bf16 v[80:83], a[24:27], v[148:151], v[80:83]\n\t"
      "ds_read_b64_tr_b16 a[66:67], v214 offset:24704\n\t"
      "buffer_load_dwordx4 v213, s[16:19], s51 offen offset:2048 lds\n\t"
      "v_mfma_f32_16x16x32_bf16 v[84:87], a[28:31], v[148:151], v[84:87]\n\t"
      "ds_read_b64_tr_b16 a[70:71], v214 offset:24712\n\t"
      "v_mfma_f32_16x16x32_bf16 v[88:91], a[32:35], v[148:151], v[88:91]\n\t"
      "v_mfma_f32_16x16x32_bf16 v[92:95], a[36:39], v[148:151], v[92:95]\n\t"
      "s_add_u32 m0, s41, 0x12000\n\t"
      "s_mov_b32 s51, 0x1e000\n\t"
      "buffer_load_dwordx4 v211, s[16:19], s51 offen lds\n\t"
      "v_mfma_f32_16x16x32_bf16 v[96:99], a[40:43], v[148:151], v[96:99]\n\t"
      "ds_read_b128 v[160:163], v198 offset:12288\n\t"
      "v_mfma_f32_16x16x32_bf16 v[100:103], a[44:47], v[148:151], v[100:103]\n\t"
      "s_waitcnt lgkmcnt(8)\n\t"
      "v_mfma_f32_16x16x32_bf16 v[104:107], a[24:27], v[152:155], v[104:107]\n\t"
      "buffer_load_dwordx4 v212, s[16:19], s51 offen offset:1024 lds\n\t"
      "v_mfma_f32_16x16x32_bf16 v[108:111], a[28:31], v[152:155], v[108:111]\n\t"
      "v_mfma_f32_16x16x32_bf16 v[112:115], a[32:35], v[152:155], v[112:115]\n\t"
      "s_waitcnt vmcnt(11)\n\t"
      "ds_read_b64_tr_b16 a[72:73], v214 offset:36864\n\t"
      "v_mfma_f32_16x16x32_bf16 v[116:119], a[36:39], v[152:155], v[116:119]\n\t"
      "ds_read_b64_tr_b16 a[76:77], v214 offset:36872\n\t"
      "buffer_load_dwordx4 v213, s[16:19], s51 offen offset:2048 lds\n\t"
      "v_mfma_f32_16x16x32_bf16 v[120:123], a[40:43], v[152:155], v[120:123]\n\t"
      "ds_read_b64_tr_b16 a[80:81], v214 offset:36928\n\t"
      "ds_read_b128 v[128:131], v198 offset:24576\n\t"
      "v_mfma_f32_16x16x32_bf16 v[132:135], a[44:47], v[152:155], v[132:135]\n\t"
      "ds_read_b64_tr_b16 a[84:85], v214 offset:36936\n\t"
      "s_waitcnt lgkmcnt(6)\n\t"
      "v_mfma_f32_16x16x32_bf16 v[32:35], a[48:51], v[156:159], v[32:35]\n\t"
      "ds_read_b64_tr_b16 a[88:89], v214 offset:36992\n\t"
      "s_add_u32 m0, s41, 0x15000\n\t"
      "s_mov_b32 s51, 0x1ec00\n\t"
      "buffer_load_dwordx4 v211, s[16:19], s51 offen lds\n\t"
      "v_mfma_f32_16x16x32_bf16 v[36:39], a[52:55], v[156:159], v[36:39]\n\t"
      "ds_read_b64_tr_b16 a[92:93], v214 offset:37000\n\t"
      "v_mfma_f32_16x16x32_bf16 v[40:43], a[56:59], v[156:159], v[40:43]\n\t"
      "v_mfma_f32_16x16x32_bf16 v[44:47], a[60:63], v[156:159], v[44:47]\n\t"
      "buffer_load_dwordx4 v212, s[16:19], s51 offen offset:1024 lds\n\t"
      "v_mfma_f32_16x16x32_bf16 v[48:51], a[64:67], v[156:159], v[48:51]\n\t"
      "ds_read_b128 v[136:139], v198 offset:36864\n\t"
      "v_mfma_f32_16x16x32_bf16 v[52:55], a[68:71], v[156:159], v[52:55]\n\t"
      "s_waitcnt lgkmcnt(8)\n\t"
      "v_mfma_f32_16x16x32_bf16 v[56:59], a[48:51], v[160:163], v[56:59]\n\t"
      "buffer_load_dwordx4 v213, s[16:19], s51 offen offset:2048 lds\n\t"
      "v_mfma_f32_16x16x32_bf16 v[60:63], a[52:55], v[160:163], v[60:63]\n\t"
      "v_mfma_f32_16x16x32_bf16 v[64:67], a[56:59], v[160:163], v[64:67]\n\t"
      "s_waitcnt vmcnt(12)\n\t"
      "ds_read_b64_tr_b16 a[74:75], v214 offset:49152\n\t"
      "v_mfma_f32_16x16x32_bf16 v[68:71], a[60:63], v[160:163], v[68:71]\n\t"
      "ds_read_b64_tr_b16 a[78:79], v214 offset:49160\n\t"
      "s_add_u32 m0, s41, 0x18000\n\t"
      "s_mov_b32 s51, 0x24000\n\t"
      "buffer_load_dwordx4 v211, s[16:19], s51 offen lds\n\t"
      "v_mfma_f32_16x16x32_bf16 v[72:75], a[64:67], v[160:163], v[72:75]\n\t"
      "ds_read_b64_tr_b16 a[82:83], v214 offset:49216\n\t"
      "ds_read_b128 v[140:143], v199 offset:0\n\t"
      "v_mfma_f32_16x16x32_bf16 v[76:79], a[68:71], v[160:163], v[76:79]\n\t"
      "ds_read_b64_tr_b16 a[86:87], v214 offset:49224\n\t"
      "s_waitcnt lgkmcnt(9)\n\t"
      "v_mfma_f32_16x16x32_bf16 v[80:83], a[48:51], v[128:131], v[80:83]\n\t"
      "ds_read_b64_tr_b16 a[90:91], v214 offset:49280\n\t"
      "buffer_load_dwordx4 v212, s[16:19], s51 offen offset:1024 lds\n\t"
      "v_mfma_f32_16x16x32_bf16 v[84:87], a[52:55], v[128:131], v[84:87]\n\t"
      "ds_read_b64_tr_b16 a[94:95], v214 offset:49288\n\t"
      "v_mfma_f32_16x16x32_bf16 v[88:91], a[56:59], v[128:131], v[88:91]\n\t"
      "v_mfma_f32_16x16x32_bf16 v[92:95], a[60:63], v[128:131], v[92:95]\n\t"
      "buffer_load_dwordx4 v213, s[16:19], s51 offen offset:2048 lds\n\t"
      "v_mfma_f32_16x16x32_bf16 v[96:99], a[64:67], v[128:131], v[96:99]\n\t"
      "ds_read_b128 v[144:147], v199 offset:12288\n\t"
      "v_mfma_f32_16x16x32_bf16 v[100:103], a[68:71], v[128:131], v[100:103]\n\t"
      "s_waitcnt lgkmcnt(8)\n\t"
      "v_mfma_f32_16x16x32_bf16 v[104:107], a[48:51], v[136:139], v[104:107]\n\t"
      "s_add_u32 m0, s41, 0x1b000\n\t"
      "s_mov_b32 s51, 0x24c00\n\t"
      "buffer_load_dwordx4 v211, s[16:19], s51 offen lds\n\t"
      "v_mfma_f32_16x16x32_bf16 v[108:111], a[52:55], v[136:139], v[108:111]\n\t"
      "v_mfma_f32_16x16x32_bf16 v[112:115], a[56:59], v[136:139], v[112:115]\n\t"
      "s_waitcnt vmcnt(13)\n\t"
      "ds_read_b64_tr_b16 a[96:97], v221 offset:0\n\t"
      "v_mfma_f32_16x16x32_bf16 v[116:119], a[60:63], v[136:139], v[116:119]\n\t"
      "ds_read_b64_tr_b16 a[100:101], v221 offset:8\n\t"
      "buffer_load_dwordx4 v212, s[16:19], s51 offen offset:1024 lds\n\t"
      "v_mfma_f32_16x16x32_bf16 v[120:123], a[64:67], v[136:139], v[120:123]\n\t"
      "ds_read_b64_tr_b16 a[104:105], v221 offset:64\n\t"
      "ds_read_b128 v[148:151], v199 offset:24576\n\t"
      "v_mfma_f32_16x16x32_bf16 v[132:135], a[68:71], v[136:139], v[132:135]\n\t"
      "ds_read_b64_tr_b16 a[108:109], v221 offset:72\n\t"
      "s_waitcnt lgkmcnt(6)\n\t"
      "v_mfma_f32_16x16x32_bf16 v[32:35], a[72:75], v[140:143], v[32:35]\n\t"
      "ds_read_b64_tr_b16 a[112:113], v221 offset:128\n\t"
      "buffer_load_dwordx4 v213, s[16:19], s51 offen offset:2048 lds\n\t"
      "v_mfma_f32_16x16x32_bf16 v[36:39], a[76:79], v[140:143], v[36:39]\n\t"
      "ds_read_b64_tr_b16 a[116:117], v221 offset:136\n\t"
      "v_mfma_f32_16x16x32_bf16 v[40:43], a[80:83], v[140:143], v[40:43]\n\t"
      "v_mfma_f32_16x16x32_bf16 v[44:47], a[84:87], v[140:143], v[44:47]\n\t"
      "s_add_u32 m0, s41, 0x1e000\n\t"
      "s_mov_b32 s51, 0x2a000\n\t"
      "buffer_load_dwordx4 v211, s[16:19], s51 offen lds\n\t"
      "v_mfma_f32_16x16x32_bf16 v[48:51], a[88:91], v[140:143], v[48:51]\n\t"
      "ds_read_b128 v[152:155], v199 offset:36864\n\t"
      "v_mfma_f32_16x16x32_bf16 v[52:55], a[92:95], v[140:143], v[52:55]\n\t"
      "s_waitcnt lgkmcnt(8)\n\t"
      "v_mfma_f32_16x16x32_bf16 v[56:59], a[72:75], v[144:147], v[56:59]\n\t"
      "buffer_load_dwordx4 v212, s[16:19], s51 offen offset:1024 lds\n\t"
      "v_mfma_f32_16x16x32_bf16 v[60:63], a[76:79], v[144:147], v[60:63]\n\t"
      "v_mfma_f32_16x16x32_bf16 v[64:67], a[80:83], v[144:147], v[64:67]\n\t"
      "s_waitcnt vmcnt(14)\n\t"
      "ds_read_b64_tr_b16 a[98:99], v221 offset:12288\n\t"
      "v_mfma_f32_16x16x32_bf16 v[68:71], a[84:87], v[144:147], v[68:71]\n\t"
      "ds_read_b64_tr_b16 a[102:103], v221 offset:12296\n\t"
      "buffer_load_dwordx4 v213, s[16:19], s51 offen offset:2048 lds\n\t"
      "v_mfma_f32_16x16x32_bf16 v[72:75], a[88:91], v[144:147], v[72:75]\n\t"
      "ds_read_b64_tr_b16 a[106:107], v221 offset:12352\n\t"
      "ds_read_b128 v[156:159], v197 offset:256\n\t"
      "v_mfma_f32_16x16x32_bf16 v[76:79], a[92:95], v[144:147], v[76:79]\n\t"
      "ds_read_b64_tr_b16 a[110:111], v221 offset:12360\n\t"
      "s_waitcnt lgkmcnt(9)\n\t"
      "v_mfma_f32_16x16x32_bf16 v[80:83], a[72:75], v[148:151], v[80:83]\n\t"
      "ds_read_b64_tr_b16 a[114:115], v221 offset:12416\n\t"
      "s_waitcnt lgkmcnt(7)\n\t"
      "s_add_u32 m0, s41, 0x21000\n\t"
      "s_mov_b32 s51, 0x2ac00\n\t"
      "buffer_load_dwordx4 v211, s[16:19], s51 offen lds\n\t"
      "v_mfma_f32_16x16x32_bf16 v[84:87], a[76:79], v[148:151], v[84:87]\n\t"
      "ds_read_b64_tr_b16 a[118:119], v221 offset:12424\n\t"
      "v_mfma_f32_16x16x32_bf16 v[88:91], a[80:83], v[148:151], v[88:91]\n\t"
      "v_mfma_f32_16x16x32_bf16 v[92:95], a[84:87], v[148:151], v[92:95]\n\t"
      "buffer_load_dwordx4 v212, s[16:19], s51 offen offset:1024 lds\n\t"
      "v_mfma_f32_16x16x32_bf16 v[96:99], a[88:91], v[148:151], v[96:99]\n\t"
      "ds_read_b128 v[160:163], v197 offset:12544\n\t"
      "v_mfma_f32_16x16x32_bf16 v[100:103], a[92:95], v[148:151], v[100:103]\n\t"
      "s_waitcnt lgkmcnt(8)\n\t"
      "v_mfma_f32_16x16x32_bf16 v[104:107], a[72:75], v[152:155], v[104:107]\n\t"
      "buffer_load_dwordx4 v213, s[16:19], s51 offen offset:2048 lds\n\t"
      "v_mfma_f32_16x16x32_bf16 v[108:111], a[76:79], v[152:155], v[108:111]\n\t"
      "v_mfma_f32_16x16x32_bf16 v[112:115], a[80:83], v[152:155], v[112:115]\n\t"
      "s_waitcnt vmcnt(15)\n\t"
      "ds_read_b64_tr_b16 a[120:121], v214 offset:0\n\t"
      "v_mfma_f32_16x16x32_bf16 v[116:119], a[84:87], v[152:155], v[116:119]\n\t"
      "ds_read_b64_tr_b16 a[124:125], v214 offset:8\n\t"
      "s_waitcnt lgkmcnt(3)\n\t"
      "s_add_u32 m0, s41, 0x24000\n\t"
      "s_mov_b32 s51, 0x30000\n\t"
      "buffer_load_dwordx4 v211, s[16:19], s51 offen lds\n\t"
      "v_mfma_f32_16x16x32_bf16 v[120:123], a[88:91], v[152:155], v[120:123]\n\t"
      "ds_read_b64_tr_b16 a[128:129], v214 offset:64\n\t"
      "ds_read_b128 v[128:131], v197 offset:24832\n\t"
      "v_mfma_f32_16x16x32_bf16 v[132:135], a[92:95], v[152:155], v[132:135]\n\t"
      "ds_read_b64_tr_b16 a[132:133], v214 offset:72\n\t"
      "v_mfma_f32_16x16x32_bf16 v[32:35], a[96:99], v[156:159], v[32:35]\n\t"
      "ds_read_b64_tr_b16 a[136:137], v214 offset:128\n\t"
      "buffer_load_dwordx4 v212, s[16:19], s51 offen offset:1024 lds\n\t"
      "v_mfma_f32_16x16x32_bf16 v[36:39], a[100:103], v[156:159], v[36:39]\n\t"
      "ds_read_b64_tr_b16 a[140:141], v214 offset:136\n\t"
      "v_mfma_f32_16x16x32_bf16 v[40:43], a[104:107], v[156:159], v[40:43]\n\t"
      "v_mfma_f32_16x16x32_bf16 v[44:47], a[108:111], v[156:159], v[44:47]\n\t"
      "buffer_load_dwordx4 v213, s[16:19], s51 offen offset:2048 lds\n\t"
      "v_mfma_f32_16x16x32_bf16 v[48:51], a[112:115], v[156:159], v[48:51]\n\t"
      "ds_read_b128 v[136:139], v197 offset:37120\n\t"
      "v_mfma_f32_16x16x32_bf16 v[52:55], a[116:119], v[156:159], v[52:55]\n\t"
      "s_waitcnt lgkmcnt(8)\n\t"
      "v_mfma_f32_16x16x32_bf16 v[56:59], a[96:99], v[160:163], v[56:59]\n\t"
      "v_mfma_f32_16x16x32_bf16 v[60:63], a[100:103], v[160:163], v[60:63]\n\t"
      "v_mfma_f32_16x16x32_bf16 v[64:67], a[104:107], v[160:163], v[64:67]\n\t"
      "s_waitcnt vmcnt(15)\n\t"
      "ds_read_b64_tr_b16 a[122:123], v214 offset:12288\n\t"
      "s_waitcnt lgkmcnt(2)\n\t"
      "s_add_u32 m0, s41, 0x12000\n\t"
      "s_mov_b32 s51, 0x30c00\n\t"
      "buffer_load_dwordx4 v211, s[16:19], s51 offen lds\n\t"
      "v_mfma_f32_16x16x32_bf16 v[68:71], a[108:111], v[160:163], v[68:71]\n\t"
      "ds_read_b64_tr_b16 a[126:127], v214 offset:12296\n\t"
      "v_mfma_f32_16x16x32_bf16 v[72:75], a[112:115], v[160:163], v[72:75]\n\t"
      "ds_read_b64_tr_b16 a[130:131], v214 offset:12352\n\t"
      "ds_read_b128 v[140:143], v198 offset:256\n\t"
      "v_mfma_f32_16x16x32_bf16 v[76:79], a[116:119], v[160:163], v[76:79]\n\t"
      "ds_read_b64_tr_b16 a[134:135], v214 offset:12360\n\t"
      "buffer_load_dwordx4 v212, s[16:19], s51 offen offset:1024 lds\n\t"
      "v_mfma_f32_16x16x32_bf16 v[80:83], a[96:99], v[128:131], v[80:83]\n\t"
      "ds_read_b64_tr_b16 a[138:139], v214 offset:12416\n\t"
      "v_mfma_f32_16x16x32_bf16 v[84:87], a[100:103], v[128:131], v[84:87]\n\t"
      "ds_read_b64_tr_b16 a[142:143], v214 offset:12424\n\t"
      "v_mfma_f32_16x16x32_bf16 v[88:91], a[104:107], v[128:131], v[88:91]\n\t"
      "buffer_load_dwordx4 v213, s[16:19], s51 offen offset:2048 lds\n\t"
      "v_mfma_f32_16x16x32_bf16 v[92:95], a[108:111], v[128:131], v[92:95]\n\t"
      "v_mfma_f32_16x16x32_bf16 v[96:99], a[112:115], v[128:131], v[96:99]\n\t"
      "ds_read_b128 v[144:147], v198 offset:12544\n\t"
      "v_mfma_f32_16x16x32_bf16 v[100:103], a[116:119], v[128:131], v[100:103]\n\t"
      "s_waitcnt lgkmcnt(8)\n\t"
      "v_mfma_f32_16x16x32_bf16 v[104:107], a[96:99], v[136:139], v[104:107]\n\t"
      "v_mfma_f32_16x16x32_bf16 v[108:111], a[100:103], v[136:139], v[108:111]\n\t"
      "v_mfma_f32_16x16x32_bf16 v[112:115], a[104:107], v[136:139], v[112:115]\n\t"
      "s_waitcnt vmcnt(15)\n\t"
      "ds_read_b64_tr_b16 a[144:145], v214 offset:24576\n\t"
      "s_waitcnt lgkmcnt(2)\n\t"
      "s_add_u32 m0, s41, 0x15000\n\t"
      "s_mov_b32 s51, 0x36000\n\t"
      "buffer_load_dwordx4 v211, s[16:19], s51 offen lds\n\t"
      "v_mfma_f32_16x16x32_bf16 v[116:119], a[108:111], v[136:139], v[116:119]\n\t"
      "ds_read_b64_tr_b16 a[148:149], v214 offset:24584\n\t"
      "v_mfma_f32_16x16x32_bf16 v[120:123], a[112:115], v[136:139], v[120:123]\n\t"
      "ds_read_b64_tr_b16 a[152:153], v214 offset:24640\n\t"
      "ds_read_b128 v[148:151], v198 offset:24832\n\t"
      "v_mfma_f32_16x16x32_bf16 v[132:135], a[116:119], v[136:139], v[132:135]\n\t"
      "ds_read_b64_tr_b16 a[156:157], v214 offset:24648\n\t"
      "buffer_load_dwordx4 v212, s[16:19], s51 offen offset:1024 lds\n\t"
      "v_mfma_f32_16x16x32_bf16 v[32:35], a[120:123], v[140:143], v[32:35]\n\t"
      "ds_read_b64_tr_b16 a[160:161], v214 offset:24704\n\t"
      "v_mfma_f32_16x16x32_bf16 v[36:39], a[124:127], v[140:143], v[36:39]\n\t"
      "ds_read_b64_tr_b16 a[164:165], v214 offset:24712\n\t"
      "v_mfma_f32_16x16x32_bf16 v[40:43], a[128:131], v[140:143], v[40:43]\n\t"
      "buffer_load_dwordx4 v213, s[16:19], s51 offen offset:2048 lds\n\t"
      "v_mfma_f32_16x16x32_bf16 v[44:47], a[132:135], v[140:143], v[44:47]\n\t"
      "v_mfma_f32_16x16x32_bf16 v[48:51], a[136:139], v[140:143], v[48:51]\n\t"
      "ds_read_b128 v[152:155], v198 offset:37120\n\t"
      "v_mfma_f32_16x16x32_bf16 v[52:55], a[140:143], v[140:143], v[52:55]\n\t"
      "s_waitcnt lgkmcnt(8)\n\t"
      "v_mfma_f32_16x16x32_bf16 v[56:59], a[120:123], v[144:147], v[56:59]\n\t"
      "v_mfma_f32_16x16x32_bf16 v[60:63], a[124:127], v[144:147], v[60:63]\n\t"
      "v_mfma_f32_16x16x32_bf16 v[64:67], a[128:131], v[144:147], v[64:67]\n\t"
      "s_waitcnt vmcnt(15)\n\t"
      "ds_read_b64_tr_b16 a[146:147], v214 offset:36864\n\t"
      "s_waitcnt lgkmcnt(2)\n\t"
      "s_add_u32 m0, s41, 0x18000\n\t"
      "s_mov_b32 s51, 0x36c00\n\t"
      "buffer_load_dwordx4 v211, s[16:19], s51 offen lds\n\t"
      "v_mfma_f32_16x16x32_bf16 v[68:71], a[132:135], v[144:147], v[68:71]\n\t"
      "ds_read_b64_tr_b16 a[150:151], v214 offset:36872\n\t"
      "v_mfma_f32_16x16x32_bf16 v[72:75], a[136:139], v[144:147], v[72:75]\n\t"
      "ds_read_b64_tr_b16 a[154:155], v214 offset:36928\n\t"
      "ds_read_b128 v[156:159], v199 offset:256\n\t"
      "v_mfma_f32_16x16x32_bf16 v[76:79], a[140:143], v[144:147], v[76:79]\n\t"
      "ds_read_b64_tr_b16 a[158:159], v214 offset:36936\n\t"
      "buffer_load_dwordx4 v212, s[16:19], s51 offen offset:1024 lds\n\t"
      "v_mfma_f32_16x16x32_bf16 v[80:83], a[120:123], v[148:151], v[80:83]\n\t"
      "ds_read_b64_tr_b16 a[162:163], v214 offset:36992\n\t"
      "v_mfma_f32_16x16x32_bf16 v[84:87], a[124:127], v[148:151], v[84:87]\n\t"
      "ds_read_b64_tr_b16 a[166:167], v214 offset:37000\n\t"
      "v_mfma_f32_16x16x32_bf16 v[88:91], a[128:131], v[148:151], v[88:91]\n\t"
      "buffer_load_dwordx4 v213, s[16:19], s51 offen offset:2048 lds\n\t"
      "v_mfma_f32_16x16x32_bf16 v[92:95], a[132:135], v[148:151], v[92:95]\n\t"
      "v_mfma_f32_16x16x32_bf16 v[96:99], a[136:139], v[148:151], v[96:99]\n\t"
      "ds_read_b128 v[160:163], v199 offset:12544\n\t"
      "v_mfma_f32_16x16x32_bf16 v[100:103], a[140:143], v[148:151], v[100:103]\n\t"
      "s_waitcnt lgkmcnt(8)\n\t"
      "v_mfma_f32_16x16x32_bf16 v[104:107], a[120:123], v[152:155], v[104:107]\n\t"
      "v_mfma_f32_16x16x32_bf16 v[108:111], a[124:127], v[152:155], v[108:111]\n\t"
      "v_mfma_f32_16x16x32_bf16 v[112:115], a[128:131], v[152:155], v[112:115]\n\t"
      "s_waitcnt vmcnt(15)\n\t"
      "ds_read_b64_tr_b16 a[168:169], v214 offset:49152\n\t"
      "s_waitcnt lgkmcnt(2)\n\t"
      "s_add_u32 m0, s41, 0x1b000\n\t"
      "s_mov_b32 s51, 0x3c000\n\t"
      "buffer_load_dwordx4 v211, s[16:19], s51 offen lds\n\t"
      "v_mfma_f32_16x16x32_bf16 v[116:119], a[132:135], v[152:155], v[116:119]\n\t"
      "ds_read_b64_tr_b16 a[172:173], v214 offset:49160\n\t"
      "v_mfma_f32_16x16x32_bf16 v[120:123], a[136:139], v[152:155], v[120:123]\n\t"
      "ds_read_b64_tr_b16 a[176:177], v214 offset:49216\n\t"
      "ds_read_b128 v[128:131], v199 offset:24832\n\t"
      "v_mfma_f32_16x16x32_bf16 v[132:135], a[140:143], v[152:155], v[132:135]\n\t"
      "ds_read_b64_tr_b16 a[180:181], v214 offset:49224\n\t"
      "buffer_load_dwordx4 v212, s[16:19], s51 offen offset:1024 lds\n\t"
      "v_mfma_f32_16x16x32_bf16 v[32:35], a[144:147], v[156:159], v[32:35]\n\t"
      "ds_read_b64_tr_b16 a[184:185], v214 offset:49280\n\t"
      "v_mfma_f32_16x16x32_bf16 v[36:39], a[148:151], v[156:159], v[36:39]\n\t"
      "ds_read_b64_tr_b16 a[188:189], v214 offset:49288\n\t"
      "v_mfma_f32_16x16x32_bf16 v[40:43], a[152:155], v[156:159], v[40:43]\n\t"
      "buffer_load_dwordx4 v213, s[16:19], s51 offen offset:2048 lds\n\t"
      "v_mfma_f32_16x16x32_bf16 v[44:47], a[156:159], v[156:159], v[44:47]\n\t"
      "v_mfma_f32_16x16x32_bf16 v[48:51], a[160:163], v[156:159], v[48:51]\n\t"
      "ds_read_b128 v[136:139], v199 offset:37120\n\t"
      "v_mfma_f32_16x16x32_bf16 v[52:55], a[164:167], v[156:159], v[52:55]\n\t"
      "s_waitcnt lgkmcnt(8)\n\t"
      "v_mfma_f32_16x16x32_bf16 v[56:59], a[144:147], v[160:163], v[56:59]\n\t"
      "v_mfma_f32_16x16x32_bf16 v[60:63], a[148:151], v[160:163], v[60:63]\n\t"
      "v_mfma_f32_16x16x32_bf16 v[64:67], a[152:155], v[160:163], v[64:67]\n\t"
      "s_waitcnt vmcnt(15)\n\t"
      "ds_read_b64_tr_b16 a[170:171], v221 offset:0\n\t"
      "s_waitcnt lgkmcnt(2)\n\t"
      "s_add_u32 m0, s41, 0x1e000\n\t"
      "s_mov_b32 s51, 0x3cc00\n\t"
      "buffer_load_dwordx4 v211, s[16:19], s51 offen lds\n\t"
      "v_mfma_f32_16x16x32_bf16 v[68:71], a[156:159], v[160:163], v[68:71]\n\t"
      "ds_read_b64_tr_b16 a[174:175], v221 offset:8\n\t"
      "v_mfma_f32_16x16x32_bf16 v[72:75], a[160:163], v[160:163], v[72:75]\n\t"
      "ds_read_b64_tr_b16 a[178:179], v221 offset:64\n\t"
      "ds_read_b128 v[140:143], v196 offset:256\n\t"
      "v_mfma_f32_16x16x32_bf16 v[76:79], a[164:167], v[160:163], v[76:79]\n\t"
      "ds_read_b64_tr_b16 a[182:183], v221 offset:72\n\t"
      "buffer_load_dwordx4 v212, s[16:19], s51 offen offset:1024 lds\n\t"
      "v_mfma_f32_16x16x32_bf16 v[80:83], a[144:147], v[128:131], v[80:83]\n\t"
      "ds_read_b64_tr_b16 a[186:187], v221 offset:128\n\t"
      "v_mfma_f32_16x16x32_bf16 v[84:87], a[148:151], v[128:131], v[84:87]\n\t"
      "ds_read_b64_tr_b16 a[190:191], v221 offset:136\n\t"
      "v_mfma_f32_16x16x32_bf16 v[88:91], a[152:155], v[128:131], v[88:91]\n\t"
      "buffer_load_dwordx4 v213, s[16:19], s51 offen offset:2048 lds\n\t"
      "v_mfma_f32_16x16x32_bf16 v[92:95], a[156:159], v[128:131], v[92:95]\n\t"
      "v_mfma_f32_16x16x32_bf16 v[96:99], a[160:163], v[128:131], v[96:99]\n\t"
      "ds_read_b128 v[144:147], v196 offset:12544\n\t"
      "v_mfma_f32_16x16x32_bf16 v[100:103], a[164:167], v[128:131], v[100:103]\n\t"
      "s_waitcnt lgkmcnt(8)\n\t"
      "v_mfma_f32_16x16x32_bf16 v[104:107], a[144:147], v[136:139], v[104:107]\n\t"
      "v_mfma_f32_16x16x32_bf16 v[108:111], a[148:151], v[136:139], v[108:111]\n\t"
      "v_mfma_f32_16x16x32_bf16 v[112:115], a[152:155], v[136:139], v[112:115]\n\t"
      "s_waitcnt vmcnt(15)\n\t"
      "ds_read_b64_tr_b16 a[192:193], v221 offset:12288\n\t"
      "s_waitcnt lgkmcnt(2)\n\t"
      "s_add_u32 m0, s41, 0x21000\n\t"
      "s_mov_b32 s51, 0x42000\n\t"
      "buffer_load_dwordx4 v211, s[16:19], s51 offen lds\n\t"
      "v_mfma_f32_16x16x32_bf16 v[116:119], a[156:159], v[136:139], v[116:119]\n\t"
      "ds_read_b64_tr_b16 a[196:197], v221 offset:12296\n\t"
      "v_mfma_f32_16x16x32_bf16 v[120:123], a[160:163], v[136:139], v[120:123]\n\t"
      "ds_read_b64_tr_b16 a[200:201], v221 offset:12352\n\t"
      "ds_read_b128 v[148:151], v196 offset:24832\n\t"
      "v_mfma_f32_16x16x32_bf16 v[132:135], a[164:167], v[136:139], v[132:135]\n\t"
      "ds_read_b64_tr_b16 a[204:205], v221 offset:12360\n\t"
      "buffer_load_dwordx4 v212, s[16:19], s51 offen offset:1024 lds\n\t"
      "v_mfma_f32_16x16x32_bf16 v[32:35], a[168:171], v[140:143], v[32:35]\n\t"
      "ds_read_b64_tr_b16 a[208:209], v221 offset:12416\n\t"
      "v_mfma_f32_16x16x32_bf16 v[36:39], a[172:175], v[140:143], v[36:39]\n\t"
      "ds_read_b64_tr_b16 a[212:213], v221 offset:12424\n\t"
      "v_mfma_f32_16x16x32_bf16 v[40:43], a[176:179], v[140:143], v[40:43]\n\t"
      "buffer_load_dwordx4 v213, s[16:19], s51 offen offset:2048 lds\n\t"
      "v_mfma_f32_16x16x32_bf16 v[44:47], a[180:183], v[140:143], v[44:47]\n\t"
      "v_mfma_f32_16x16x32_bf16 v[48:51], a[184:187], v[140:143], v[48:51]\n\t"
      "ds_read_b128 v[152:155], v196 offset:37120\n\t"
      "v_mfma_f32_16x16x32_bf16 v[52:55], a[188:191], v[140:143], v[52:55]\n\t"
      "s_waitcnt lgkmcnt(8)\n\t"
      "v_mfma_f32_16x16x32_bf16 v[56:59], a[168:171], v[144:147], v[56:59]\n\t"
      "v_mfma_f32_16x16x32_bf16 v[60:63], a[172:175], v[144:147], v[60:63]\n\t"
      "v_mfma_f32_16x16x32_bf16 v[64:67], a[176:179], v[144:147], v[64:67]\n\t"
      "s_waitcnt vmcnt(15)\n\t"
      "ds_read_b64_tr_b16 a[194:195], v214 offset:0\n\t"
      "s_waitcnt lgkmcnt(2)\n\t"
      "s_add_u32 m0, s41, 0x24000\n\t"
      "s_mov_b32 s51, 0x42c00\n\t"
      "buffer_load_dwordx4 v211, s[16:19], s51 offen lds\n\t"
      "v_mfma_f32_16x16x32_bf16 v[68:71], a[180:183], v[144:147], v[68:71]\n\t"
      "ds_read_b64_tr_b16 a[198:199], v214 offset:8\n\t"
      "v_mfma_f32_16x16x32_bf16 v[72:75], a[184:187], v[144:147], v[72:75]\n\t"
      "ds_read_b64_tr_b16 a[202:203], v214 offset:64\n\t"
      "ds_read_b128 v[156:159], v198 offset:512\n\t"
      "v_mfma_f32_16x16x32_bf16 v[76:79], a[188:191], v[144:147], v[76:79]\n\t"
      "ds_read_b64_tr_b16 a[206:207], v214 offset:72\n\t"
      "buffer_load_dwordx4 v212, s[16:19], s51 offen offset:1024 lds\n\t"
      "v_mfma_f32_16x16x32_bf16 v[80:83], a[168:171], v[148:151], v[80:83]\n\t"
      "ds_read_b64_tr_b16 a[210:211], v214 offset:128\n\t"
      "v_mfma_f32_16x16x32_bf16 v[84:87], a[172:175], v[148:151], v[84:87]\n\t"
      "ds_read_b64_tr_b16 a[214:215], v214 offset:136\n\t"
      "v_mfma_f32_16x16x32_bf16 v[88:91], a[176:179], v[148:151], v[88:91]\n\t"
      "buffer_load_dwordx4 v213, s[16:19], s51 offen offset:2048 lds\n\t"
      "v_mfma_f32_16x16x32_bf16 v[92:95], a[180:183], v[148:151], v[92:95]\n\t"
      "v_mfma_f32_16x16x32_bf16 v[96:99], a[184:187], v[148:151], v[96:99]\n\t"
      "ds_read_b128 v[160:163], v198 offset:12800\n\t"
      "v_mfma_f32_16x16x32_bf16 v[100:103], a[188:191], v[148:151], v[100:103]\n\t"
      "s_waitcnt lgkmcnt(8)\n\t"
      "v_mfma_f32_16x16x32_bf16 v[104:107], a[168:171], v[152:155], v[104:107]\n\t"
      "s_add_u32 m0, s41, 0xc000\n\t"
      "s_add_u32 s51, s40, 0xc00000\n\t"
      "buffer_load_dwordx4 v208, s[20:23], s51 offen lds\n\t"
      "v_mfma_f32_16x16x32_bf16 v[108:111], a[172:175], v[152:155], v[108:111]\n\t"
      "v_mfma_f32_16x16x32_bf16 v[112:115], a[176:179], v[152:155], v[112:115]\n\t"
      "s_waitcnt vmcnt(16)\n\t"
      "ds_read_b64_tr_b16 a[216:217], v214 offset:12288\n\t"
      "buffer_load_dwordx4 v208, s[20:23], s51 offen offset:1024 lds\n\t"
      "v_mfma_f32_16x16x32_bf16 v[116:119], a[180:183], v[152:155], v[116:119]\n\t"
      "ds_read_b64_tr_b16 a[220:221], v214 offset:12296\n\t"
      "v_mfma_f32_16x16x32_bf16 v[120:123], a[184:187], v[152:155], v[120:123]\n\t"
      "ds_read_b64_tr_b16 a[224:225], v214 offset:12352\n\t"
      "ds_read_b128 v[128:131], v198 offset:25088\n\t"
      "buffer_load_dwordx4 v208, s[20:23], s51 offen offset:2048 lds\n\t"
      "v_mfma_f32_16x16x32_bf16 v[132:135], a[188:191], v[152:155], v[132:135]\n\t"
      "ds_read_b64_tr_b16 a[228:229], v214 offset:12360\n\t"
      "s_waitcnt lgkmcnt(6)\n\t"
      "v_mfma_f32_16x16x32_bf16 v[32:35], a[192:195], v[156:159], v[32:35]\n\t"
      "ds_read_b64_tr_b16 a[232:233], v214 offset:12416\n\t"
      "s_add_u32 m0, s41, 0xf000\n\t"
      "s_add_u32 s51, s40, 0xc00000\n\t"
      "buffer_load_dwordx4 v209, s[20:23], s51 offen lds\n\t"
      "v_mfma_f32_16x16x32_bf16 v[36:39], a[196:199], v[156:159], v[36:39]\n\t"
      "ds_read_b64_tr_b16 a[236:237], v214 offset:12424\n\t"
      "v_mfma_f32_16x16x32_bf16 v[40:43], a[200:203], v[156:159], v[40:43]\n\t"
      "buffer_load_dwordx4 v209, s[20:23], s51 offen offset:1024 lds\n\t"
      "v_mfma_f32_16x16x32_bf16 v[44:47], a[204:207], v[156:159], v[44:47]\n\t"
      "v_mfma_f32_16x16x32_bf16 v[48:51], a[208:211], v[156:159], v[48:51]\n\t"
      "ds_read_b128 v[136:139], v198 offset:37376\n\t"
      "buffer_load_dwordx4 v209, s[20:23], s51 offen offset:2048 lds\n\t"
      "v_mfma_f32_16x16x32_bf16 v[52:55], a[212:215], v[156:159], v[52:55]\n\t"
      "s_waitcnt lgkmcnt(8)\n\t"
      "v_mfma_f32_16x16x32_bf16 v[56:59], a[192:195], v[160:163], v[56:59]\n\t"
      "v_mfma_f32_16x16x32_bf16 v[60:63], a[196:199], v[160:163], v[60:63]\n\t"
      "v_mfma_f32_16x16x32_bf16 v[64:67], a[200:203], v[160:163], v[64:67]\n\t"
      "s_waitcnt vmcnt(18)\n\t"
      "ds_read_b64_tr_b16 a[218:219], v214 offset:24576\n\t"
      "v_mfma_f32_16x16x32_bf16 v[68:71], a[204:207], v[160:163], v[68:71]\n\t"
      "ds_read_b64_tr_b16 a[222:223], v214 offset:24584\n\t"
      "v_mfma_f32_16x16x32_bf16 v[72:75], a[208:211], v[160:163], v[72:75]\n\t"
      "ds_read_b64_tr_b16 a[226:227], v214 offset:24640\n\t"
      "ds_read_b128 v[140:143], v199 offset:512\n\t"
      "v_mfma_f32_16x16x32_bf16 v[76:79], a[212:215], v[160:163], v[76:79]\n\t"
      "ds_read_b64_tr_b16 a[230:231], v214 offset:24648\n\t"
      "s_waitcnt lgkmcnt(9)\n\t"
      "v_mfma_f32_16x16x32_bf16 v[80:83], a[192:195], v[128:131], v[80:83]\n\t"
      "ds_read_b64_tr_b16 a[234:235], v214 offset:24704\n\t"
      "v_mfma_f32_16x16x32_bf16 v[84:87], a[196:199], v[128:131], v[84:87]\n\t"
      "ds_read_b64_tr_b16 a[238:239], v214 offset:24712\n\t"
      "v_mfma_f32_16x16x32_bf16 v[88:91], a[200:203], v[128:131], v[88:91]\n\t"
      "v_mfma_f32_16x16x32_bf16 v[92:95], a[204:207], v[128:131], v[92:95]\n\t"
      "v_mfma_f32_16x16x32_bf16 v[96:99], a[208:211], v[128:131], v[96:99]\n\t"
      "ds_read_b128 v[144:147], v199 offset:12800\n\t"
      "v_mfma_f32_16x16x32_bf16 v[100:103], a[212:215], v[128:131], v[100:103]\n\t"
      "s_waitcnt lgkmcnt(8)\n\t"
      "v_mfma_f32_16x16x32_bf16 v[104:107], a[192:195], v[136:139], v[104:107]\n\t"
      "v_mfma_f32_16x16x32_bf16 v[108:111], a[196:199], v[136:139], v[108:111]\n\t"
      "v_mfma_f32_16x16x32_bf16 v[112:115], a[200:203], v[136:139], v[112:115]\n\t"
      "s_waitcnt vmcnt(15)\n\t"
      "ds_read_b64_tr_b16 a[240:241], v214 offset:36864\n\t"
      "v_mfma_f32_16x16x32_bf16 v[116:119], a[204:207], v[136:139], v[116:119]\n\t"
      "ds_read_b64_tr_b16 a[244:245], v214 offset:36872\n\t"
      "v_mfma_f32_16x16x32_bf16 v[120:123], a[208:211], v[136:139], v[120:123]\n\t"
      "ds_read_b64_tr_b16 a[248:249], v214 offset:36928\n\t"
      "ds_read_b128 v[148:151], v199 offset:25088\n\t"
      "v_mfma_f32_16x16x32_bf16 v[132:135], a[212:215], v[136:139], v[132:135]\n\t"
      "ds_read_b64_tr_b16 a[252:253], v214 offset:36936\n\t"
      "s_waitcnt lgkmcnt(6)\n\t"
      "v_mfma_f32_16x16x32_bf16 v[32:35], a[216:219], v[140:143], v[32:35]\n\t"
      "ds_read_b64_tr_b16 v[164:165], v214 offset:36992\n\t"
      "v_mfma_f32_16x16x32_bf16 v[36:39], a[220:223], v[140:143], v[36:39]\n\t"
      "ds_read_b64_tr_b16 v[168:169], v214 offset:37000\n\t"
      "v_mfma_f32_16x16x32_bf16 v[40:43], a[224:227], v[140:143], v[40:43]\n\t"
      "s_waitcnt vmcnt(12)\n\t"
      "ds_read_b64_tr_b16 a[242:243], v214 offset:49152\n\t"
      "v_mfma_f32_16x16x32_bf16 v[44:47], a[228:231], v[140:143], v[44:47]\n\t"
      "ds_read_b64_tr_b16 a[246:247], v214 offset:49160\n\t"
      "v_mfma_f32_16x16x32_bf16 v[48:51], a[232:235], v[140:143], v[48:51]\n\t"
      "ds_read_b64_tr_b16 a[250:251], v214 offset:49216\n\t"
      "ds_read_b128 v[152:155], v199 offset:37376\n\t"
      "v_mfma_f32_16x16x32_bf16 v[52:55], a[236:239], v[140:143], v[52:55]\n\t"
      "ds_read_b64_tr_b16 a[254:255], v214 offset:49224\n\t"
      "s_waitcnt lgkmcnt(12)\n\t"
      "v_mfma_f32_16x16x32_bf16 v[56:59], a[216:219], v[144:147], v[56:59]\n\t"
      "ds_read_b64_tr_b16 v[166:167], v214 offset:49280\n\t"
      "v_mfma_f32_16x16x32_bf16 v[60:63], a[220:223], v[144:147], v[60:63]\n\t"
      "ds_read_b64_tr_b16 v[170:171], v214 offset:49288\n\t"
      "v_mfma_f32_16x16x32_bf16 v[64:67], a[224:227], v[144:147], v[64:67]\n\t"
      "s_waitcnt vmcnt(9)\n\t"
      "ds_read_b64_tr_b16 v[172:173], v221 offset:0\n\t"
      "v_mfma_f32_16x16x32_bf16 v[68:71], a[228:231], v[144:147], v[68:71]\n\t"
      "ds_read_b64_tr_b16 v[176:177], v221 offset:8\n\t"
      "v_mfma_f32_16x16x32_bf16 v[72:75], a[232:235], v[144:147], v[72:75]\n\t"
      "ds_read_b64_tr_b16 v[180:181], v221 offset:64\n\t"
      "ds_read_b128 v[156:159], v196 offset:512\n\t"
      "v_mfma_f32_16x16x32_bf16 v[76:79], a[236:239], v[144:147], v[76:79]\n\t"
      "ds_read_b64_tr_b16 v[184:185], v221 offset:72\n\t"
      "s_waitcnt lgkmcnt(14)\n\t"
      "v_mfma_f32_16x16x32_bf16 v[80:83], a[216:219], v[148:151], v[80:83]\n\t"
      "ds_read_b64_tr_b16 v[188:189], v221 offset:128\n\t"
      "v_mfma_f32_16x16x32_bf16 v[84:87], a[220:223], v[148:151], v[84:87]\n\t"
      "ds_read_b64_tr_b16 v[192:193], v221 offset:136\n\t"
      "v_mfma_f32_16x16x32_bf16 v[88:91], a[224:227], v[148:151], v[88:91]\n\t"
      "v_mfma_f32_16x16x32_bf16 v[92:95], a[228:231], v[148:151], v[92:95]\n\t"
      "v_mfma_f32_16x16x32_bf16 v[96:99], a[232:235], v[148:151], v[96:99]\n\t"
      "ds_read_b128 v[160:163], v196 offset:12800\n\t"
      "v_mfma_f32_16x16x32_bf16 v[100:103], a[236:239], v[148:151], v[100:103]\n\t"
      "s_waitcnt lgkmcnt(11)\n\t"
      "v_mfma_f32_16x16x32_bf16 v[104:107], a[216:219], v[152:155], v[104:107]\n\t"
      "v_mfma_f32_16x16x32_bf16 v[108:111], a[220:223], v[152:155], v[108:111]\n\t"
      "v_mfma_f32_16x16x32_bf16 v[112:115], a[224:227], v[152:155], v[112:115]\n\t"
      "s_waitcnt vmcnt(6)\n\t"
      "ds_read_b64_tr_b16 v[174:175], v221 offset:12288\n\t"
      "buffer_load_dwordx2 v[240:241], v245, s[24:27], 0 offen sc1\n\t"
      "v_mfma_f32_16x16x32_bf16 v[116:119], a[228:231], v[152:155], v[116:119]\n\t"
      "ds_read_b64_tr_b16 v[178:179], v221 offset:12296\n\t"
      "s_cmp_eq_u32 s41, s43\n\t"
      "s_cbranch_scc0 .Lc3_%=\n\t"
      "buffer_load_dword v254, v253, s[24:27], 0 offen sc1\n\t"
      ".Lc3_%=:\n\t"
      "v_mfma_f32_16x16x32_bf16 v[120:123], a[232:235], v[152:155], v[120:123]\n\t"
      "ds_read_b64_tr_b16 v[182:183], v221 offset:12352\n\t"
      "ds_read_b128 v[140:143], v197 offset:512\n\t"
      "v_mfma_f32_16x16x32_bf16 v[132:135], a[236:239], v[152:155], v[132:135]\n\t"
      "ds_read_b64_tr_b16 v[186:187], v221 offset:12360\n\t"
      "s_waitcnt lgkmcnt(9)\n\t"
      "v_mfma_f32_16x16x32_bf16 v[32:35], a[240:243], v[156:159], v[32:35]\n\t"
      "ds_read_b64_tr_b16 v[190:191], v221 offset:12416\n\t"
      "v_mfma_f32_16x16x32_bf16 v[36:39], a[244:247], v[156:159], v[36:39]\n\t"
      "ds_read_b64_tr_b16 v[194:195], v221 offset:12424\n\t"
      "v_mfma_f32_16x16x32_bf16 v[40:43], a[248:251], v[156:159], v[40:43]\n\t"
      "v_mfma_f32_16x16x32_bf16 v[44:47], a[252:255], v[156:159], v[44:47]\n\t"
      "v_mfma_f32_16x16x32_bf16 v[48:51], v[164:167], v[156:159], v[48:51]\n\t"
      "ds_read_b128 v[144:147], v197 offset:12800\n\t"
      "v_mfma_f32_16x16x32_bf16 v[52:55], v[168:171], v[156:159], v[52:55]\n\t"
      "s_waitcnt lgkmcnt(8)\n\t"
      "v_mfma_f32_16x16x32_bf16 v[56:59], a[240:243], v[160:163], v[56:59]\n\t"
      "v_mfma_f32_16x16x32_bf16 v[60:63], a[244:247], v[160:163], v[60:63]\n\t"
      "v_mfma_f32_16x16x32_bf16 v[64:67], a[248:251], v[160:163], v[64:67]\n\t"
      "v_mfma_f32_16x16x32_bf16 v[68:71], a[252:255], v[160:163], v[68:71]\n\t"
      "v_mfma_f32_16x16x32_bf16 v[72:75], v[164:167], v[160:163], v[72:75]\n\t"
      "ds_read_b128 v[128:131], v196 offset:25088\n\t"
      "v_mfma_f32_16x16x32_bf16 v[76:79], v[168:171], v[160:163], v[76:79]\n\t"
      "s_waitcnt lgkmcnt(2)\n\t"
      "v_mfma_f32_16x16x32_bf16 v[32:35], v[172:175], v[140:143], v[32:35]\n\t"
      "v_mfma_f32_16x16x32_bf16 v[36:39], v[176:179], v[140:143], v[36:39]\n\t"
      "v_mfma_f32_16x16x32_bf16 v[40:43], v[180:183], v[140:143], v[40:43]\n\t"
      "v_mfma_f32_16x16x32_bf16 v[44:47], v[184:187], v[140:143], v[44:47]\n\t"
      "v_mfma_f32_16x16x32_bf16 v[48:51], v[188:191], v[140:143], v[48:51]\n\t"
      "ds_read_b128 v[136:139], v196 offset:37376\n\t"
      "v_mfma_f32_16x16x32_bf16 v[52:55], v[192:195], v[140:143], v[52:55]\n\t"
      "s_waitcnt lgkmcnt(2)\n\t"
      "v_mfma_f32_16x16x32_bf16 v[56:59], v[172:175], v[144:147], v[56:59]\n\t"
      "v_mfma_f32_16x16x32_bf16 v[60:63], v[176:179], v[144:147], v[60:63]\n\t"
      "s_waitcnt vmcnt(4) lgkmcnt(2)\n\t"
      "s_barrier\n\t"
      "v_mfma_f32_16x16x32_bf16 v[64:67], v[180:183], v[144:147], v[64:67]\n\t"
      "v_cvt_pk_bf16_f32 v32, v32, v33\n\t"
      "v_cvt_pk_bf16_f32 v33, v34, v35\n\t"
      "v_mfma_f32_16x16x32_bf16 v[68:71], v[184:187], v[144:147], v[68:71]\n\t"
      "v_cvt_pk_bf16_f32 v34, v36, v37\n\t"
      "v_cvt_pk_bf16_f32 v35, v38, v39\n\t"
      "v_mfma_f32_16x16x32_bf16 v[72:75], v[188:191], v[144:147], v[72:75]\n\t"
      "ds_read_b128 v[148:151], v197 offset:25088\n\t"
      "ds_write_b128 v217, v[32:35] offset:24576\n\t"
      "v_cvt_pk_bf16_f32 v40, v40, v41\n\t"
      "v_mfma_f32_16x16x32_bf16 v[76:79], v[192:195], v[144:147], v[76:79]\n\t"
      "v_cvt_pk_bf16_f32 v41, v42, v43\n\t"
      "v_cvt_pk_bf16_f32 v42, v44, v45\n\t"
      "s_mov_b32 m0, s41\n\t"
      "s_add_u32 s51, s40, 0x1200000\n\t"
      "buffer_load_dwordx4 v208, s[20:23], s51 offen lds\n\t"
      "s_waitcnt lgkmcnt(3)\n\t"
      "v_mfma_f32_16x16x32_bf16 v[80:83], a[240:243], v[128:131], v[80:83]\n\t"
      "v_cvt_pk_bf16_f32 v43, v46, v47\n\t"
      "ds_write_b128 v218, v[40:43] offset:24576\n\t"
      "ds_read_b128 v[32:35], v196 offset:49152\n\t"
      "v_mfma_f32_16x16x32_bf16 v[84:87], a[244:247], v[128:131], v[84:87]\n\t"
      "v_cvt_pk_bf16_f32 v48, v48, v49\n\t"
      "v_cvt_pk_bf16_f32 v49, v50, v51\n\t"
      "v_mfma_f32_16x16x32_bf16 v[88:91], a[248:251], v[128:131], v[88:91]\n\t"
      "v_cvt_pk_bf16_f32 v50, v52, v53\n\t"
      "v_cvt_pk_bf16_f32 v51, v54, v55\n\t"
      "buffer_load_dwordx4 v208, s[20:23], s51 offen offset:1024 lds\n\t"
      "v_mfma_f32_16x16x32_bf16 v[92:95], a[252:255], v[128:131], v[92:95]\n\t"
      "ds_write_b128 v219, v[48:51] offset:24576\n\t"
      "ds_read_b128 v[36:39], v197 offset:49152\n\t"
      "v_cvt_pk_bf16_f32 v56, v56, v57\n\t"
      "v_mfma_f32_16x16x32_bf16 v[96:99], v[164:167], v[128:131], v[96:99]\n\t"
      "ds_read_b128 v[152:155], v197 offset:37376\n\t"
      "v_cvt_pk_bf16_f32 v57, v58, v59\n\t"
      "v_cvt_pk_bf16_f32 v58, v60, v61\n\t"
      "v_mfma_f32_16x16x32_bf16 v[100:103], v[168:171], v[128:131], v[100:103]\n\t"
      "v_cvt_pk_bf16_f32 v59, v62, v63\n\t"
      "ds_write_b128 v217, v[56:59] offset:36864\n\t"
      "ds_read_b128 v[40:43], v198 offset:49152\n\t"
      "s_waitcnt lgkmcnt(9)\n\t"
      "v_mfma_f32_16x16x32_bf16 v[104:107], a[240:243], v[136:139], v[104:107]\n\t"
      "v_cvt_pk_bf16_f32 v64, v64, v65\n\t"
      "v_cvt_pk_bf16_f32 v65, v66, v67\n\t"
      "buffer_load_dwordx4 v208, s[20:23], s51 offen offset:2048 lds\n\t"
      "v_mfma_f32_16x16x32_bf16 v[108:111], a[244:247], v[136:139], v[108:111]\n\t"
      "v_cvt_pk_bf16_f32 v66, v68, v69\n\t"
      "v_cvt_pk_bf16_f32 v67, v70, v71\n\t"
      "v_mfma_f32_16x16x32_bf16 v[112:115], a[248:251], v[136:139], v[112:115]\n\t"
      "ds_write_b128 v218, v[64:67] offset:36864\n\t"
      "ds_read_b128 v[44:47], v199 offset:49152\n\t"
      "v_cvt_pk_bf16_f32 v72, v72, v73\n\t"
      "v_mfma_f32_16x16x32_bf16 v[116:119], a[252:255], v[136:139], v[116:119]\n\t"
      "v_cvt_pk_bf16_f32 v73, v74, v75\n\t"
      "v_cvt_pk_bf16_f32 v74, v76, v77\n\t"
      "v_mfma_f32_16x16x32_bf16 v[120:123], v[164:167], v[136:139], v[120:123]\n\t"
      "v_cvt_pk_bf16_f32 v75, v78, v79\n\t"
      "ds_write_b128 v219, v[72:75] offset:36864\n\t"
      "ds_read_b128 v[48:51], v197 offset:49408\n\t"
      "v_mfma_f32_16x16x32_bf16 v[132:135], v[168:171], v[136:139], v[132:135]\n\t"
      "s_add_u32 m0, s41, 0x3000\n\t"
      "s_add_u32 s51, s40, 0x1200000\n\t"
      "buffer_load_dwordx4 v209, s[20:23], s51 offen lds\n\t"
      "ds_read_b128 v[52:55], v198 offset:49408\n\t"
      "s_waitcnt lgkmcnt(13)\n\t"
      "v_mfma_f32_16x16x32_bf16 v[80:83], v[172:175], v[148:151], v[80:83]\n\t"
      "ds_read_b128 v[56:59], v199 offset:49408\n\t"
      "ds_read_b128 v[60:63], v196 offset:49408\n\t"
      "v_mfma_f32_16x16x32_bf16 v[84:87], v[176:179], v[148:151], v[84:87]\n\t"
      "ds_read_b128 v[64:67], v198 offset:49664\n\t"
      "s_memrealtime s[80:81]\n\t"
      "v_mfma_f32_16x16x32_bf16 v[88:91], v[180:183], v[148:151], v[88:91]\n\t"
      "buffer_load_dwordx4 v209, s[20:23], s51 offen offset:1024 lds\n\t"
      "ds_read_b128 v[68:71], v199 offset:49664\n\t"
      "v_mfma_f32_16x16x32_bf16 v[92:95], v[184:187], v[148:151], v[92:95]\n\t"
      "ds_read_b128 v[72:75], v196 offset:49664\n\t"
      "ds_read_b128 v[76:79], v197 offset:49664\n\t"
      "v_mfma_f32_16x16x32_bf16 v[96:99], v[188:191], v[148:151], v[96:99]\n\t"
      "v_mfma_f32_16x16x32_bf16 v[100:103], v[192:195], v[148:151], v[100:103]\n\t"
      "buffer_load_dwordx4 v209, s[20:23], s51 offen offset:2048 lds\n\t"
      "s_waitcnt lgkmcnt(13)\n\t"
      "v_mfma_f32_16x16x32_bf16 v[104:107], v[172:175], v[152:155], v[104:107]\n\t"
      "v_mfma_f32_16x16x32_bf16 v[108:111], v[176:179], v[152:155], v[108:111]\n\t"
      "v_mfma_f32_16x16x32_bf16 v[112:115], v[180:183], v[152:155], v[112:115]\n\t"
      "v_mfma_f32_16x16x32_bf16 v[116:119], v[184:187], v[152:155], v[116:119]\n\t"
      "v_mfma_f32_16x16x32_bf16 v[120:123], v[188:191], v[152:155], v[120:123]\n\t"
      "v_mfma_f32_16x16x32_bf16 v[132:135], v[192:195], v[152:155], v[132:135]\n\t"
      "v_add_u32_e32 v196, s46, v200\n\t"
      "v_add_u32_e32 v197, s46, v201\n\t"
      "v_add_u32_e32 v198, s46, v202\n\t"
      "v_add_u32_e32 v199, s46, v203\n\t"
      "s_mov_b32 s86, 80\n\t"
      "s_mov_b32 s84, 92\n\t"
      "s_branch .Lp2_%=\n\t"
      ".Lend_%=:\n\t"
      ".Lconv_end:\n\t"
      :
      : "{s[16:31]}"(d16), "{s[36:39]}"(db), "{s[52:53]}"(rs), "{s40}"(t0), "{s41}"(p0), "{s43}"(lds0), "{s94}"(ty), "{s95}"(tres), "{s[60:61]}"(pres),
      "{s69}"(pre1), "{s74}"(plm), "{s75}"(plb), "{s66}"(sst ? 24576u : 0u - 24576u), "{s89}"((blk >> 4) % 5u), "{v208}"(ga), "{v209}"(gb),
      "{v211}"(woff[0]), "{v212}"(woff[1]), "{v213}"(woff[2]), "{v248}"(ta), "{v249}"(ta), "{v250}"(tp), "{v251}"(t1), "{v252}"(mk), "{v245}"(vgv),
      "{v246}"(vxw), "{v247}"(vxr), "{v253}"(vcell), "{v200}"(cfa[0]), "{v201}"(cfa[1]), "{v202}"(cfa[2]), "{v203}"(cfa[3]), "{v204}"(csw[0]),
      "{v205}"(csw[1]), "{v206}"(csw[2]), "{v207}"(csr), "{v214}"(tbase + 73728u), "{v221}"(tbase + 135168u), "{v[226:229]}"(br0), "{v[230:233]}"(br1),
      "{v[234:237]}"(br2), "{v4}"(tc0), "{v5}"(tc1), "{v6}"(cpo)
      : "memory", "scc", "vcc",
      "s42", "s44", "s45", "s46", "s47", "s48", "s49", "s50", "s51", "s54", "s55", "s56", "s57", "s58", "s59", "s62", "s63", "s64", "s65", "s67",
      "s68", "s70", "s71", "s72", "s73", "s76", "s77", "s78", "s79", "s80", "s81", "s82", "s83", "s84", "s85", "s86", "s87", "s88", "s90", "s91",
      "s92", "s93", "s96", "s97", "s98", "s99",
      "v1", "v2", "v3", "v7", "v8", "v9", "v10", "v11", "v12", "v13", "v14", "v15", "v16", "v17", "v18", "v19", "v20", "v21", "v22", "v23", "v24",
      "v25", "v26", "v27", "v28", "v29", "v30", "v31", "v32", "v33", "v34", "v35", "v36", "v37", "v38", "v39", "v40", "v41", "v42", "v43", "v44",
      "v45", "v46", "v47", "v48", "v49", "v50", "v51", "v52", "v53", "v54", "v55", "v56", "v57", "v58", "v59", "v60", "v61", "v62", "v63", "v64",
      "v65", "v66", "v67", "v68", "v69", "v70", "v71", "v72", "v73", "v74", "v75", "v76", "v77", "v78", "v79", "v80", "v81", "v82", "v83", "v84",
      "v85", "v86", "v87", "v88", "v89", "v90", "v91", "v92", "v93", "v94", "v95", "v96", "v97", "v98", "v99", "v100", "v101", "v102", "v103", "v104",
      "v105", "v106", "v107", "v108", "v109", "v110", "v111", "v112", "v113", "v114", "v115", "v116", "v117", "v118", "v119", "v120", "v121", "v122",
      "v123", "v124", "v125", "v126", "v127", "v128", "v129", "v130", "v131", "v132", "v133", "v134", "v135", "v136", "v137", "v138", "v139", "v140",
      "v141", "v142", "v143", "v144", "v145", "v146", "v147", "v148", "v149", "v150", "v151", "v152", "v153", "v154", "v155", "v156", "v157", "v158",
      "v159", "v160", "v161", "v162", "v163", "v164", "v165", "v166", "v167", "v168", "v169", "v170", "v171", "v172", "v173", "v174", "v175", "v176",
      "v177", "v178", "v179", "v180", "v181", "v182", "v183", "v184", "v185", "v186", "v187", "v188", "v189", "v190", "v191", "v192", "v193", "v194",
      "v195", "v196", "v197", "v198", "v199", "v210", "v215", "v216", "v217", "v218", "v219", "v220", "v222", "v223", "v224", "v225", "v238", "v239",
      "v240", "v241", "v242", "v243", "v244", "v254", "v255",
      "a0", "a1", "a2", "a3", "a4", "a5", "a6", "a7", "a8", "a9", "a10", "a11", "a12", "a13", "a14", "a15", "a16", "a17", "a18", "a19", "a20", "a21",
      "a22", "a23", "a24", "a25", "a26", "a27", "a28", "a29", "a30", "a31", "a32", "a33", "a34", "a35", "a36", "a37", "a38", "a39", "a40", "a41",
      "a42", "a43", "a44", "a45", "a46", "a47", "a48", "a49", "a50", "a51", "a52", "a53", "a54", "a55", "a56", "a57", "a58", "a59", "a60", "a61",
      "a62", "a63", "a64", "a65", "a66", "a67", "a68", "a69", "a70", "a71", "a72", "a73", "a74", "a75", "a76", "a77", "a78", "a79", "a80", "a81",
      "a82", "a83", "a84", "a85", "a86", "a87", "a88", "a89", "a90", "a91", "a92", "a93", "a94", "a95", "a96", "a97", "a98", "a99", "a100", "a101",
      "a102", "a103", "a104", "a105", "a106", "a107", "a108", "a109", "a110", "a111", "a112", "a113", "a114", "a115", "a116", "a117", "a118", "a119",
      "a120", "a121", "a122", "a123", "a124", "a125", "a126", "a127", "a128", "a129", "a130", "a131", "a132", "a133", "a134", "a135", "a136", "a137",
      "a138", "a139", "a140", "a141", "a142", "a143", "a144", "a145", "a146", "a147", "a148", "a149", "a150", "a151", "a152", "a153", "a154", "a155",
      "a156", "a157", "a158", "a159", "a160", "a161", "a162", "a163", "a164", "a165", "a166", "a167", "a168", "a169", "a170", "a171", "a172", "a173",
      "a174", "a175", "a176", "a177", "a178", "a179", "a180", "a181", "a182", "a183", "a184", "a185", "a186", "a187", "a188", "a189", "a190", "a191",
      "a192", "a193", "a194", "a195", "a196", "a197", "a198", "a199", "a200", "a201", "a202", "a203", "a204", "a205", "a206", "a207", "a208", "a209",
      "a210", "a211", "a212", "a213", "a214", "a215", "a216", "a217", "a218", "a219", "a220", "a221", "a222", "a223", "a224", "a225", "a226", "a227",
      "a228", "a229", "a230", "a231", "a232", "a233", "a234", "a235", "a236", "a237", "a238", "a239", "a240", "a241", "a242", "a243", "a244", "a245",
      "a246", "a247", "a248", "a249", "a250", "a251", "a252", "a253", "a254", "a255");
}

extern "C" void kernel_launch(void* const* d_in, const int* in_sizes, int n_in,
                              void* d_out, int out_size, void* d_ws, size_t ws_size,
                              hipStream_t stream) {
  conv1x1_kernel<<<256, 256, 0, stream>>>(d_in[1], 0x0002000000048000ull, d_in[0], 0x0002000006c00000ull, d_ws, 0x0002000000000c00ull, d_out, 0x0002000006c00000ull,
                                          d_in[2], 0x0002000000000300ull);
}
```

```cpp
#include <hip/hip_runtime.h>
#include <stdint.h>

#define LAS __attribute__((address_space(3)))
typedef __attribute__((ext_vector_type(16))) unsigned u32x16;
typedef __attribute__((ext_vector_type(4))) unsigned u32x4;
typedef unsigned long long u64;

__global__ __launch_bounds__(256, 1) void conv1x1_kernel(const void* w, u64 cw, const void* x, u64 cx, void* ws, u64 cs, void* out, u64 co,
                                                         const void* bias, u64 cb) {
  __shared__ __attribute__((aligned(1024))) unsigned char lds[160768];

  u32x16 d16;
  u32x4 db;
  u64 rs;
  asm volatile(
      "s_load_dwordx16 s[16:31], %3, 0x0\n\t"
      "s_load_dwordx4 s[36:39], %3, 0x40\n\t"
      "s_memrealtime s[52:53]\n\t"
      : "={s[16:31]}"(d16), "={s[36:39]}"(db), "={s[52:53]}"(rs)
      : "s"(__builtin_amdgcn_kernarg_segment_ptr())
      : "memory");
  unsigned cpo = ((blockIdx.x >> 3) * 256u + threadIdx.x) * 4u;
  asm volatile(
      "s_getpc_b64 s[92:93]\n\t"
      ".Lconv_cp:\n\t"
      "v_min_u32_e32 v6, .Lconv_end-.Lconv_cp-4, v6\n\t"
      "global_load_dword v6, v6, s[92:93]\n\t"
      : "+{v6}"(cpo)
      :
      : "memory", "s92", "s93");
  __builtin_amdgcn_sched_barrier(0);
  unsigned tid  = threadIdx.x;
  unsigned lane = tid & 63;
  unsigned wv   = __builtin_amdgcn_readfirstlane(tid >> 6);
  const unsigned lds0 = (unsigned)(size_t)(LAS unsigned char*)lds;
  unsigned blk  = blockIdx.x;
  const unsigned p0   = lds0 + 3072u * wv;
  const unsigned lq = lane >> 4, ph = (lane >> 2) & 3, pl = lane & 3;
  const unsigned gs = 256u * lq + 64u * ((ph - lq) & 3) + 16u * (pl ^ ((4u - wv) & 3));
  const unsigned ga = gs + 3072u * (wv < 3u ? wv : 5u);
  const unsigned gb = gs + 3072u * (wv < 2u ? wv + 6u : wv + 1u);
  const unsigned boff = 192u * wv + 16u * lq;
  const unsigned role = (blk >> 2) & 1u;
  unsigned pk = (blk & 3u) * 32u + (blk >> 3);
  const unsigned sst  = (role ^ (blk >> 4)) & 1u;
  const unsigned t0   = pk * 49152u + sst * 24576u;
  const unsigned pv = blk >> 7, ps = ((blk & 7u) | ((blk >> 1) & 56u)) ^ (4u * pv), pgr = (ps >> 2) & 7u, pi = (ps & 3u) * 2u + pv;
  const unsigned ppg = min(pgr * 8u, 52u) + (pi & (0x13777777u >> (4u * pgr)) & 7u);
  const unsigned pop = (ppg << 21) + ((ps & 3u) * 4u + pv) * 0x1c080u + 0x24080u;
  unsigned pfire = ((role ^ ppg) & 1u) * 2u, psel = ps >> 5;
  const unsigned vpub = lane == 0u ? 0u + (pv * 64u + ps) * 8u : 0x40000000u;
  const unsigned pfo = (blk >> 4) * 18432u;
  const unsigned pfv = tid * 16u;
  const unsigned ty = (blk >> 3) & 1u;
  const unsigned tidx = (blk >> 3) * 4u + wv, tsel = tidx >> 6;
  const unsigned toff = (tidx & 63u) < 54u ? (tidx & 63u) << 21 : 113246204u;
  unsigned tres, pre1;
  u64 pres;
  u32x4 br0, br1, br2;
  asm volatile(
      "s_add_u32 m0, s43, 0x27000\n\t"
      "s_waitcnt lgkmcnt(0)\n\t"
      "s_cmp_eq_u32 s94, 1\n\t"
      "s_cbranch_scc0 .Lc1_%=\n\t"
      "s_cmp_eq_u32 s41, s43\n\t"
      "s_cbranch_scc0 .Lc0_%=\n\t"
      "s_cmp_eq_u32 s78, 0\n\t"
      "s_cselect_b64 s[96:97], s[20:21], s[28:29]\n\t"
      "s_load_dword s69, s[96:97], s76 offset:0x0\n\t"
      "s_load_dwordx2 s[60:61], s[96:97], s76 offset:0x3300\n\t"
      ".Lc0_%=:\n\t"
      "buffer_load_dwordx4 v216, s[16:19], s42 offen lds\n\t"
      "s_add_u32 s51, s42, 0x1000\n\t"
      "buffer_load_dwordx4 v216, s[16:19], s51 offen lds\n\t"
      "s_add_u32 s51, s42, 0x2000\n\t"
      "buffer_load_dwordx4 v216, s[16:19], s51 offen lds\n\t"
      "s_add_u32 s51, s42, 0x3000\n\t"
      "buffer_load_dwordx4 v216, s[16:19], s51 offen lds\n\t"
      "s_add_u32 s51, s42, 0x4000\n\t"
      "buffer_load_dwordx4 v216, s[16:19], s51 offen lds\n\t"
      ".Lc1_%=:\n\t"
      "buffer_load_dwordx4 v[226:229], v215, s[36:39], 0 offen offset:0\n\t"
      "buffer_load_dwordx4 v[230:233], v215, s[36:39], 0 offen offset:64\n\t"
      "buffer_load_dwordx4 v[234:237], v215, s[36:39], 0 offen offset:128\n\t"
      "s_mov_b32 m0, s41\n\t"
      "s_nop 0\n\t"
      "buffer_load_dwordx4 v208, s[20:23], s40 offen lds\n\t"
      "buffer_load_dwordx4 v208, s[20:23], s40 offen offset:1024 lds\n\t"
      "buffer_load_dwordx4 v208, s[20:23], s40 offen offset:2048 lds\n\t"
      "s_cmp_eq_u32 s94, 0\n\t"
      "s_cbranch_scc0 .Lvb0_%=\n\t"
      "s_add_u32 m0, s41, 0x3000\n\t"
      "s_nop 0\n\t"
      "buffer_load_dwordx4 v209, s[20:23], s40 offen lds\n\t"
      "buffer_load_dwordx4 v209, s[20:23], s40 offen offset:1024 lds\n\t"
      "buffer_load_dwordx4 v209, s[20:23], s40 offen offset:2048 lds\n\t"
      "s_cmp_eq_u32 s99, 0\n\t"
      "s_cselect_b64 s[96:97], s[20:21], s[28:29]\n\t"
      "s_load_dword s95, s[96:97], s98\n\t"
      "s_branch .Lvb1_%=\n\t"
      ".Lvb0_%=:\n\t"
      "s_cmp_eq_u32 s41, s43\n\t"
      "s_cbranch_scc1 .Lev0_%=\n\t"
      "s_cmp_eq_u32 s99, 0\n\t"
      "s_cselect_b64 s[96:97], s[20:21], s[28:29]\n\t"
      "s_load_dword s95, s[96:97], s98\n\t"
      "s_cmp_eq_u32 s94, 0\n\t"
      "s_cbranch_scc1 .Lev1_%=\n\t"
      "s_sleep 8\n\t"
      "s_branch .Lev1_%=\n\t"
      ".Lev0_%=:\n\t"
      "s_andn2_b32 s79, s52, 15\n\t"
      "v_mov_b32_e32 v241, s53\n\t"
      "s_waitcnt lgkmcnt(2)\n\t"
      "s_nop 7\n\t"
      "s_getreg_b32 s78, hwreg(HW_REG_IB_STS, 8, 4)\n\t"
      "s_cmp_eq_u32 s78, 1\n\t"
      "s_cselect_b32 s51, 2, 0\n\t"
      "s_xor_b32 s77, s77, s51\n\t"
      "s_cmp_lg_u32 s78, 0\n\t"
      "s_cselect_b32 s77, s77, 1\n\t"
      "s_or_b32 s79, s79, s77\n\t"
      "v_mov_b32_e32 v240, s79\n\t"
      "buffer_store_dwordx2 v[240:241], v244, s[24:27], 0 offen sc1\n\t"
      "s_cmp_eq_u32 s99, 0\n\t"
      "s_cselect_b64 s[96:97], s[20:21], s[28:29]\n\t"
      "s_load_dword s95, s[96:97], s98\n\t"
      ".Lev1_%=:\n\t"
      "s_add_u32 m0, s41, 0x3000\n\t"
      "s_nop 0\n\t"
      "buffer_load_dwordx4 v209, s[20:23], s40 offen lds\n\t"
      "buffer_load_dwordx4 v209, s[20:23], s40 offen offset:1024 lds\n\t"
      "buffer_load_dwordx4 v209, s[20:23], s40 offen offset:2048 lds\n\t"
      "s_add_u32 m0, s41, 0x6000\n\t"
      "s_add_u32 s51, s40, 0x600000\n\t"
      "buffer_load_dwordx4 v208, s[20:23], s51 offen lds\n\t"
      "buffer_load_dwordx4 v208, s[20:23], s51 offen offset:1024 lds\n\t"
      "buffer_load_dwordx4 v208, s[20:23], s51 offen offset:2048 lds\n\t"
      "s_add_u32 m0, s41, 0x9000\n\t"
      "s_add_u32 s51, s40, 0x600000\n\t"
      "buffer_load_dwordx4 v209, s[20:23], s51 offen lds\n\t"
      "buffer_load_dwordx4 v209, s[20:23], s51 offen offset:1024 lds\n\t"
      "buffer_load_dwordx4 v209, s[20:23], s51 offen offset:2048 lds\n\t"
      ".Lvb1_%=:\n\t"
      : "={s95}"(tres), "={s[60:61]}"(pres), "={s69}"(pre1), "={v[226:229]}"(br0), "={v[230:233]}"(br1), "={v[234:237]}"(br2),
        "+v"(lane), "+{s77}"(pfire), "+{s78}"(psel), "+{v6}"(cpo)
      : "{s[16:31]}"(d16), "{s[36:39]}"(db), "{s40}"(t0), "{s41}"(p0), "{s43}"(lds0), "{s94}"(ty),
        "{v208}"(ga), "{v209}"(gb), "{v215}"(boff), "{v216}"(pfv),
        "{s42}"(pfo), "{s98}"(toff), "{s99}"(tsel), "{s76}"(pop), "{s[52:53]}"(rs), "{v244}"(vpub)
      : "memory", "scc", "s51", "s92", "s93", "s96", "s97", "s79", "v240", "v241");
  asm volatile("" : "+s"(wv), "+s"(pk));
  __builtin_amdgcn_sched_barrier(0);
  const unsigned vcell = lane < 2u ? 2048u + pk * 8u + 4u * (role ^ lane) : 0x40000000u;
  const unsigned ob = (unsigned)(((lane < 12u ? 0x41ccd62ca418820ull >> (5u * lane) : 0x22a4c1eeull >> (5u * ((lane - 12u) & 7u)))) & 31u) * 6291456u + pk * 49152u;
  const unsigned pg = (ob + 24576u) >> 21;
  const unsigned ta = lane < 18u ? ob + 24576u * sst : 113246208u, t1 = ob + 24576u * (sst ^ 1u);
  const unsigned tq = (lane >= 9u ? 82u : 80u), tp = lane >= 14u ? 0u : tq;
  const unsigned mk = lane >= 4u + ty && lane < 18u ? 1u << (pg < 52u ? (pg >> 3) * 4u : 28u) : 0u;
  const unsigned plm = (unsigned)__builtin_amdgcn_ballot_w64(((pg ^ role ^ sst) & 1u) != 0u), plb = wv < 2u ? plm : ~plm;
  const unsigned vgv = 0u + (wv & 1u) * 512u + lane * 8u;
  const unsigned vxr = lds0 + 159744u + lane * 16u, vxw = vxr + wv * 4u;
  const unsigned li = lane & 15, lg = lane >> 4, rl = li & 3, hi = li >> 2;
  const unsigned rb = 768u * li + 16u * (lg ^ ((4u - hi) & 3)) - 3072u * wv;
  unsigned cfa[4], csw[3];
#pragma unroll
  for (unsigned u = 0; u < 4; ++u) cfa[u] = rb + 64u * ((u - rl) & 3);
#pragma unroll
  for (unsigned j = 0; j < 3; ++j) {
    const unsigned e = 3u * wv + j;
    csw[j] = rb + 256u * (e >> 2) + 64u * (((e & 3) + (e >> 2) - rl) & 3);
  }
  const unsigned csr = 16u * lane;
  const unsigned tbase = p0 + (4u * lg + (li >> 2)) * 192u + 16u * (li & 3);
  unsigned woff[3];
#pragma unroll
  for (unsigned j = 0; j < 3; ++j) {
    const unsigned q = 64u * j + lane, rr = q / 12u;
    woff[j] = (8u * (rr >> 2) + (rr & 3)) * 768u + 192u * wv + 16u * (q % 12u) - 1024u * j;
  }
  asm volatile(
      "s_cmp_eq_u32 s94, 0\n\t"
      "s_cbranch_scc0 .Lc2_%=\n\t"
      "s_add_u32 m0, s41, 0x1e000\n\t"
      "s_mov_b32 s51, 0x0\n\t"
      "buffer_load_dwordx4 v211, s[16:19], s51 offen lds\n\t"
      "buffer_load_dwordx4 v212, s[16:19], s51 offen offset:1024 lds\n\t"
      "buffer_load_dwordx4 v213, s[16:19], s51 offen offset:2048 lds\n\t"
      ".Lc2_%=:\n\t"
      "s_add_u32 s49, s89, 5\n\t"
      "s_mov_b32 s62, 0\n\t"
      "s_mov_b32 s70, 0\n\t"
      "s_add_u32 s50, s41, 0x12000\n\t"
      "s_add_u32 s51, s41, 0x12000\n\t"
      "s_sub_u32 s63, s52, 800\n\t"
      "s_andn2_b32 s63, s63, 15\n\t"
      "v_add_u32_e32 v196, s41, v200\n\t"
      "v_add_u32_e32 v197, s41, v201\n\t"
      "v_add_u32_e32 v198, s41, v202\n\t"
      "v_add_u32_e32 v199, s41, v203\n\t"
      "v_add_u32_e32 v217, s51, v204\n\t"
      "v_add_u32_e32 v218, s51, v205\n\t"
      "v_add_u32_e32 v219, s51, v206\n\t"
      "v_add_u32_e32 v220, s51, v207\n\t"
      "v_add_u32_e32 v238, s51, v207\n\t"
      "s_cmp_eq_u32 s94, 0\n\t"
      "s_cbranch_scc0 .Lpb_%=\n\t"
      "s_add_u32 m0, s41, 0x21000\n\t"
      "s_mov_b32 s51, 0xc00\n\t"
      "buffer_load_dwordx4 v211, s[16:19], s51 offen lds\n\t"
      "buffer_load_dwordx4 v212, s[16:19], s51 offen offset:1024 lds\n\t"
      "buffer_load_dwordx4 v213, s[16:19], s51 offen offset:2048 lds\n\t"
      "s_add_u32 m0, s41, 0x24000\n\t"
      "s_mov_b32 s51, 0x6000\n\t"
      "buffer_load_dwordx4 v211, s[16:19], s51 offen lds\n\t"
      "buffer_load_dwordx4 v212, s[16:19], s51 offen offset:1024 lds\n\t"
      "buffer_load_dwordx4 v213, s[16:19], s51 offen offset:2048 lds\n\t"
      "s_waitcnt vmcnt(15)\n\t"
      "v_lshlrev_b32_e32 v8, 16, v226\n\t"
      "v_and_b32_e32 v9, 0xffff0000, v226\n\t"
      "v_lshlrev_b32_e32 v10, 16, v227\n\t"
      "v_and_b32_e32 v11, 0xffff0000, v227\n\t"
      "v_lshlrev_b32_e32 v12, 16, v228\n\t"
      "v_and_b32_e32 v13, 0xffff0000, v228\n\t"
      "v_lshlrev_b32_e32 v14, 16, v229\n\t"
      "v_and_b32_e32 v15, 0xffff0000, v229\n\t"
      "v_lshlrev_b32_e32 v16, 16, v230\n\t"
      "v_and_b32_e32 v17, 0xffff0000, v230\n\t"
      "v_lshlrev_b32_e32 v18, 16, v231\n\t"
      "v_and_b32_e32 v19, 0xffff0000, v231\n\t"
      "v_lshlrev_b32_e32 v20, 16, v232\n\t"
      "v_and_b32_e32 v21, 0xffff0000, v232\n\t"
      "v_lshlrev_b32_e32 v22, 16, v233\n\t"
      "v_and_b32_e32 v23, 0xffff0000, v233\n\t"
      "v_lshlrev_b32_e32 v24, 16, v234\n\t"
      "v_and_b32_e32 v25, 0xffff0000, v234\n\t"
      "v_lshlrev_b32_e32 v26, 16, v235\n\t"
      "v_and_b32_e32 v27, 0xffff0000, v235\n\t"
      "v_lshlrev_b32_e32 v28, 16, v236\n\t"
      "v_and_b32_e32 v29, 0xffff0000, v236\n\t"
      "v_lshlrev_b32_e32 v30, 16, v237\n\t"
      "v_and_b32_e32 v31, 0xffff0000, v237\n\t"
      "s_add_u32 m0, s41, 0x12000\n\t"
      "s_mov_b32 s51, 0x6c00\n\t"
      "buffer_load_dwordx4 v211, s[16:19], s51 offen lds\n\t"
      "buffer_load_dwordx4 v212, s[16:19], s51 offen offset:1024 lds\n\t"
      "buffer_load_dwordx4 v213, s[16:19], s51 offen offset:2048 lds\n\t"
      "s_add_u32 m0, s41, 0x15000\n\t"
      "s_mov_b32 s51, 0xc000\n\t"
      "buffer_load_dwordx4 v211, s[16:19], s51 offen lds\n\t"
      "buffer_load_dwordx4 v212, s[16:19], s51 offen offset:1024 lds\n\t"
      "buffer_load_dwordx4 v213, s[16:19], s51 offen offset:2048 lds\n\t"
      "s_add_u32 m0, s41, 0x18000\n\t"
      "s_mov_b32 s51, 0xcc00\n\t"
      "buffer_load_dwordx4 v211, s[16:19], s51 offen lds\n\t"
      "buffer_load_dwordx4 v212, s[16:19], s51 offen offset:1024 lds\n\t"
      "buffer_load_dwordx4 v213, s[16:19], s51 offen offset:2048 lds\n\t"
      "s_add_u32 m0, s41, 0x1b000\n\t"
      "s_mov_b32 s51, 0x12000\n\t"
      "buffer_load_dwordx4 v211, s[16:19], s51 offen lds\n\t"
      "buffer_load_dwordx4 v212, s[16:19], s51 offen offset:1024 lds\n\t"
      "buffer_load_dwordx4 v213, s[16:19], s51 offen offset:2048 lds\n\t"
      "s_mov_b32 s44, 0\n\t"
      "s_mov_b32 s85, 3\n\t"
      "s_mov_b32 s45, s41\n\t"
      "s_add_u32 s46, s41, 0x6000\n\t"
      "s_add_u32 s47, s41, 0xc000\n\t"
      "s_mov_b64 vcc, 0\n\t"
      "s_waitcnt vmcnt(18)\n\t"
      "s_barrier\n\t"
      "ds_read_b64_tr_b16 a[0:1], v214 offset:49152\n\t"
      "ds_read_b64_tr_b16 a[4:5], v214 offset:49160\n\t"
      "ds_read_b64_tr_b16 a[8:9], v214 offset:49216\n\t"
      "ds_read_b64_tr_b16 a[12:13], v214 offset:49224\n\t"
      "ds_read_b64_tr_b16 a[16:17], v214 offset:49280\n\t"
      "ds_read_b64_tr_b16 a[20:21], v214 offset:49288\n\t"
      "ds_read_b128 v[140:143], v196 offset:0\n\t"
      "s_waitcnt vmcnt(15)\n\t"
      "ds_read_b64_tr_b16 a[2:3], v221 offset:0\n\t"
      "ds_read_b64_tr_b16 a[6:7], v221 offset:8\n\t"
      "ds_read_b64_tr_b16 a[10:11], v221 offset:64\n\t"
      "ds_read_b64_tr_b16 a[14:15], v221 offset:72\n\t"
      "ds_read_b64_tr_b16 a[18:19], v221 offset:128\n\t"
      "ds_read_b64_tr_b16 a[22:23], v221 offset:136\n\t"
      "ds_read_b128 v[152:155], v196 offset:12288\n\t"
      "s_waitcnt lgkmcnt(8)\n\t"
      "s_add_u32 m0, s41, 0x1e000\n\t"
      "s_mov_b32 s51, 0x12c00\n\t"
      "buffer_load_dwordx4 v211, s[16:19], s51 offen lds\n\t"
      "buffer_load_dwordx4 v212, s[16:19], s51 offen offset:1024 lds\n\t"
      "buffer_load_dwordx4 v213, s[16:19], s51 offen offset:2048 lds\n\t"
      "s_waitcnt vmcnt(15)\n\t"
      "ds_read_b64_tr_b16 a[24:25], v221 offset:12288\n\t"
      "ds_read_b64_tr_b16 a[28:29], v221 offset:12296\n\t"
      "ds_read_b64_tr_b16 a[32:33], v221 offset:12352\n\t"
      "ds_read_b64_tr_b16 a[36:37], v221 offset:12360\n\t"
      "ds_read_b64_tr_b16 a[40:41], v221 offset:12416\n\t"
      "ds_read_b64_tr_b16 a[44:45], v221 offset:12424\n\t"
      "ds_read_b128 v[144:147], v197 offset:0\n\t"
      "s_waitcnt lgkmcnt(8)\n\t"
      "v_mfma_f32_16x16x32_bf16 v[80:83], a[0:3], v[140:143], v[8:11]\n\t"
      "s_add_u32 m0, s41, 0x21000\n\t"
      "s_mov_b32 s51, 0x18000\n\t"
      "buffer_load_dwordx4 v211, s[16:19], s51 offen lds\n\t"
      "v_mfma_f32_16x16x32_bf16 v[84:87], a[4:7], v[140:143], v[12:15]\n\t"
      "buffer_load_dwordx4 v212, s[16:19], s51 offen offset:1024 lds\n\t"
      "v_mfma_f32_16x16x32_bf16 v[88:91], a[8:11], v[140:143], v[16:19]\n\t"
      "buffer_load_dwordx4 v213, s[16:19], s51 offen offset:2048 lds\n\t"
      "s_waitcnt vmcnt(15)\n\t"
      "ds_read_b64_tr_b16 a[26:27], v214 offset:0\n\t"
      "v_mfma_f32_16x16x32_bf16 v[92:95], a[12:15], v[140:143], v[20:23]\n\t"
      "ds_read_b64_tr_b16 a[30:31], v214 offset:8\n\t"
      "ds_read_b64_tr_b16 a[34:35], v214 offset:64\n\t"
      "v_mfma_f32_16x16x32_bf16 v[96:99], a[16:19], v[140:143], v[24:27]\n\t"
      "ds_read_b64_tr_b16 a[38:39], v214 offset:72\n\t"
      "ds_read_b64_tr_b16 a[42:43], v214 offset:128\n\t"
      "v_mfma_f32_16x16x32_bf16 v[100:103], a[20:23], v[140:143], v[28:31]\n\t"
      "ds_read_b64_tr_b16 a[46:47], v214 offset:136\n\t"
      "ds_read_b128 v[156:159], v197 offset:12288\n\t"
      "s_waitcnt lgkmcnt(8)\n\t"
      "v_mfma_f32_16x16x32_bf16 v[104:107], a[0:3], v[152:155], v[8:11]\n\t"
      "s_add_u32 m0, s41, 0x24000\n\t"
      "s_mov_b32 s51, 0x18c00\n\t"
      "buffer_load_dwordx4 v211, s[16:19], s51 offen lds\n\t"
      "v_mfma_f32_16x16x32_bf16 v[108:111], a[4:7], v[152:155], v[12:15]\n\t"
      "buffer_load_dwordx4 v212, s[16:19], s51 offen offset:1024 lds\n\t"
      "v_mfma_f32_16x16x32_bf16 v[112:115], a[8:11], v[152:155], v[16:19]\n\t"
      "buffer_load_dwordx4 v213, s[16:19], s51 offen offset:2048 lds\n\t"
      "s_waitcnt vmcnt(15)\n\t"
      "ds_read_b64_tr_b16 a[48:49], v214 offset:12288\n\t"
      "v_mfma_f32_16x16x32_bf16 v[116:119], a[12:15], v[152:155], v[20:23]\n\t"
      "ds_read_b64_tr_b16 a[52:53], v214 offset:12296\n\t"
      "ds_read_b64_tr_b16 a[56:57], v214 offset:12352\n\t"
      "v_mfma_f32_16x16x32_bf16 v[120:123], a[16:19], v[152:155], v[24:27]\n\t"
      "ds_read_b64_tr_b16 a[60:61], v214 offset:12360\n\t"
      "ds_read_b64_tr_b16 a[64:65], v214 offset:12416\n\t"
      "v_mfma_f32_16x16x32_bf16 v[132:135], a[20:23], v[152:155], v[28:31]\n\t"
      "ds_read_b64_tr_b16 a[68:69], v214 offset:12424\n\t"
      "ds_read_b128 v[148:151], v198 offset:0\n\t"
      "s_waitcnt lgkmcnt(8)\n\t"
      "v_mfma_f32_16x16x32_bf16 v[80:83], a[24:27], v[144:147], v[80:83]\n\t"
      "s_add_u32 m0, s41, 0x12000\n\t"
      "s_mov_b32 s51, 0x1e000\n\t"
      "buffer_load_dwordx4 v211, s[16:19], s51 offen lds\n\t"
      "v_mfma_f32_16x16x32_bf16 v[84:87], a[28:31], v[144:147], v[84:87]\n\t"
      "buffer_load_dwordx4 v212, s[16:19], s51 offen offset:1024 lds\n\t"
      "v_mfma_f32_16x16x32_bf16 v[88:91], a[32:35], v[144:147], v[88:91]\n\t"
      "buffer_load_dwordx4 v213, s[16:19], s51 offen offset:2048 lds\n\t"
      "s_waitcnt vmcnt(15)\n\t"
      "ds_read_b64_tr_b16 a[50:51], v214 offset:24576\n\t"
      "v_mfma_f32_16x16x32_bf16 v[92:95], a[36:39], v[144:147], v[92:95]\n\t"
      "ds_read_b64_tr_b16 a[54:55], v214 offset:24584\n\t"
      "ds_read_b64_tr_b16 a[58:59], v214 offset:24640\n\t"
      "v_mfma_f32_16x16x32_bf16 v[96:99], a[40:43], v[144:147], v[96:99]\n\t"
      "ds_read_b64_tr_b16 a[62:63], v214 offset:24648\n\t"
      "ds_read_b64_tr_b16 a[66:67], v214 offset:24704\n\t"
      "v_mfma_f32_16x16x32_bf16 v[100:103], a[44:47], v[144:147], v[100:103]\n\t"
      "ds_read_b64_tr_b16 a[70:71], v214 offset:24712\n\t"
      "ds_read_b128 v[160:163], v198 offset:12288\n\t"
      "s_waitcnt lgkmcnt(8)\n\t"
      "v_mfma_f32_16x16x32_bf16 v[104:107], a[24:27], v[156:159], v[104:107]\n\t"
      "s_add_u32 m0, s41, 0x15000\n\t"
      "s_mov_b32 s51, 0x1ec00\n\t"
      "buffer_load_dwordx4 v211, s[16:19], s51 offen lds\n\t"
      "v_mfma_f32_16x16x32_bf16 v[108:111], a[28:31], v[156:159], v[108:111]\n\t"
      "buffer_load_dwordx4 v212, s[16:19], s51 offen offset:1024 lds\n\t"
      "v_mfma_f32_16x16x32_bf16 v[112:115], a[32:35], v[156:159], v[112:115]\n\t"
      "buffer_load_dwordx4 v213, s[16:19], s51 offen offset:2048 lds\n\t"
      "s_waitcnt vmcnt(15)\n\t"
      "ds_read_b64_tr_b16 a[72:73], v214 offset:36864\n\t"
      "v_mfma_f32_16x16x32_bf16 v[116:119], a[36:39], v[156:159], v[116:119]\n\t"
      "ds_read_b64_tr_b16 a[76:77], v214 offset:36872\n\t"
      "ds_read_b64_tr_b16 a[80:81], v214 offset:36928\n\t"
      "v_mfma_f32_16x16x32_bf16 v[120:123], a[40:43], v[156:159], v[120:123]\n\t"
      "ds_read_b64_tr_b16 a[84:85], v214 offset:36936\n\t"
      "ds_read_b64_tr_b16 a[88:89], v214 offset:36992\n\t"
      "v_mfma_f32_16x16x32_bf16 v[132:135], a[44:47], v[156:159], v[132:135]\n\t"
      "ds_read_b64_tr_b16 a[92:93], v214 offset:37000\n\t"
      "ds_read_b128 v[140:143], v199 offset:0\n\t"
      "s_waitcnt lgkmcnt(8)\n\t"
      "v_mfma_f32_16x16x32_bf16 v[80:83], a[48:51], v[148:151], v[80:83]\n\t"
      "s_add_u32 m0, s41, 0x18000\n\t"
      "s_mov_b32 s51, 0x24000\n\t"
      "buffer_load_dwordx4 v211, s[16:19], s51 offen lds\n\t"
      "v_mfma_f32_16x16x32_bf16 v[84:87], a[52:55], v[148:151], v[84:87]\n\t"
      "buffer_load_dwordx4 v212, s[16:19], s51 offen offset:1024 lds\n\t"
      "v_mfma_f32_16x16x32_bf16 v[88:91], a[56:59], v[148:151], v[88:91]\n\t"
      "buffer_load_dwordx4 v213, s[16:19], s51 offen offset:2048 lds\n\t"
      "s_waitcnt vmcnt(15)\n\t"
      "ds_read_b64_tr_b16 a[74:75], v214 offset:49152\n\t"
      "v_mfma_f32_16x16x32_bf16 v[92:95], a[60:63], v[148:151], v[92:95]\n\t"
      "ds_read_b64_tr_b16 a[78:79], v214 offset:49160\n\t"
      "ds_read_b64_tr_b16 a[82:83], v214 offset:49216\n\t"
      "v_mfma_f32_16x16x32_bf16 v[96:99], a[64:67], v[148:151], v[96:99]\n\t"
      "ds_read_b64_tr_b16 a[86:87], v214 offset:49224\n\t"
      "ds_read_b64_tr_b16 a[90:91], v214 offset:49280\n\t"
      "v_mfma_f32_16x16x32_bf16 v[100:103], a[68:71], v[148:151], v[100:103]\n\t"
      "ds_read_b64_tr_b16 a[94:95], v214 offset:49288\n\t"
      "ds_read_b128 v[152:155], v199 offset:12288\n\t"
      "s_waitcnt lgkmcnt(8)\n\t"
      "v_mfma_f32_16x16x32_bf16 v[104:107], a[48:51], v[160:163], v[104:107]\n\t"
      "s_add_u32 m0, s41, 0x1b000\n\t"
      "s_mov_b32 s51, 0x24c00\n\t"
      "buffer_load_dwordx4 v211, s[16:19], s51 offen lds\n\t"
      "v_mfma_f32_16x16x32_bf16 v[108:111], a[52:55], v[160:163], v[108:111]\n\t"
      "buffer_load_dwordx4 v212, s[16:19], s51 offen offset:1024 lds\n\t"
      "v_mfma_f32_16x16x32_bf16 v[112:115], a[56:59], v[160:163], v[112:115]\n\t"
      "buffer_load_dwordx4 v213, s[16:19], s51 offen offset:2048 lds\n\t"
      "s_waitcnt vmcnt(15)\n\t"
      "ds_read_b64_tr_b16 a[96:97], v221 offset:0\n\t"
      "v_mfma_f32_16x16x32_bf16 v[116:119], a[60:63], v[160:163], v[116:119]\n\t"
      "ds_read_b64_tr_b16 a[100:101], v221 offset:8\n\t"
      "ds_read_b64_tr_b16 a[104:105], v221 offset:64\n\t"
      "v_mfma_f32_16x16x32_bf16 v[120:123], a[64:67], v[160:163], v[120:123]\n\t"
      "ds_read_b64_tr_b16 a[108:109], v221 offset:72\n\t"
      "ds_read_b64_tr_b16 a[112:113], v221 offset:128\n\t"
      "v_mfma_f32_16x16x32_bf16 v[132:135], a[68:71], v[160:163], v[132:135]\n\t"
      "ds_read_b64_tr_b16 a[116:117], v221 offset:136\n\t"
      "ds_read_b128 v[144:147], v197 offset:256\n\t"
      "s_waitcnt lgkmcnt(8)\n\t"
      "v_mfma_f32_16x16x32_bf16 v[80:83], a[72:75], v[140:143], v[80:83]\n\t"
      "s_add_u32 m0, s41, 0x1e000\n\t"
      "s_mov_b32 s51, 0x2a000\n\t"
      "buffer_load_dwordx4 v211, s[16:19], s51 offen lds\n\t"
      "v_mfma_f32_16x16x32_bf16 v[84:87], a[76:79], v[140:143], v[84:87]\n\t"
      "buffer_load_dwordx4 v212, s[16:19], s51 offen offset:1024 lds\n\t"
      "v_mfma_f32_16x16x32_bf16 v[88:91], a[80:83], v[140:143], v[88:91]\n\t"
      "buffer_load_dwordx4 v213, s[16:19], s51 offen offset:2048 lds\n\t"
      "s_waitcnt vmcnt(15)\n\t"
      "ds_read_b64_tr_b16 a[98:99], v221 offset:12288\n\t"
      "v_mfma_f32_16x16x32_bf16 v[92:95], a[84:87], v[140:143], v[92:95]\n\t"
      "ds_read_b64_tr_b16 a[102:103], v221 offset:12296\n\t"
      "ds_read_b64_tr_b16 a[106:107], v221 offset:12352\n\t"
      "v_mfma_f32_16x16x32_bf16 v[96:99], a[88:91], v[140:143], v[96:99]\n\t"
      "ds_read_b64_tr_b16 a[110:111], v221 offset:12360\n\t"
      "ds_read_b64_tr_b16 a[114:115], v221 offset:12416\n\t"
      "v_mfma_f32_16x16x32_bf16 v[100:103], a[92:95], v[140:143], v[100:103]\n\t"
      "ds_read_b64_tr_b16 a[118:119], v221 offset:12424\n\t"
      "ds_read_b128 v[156:159], v197 offset:12544\n\t"
      "s_waitcnt lgkmcnt(8)\n\t"
      "v_mfma_f32_16x16x32_bf16 v[104:107], a[72:75], v[152:155], v[104:107]\n\t"
      "s_add_u32 m0, s41, 0x21000\n\t"
      "s_mov_b32 s51, 0x2ac00\n\t"
      "buffer_load_dwordx4 v211, s[16:19], s51 offen lds\n\t"
      "v_mfma_f32_16x16x32_bf16 v[108:111], a[76:79], v[152:155], v[108:111]\n\t"
      "buffer_load_dwordx4 v212, s[16:19], s51 offen offset:1024 lds\n\t"
      "v_mfma_f32_16x16x32_bf16 v[112:115], a[80:83], v[152:155], v[112:115]\n\t"
      "buffer_load_dwordx4 v213, s[16:19], s51 offen offset:2048 lds\n\t"
      "s_waitcnt vmcnt(15)\n\t"
      "ds_read_b64_tr_b16 a[120:121], v214 offset:0\n\t"
      "v_mfma_f32_16x16x32_bf16 v[116:119], a[84:87], v[152:155], v[116:119]\n\t"
      "ds_read_b64_tr_b16 a[124:125], v214 offset:8\n\t"
      "ds_read_b64_tr_b16 a[128:129], v214 offset:64\n\t"
      "v_mfma_f32_16x16x32_bf16 v[120:123], a[88:91], v[152:155], v[120:123]\n\t"
      "ds_read_b64_tr_b16 a[132:133], v214 offset:72\n\t"
      "ds_read_b64_tr_b16 a[136:137], v214 offset:128\n\t"
      "v_mfma_f32_16x16x32_bf16 v[132:135], a[92:95], v[152:155], v[132:135]\n\t"
      "ds_read_b64_tr_b16 a[140:141], v214 offset:136\n\t"
      "ds_read_b128 v[148:151], v198 offset:256\n\t"
      "s_waitcnt lgkmcnt(8)\n\t"
      "v_mfma_f32_16x16x32_bf16 v[80:83], a[96:99], v[144:147], v[80:83]\n\t"
      "s_add_u32 m0, s41, 0x24000\n\t"
      "s_mov_b32 s51, 0x30000\n\t"
      "buffer_load_dwordx4 v211, s[16:19], s51 offen lds\n\t"
      "v_mfma_f32_16x16x32_bf16 v[84:87], a[100:103], v[144:147], v[84:87]\n\t"
      "buffer_load_dwordx4 v212, s[16:19], s51 offen offset:1024 lds\n\t"
      "v_mfma_f32_16x16x32_bf16 v[88:91], a[104:107], v[144:147], v[88:91]\n\t"
      "buffer_load_dwordx4 v213, s[16:19], s51 offen offset:2048 lds\n\t"
      "s_waitcnt vmcnt(15)\n\t"
      "ds_read_b64_tr_b16 a[122:123], v214 offset:12288\n\t"
      "v_mfma_f32_16x16x32_bf16 v[92:95], a[108:111], v[144:147], v[92:95]\n\t"
      "ds_read_b64_tr_b16 a[126:127], v214 offset:12296\n\t"
      "ds_read_b64_tr_b16 a[130:131], v214 offset:12352\n\t"
      "v_mfma_f32_16x16x32_bf16 v[96:99], a[112:115], v[144:147], v[96:99]\n\t"
      "ds_read_b64_tr_b16 a[134:135], v214 offset:12360\n\t"
      "ds_read_b64_tr_b16 a[138:139], v214 offset:12416\n\t"
      "v_mfma_f32_16x16x32_bf16 v[100:103], a[116:119], v[144:147], v[100:103]\n\t"
      "ds_read_b64_tr_b16 a[142:143], v214 offset:12424\n\t"
      "ds_read_b128 v[160:163], v198 offset:12544\n\t"
      "s_waitcnt lgkmcnt(8)\n\t"
      "v_mfma_f32_16x16x32_bf16 v[104:107], a[96:99], v[156:159], v[104:107]\n\t"
      "s_add_u32 m0, s41, 0x12000\n\t"
      "s_mov_b32 s51, 0x30c00\n\t"
      "buffer_load_dwordx4 v211, s[16:19], s51 offen lds\n\t"
      "v_mfma_f32_16x16x32_bf16 v[108:111], a[100:103], v[156:159], v[108:111]\n\t"
      "buffer_load_dwordx4 v212, s[16:19], s51 offen offset:1024 lds\n\t"
      "v_mfma_f32_16x16x32_bf16 v[112:115], a[104:107], v[156:159], v[112:115]\n\t"
      "buffer_load_dwordx4 v213, s[16:19], s51 offen offset:2048 lds\n\t"
      "s_waitcnt vmcnt(15)\n\t"
      "ds_read_b64_tr_b16 a[144:145], v214 offset:24576\n\t"
      "v_mfma_f32_16x16x32_bf16 v[116:119], a[108:111], v[156:159], v[116:119]\n\t"
      "ds_read_b64_tr_b16 a[148:149], v214 offset:24584\n\t"
      "ds_read_b64_tr_b16 a[152:153], v214 offset:24640\n\t"
      "v_mfma_f32_16x16x32_bf16 v[120:123], a[112:115], v[156:159], v[120:123]\n\t"
      "ds_read_b64_tr_b16 a[156:157], v214 offset:24648\n\t"
      "ds_read_b64_tr_b16 a[160:161], v214 offset:24704\n\t"
      "v_mfma_f32_16x16x32_bf16 v[132:135], a[116:119], v[156:159], v[132:135]\n\t"
      "ds_read_b64_tr_b16 a[164:165], v214 offset:24712\n\t"
      "ds_read_b128 v[140:143], v199 offset:256\n\t"
      "s_waitcnt lgkmcnt(8)\n\t"
      "v_mfma_f32_16x16x32_bf16 v[80:83], a[120:123], v[148:151], v[80:83]\n\t"
      "s_add_u32 m0, s41, 0x15000\n\t"
      "s_mov_b32 s51, 0x36000\n\t"
      "buffer_load_dwordx4 v211, s[16:19], s51 offen lds\n\t"
      "v_mfma_f32_16x16x32_bf16 v[84:87], a[124:127], v[148:151], v[84:87]\n\t"
      "buffer_load_dwordx4 v212, s[16:19], s51 offen offset:1024 lds\n\t"
      "v_mfma_f32_16x16x32_bf16 v[88:91], a[128:131], v[148:151], v[88:91]\n\t"
      "buffer_load_dwordx4 v213, s[16:19], s51 offen offset:2048 lds\n\t"
      "s_waitcnt vmcnt(15)\n\t"
      "ds_read_b64_tr_b16 a[146:147], v214 offset:36864\n\t"
      "v_mfma_f32_16x16x32_bf16 v[92:95], a[132:135], v[148:151], v[92:95]\n\t"
      "ds_read_b64_tr_b16 a[150:151], v214 offset:36872\n\t"
      "ds_read_b64_tr_b16 a[154:155], v214 offset:36928\n\t"
      "v_mfma_f32_16x16x32_bf16 v[96:99], a[136:139], v[148:151], v[96:99]\n\t"
      "ds_read_b64_tr_b16 a[158:159], v214 offset:36936\n\t"
      "ds_read_b64_tr_b16 a[162:163], v214 offset:36992\n\t"
      "v_mfma_f32_16x16x32_bf16 v[100:103], a[140:143], v[148:151], v[100:103]\n\t"
      "ds_read_b64_tr_b16 a[166:167], v214 offset:37000\n\t"
      "ds_read_b128 v[152:155], v199 offset:12544\n\t"
      "s_waitcnt lgkmcnt(8)\n\t"
      "v_mfma_f32_16x16x32_bf16 v[104:107], a[120:123], v[160:163], v[104:107]\n\t"
      "s_add_u32 m0, s41, 0x18000\n\t"
      "s_mov_b32 s51, 0x36c00\n\t"
      "buffer_load_dwordx4 v211, s[16:19], s51 offen lds\n\t"
      "v_mfma_f32_16x16x32_bf16 v[108:111], a[124:127], v[160:163], v[108:111]\n\t"
      "buffer_load_dwordx4 v212, s[16:19], s51 offen offset:1024 lds\n\t"
      "v_mfma_f32_16x16x32_bf16 v[112:115], a[128:131], v[160:163], v[112:115]\n\t"
      "buffer_load_dwordx4 v213, s[16:19], s51 offen offset:2048 lds\n\t"
      "s_waitcnt vmcnt(15)\n\t"
      "ds_read_b64_tr_b16 a[168:169], v214 offset:49152\n\t"
      "v_mfma_f32_16x16x32_bf16 v[116:119], a[132:135], v[160:163], v[116:119]\n\t"
      "ds_read_b64_tr_b16 a[172:173], v214 offset:49160\n\t"
      "ds_read_b64_tr_b16 a[176:177], v214 offset:49216\n\t"
      "v_mfma_f32_16x16x32_bf16 v[120:123], a[136:139], v[160:163], v[120:123]\n\t"
      "ds_read_b64_tr_b16 a[180:181], v214 offset:49224\n\t"
      "ds_read_b64_tr_b16 a[184:185], v214 offset:49280\n\t"
      "v_mfma_f32_16x16x32_bf16 v[132:135], a[140:143], v[160:163], v[132:135]\n\t"
      "ds_read_b64_tr_b16 a[188:189], v214 offset:49288\n\t"
      "ds_read_b128 v[144:147], v196 offset:256\n\t"
      "s_waitcnt lgkmcnt(8)\n\t"
      "v_mfma_f32_16x16x32_bf16 v[80:83], a[144:147], v[140:143], v[80:83]\n\t"
      "s_add_u32 m0, s41, 0x1b000\n\t"
      "s_mov_b32 s51, 0x3c000\n\t"
      "buffer_load_dwordx4 v211, s[16:19], s51 offen lds\n\t"
      "v_mfma_f32_16x16x32_bf16 v[84:87], a[148:151], v[140:143], v[84:87]\n\t"
      "buffer_load_dwordx4 v212, s[16:19], s51 offen offset:1024 lds\n\t"
      "v_mfma_f32_16x16x32_bf16 v[88:91], a[152:155], v[140:143], v[88:91]\n\t"
      "buffer_load_dwordx4 v213, s[16:19], s51 offen offset:2048 lds\n\t"
      "s_waitcnt vmcnt(15)\n\t"
      "ds_read_b64_tr_b16 a[170:171], v221 offset:0\n\t"
      "v_mfma_f32_16x16x32_bf16 v[92:95], a[156:159], v[140:143], v[92:95]\n\t"
      "ds_read_b64_tr_b16 a[174:175], v221 offset:8\n\t"
      "ds_read_b64_tr_b16 a[178:179], v221 offset:64\n\t"
      "v_mfma_f32_16x16x32_bf16 v[96:99], a[160:163], v[140:143], v[96:99]\n\t"
      "ds_read_b64_tr_b16 a[182:183], v221 offset:72\n\t"
      "ds_read_b64_tr_b16 a[186:187], v221 offset:128\n\t"
      "v_mfma_f32_16x16x32_bf16 v[100:103], a[164:167], v[140:143], v[100:103]\n\t"
      "ds_read_b64_tr_b16 a[190:191], v221 offset:136\n\t"
      "ds_read_b128 v[156:159], v196 offset:12544\n\t"
      "s_waitcnt lgkmcnt(8)\n\t"
      "v_mfma_f32_16x16x32_bf16 v[104:107], a[144:147], v[152:155], v[104:107]\n\t"
      "s_add_u32 m0, s41, 0x1e000\n\t"
      "s_mov_b32 s51, 0x3cc00\n\t"
      "buffer_load_dwordx4 v211, s[16:19], s51 offen lds\n\t"
      "v_mfma_f32_16x16x32_bf16 v[108:111], a[148:151], v[152:155], v[108:111]\n\t"
      "buffer_load_dwordx4 v212, s[16:19], s51 offen offset:1024 lds\n\t"
      "v_mfma_f32_16x16x32_bf16 v[112:115], a[152:155], v[152:155], v[112:115]\n\t"
      "buffer_load_dwordx4 v213, s[16:19], s51 offen offset:2048 lds\n\t"
      "s_waitcnt vmcnt(15)\n\t"
      "ds_read_b64_tr_b16 a[192:193], v221 offset:12288\n\t"
      "v_mfma_f32_16x16x32_bf16 v[116:119], a[156:159], v[152:155], v[116:119]\n\t"
      "ds_read_b64_tr_b16 a[196:197], v221 offset:12296\n\t"
      "ds_read_b64_tr_b16 a[200:201], v221 offset:12352\n\t"
      "v_mfma_f32_16x16x32_bf16 v[120:123], a[160:163], v[152:155], v[120:123]\n\t"
      "ds_read_b64_tr_b16 a[204:205], v221 offset:12360\n\t"
      "ds_read_b64_tr_b16 a[208:209], v221 offset:12416\n\t"
      "v_mfma_f32_16x16x32_bf16 v[132:135], a[164:167], v[152:155], v[132:135]\n\t"
      "ds_read_b64_tr_b16 a[212:213], v221 offset:12424\n\t"
      "ds_read_b128 v[148:151], v198 offset:512\n\t"
      "s_waitcnt lgkmcnt(8)\n\t"
      "v_mfma_f32_16x16x32_bf16 v[80:83], a[168:171], v[144:147], v[80:83]\n\t"
      "s_add_u32 m0, s41, 0x21000\n\t"
      "s_mov_b32 s51, 0x42000\n\t"
      "buffer_load_dwordx4 v211, s[16:19], s51 offen lds\n\t"
      "v_mfma_f32_16x16x32_bf16 v[84:87], a[172:175], v[144:147], v[84:87]\n\t"
      "buffer_load_dwordx4 v212, s[16:19], s51 offen offset:1024 lds\n\t"
      "v_mfma_f32_16x16x32_bf16 v[88:91], a[176:179], v[144:147], v[88:91]\n\t"
      "buffer_load_dwordx4 v213, s[16:19], s51 offen offset:2048 lds\n\t"
      "s_waitcnt vmcnt(15)\n\t"
      "ds_read_b64_tr_b16 a[194:195], v214 offset:0\n\t"
      "v_mfma_f32_16x16x32_bf16 v[92:95], a[180:183], v[144:147], v[92:95]\n\t"
      "ds_read_b64_tr_b16 a[198:199], v214 offset:8\n\t"
      "ds_read_b64_tr_b16 a[202:203], v214 offset:64\n\t"
      "v_mfma_f32_16x16x32_bf16 v[96:99], a[184:187], v[144:147], v[96:99]\n\t"
      "ds_read_b64_tr_b16 a[206:207], v214 offset:72\n\t"
      "ds_read_b64_tr_b16 a[210:211], v214 offset:128\n\t"
      "v_mfma_f32_16x16x32_bf16 v[100:103], a[188:191], v[144:147], v[100:103]\n\t"
      "ds_read_b64_tr_b16 a[214:215], v214 offset:136\n\t"
      "ds_read_b128 v[160:163], v198 offset:12800\n\t"
      "s_waitcnt lgkmcnt(8)\n\t"
      "v_mfma_f32_16x16x32_bf16 v[104:107], a[168:171], v[156:159], v[104:107]\n\t"
      "s_add_u32 m0, s41, 0x24000\n\t"
      "s_mov_b32 s51, 0x42c00\n\t"
      "buffer_load_dwordx4 v211, s[16:19], s51 offen lds\n\t"
      "v_mfma_f32_16x16x32_bf16 v[108:111], a[172:175], v[156:159], v[108:111]\n\t"
      "buffer_load_dwordx4 v212, s[16:19], s51 offen offset:1024 lds\n\t"
      "v_mfma_f32_16x16x32_bf16 v[112:115], a[176:179], v[156:159], v[112:115]\n\t"
      "buffer_load_dwordx4 v213, s[16:19], s51 offen offset:2048 lds\n\t"
      "s_waitcnt vmcnt(15)\n\t"
      "ds_read_b64_tr_b16 a[216:217], v214 offset:12288\n\t"
      "v_mfma_f32_16x16x32_bf16 v[116:119], a[180:183], v[156:159], v[116:119]\n\t"
      "ds_read_b64_tr_b16 a[220:221], v214 offset:12296\n\t"
      "ds_read_b64_tr_b16 a[224:225], v214 offset:12352\n\t"
      "v_mfma_f32_16x16x32_bf16 v[120:123], a[184:187], v[156:159], v[120:123]\n\t"
      "ds_read_b64_tr_b16 a[228:229], v214 offset:12360\n\t"
      "ds_read_b64_tr_b16 a[232:233], v214 offset:12416\n\t"
      "v_mfma_f32_16x16x32_bf16 v[132:135], a[188:191], v[156:159], v[132:135]\n\t"
      "ds_read_b64_tr_b16 a[236:237], v214 offset:12424\n\t"
      "ds_read_b128 v[140:143], v199 offset:512\n\t"
      "s_waitcnt lgkmcnt(8)\n\t"
      "v_mfma_f32_16x16x32_bf16 v[80:83], a[192:195], v[148:151], v[80:83]\n\t"
      "s_add_u32 m0, s41, 0x6000\n\t"
      "s_add_u32 s51, s40, 0x600000\n\t"
      "buffer_load_dwordx4 v208, s[20:23], s51 offen lds\n\t"
      "v_mfma_f32_16x16x32_bf16 v[84:87], a[196:199], v[148:151], v[84:87]\n\t"
      "buffer_load_dwordx4 v208, s[20:23], s51 offen offset:1024 lds\n\t"
      "v_mfma_f32_16x16x32_bf16 v[88:91], a[200:203], v[148:151], v[88:91]\n\t"
      "buffer_load_dwordx4 v208, s[20:23], s51 offen offset:2048 lds\n\t"
      "s_waitcnt vmcnt(15)\n\t"
      "ds_read_b64_tr_b16 a[218:219], v214 offset:24576\n\t"
      "v_mfma_f32_16x16x32_bf16 v[92:95], a[204:207], v[148:151], v[92:95]\n\t"
      "ds_read_b64_tr_b16 a[222:223], v214 offset:24584\n\t"
      "ds_read_b64_tr_b16 a[226:227], v214 offset:24640\n\t"
      "v_mfma_f32_16x16x32_bf16 v[96:99], a[208:211], v[148:151], v[96:99]\n\t"
      "ds_read_b64_tr_b16 a[230:231], v214 offset:24648\n\t"
      "ds_read_b64_tr_b16 a[234:235], v214 offset:24704\n\t"
      "v_mfma_f32_16x16x32_bf16 v[100:103], a[212:215], v[148:151], v[100:103]\n\t"
      "ds_read_b64_tr_b16 a[238:239], v214 offset:24712\n\t"
      "ds_read_b128 v[152:155], v199 offset:12800\n\t"
      "s_waitcnt lgkmcnt(14)\n\t"
      "v_mfma_f32_16x16x32_bf16 v[104:107], a[192:195], v[160:163], v[104:107]\n\t"
      "s_add_u32 m0, s41, 0x9000\n\t"
      "s_add_u32 s51, s40, 0x600000\n\t"
      "buffer_load_dwordx4 v209, s[20:23], s51 offen lds\n\t"
      "v_mfma_f32_16x16x32_bf16 v[108:111], a[196:199], v[160:163], v[108:111]\n\t"
      "buffer_load_dwordx4 v209, s[20:23], s51 offen offset:1024 lds\n\t"
      "v_mfma_f32_16x16x32_bf16 v[112:115], a[200:203], v[160:163], v[112:115]\n\t"
      "buffer_load_dwordx4 v209, s[20:23], s51 offen offset:2048 lds\n\t"
      "s_waitcnt vmcnt(15)\n\t"
      "ds_read_b64_tr_b16 a[240:241], v214 offset:36864\n\t"
      "v_mfma_f32_16x16x32_bf16 v[116:119], a[204:207], v[160:163], v[116:119]\n\t"
      "ds_read_b64_tr_b16 a[244:245], v214 offset:36872\n\t"
      "ds_read_b64_tr_b16 a[248:249], v214 offset:36928\n\t"
      "v_mfma_f32_16x16x32_bf16 v[120:123], a[208:211], v[160:163], v[120:123]\n\t"
      "ds_read_b64_tr_b16 a[252:253], v214 offset:36936\n\t"
      "ds_read_b64_tr_b16 v[164:165], v214 offset:36992\n\t"
      "v_mfma_f32_16x16x32_bf16 v[132:135], a[212:215], v[160:163], v[132:135]\n\t"
      "ds_read_b64_tr_b16 v[168:169], v214 offset:37000\n\t"
      "ds_read_b128 v[144:147], v196 offset:512\n\t"
      "s_waitcnt lgkmcnt(8)\n\t"
      "v_mfma_f32_16x16x32_bf16 v[80:83], a[216:219], v[140:143], v[80:83]\n\t"
      "s_add_u32 m0, s41, 0xc000\n\t"
      "s_add_u32 s51, s40, 0xc00000\n\t"
      "buffer_load_dwordx4 v208, s[20:23], s51 offen lds\n\t"
      "v_mfma_f32_16x16x32_bf16 v[84:87], a[220:223], v[140:143], v[84:87]\n\t"
      "buffer_load_dwordx4 v208, s[20:23], s51 offen offset:1024 lds\n\t"
      "v_mfma_f32_16x16x32_bf16 v[88:91], a[224:227], v[140:143], v[88:91]\n\t"
      "buffer_load_dwordx4 v208, s[20:23], s51 offen offset:2048 lds\n\t"
      "s_waitcnt vmcnt(15)\n\t"
      "ds_read_b64_tr_b16 a[242:243], v214 offset:49152\n\t"
      "v_mfma_f32_16x16x32_bf16 v[92:95], a[228:231], v[140:143], v[92:95]\n\t"
      "ds_read_b64_tr_b16 a[246:247], v214 offset:49160\n\t"
      "ds_read_b64_tr_b16 a[250:251], v214 offset:49216\n\t"
      "v_mfma_f32_16x16x32_bf16 v[96:99], a[232:235], v[140:143], v[96:99]\n\t"
      "ds_read_b64_tr_b16 a[254:255], v214 offset:49224\n\t"
      "ds_read_b64_tr_b16 v[166:167], v214 offset:49280\n\t"
      "v_mfma_f32_16x16x32_bf16 v[100:103], a[236:239], v[140:143], v[100:103]\n\t"
      "ds_read_b64_tr_b16 v[170:171], v214 offset:49288\n\t"
      "ds_read_b128 v[156:159], v196 offset:12800\n\t"
      "s_waitcnt lgkmcnt(14)\n\t"
      "v_mfma_f32_16x16x32_bf16 v[104:107], a[216:219], v[152:155], v[104:107]\n\t"
      "s_add_u32 m0, s41, 0xf000\n\t"
      "s_add_u32 s51, s40, 0xc00000\n\t"
      "buffer_load_dwordx4 v209, s[20:23], s51 offen lds\n\t"
      "v_mfma_f32_16x16x32_bf16 v[108:111], a[220:223], v[152:155], v[108:111]\n\t"
      "buffer_load_dwordx4 v209, s[20:23], s51 offen offset:1024 lds\n\t"
      "v_mfma_f32_16x16x32_bf16 v[112:115], a[224:227], v[152:155], v[112:115]\n\t"
      "buffer_load_dwordx4 v209, s[20:23], s51 offen offset:2048 lds\n\t"
      "s_waitcnt vmcnt(15)\n\t"
      "ds_read_b64_tr_b16 v[172:173], v221 offset:0\n\t"
      "v_mfma_f32_16x16x32_bf16 v[116:119], a[228:231], v[152:155], v[116:119]\n\t"
      "ds_read_b64_tr_b16 v[176:177], v221 offset:8\n\t"
      "ds_read_b64_tr_b16 v[180:181], v221 offset:64\n\t"
      "ds_read_b64_tr_b16 v[184:185], v221 offset:72\n\t"
      "v_mfma_f32_16x16x32_bf16 v[120:123], a[232:235], v[152:155], v[120:123]\n\t"
      "ds_read_b64_tr_b16 v[188:189], v221 offset:128\n\t"
      "ds_read_b64_tr_b16 v[192:193], v221 offset:136\n\t"
      "v_mfma_f32_16x16x32_bf16 v[132:135], a[236:239], v[152:155], v[132:135]\n\t"
      "ds_read_b128 v[148:151], v197 offset:512\n\t"
      "ds_read_b128 v[160:163], v197 offset:12800\n\t"
      "s_waitcnt lgkmcnt(9)\n\t"
      "v_mfma_f32_16x16x32_bf16 v[80:83], a[240:243], v[144:147], v[80:83]\n\t"
      "buffer_load_dwordx2 v[240:241], v245, s[24:27], 0 offen sc1\n\t"
      "v_mfma_f32_16x16x32_bf16 v[84:87], a[244:247], v[144:147], v[84:87]\n\t"
      "s_cmp_eq_u32 s41, s43\n\t"
      "s_cbranch_scc0 .Lc3_%=\n\t"
      "buffer_load_dword v254, v253, s[24:27], 0 offen sc1\n\t"
      ".Lc3_%=:\n\t"
      "v_mfma_f32_16x16x32_bf16 v[88:91], a[248:251], v[144:147], v[88:91]\n\t"
      "s_waitcnt vmcnt(13)\n\t"
      "ds_read_b64_tr_b16 v[174:175], v221 offset:12288\n\t"
      "v_mfma_f32_16x16x32_bf16 v[92:95], a[252:255], v[144:147], v[92:95]\n\t"
      "ds_read_b64_tr_b16 v[178:179], v221 offset:12296\n\t"
      "ds_read_b64_tr_b16 v[182:183], v221 offset:12352\n\t"
      "v_mfma_f32_16x16x32_bf16 v[96:99], v[164:167], v[144:147], v[96:99]\n\t"
      "ds_read_b64_tr_b16 v[186:187], v221 offset:12360\n\t"
      "ds_read_b64_tr_b16 v[190:191], v221 offset:12416\n\t"
      "v_mfma_f32_16x16x32_bf16 v[100:103], v[168:171], v[144:147], v[100:103]\n\t"
      "ds_read_b64_tr_b16 v[194:195], v221 offset:12424\n\t"
      "s_memrealtime s[80:81]\n\t"
      "v_add_u32_e32 v196, s46, v200\n\t"
      "v_add_u32_e32 v197, s46, v201\n\t"
      "v_add_u32_e32 v198, s46, v202\n\t"
      "v_add_u32_e32 v199, s46, v203\n\t"
      "s_waitcnt vmcnt(10)\n\t"
      "s_waitcnt lgkmcnt(6)\n\t"
      "s_barrier\n\t"
      "v_mfma_f32_16x16x32_bf16 v[104:107], a[240:243], v[156:159], v[104:107]\n\t"
      "ds_read_b128 v[32:35], v196 offset:0\n\t"
      "v_mfma_f32_16x16x32_bf16 v[108:111], a[244:247], v[156:159], v[108:111]\n\t"
      "ds_read_b128 v[36:39], v197 offset:0\n\t"
      "v_mfma_f32_16x16x32_bf16 v[112:115], a[248:251], v[156:159], v[112:115]\n\t"
      "ds_read_b128 v[40:43], v198 offset:0\n\t"
      "v_mfma_f32_16x16x32_bf16 v[116:119], a[252:255], v[156:159], v[116:119]\n\t"
      "ds_read_b128 v[44:47], v199 offset:0\n\t"
      "v_mfma_f32_16x16x32_bf16 v[120:123], v[164:167], v[156:159], v[120:123]\n\t"
      "ds_read_b128 v[48:51], v197 offset:256\n\t"
      "v_mfma_f32_16x16x32_bf16 v[132:135], v[168:171], v[156:159], v[132:135]\n\t"
      "s_waitcnt lgkmcnt(5)\n\t"
      "s_mov_b32 s87, 76\n\t"
      "s_mov_b32 s86, 80\n\t"
      "s_mov_b32 s84, 100\n\t"
      "v_mfma_f32_16x16x32_bf16 v[80:83], v[172:175], v[148:151], v[80:83]\n\t"
      "ds_read_b128 v[52:55], v198 offset:256\n\t"
      "v_mfma_f32_16x16x32_bf16 v[84:87], v[176:179], v[148:151], v[84:87]\n\t"
      "ds_read_b128 v[56:59], v199 offset:256\n\t"
      "v_mfma_f32_16x16x32_bf16 v[88:91], v[180:183], v[148:151], v[88:91]\n\t"
      "ds_read_b128 v[60:63], v196 offset:256\n\t"
      "v_mfma_f32_16x16x32_bf16 v[92:95], v[184:187], v[148:151], v[92:95]\n\t"
      "ds_read_b128 v[64:67], v198 offset:512\n\t"
      "v_mfma_f32_16x16x32_bf16 v[96:99], v[188:191], v[148:151], v[96:99]\n\t"
      "ds_read_b128 v[68:71], v199 offset:512\n\t"
      "v_mfma_f32_16x16x32_bf16 v[100:103], v[192:195], v[148:151], v[100:103]\n\t"
      "ds_read_b128 v[72:75], v196 offset:512\n\t"
      "v_mfma_f32_16x16x32_bf16 v[104:107], v[172:175], v[160:163], v[104:107]\n\t"
      "ds_read_b128 v[76:79], v197 offset:512\n\t"
      "v_mfma_f32_16x16x32_bf16 v[108:111], v[176:179], v[160:163], v[108:111]\n\t"
      "v_mfma_f32_16x16x32_bf16 v[112:115], v[180:183], v[160:163], v[112:115]\n\t"
      "v_mfma_f32_16x16x32_bf16 v[116:119], v[184:187], v[160:163], v[116:119]\n\t"
      "v_mfma_f32_16x16x32_bf16 v[120:123], v[188:191], v[160:163], v[120:123]\n\t"
      "v_mfma_f32_16x16x32_bf16 v[132:135], v[192:195], v[160:163], v[132:135]\n\t"
      ".Lp2_%=:\n\t"
      "s_cbranch_vccnz .Lwsb_%=\n\t"
      "s_waitcnt lgkmcnt(8)\n\t"
      "v_mfma_f32_16x16x32_bf16 v[128:131], a[0:3], v[32:35], v[8:11]\n\t"
      "v_cvt_pk_bf16_f32 v80, v80, v81\n\t"
      "v_cvt_pk_bf16_f32 v81, v82, v83\n\t"
      "v_mfma_f32_16x16x32_bf16 v[128:131], a[24:27], v[36:39], v[128:131]\n\t"
      "v_cvt_pk_bf16_f32 v136, v120, v121\n\t"
      "v_cvt_pk_bf16_f32 v137, v122, v123\n\t"
      "v_mfma_f32_16x16x32_bf16 v[128:131], a[48:51], v[40:43], v[128:131]\n\t"
      "v_cvt_pk_bf16_f32 v138, v132, v133\n\t"
      "v_mfma_f32_16x16x32_bf16 v[128:131], a[72:75], v[44:47], v[128:131]\n\t"
      "v_cvt_pk_bf16_f32 v139, v134, v135\n\t"
      "s_waitcnt lgkmcnt(4)\n\t"
      "v_mfma_f32_16x16x32_bf16 v[128:131], a[96:99], v[48:51], v[128:131]\n\t"
      "ds_write_b128 v219, v[136:139] offset:12288\n\t"
      "v_mfma_f32_16x16x32_bf16 v[128:131], a[120:123], v[52:55], v[128:131]\n\t"
      "v_cvt_pk_bf16_f32 v82, v84, v85\n\t"
      "v_cvt_pk_bf16_f32 v83, v86, v87\n\t"
      "v_mfma_f32_16x16x32_bf16 v[128:131], a[144:147], v[56:59], v[128:131]\n\t"
      "v_cvt_pk_bf16_f32 v88, v88, v89\n\t"
      "v_cvt_pk_bf16_f32 v89, v90, v91\n\t"
      "v_mfma_f32_16x16x32_bf16 v[128:131], a[168:171], v[60:63], v[128:131]\n\t"
      "ds_write_b128 v217, v[80:83] offset:0\n\t"
      "v_cvt_pk_bf16_f32 v90, v92, v93\n\t"
      "s_waitcnt lgkmcnt(2)\n\t"
      "v_mfma_f32_16x16x32_bf16 v[128:131], a[192:195], v[64:67], v[128:131]\n\t"
      "v_cvt_pk_bf16_f32 v91, v94, v95\n\t"
      "v_cvt_pk_bf16_f32 v96, v96, v97\n\t"
      "v_mfma_f32_16x16x32_bf16 v[128:131], a[216:219], v[68:71], v[128:131]\n\t"
      "ds_write_b128 v218, v[88:91] offset:0\n\t"
      "v_cvt_pk_bf16_f32 v97, v98, v99\n\t"
      "v_mfma_f32_16x16x32_bf16 v[128:131], a[240:243], v[72:75], v[128:131]\n\t"
      "v_cvt_pk_bf16_f32 v98, v100, v101\n\t"
      "v_cvt_pk_bf16_f32 v99, v102, v103\n\t"
      "v_mfma_f32_16x16x32_bf16 v[128:131], v[172:175], v[76:79], v[128:131]\n\t"
      "v_cvt_pk_bf16_f32 v104, v104, v105\n\t"
      "v_cvt_pk_bf16_f32 v105, v106, v107\n\t"
      "v_mfma_f32_16x16x32_bf16 v[132:135], a[4:7], v[32:35], v[12:15]\n\t"
      "ds_write_b128 v219, v[96:99] offset:0\n\t"
      "v_cvt_pk_bf16_f32 v106, v108, v109\n\t"
      "v_mfma_f32_16x16x32_bf16 v[132:135], a[28:31], v[36:39], v[132:135]\n\t"
      "v_cvt_pk_bf16_f32 v107, v110, v111\n\t"
      "v_cvt_pk_bf16_f32 v112, v112, v113\n\t"
      "v_mfma_f32_16x16x32_bf16 v[132:135], a[52:55], v[40:43], v[132:135]\n\t"
      "v_cvt_pk_bf16_f32 v136, v128, v129\n\t"
      "v_mfma_f32_16x16x32_bf16 v[132:135], a[76:79], v[44:47], v[132:135]\n\t"
      "v_cvt_pk_bf16_f32 v137, v130, v131\n\t"
      "v_mfma_f32_16x16x32_bf16 v[132:135], a[100:103], v[48:51], v[132:135]\n\t"
      "ds_write_b128 v217, v[104:107] offset:12288\n\t"
      "v_cvt_pk_bf16_f32 v113, v114, v115\n\t"
      "s_add_u32 s48, s47, 0x6000\n\t"
      "v_mfma_f32_16x16x32_bf16 v[132:135], a[124:127], v[52:55], v[132:135]\n\t"
      "v_cvt_pk_bf16_f32 v114, v116, v117\n\t"
      "v_cvt_pk_bf16_f32 v115, v118, v119\n\t"
      "s_cmp_eq_u32 s48, s50\n\t"
      "s_cselect_b32 s48, s41, s48\n\t"
      "v_mfma_f32_16x16x32_bf16 v[132:135], a[148:151], v[56:59], v[132:135]\n\t"
      "ds_write_b128 v218, v[112:115] offset:12288\n\t"
      "v_mfma_f32_16x16x32_bf16 v[132:135], a[172:175], v[60:63], v[132:135]\n\t"
      "v_add_u32_e32 v217, s46, v204\n\t"
      "v_mfma_f32_16x16x32_bf16 v[132:135], a[196:199], v[64:67], v[132:135]\n\t"
      "v_add_u32_e32 v218, s46, v205\n\t"
      "v_mfma_f32_16x16x32_bf16 v[132:135], a[220:223], v[68:71], v[132:135]\n\t"
      "v_readlane_b32 s73, v248, s85\n\t"
      "v_add_u32_e32 v219, s46, v206\n\t"
      "v_mfma_f32_16x16x32_bf16 v[132:135], a[244:247], v[72:75], v[132:135]\n\t"
      "s_mov_b32 m0, s45\n\t"
      "v_mfma_f32_16x16x32_bf16 v[132:135], v[176:179], v[76:79], v[132:135]\n\t"
      "s_waitcnt lgkmcnt(0)\n\t"
      "s_barrier\n\t"
      "v_mfma_f32_16x16x32_bf16 v[128:131], a[8:11], v[32:35], v[16:19]\n\t"
      "s_mov_b32 s82, s80\n\t"
      "v_mfma_f32_16x16x32_bf16 v[128:131], a[32:35], v[36:39], v[128:131]\n\t"
      "s_memrealtime s[80:81]\n\t"
      "v_mfma_f32_16x16x32_bf16 v[128:131], a[56:59], v[40:43], v[128:131]\n\t"
      "v_cvt_pk_bf16_f32 v138, v132, v133\n\t"
      "v_mfma_f32_16x16x32_bf16 v[128:131], a[80:83], v[44:47], v[128:131]\n\t"
      "v_cvt_pk_bf16_f32 v139, v134, v135\n\t"
      "v_mfma_f32_16x16x32_bf16 v[128:131], a[104:107], v[48:51], v[128:131]\n\t"
      "ds_write_b128 v217, v[136:139] offset:0\n\t"
      "v_mfma_f32_16x16x32_bf16 v[128:131], a[128:131], v[52:55], v[128:131]\n\t"
      "buffer_load_dwordx4 v208, s[20:23], s73 offen lds\n\t"
      "v_mfma_f32_16x16x32_bf16 v[128:131], a[152:155], v[56:59], v[128:131]\n\t"
      "v_mfma_f32_16x16x32_bf16 v[128:131], a[176:179], v[60:63], v[128:131]\n\t"
      "v_mfma_f32_16x16x32_bf16 v[128:131], a[200:203], v[64:67], v[128:131]\n\t"
      "v_mfma_f32_16x16x32_bf16 v[128:131], a[224:227], v[68:71], v[128:131]\n\t"
      "v_mfma_f32_16x16x32_bf16 v[128:131], a[248:251], v[72:75], v[128:131]\n\t"
      "v_mfma_f32_16x16x32_bf16 v[128:131], v[180:183], v[76:79], v[128:131]\n\t"
      "v_mfma_f32_16x16x32_bf16 v[132:135], a[12:15], v[32:35], v[20:23]\n\t"
      "v_mfma_f32_16x16x32_bf16 v[132:135], a[36:39], v[36:39], v[132:135]\n\t"
      "buffer_load_dwordx4 v208, s[20:23], s73 offen offset:1024 lds\n\t"
      "v_mfma_f32_16x16x32_bf16 v[132:135], a[60:63], v[40:43], v[132:135]\n\t"
      "v_cvt_pk_bf16_f32 v136, v128, v129\n\t"
      "v_mfma_f32_16x16x32_bf16 v[132:135], a[84:87], v[44:47], v[132:135]\n\t"
      "v_cvt_pk_bf16_f32 v137, v130, v131\n\t"
      "v_mfma_f32_16x16x32_bf16 v[132:135], a[108:111], v[48:51], v[132:135]\n\t"
      "v_mfma_f32_16x16x32_bf16 v[132:135], a[132:135], v[52:55], v[132:135]\n\t"
      "v_mfma_f32_16x16x32_bf16 v[132:135], a[156:159], v[56:59], v[132:135]\n\t"
      "v_mfma_f32_16x16x32_bf16 v[132:135], a[180:183], v[60:63], v[132:135]\n\t"
      "v_mfma_f32_16x16x32_bf16 v[132:135], a[204:207], v[64:67], v[132:135]\n\t"
      "v_mfma_f32_16x16x32_bf16 v[132:135], a[228:231], v[68:71], v[132:135]\n\t"
      "buffer_load_dwordx4 v208, s[20:23], s73 offen offset:2048 lds\n\t"
      "v_mfma_f32_16x16x32_bf16 v[132:135], a[252:255], v[72:75], v[132:135]\n\t"
      "v_readlane_b32 s73, v249, s85\n\t"
      "s_add_u32 m0, s45, 0x3000\n\t"
      "v_mfma_f32_16x16x32_bf16 v[132:135], v[184:187], v[76:79], v[132:135]\n\t"
      "s_waitcnt vmcnt(10)\n\t"
      "s_barrier\n\t"
      "v_mfma_f32_16x16x32_bf16 v[128:131], a[16:19], v[32:35], v[24:27]\n\t"
      "ds_read_b128 v[80:83], v196 offset:12288\n\t"
      "v_mfma_f32_16x16x32_bf16 v[128:131], a[40:43], v[36:39], v[128:131]\n\t"
      "v_mfma_f32_16x16x32_bf16 v[128:131], a[64:67], v[40:43], v[128:131]\n\t"
      "ds_read_b128 v[84:87], v197 offset:12288\n\t"
      "v_cvt_pk_bf16_f32 v138, v132, v133\n\t"
      "v_mfma_f32_16x16x32_bf16 v[128:131], a[88:91], v[44:47], v[128:131]\n\t"
      "ds_read_b128 v[88:91], v198 offset:12288\n\t"
      "v_cvt_pk_bf16_f32 v139, v134, v135\n\t"
      "v_mfma_f32_16x16x32_bf16 v[128:131], a[112:115], v[48:51], v[128:131]\n\t"
      "ds_write_b128 v218, v[136:139] offset:0\n\t"
      "v_mfma_f32_16x16x32_bf16 v[128:131], a[136:139], v[52:55], v[128:131]\n\t"
      "buffer_load_dwordx4 v209, s[20:23], s73 offen lds\n\t"
      "s_waitcnt vmcnt(10)\n\t"
      "s_cmp_eq_u32 s94, 0\n\t"
      "s_cbranch_scc0 .Lwgvw_0_%=\n\t"
      "s_waitcnt vmcnt(4)\n\t"
      ".Lwgvw_0_%=:\n\t"
      "v_subrev_u32_e32 v242, s63, v240\n\t"
      "v_xor_b32_e32 v241, s53, v241\n\t"
      "v_mfma_f32_16x16x32_bf16 v[128:131], a[160:163], v[56:59], v[128:131]\n\t"
      "ds_read_b128 v[92:95], v199 offset:12288\n\t"
      "v_lshrrev_b32_e32 v240, 13, v242\n\t"
      "v_or_b32_e32 v240, v240, v241\n\t"
      "v_mfma_f32_16x16x32_bf16 v[128:131], a[184:187], v[60:63], v[128:131]\n\t"
      "ds_read_b128 v[96:99], v197 offset:12544\n\t"
      "v_mfma_f32_16x16x32_bf16 v[128:131], a[208:211], v[64:67], v[128:131]\n\t"
      "v_cmp_ne_u32_e32 vcc, 0, v240\n\t"
      "v_and_b32_e32 v242, 15, v242\n\t"
      "v_mfma_f32_16x16x32_bf16 v[128:131], a[232:235], v[68:71], v[128:131]\n\t"
      "v_mfma_f32_16x16x32_bf16 v[128:131], v[164:167], v[72:75], v[128:131]\n\t"
      "ds_read_b128 v[100:103], v198 offset:12544\n\t"
      "s_cbranch_vccnz .Lgs_%=\n\t"
      ".Lgr_%=:\n\t"
      "ds_write_b32 v246, v242\n\t"
      "v_mfma_f32_16x16x32_bf16 v[128:131], v[188:191], v[76:79], v[128:131]\n\t"
      "ds_read_b128 v[104:107], v199 offset:12544\n\t"
      "v_mfma_f32_16x16x32_bf16 v[132:135], a[20:23], v[32:35], v[28:31]\n\t"
      "ds_read_b128 v[108:111], v196 offset:12544\n\t"
      "v_mfma_f32_16x16x32_bf16 v[132:135], a[44:47], v[36:39], v[132:135]\n\t"
      "buffer_load_dwordx4 v209, s[20:23], s73 offen offset:1024 lds\n\t"
      "v_mfma_f32_16x16x32_bf16 v[132:135], a[68:71], v[40:43], v[132:135]\n\t"
      "ds_read_b128 v[112:115], v198 offset:12800\n\t"
      "v_cvt_pk_bf16_f32 v136, v128, v129\n\t"
      "v_add_u32_e32 v220, s46, v207\n\t"
      "v_mfma_f32_16x16x32_bf16 v[132:135], a[92:95], v[44:47], v[132:135]\n\t"
      "ds_read_b128 v[116:119], v199 offset:12800\n\t"
      "v_cvt_pk_bf16_f32 v137, v130, v131\n\t"
      "s_sub_u32 s84, s84, 4\n\t"
      "s_max_i32 s84, s84, s86\n\t"
      "v_mfma_f32_16x16x32_bf16 v[132:135], a[116:119], v[48:51], v[132:135]\n\t"
      "s_sub_u32 s83, s82, s80\n\t"
      "v_mfma_f32_16x16x32_bf16 v[132:135], a[140:143], v[52:55], v[132:135]\n\t"
      "s_max_i32 s83, s83, 0\n\t"
      "s_add_u32 s82, s80, s83\n\t"
      "v_mfma_f32_16x16x32_bf16 v[132:135], a[164:167], v[56:59], v[132:135]\n\t"
      "ds_read_b128 v[120:123], v196 offset:12800\n\t"
      "v_mfma_f32_16x16x32_bf16 v[132:135], a[188:191], v[60:63], v[132:135]\n\t"
      "ds_read_b128 v[124:127], v197 offset:12800\n\t"
      "v_mfma_f32_16x16x32_bf16 v[132:135], a[212:215], v[64:67], v[132:135]\n\t"
      "v_mfma_f32_16x16x32_bf16 v[132:135], a[236:239], v[68:71], v[132:135]\n\t"
      "s_add_u32 s82, s82, s84\n\t"
      "v_mfma_f32_16x16x32_bf16 v[132:135], v[168:171], v[72:75], v[132:135]\n\t"
      "s_add_u32 s44, s44, 1\n\t"
      "s_add_u32 s85, s85, 1\n\t"
      "v_mfma_f32_16x16x32_bf16 v[132:135], v[192:195], v[76:79], v[132:135]\n\t"
      "s_waitcnt lgkmcnt(9)\n\t"
      "v_mfma_f32_16x16x32_bf16 v[128:131], a[0:3], v[80:83], v[8:11]\n\t"
      "v_mfma_f32_16x16x32_bf16 v[128:131], a[24:27], v[84:87], v[128:131]\n\t"
      "v_readlane_b32 s72, v248, s44\n\t"
      "v_mfma_f32_16x16x32_bf16 v[128:131], a[48:51], v[88:91], v[128:131]\n\t"
      "v_cvt_pk_bf16_f32 v138, v132, v133\n\t"
      "v_mfma_f32_16x16x32_bf16 v[128:131], a[72:75], v[92:95], v[128:131]\n\t"
      "v_cvt_pk_bf16_f32 v139, v134, v135\n\t"
      "s_waitcnt lgkmcnt(4)\n\t"
      "v_mfma_f32_16x16x32_bf16 v[128:131], a[96:99], v[96:99], v[128:131]\n\t"
      "ds_write_b128 v219, v[136:139] offset:0\n\t"
      "v_mfma_f32_16x16x32_bf16 v[128:131], a[120:123], v[100:103], v[128:131]\n\t"
      "v_mfma_f32_16x16x32_bf16 v[128:131], a[144:147], v[104:107], v[128:131]\n\t"
      "buffer_load_dwordx4 v209, s[20:23], s73 offen offset:2048 lds\n\t"
      "v_mfma_f32_16x16x32_bf16 v[128:131], a[168:171], v[108:111], v[128:131]\n\t"
      "v_add_u32_e32 v196, s47, v200\n\t"
      "s_waitcnt lgkmcnt(1)\n\t"
      "v_mfma_f32_16x16x32_bf16 v[128:131], a[192:195], v[112:115], v[128:131]\n\t"
      "v_add_u32_e32 v197, s47, v201\n\t"
      "v_mfma_f32_16x16x32_bf16 v[128:131], a[216:219], v[116:119], v[128:131]\n\t"
      "v_add_u32_e32 v198, s47, v202\n\t"
      "v_mfma_f32_16x16x32_bf16 v[128:131], a[240:243], v[120:123], v[128:131]\n\t"
      "v_add_u32_e32 v199, s47, v203\n\t"
      "v_mfma_f32_16x16x32_bf16 v[128:131], v[172:175], v[124:127], v[128:131]\n\t"
      "s_cmp_gt_i32 s83, 1\n\t"
      "s_cbranch_scc1 .LslpP3_%=\n\t"
      ".LretP3_%=:\n\t"
      "s_cmp_eq_u32 s94, 1\n\t"
      "s_cbranch_scc0 .LwP3barv_1_%=\n\t"
      "s_waitcnt vmcnt(9)\n\t"
      ".LwP3barv_1_%=:\n\t"
      "s_waitcnt lgkmcnt(0)\n\t"
      "s_barrier\n\t"
      "v_mfma_f32_16x16x32_bf16 v[132:135], a[4:7], v[80:83], v[12:15]\n\t"
      "ds_read_b128 v[140:143], v220 offset:0\n\t"
      "v_mfma_f32_16x16x32_bf16 v[132:135], a[28:31], v[84:87], v[132:135]\n\t"
      "ds_read_b128 v[240:243], v247 offset:0\n\t"
      "v_mfma_f32_16x16x32_bf16 v[132:135], a[52:55], v[88:91], v[132:135]\n\t"
      "v_cvt_pk_bf16_f32 v136, v128, v129\n\t"
      "v_mfma_f32_16x16x32_bf16 v[132:135], a[76:79], v[92:95], v[132:135]\n\t"
      "ds_read_b128 v[32:35], v196 offset:0\n\t"
      "v_cvt_pk_bf16_f32 v137, v130, v131\n\t"
      "v_mfma_f32_16x16x32_bf16 v[132:135], a[100:103], v[96:99], v[132:135]\n\t"
      "v_readlane_b32 s68, v248, s94\n\t"
      "v_mfma_f32_16x16x32_bf16 v[132:135], a[124:127], v[100:103], v[132:135]\n\t"
      "ds_read_b128 v[152:155], v238 offset:0\n\t"
      "v_mfma_f32_16x16x32_bf16 v[132:135], a[148:151], v[104:107], v[132:135]\n\t"
      "ds_read_b128 v[36:39], v197 offset:0\n\t"
      "v_mfma_f32_16x16x32_bf16 v[132:135], a[172:175], v[108:111], v[132:135]\n\t"
      "v_mfma_f32_16x16x32_bf16 v[132:135], a[196:199], v[112:115], v[132:135]\n\t"
      "s_waitcnt lgkmcnt(3)\n\t"
      "v_add3_u32 v240, v240, v241, v242\n\t"
      "v_mfma_f32_16x16x32_bf16 v[132:135], a[220:223], v[116:119], v[132:135]\n\t"
      "ds_read_b128 v[40:43], v198 offset:0\n\t"
      "v_mfma_f32_16x16x32_bf16 v[132:135], a[244:247], v[120:123], v[132:135]\n\t"
      "v_add_u32_e32 v240, v240, v243\n\t"
      "v_mfma_f32_16x16x32_bf16 v[132:135], v[176:179], v[124:127], v[132:135]\n\t"
      "buffer_store_dwordx4 v[140:143], v208, s[28:31], s72 offen sc1\n\t"
      "v_mfma_f32_16x16x32_bf16 v[128:131], a[8:11], v[80:83], v[16:19]\n\t"
      "ds_read_b128 v[44:47], v199 offset:0\n\t"
      "v_mfma_f32_16x16x32_bf16 v[128:131], a[32:35], v[84:87], v[128:131]\n\t"
      "v_add_u32_dpp v241, v240, v240 quad_perm:[1,0,3,2] row_mask:0xf bank_mask:0xf\n\t"
      "s_memrealtime s[80:81]\n\t"
      "v_mfma_f32_16x16x32_bf16 v[128:131], a[56:59], v[88:91], v[128:131]\n\t"
      "v_cvt_pk_bf16_f32 v138, v132, v133\n\t"
      "v_mfma_f32_16x16x32_bf16 v[128:131], a[80:83], v[92:95], v[128:131]\n\t"
      "v_cvt_pk_bf16_f32 v139, v134, v135\n\t"
      "v_mfma_f32_16x16x32_bf16 v[128:131], a[104:107], v[96:99], v[128:131]\n\t"
      "ds_write_b128 v217, v[136:139] offset:12288\n\t"
      "v_add_u32_dpp v240, v241, v241 quad_perm:[2,3,0,1] row_mask:0xf bank_mask:0xf\n\t"
      "v_mfma_f32_16x16x32_bf16 v[128:131], a[128:131], v[100:103], v[128:131]\n\t"
      "s_mov_b32 m0, s46\n\t"
      "v_mfma_f32_16x16x32_bf16 v[128:131], a[152:155], v[104:107], v[128:131]\n\t"
      "s_waitcnt lgkmcnt(4)\n\t"
      "buffer_store_dwordx4 v[152:155], v208, s[28:31], s68 offen sc1\n\t"
      "v_cmp_le_u32_e64 s[76:77], 18, v240\n\t"
      "v_cmp_ge_u32_e64 s[78:79], 14, v240\n\t"
      "v_mfma_f32_16x16x32_bf16 v[128:131], a[176:179], v[108:111], v[128:131]\n\t"
      "ds_read_b128 v[48:51], v197 offset:256\n\t"
      "v_mfma_f32_16x16x32_bf16 v[128:131], a[200:203], v[112:115], v[128:131]\n\t"
      "v_cmp_lt_u32_e32 vcc, 63, v240\n\t"
      "s_and_b32 s65, s76, s77\n\t"
      "v_mfma_f32_16x16x32_bf16 v[128:131], a[224:227], v[116:119], v[128:131]\n\t"
      "ds_read_b128 v[52:55], v198 offset:256\n\t"
      "v_mfma_f32_16x16x32_bf16 v[128:131], a[248:251], v[120:123], v[128:131]\n\t"
      "ds_read_b128 v[156:159], v238 offset:1024\n\t"
      "s_and_b32 s78, s78, s79\n\t"
      "s_and_b32 s67, s65, 0x11111111\n\t"
      "v_mfma_f32_16x16x32_bf16 v[128:131], v[180:183], v[124:127], v[128:131]\n\t"
      "v_mfma_f32_16x16x32_bf16 v[132:135], a[12:15], v[80:83], v[20:23]\n\t"
      "ds_read_b128 v[56:59], v199 offset:256\n\t"
      "s_or_b32 s65, s65, s78\n\t"
      "s_cmp_lg_u64 vcc, 0\n\t"
      "s_cselect_b32 s65, 0, s65\n\t"
      "s_cselect_b32 s67, 0, s67\n\t"
      "v_mfma_f32_16x16x32_bf16 v[132:135], a[36:39], v[84:87], v[132:135]\n\t"
      "v_and_b32_e32 v240, s67, v252\n\t"
      "v_and_b32_e32 v241, s65, v252\n\t"
      "v_mfma_f32_16x16x32_bf16 v[132:135], a[60:63], v[88:91], v[132:135]\n\t"
      "v_cvt_pk_bf16_f32 v136, v128, v129\n\t"
      "v_mfma_f32_16x16x32_bf16 v[132:135], a[84:87], v[92:95], v[132:135]\n\t"
      "v_cvt_pk_bf16_f32 v137, v130, v131\n\t"
      "ds_read_b128 v[144:147], v220 offset:1024\n\t"
      "v_mfma_f32_16x16x32_bf16 v[132:135], a[108:111], v[96:99], v[132:135]\n\t"
      "v_cmp_ne_u32_e64 s[76:77], 0, v240\n\t"
      "v_cmp_ne_u32_e64 s[78:79], 0, v241\n\t"
      "v_mfma_f32_16x16x32_bf16 v[132:135], a[132:135], v[100:103], v[132:135]\n\t"
      "s_waitcnt lgkmcnt(2)\n\t"
      "buffer_store_dwordx4 v[156:159], v208, s[28:31], s68 offen offset:1024 sc1\n\t"
      "v_mov_b32_e32 v243, s87\n\t"
      "s_and_b32 s64, s65, 0x22222222\n\t"
      "v_mfma_f32_16x16x32_bf16 v[132:135], a[156:159], v[104:107], v[132:135]\n\t"
      "ds_read_b128 v[60:63], v196 offset:256\n\t"
      "s_add_u32 s87, s87, 2\n\t"
      "v_min_u32_e32 v242, s87, v250\n\t"
      "v_mfma_f32_16x16x32_bf16 v[132:135], a[180:183], v[108:111], v[132:135]\n\t"
      "s_xor_b32 s75, s75, s76\n\t"
      "s_xor_b32 s74, s74, s76\n\t"
      "v_mfma_f32_16x16x32_bf16 v[132:135], a[204:207], v[112:115], v[132:135]\n\t"
      "s_and_b32 s76, s75, s78\n\t"
      "v_cndmask_b32_e64 v249, v248, v251, s[76:77]\n\t"
      "v_mfma_f32_16x16x32_bf16 v[132:135], a[228:231], v[116:119], v[132:135]\n\t"
      "ds_read_b128 v[64:67], v198 offset:512\n\t"
      "s_bcnt1_i32_b32 s51, s78\n\t"
      "s_cmp_lt_u32 s51, 4\n\t"
      "s_cselect_b32 s51, 2, 0\n\t"
      "v_mfma_f32_16x16x32_bf16 v[132:135], a[252:255], v[120:123], v[132:135]\n\t"
      "s_and_b32 s76, s74, s78\n\t"
      "v_cndmask_b32_e64 v248, v248, v251, s[76:77]\n\t"
      "v_mfma_f32_16x16x32_bf16 v[132:135], v[184:187], v[124:127], v[132:135]\n\t"
      "s_waitcnt lgkmcnt(2)\n\t"
      "buffer_store_dwordx4 v[144:147], v208, s[28:31], s72 offen offset:1024 sc1\n\t"
      "v_mfma_f32_16x16x32_bf16 v[128:131], a[16:19], v[80:83], v[24:27]\n\t"
      "ds_read_b128 v[68:71], v199 offset:512\n\t"
      "v_add_u32_e32 v250, s51, v250\n\t"
      "v_mfma_f32_16x16x32_bf16 v[128:131], a[40:43], v[84:87], v[128:131]\n\t"
      "s_lshr_b32 s74, s78, 3\n\t"
      "s_or_b32 s76, s74, s78\n\t"
      "v_mfma_f32_16x16x32_bf16 v[128:131], a[64:67], v[88:91], v[128:131]\n\t"
      "v_cvt_pk_bf16_f32 v138, v132, v133\n\t"
      "v_readlane_b32 s73, v248, s85\n\t"
      "v_mfma_f32_16x16x32_bf16 v[128:131], a[88:91], v[92:95], v[128:131]\n\t"
      "v_cvt_pk_bf16_f32 v139, v134, v135\n\t"
      "ds_read_b128 v[160:163], v238 offset:2048\n\t"
      "v_mfma_f32_16x16x32_bf16 v[128:131], a[112:115], v[96:99], v[128:131]\n\t"
      "ds_write_b128 v218, v[136:139] offset:12288\n\t"
      "v_mfma_f32_16x16x32_bf16 v[128:131], a[136:139], v[100:103], v[128:131]\n\t"
      "v_cndmask_b32_e64 v250, v250, v242, s[76:77]\n\t"
      "s_and_b32 s78, s74, s78\n\t"
      "v_mfma_f32_16x16x32_bf16 v[128:131], a[160:163], v[104:107], v[128:131]\n\t"
      "s_and_b32 s78, s78, 0x3fff\n\t"
      "v_cndmask_b32_e64 v250, v250, v243, s[78:79]\n\t"
      "v_mfma_f32_16x16x32_bf16 v[128:131], a[184:187], v[108:111], v[128:131]\n\t"
      "ds_read_b128 v[148:151], v220 offset:2048\n\t"
      "v_mfma_f32_16x16x32_bf16 v[128:131], a[208:211], v[112:115], v[128:131]\n\t"
      "buffer_load_dwordx4 v208, s[20:23], s73 offen lds\n\t"
      "v_mfma_f32_16x16x32_bf16 v[128:131], a[232:235], v[116:119], v[128:131]\n\t"
      "ds_read_b128 v[72:75], v196 offset:512\n\t"
      "s_or_b32 s64, s64, s67\n\t"
      "v_mfma_f32_16x16x32_bf16 v[128:131], v[164:167], v[120:123], v[128:131]\n\t"
      "v_readlane_b32 s86, v250, s44\n\t"
      "v_mfma_f32_16x16x32_bf16 v[128:131], v[188:191], v[124:127], v[128:131]\n\t"
      "v_mfma_f32_16x16x32_bf16 v[132:135], a[20:23], v[80:83], v[28:31]\n\t"
      "ds_read_b128 v[76:79], v197 offset:512\n\t"
      "v_mfma_f32_16x16x32_bf16 v[132:135], a[44:47], v[84:87], v[132:135]\n\t"
      "buffer_load_dwordx4 v208, s[20:23], s73 offen offset:1024 lds\n\t"
      "v_mfma_f32_16x16x32_bf16 v[132:135], a[68:71], v[88:91], v[132:135]\n\t"
      "v_cvt_pk_bf16_f32 v136, v128, v129\n\t"
      "v_mfma_f32_16x16x32_bf16 v[132:135], a[92:95], v[92:95], v[132:135]\n\t"
      "v_cvt_pk_bf16_f32 v137, v130, v131\n\t"
      "s_sub_u32 s84, s84, 4\n\t"
      "s_max_i32 s84, s84, s86\n\t"
      "v_mfma_f32_16x16x32_bf16 v[132:135], a[116:119], v[96:99], v[132:135]\n\t"
      "s_sub_u32 s83, s82, s80\n\t"
      "v_mfma_f32_16x16x32_bf16 v[132:135], a[140:143], v[100:103], v[132:135]\n\t"
      "s_waitcnt lgkmcnt(4)\n\t"
      "buffer_store_dwordx4 v[160:163], v208, s[28:31], s68 offen offset:2048 sc1\n\t"
      "s_max_i32 s83, s83, 0\n\t"
      "s_add_u32 s82, s80, s83\n\t"
      "v_mfma_f32_16x16x32_bf16 v[132:135], a[164:167], v[104:107], v[132:135]\n\t"
      "s_mov_b32 s45, s46\n\t"
      "v_mfma_f32_16x16x32_bf16 v[132:135], a[188:191], v[108:111], v[132:135]\n\t"
      "s_mov_b32 s46, s47\n\t"
      "v_mfma_f32_16x16x32_bf16 v[132:135], a[212:215], v[112:115], v[132:135]\n\t"
      "s_mov_b32 s47, s48\n\t"
      "v_mfma_f32_16x16x32_bf16 v[132:135], a[236:239], v[116:119], v[132:135]\n\t"
      "s_add_u32 s82, s82, s84\n\t"
      "v_mfma_f32_16x16x32_bf16 v[132:135], v[168:171], v[120:123], v[132:135]\n\t"
      "s_cmp_lg_u32 s44, s49\n\t"
      "v_mfma_f32_16x16x32_bf16 v[132:135], v[192:195], v[124:127], v[132:135]\n\t"
      "s_waitcnt lgkmcnt(2)\n\t"
      "buffer_store_dwordx4 v[148:151], v208, s[28:31], s72 offen offset:2048 sc1\n\t"
      "v_mfma_f32_16x16x32_bf16 v[128:131], a[0:3], v[32:35], v[8:11]\n\t"
      "v_mfma_f32_16x16x32_bf16 v[128:131], a[24:27], v[36:39], v[128:131]\n\t"
      "v_readlane_b32 s72, v249, s44\n\t"
      "v_mfma_f32_16x16x32_bf16 v[128:131], a[48:51], v[40:43], v[128:131]\n\t"
      "v_cvt_pk_bf16_f32 v138, v132, v133\n\t"
      "v_mfma_f32_16x16x32_bf16 v[128:131], a[72:75], v[44:47], v[128:131]\n\t"
      "v_cvt_pk_bf16_f32 v139, v134, v135\n\t"
      "v_mfma_f32_16x16x32_bf16 v[128:131], a[96:99], v[48:51], v[128:131]\n\t"
      "ds_write_b128 v219, v[136:139] offset:12288\n\t"
      "v_mfma_f32_16x16x32_bf16 v[128:131], a[120:123], v[52:55], v[128:131]\n\t"
      "v_mfma_f32_16x16x32_bf16 v[128:131], a[144:147], v[56:59], v[128:131]\n\t"
      "buffer_load_dwordx4 v208, s[20:23], s73 offen offset:2048 lds\n\t"
      "v_mfma_f32_16x16x32_bf16 v[128:131], a[168:171], v[60:63], v[128:131]\n\t"
      "s_waitcnt lgkmcnt(1)\n\t"
      "v_mfma_f32_16x16x32_bf16 v[128:131], a[192:195], v[64:67], v[128:131]\n\t"
      "v_add_u32_e32 v217, s46, v204\n\t"
      "v_mfma_f32_16x16x32_bf16 v[128:131], a[216:219], v[68:71], v[128:131]\n\t"
      "v_add_u32_e32 v218, s46, v205\n\t"
      "v_mfma_f32_16x16x32_bf16 v[128:131], a[240:243], v[72:75], v[128:131]\n\t"
      "v_add_u32_e32 v219, s46, v206\n\t"
      "v_mfma_f32_16x16x32_bf16 v[128:131], v[172:175], v[76:79], v[128:131]\n\t"
      "s_cmp_gt_i32 s83, 1\n\t"
      "s_cbranch_scc1 .LslpP4_%=\n\t"
      ".LretP4_%=:\n\t"
      "s_waitcnt vmcnt(15) lgkmcnt(0)\n\t"
      "s_barrier\n\t"
      "v_mfma_f32_16x16x32_bf16 v[132:135], a[4:7], v[32:35], v[12:15]\n\t"
      "ds_read_b128 v[140:143], v220 offset:12288\n\t"
      "v_mfma_f32_16x16x32_bf16 v[132:135], a[28:31], v[36:39], v[132:135]\n\t"
      "v_mfma_f32_16x16x32_bf16 v[132:135], a[52:55], v[40:43], v[132:135]\n\t"
      "v_cvt_pk_bf16_f32 v136, v128, v129\n\t"
      "v_mfma_f32_16x16x32_bf16 v[132:135], a[76:79], v[44:47], v[132:135]\n\t"
      "ds_read_b128 v[80:83], v196 offset:12288\n\t"
      "v_cvt_pk_bf16_f32 v137, v130, v131\n\t"
      "v_mfma_f32_16x16x32_bf16 v[132:135], a[100:103], v[48:51], v[132:135]\n\t"
      "v_readlane_b32 s68, v248, s94\n\t"
      "s_add_u32 s48, s47, 0x6000\n\t"
      "v_mfma_f32_16x16x32_bf16 v[132:135], a[124:127], v[52:55], v[132:135]\n\t"
      "ds_read_b128 v[152:155], v238 offset:12288\n\t"
      "s_cmp_eq_u32 s48, s50\n\t"
      "s_cselect_b32 s48, s41, s48\n\t"
      "v_mfma_f32_16x16x32_bf16 v[132:135], a[148:151], v[56:59], v[132:135]\n\t"
      "ds_read_b128 v[84:87], v197 offset:12288\n\t"
      "v_mfma_f32_16x16x32_bf16 v[132:135], a[172:175], v[60:63], v[132:135]\n\t"
      "v_readlane_b32 s73, v249, s85\n\t"
      "v_mfma_f32_16x16x32_bf16 v[132:135], a[196:199], v[64:67], v[132:135]\n\t"
      "v_mfma_f32_16x16x32_bf16 v[132:135], a[220:223], v[68:71], v[132:135]\n\t"
      "ds_read_b128 v[88:91], v198 offset:12288\n\t"
      "v_mfma_f32_16x16x32_bf16 v[132:135], a[244:247], v[72:75], v[132:135]\n\t"
      "v_mfma_f32_16x16x32_bf16 v[132:135], v[176:179], v[76:79], v[132:135]\n\t"
      "s_waitcnt lgkmcnt(4)\n\t"
      "buffer_store_dwordx4 v[140:143], v209, s[28:31], s72 offen sc1\n\t"
      "v_mfma_f32_16x16x32_bf16 v[128:131], a[8:11], v[32:35], v[16:19]\n\t"
      "ds_read_b128 v[92:95], v199 offset:12288\n\t"
      "v_mfma_f32_16x16x32_bf16 v[128:131], a[32:35], v[36:39], v[128:131]\n\t"
      "s_memrealtime s[80:81]\n\t"
      "v_mfma_f32_16x16x32_bf16 v[128:131], a[56:59], v[40:43], v[128:131]\n\t"
      "v_cvt_pk_bf16_f32 v138, v132, v133\n\t"
      "v_mfma_f32_16x16x32_bf16 v[128:131], a[80:83], v[44:47], v[128:131]\n\t"
      "v_cvt_pk_bf16_f32 v139, v134, v135\n\t"
      "v_mfma_f32_16x16x32_bf16 v[128:131], a[104:107], v[48:51], v[128:131]\n\t"
      "ds_write_b128 v217, v[136:139] offset:0\n\t"
      "v_mfma_f32_16x16x32_bf16 v[128:131], a[128:131], v[52:55], v[128:131]\n\t"
      "s_add_u32 m0, s45, 0x3000\n\t"
      "v_mfma_f32_16x16x32_bf16 v[128:131], a[152:155], v[56:59], v[128:131]\n\t"
      "s_waitcnt lgkmcnt(4)\n\t"
      "buffer_store_dwordx4 v[152:155], v209, s[28:31], s68 offen sc1\n\t"
      "v_mfma_f32_16x16x32_bf16 v[128:131], a[176:179], v[60:63], v[128:131]\n\t"
      "ds_read_b128 v[96:99], v197 offset:12544\n\t"
      "v_mfma_f32_16x16x32_bf16 v[128:131], a[200:203], v[64:67], v[128:131]\n\t"
      "v_mfma_f32_16x16x32_bf16 v[128:131], a[224:227], v[68:71], v[128:131]\n\t"
      "ds_read_b128 v[100:103], v198 offset:12544\n\t"
      "v_mfma_f32_16x16x32_bf16 v[128:131], a[248:251], v[72:75], v[128:131]\n\t"
      "ds_read_b128 v[156:159], v238 offset:13312\n\t"
      "v_mfma_f32_16x16x32_bf16 v[128:131], v[180:183], v[76:79], v[128:131]\n\t"
      "buffer_load_dwordx4 v209, s[20:23], s73 offen lds\n\t"
      "v_mfma_f32_16x16x32_bf16 v[132:135], a[12:15], v[32:35], v[20:23]\n\t"
      "ds_read_b128 v[104:107], v199 offset:12544\n\t"
      "v_mfma_f32_16x16x32_bf16 v[132:135], a[36:39], v[36:39], v[132:135]\n\t"
      "v_mfma_f32_16x16x32_bf16 v[132:135], a[60:63], v[40:43], v[132:135]\n\t"
      "v_cvt_pk_bf16_f32 v136, v128, v129\n\t"
      "v_mfma_f32_16x16x32_bf16 v[132:135], a[84:87], v[44:47], v[132:135]\n\t"
      "v_cvt_pk_bf16_f32 v137, v130, v131\n\t"
      "ds_read_b128 v[144:147], v220 offset:13312\n\t"
      "v_mfma_f32_16x16x32_bf16 v[132:135], a[108:111], v[48:51], v[132:135]\n\t"
      "v_mfma_f32_16x16x32_bf16 v[132:135], a[132:135], v[52:55], v[132:135]\n\t"
      "s_waitcnt lgkmcnt(2)\n\t"
      "buffer_store_dwordx4 v[156:159], v209, s[28:31], s68 offen offset:1024 sc1\n\t"
      "v_mfma_f32_16x16x32_bf16 v[132:135], a[156:159], v[56:59], v[132:135]\n\t"
      "ds_read_b128 v[108:111], v196 offset:12544\n\t"
      "v_mfma_f32_16x16x32_bf16 v[132:135], a[180:183], v[60:63], v[132:135]\n\t"
      "v_mfma_f32_16x16x32_bf16 v[132:135], a[204:207], v[64:67], v[132:135]\n\t"
      "v_mfma_f32_16x16x32_bf16 v[132:135], a[228:231], v[68:71], v[132:135]\n\t"
      "ds_read_b128 v[112:115], v198 offset:12800\n\t"
      "v_mfma_f32_16x16x32_bf16 v[132:135], a[252:255], v[72:75], v[132:135]\n\t"
      "v_mfma_f32_16x16x32_bf16 v[132:135], v[184:187], v[76:79], v[132:135]\n\t"
      "s_waitcnt lgkmcnt(2)\n\t"
      "buffer_store_dwordx4 v[144:147], v209, s[28:31], s72 offen offset:1024 sc1\n\t"
      "v_mfma_f32_16x16x32_bf16 v[128:131], a[16:19], v[32:35], v[24:27]\n\t"
      "ds_read_b128 v[116:119], v199 offset:12800\n\t"
      "v_mfma_f32_16x16x32_bf16 v[128:131], a[40:43], v[36:39], v[128:131]\n\t"
      "v_mfma_f32_16x16x32_bf16 v[128:131], a[64:67], v[40:43], v[128:131]\n\t"
      "v_cvt_pk_bf16_f32 v138, v132, v133\n\t"
      "v_mfma_f32_16x16x32_bf16 v[128:131], a[88:91], v[44:47], v[128:131]\n\t"
      "v_cvt_pk_bf16_f32 v139, v134, v135\n\t"
      "ds_read_b128 v[160:163], v238 offset:14336\n\t"
      "v_mfma_f32_16x16x32_bf16 v[128:131], a[112:115], v[48:51], v[128:131]\n\t"
      "ds_write_b128 v218, v[136:139] offset:0\n\t"
      "v_mfma_f32_16x16x32_bf16 v[128:131], a[136:139], v[52:55], v[128:131]\n\t"
      "v_mfma_f32_16x16x32_bf16 v[128:131], a[160:163], v[56:59], v[128:131]\n\t"
      "v_mfma_f32_16x16x32_bf16 v[128:131], a[184:187], v[60:63], v[128:131]\n\t"
      "ds_read_b128 v[148:151], v220 offset:14336\n\t"
      "v_mfma_f32_16x16x32_bf16 v[128:131], a[208:211], v[64:67], v[128:131]\n\t"
      "v_mfma_f32_16x16x32_bf16 v[128:131], a[232:235], v[68:71], v[128:131]\n\t"
      "ds_read_b128 v[120:123], v196 offset:12800\n\t"
      "v_mfma_f32_16x16x32_bf16 v[128:131], v[164:167], v[72:75], v[128:131]\n\t"
      "v_mfma_f32_16x16x32_bf16 v[128:131], v[188:191], v[76:79], v[128:131]\n\t"
      "buffer_load_dwordx4 v209, s[20:23], s73 offen offset:1024 lds\n\t"
      "v_mfma_f32_16x16x32_bf16 v[132:135], a[20:23], v[32:35], v[28:31]\n\t"
      "ds_read_b128 v[124:127], v197 offset:12800\n\t"
      "v_mfma_f32_16x16x32_bf16 v[132:135], a[44:47], v[36:39], v[132:135]\n\t"
      "v_mfma_f32_16x16x32_bf16 v[132:135], a[68:71], v[40:43], v[132:135]\n\t"
      "v_cvt_pk_bf16_f32 v136, v128, v129\n\t"
      "v_add_u32_e32 v220, s46, v207\n\t"
      "v_mfma_f32_16x16x32_bf16 v[132:135], a[92:95], v[44:47], v[132:135]\n\t"
      "v_cvt_pk_bf16_f32 v137, v130, v131\n\t"
      "s_sub_u32 s84, s84, 4\n\t"
      "s_max_i32 s84, s84, s86\n\t"
      "v_mfma_f32_16x16x32_bf16 v[132:135], a[116:119], v[48:51], v[132:135]\n\t"
      "s_sub_u32 s83, s82, s80\n\t"
      "v_mfma_f32_16x16x32_bf16 v[132:135], a[140:143], v[52:55], v[132:135]\n\t"
      "s_waitcnt lgkmcnt(4)\n\t"
      "buffer_store_dwordx4 v[160:163], v209, s[28:31], s68 offen offset:2048 sc1\n\t"
      "s_max_i32 s83, s83, 0\n\t"
      "s_add_u32 s82, s80, s83\n\t"
      "v_mfma_f32_16x16x32_bf16 v[132:135], a[164:167], v[56:59], v[132:135]\n\t"
      "v_mfma_f32_16x16x32_bf16 v[132:135], a[188:191], v[60:63], v[132:135]\n\t"
      "v_mfma_f32_16x16x32_bf16 v[132:135], a[212:215], v[64:67], v[132:135]\n\t"
      "s_add_u32 s44, s44, 1\n\t"
      "v_mfma_f32_16x16x32_bf16 v[132:135], a[236:239], v[68:71], v[132:135]\n\t"
      "s_add_u32 s82, s82, s84\n\t"
      "v_mfma_f32_16x16x32_bf16 v[132:135], v[168:171], v[72:75], v[132:135]\n\t"
      "s_add_u32 s85, s85, 1\n\t"
      "v_mfma_f32_16x16x32_bf16 v[132:135], v[192:195], v[76:79], v[132:135]\n\t"
      "s_waitcnt lgkmcnt(2)\n\t"
      "buffer_store_dwordx4 v[148:151], v209, s[28:31], s72 offen offset:2048 sc1\n\t"
      "v_mfma_f32_16x16x32_bf16 v[128:131], a[0:3], v[80:83], v[8:11]\n\t"
      "v_mfma_f32_16x16x32_bf16 v[128:131], a[24:27], v[84:87], v[128:131]\n\t"
      "v_readlane_b32 s72, v248, s44\n\t"
      "v_mfma_f32_16x16x32_bf16 v[128:131], a[48:51], v[88:91], v[128:131]\n\t"
      "v_cvt_pk_bf16_f32 v138, v132, v133\n\t"
      "v_mfma_f32_16x16x32_bf16 v[128:131], a[72:75], v[92:95], v[128:131]\n\t"
      "v_cvt_pk_bf16_f32 v139, v134, v135\n\t"
      "v_mfma_f32_16x16x32_bf16 v[128:131], a[96:99], v[96:99], v[128:131]\n\t"
      "ds_write_b128 v219, v[136:139] offset:0\n\t"
      "v_mfma_f32_16x16x32_bf16 v[128:131], a[120:123], v[100:103], v[128:131]\n\t"
      "v_mfma_f32_16x16x32_bf16 v[128:131], a[144:147], v[104:107], v[128:131]\n\t"
      "buffer_load_dwordx4 v209, s[20:23], s73 offen offset:2048 lds\n\t"
      "v_mfma_f32_16x16x32_bf16 v[128:131], a[168:171], v[108:111], v[128:131]\n\t"
      "v_add_u32_e32 v196, s47, v200\n\t"
      "s_waitcnt lgkmcnt(1)\n\t"
      "v_mfma_f32_16x16x32_bf16 v[128:131], a[192:195], v[112:115], v[128:131]\n\t"
      "v_add_u32_e32 v197, s47, v201\n\t"
      "v_mfma_f32_16x16x32_bf16 v[128:131], a[216:219], v[116:119], v[128:131]\n\t"
      "v_add_u32_e32 v198, s47, v202\n\t"
      "v_mfma_f32_16x16x32_bf16 v[128:131], a[240:243], v[120:123], v[128:131]\n\t"
      "v_add_u32_e32 v199, s47, v203\n\t"
      "v_mfma_f32_16x16x32_bf16 v[128:131], v[172:175], v[124:127], v[128:131]\n\t"
      "s_cmp_gt_i32 s83, 1\n\t"
      "s_cbranch_scc1 .LslpP5_%=\n\t"
      ".LretP5_%=:\n\t"
      "s_waitcnt vmcnt(21) lgkmcnt(0)\n\t"
      "s_barrier\n\t"
      "v_mfma_f32_16x16x32_bf16 v[132:135], a[4:7], v[80:83], v[12:15]\n\t"
      "ds_read_b128 v[140:143], v220 offset:0\n\t"
      "v_mfma_f32_16x16x32_bf16 v[132:135], a[28:31], v[84:87], v[132:135]\n\t"
      "v_mfma_f32_16x16x32_bf16 v[132:135], a[52:55], v[88:91], v[132:135]\n\t"
      "v_cvt_pk_bf16_f32 v136, v128, v129\n\t"
      "v_mfma_f32_16x16x32_bf16 v[132:135], a[76:79], v[92:95], v[132:135]\n\t"
      "ds_read_b128 v[32:35], v196 offset:0\n\t"
      "v_cvt_pk_bf16_f32 v137, v130, v131\n\t"
      "v_mfma_f32_16x16x32_bf16 v[132:135], a[100:103], v[96:99], v[132:135]\n\t"
      "v_mfma_f32_16x16x32_bf16 v[132:135], a[124:127], v[100:103], v[132:135]\n\t"
      "v_mfma_f32_16x16x32_bf16 v[132:135], a[148:151], v[104:107], v[132:135]\n\t"
      "ds_read_b128 v[36:39], v197 offset:0\n\t"
      "v_mfma_f32_16x16x32_bf16 v[132:135], a[172:175], v[108:111], v[132:135]\n\t"
      "v_readlane_b32 s73, v248, s85\n\t"
      "v_mfma_f32_16x16x32_bf16 v[132:135], a[196:199], v[112:115], v[132:135]\n\t"
      "v_mfma_f32_16x16x32_bf16 v[132:135], a[220:223], v[116:119], v[132:135]\n\t"
      "ds_read_b128 v[40:43], v198 offset:0\n\t"
      "v_mfma_f32_16x16x32_bf16 v[132:135], a[244:247], v[120:123], v[132:135]\n\t"
      "v_mfma_f32_16x16x32_bf16 v[132:135], v[176:179], v[124:127], v[132:135]\n\t"
      "s_waitcnt lgkmcnt(3)\n\t"
      "buffer_store_dwordx4 v[140:143], v208, s[28:31], s72 offen sc1\n\t"
      "v_mfma_f32_16x16x32_bf16 v[128:131], a[8:11], v[80:83], v[16:19]\n\t"
      "ds_read_b128 v[44:47], v199 offset:0\n\t"
      "v_mfma_f32_16x16x32_bf16 v[128:131], a[32:35], v[84:87], v[128:131]\n\t"
      "s_memrealtime s[80:81]\n\t"
      "v_mfma_f32_16x16x32_bf16 v[128:131], a[56:59], v[88:91], v[128:131]\n\t"
      "v_cvt_pk_bf16_f32 v138, v132, v133\n\t"
      "v_mfma_f32_16x16x32_bf16 v[128:131], a[80:83], v[92:95], v[128:131]\n\t"
      "v_cvt_pk_bf16_f32 v139, v134, v135\n\t"
      "v_mfma_f32_16x16x32_bf16 v[128:131], a[104:107], v[96:99], v[128:131]\n\t"
      "ds_write_b128 v217, v[136:139] offset:12288\n\t"
      "v_mfma_f32_16x16x32_bf16 v[128:131], a[128:131], v[100:103], v[128:131]\n\t"
      "s_mov_b32 m0, s46\n\t"
      "v_mfma_f32_16x16x32_bf16 v[128:131], a[152:155], v[104:107], v[128:131]\n\t"
      "v_mfma_f32_16x16x32_bf16 v[128:131], a[176:179], v[108:111], v[128:131]\n\t"
      "ds_read_b128 v[48:51], v197 offset:256\n\t"
      "v_mfma_f32_16x16x32_bf16 v[128:131], a[200:203], v[112:115], v[128:131]\n\t"
      "v_mfma_f32_16x16x32_bf16 v[128:131], a[224:227], v[116:119], v[128:131]\n\t"
      "ds_read_b128 v[52:55], v198 offset:256\n\t"
      "v_mfma_f32_16x16x32_bf16 v[128:131], a[248:251], v[120:123], v[128:131]\n\t"
      "v_mfma_f32_16x16x32_bf16 v[128:131], v[180:183], v[124:127], v[128:131]\n\t"
      "buffer_load_dwordx4 v208, s[20:23], s73 offen lds\n\t"
      "v_mfma_f32_16x16x32_bf16 v[132:135], a[12:15], v[80:83], v[20:23]\n\t"
      "ds_read_b128 v[56:59], v199 offset:256\n\t"
      "v_mfma_f32_16x16x32_bf16 v[132:135], a[36:39], v[84:87], v[132:135]\n\t"
      "v_mfma_f32_16x16x32_bf16 v[132:135], a[60:63], v[88:91], v[132:135]\n\t"
      "v_cvt_pk_bf16_f32 v136, v128, v129\n\t"
      "v_mfma_f32_16x16x32_bf16 v[132:135], a[84:87], v[92:95], v[132:135]\n\t"
      "v_cvt_pk_bf16_f32 v137, v130, v131\n\t"
      "ds_read_b128 v[144:147], v220 offset:1024\n\t"
      "v_mfma_f32_16x16x32_bf16 v[132:135], a[108:111], v[96:99], v[132:135]\n\t"
      "v_mfma_f32_16x16x32_bf16 v[132:135], a[132:135], v[100:103], v[132:135]\n\t"
      "v_mfma_f32_16x16x32_bf16 v[132:135], a[156:159], v[104:107], v[132:135]\n\t"
      "ds_read_b128 v[60:63], v196 offset:256\n\t"
      "v_mfma_f32_16x16x32_bf16 v[132:135], a[180:183], v[108:111], v[132:135]\n\t"
      "v_mfma_f32_16x16x32_bf16 v[132:135], a[204:207], v[112:115], v[132:135]\n\t"
      "v_mfma_f32_16x16x32_bf16 v[132:135], a[228:231], v[116:119], v[132:135]\n\t"
      "ds_read_b128 v[64:67], v198 offset:512\n\t"
      "v_mfma_f32_16x16x32_bf16 v[132:135], a[252:255], v[120:123], v[132:135]\n\t"
      "v_mfma_f32_16x16x32_bf16 v[132:135], v[184:187], v[124:127], v[132:135]\n\t"
      "s_waitcnt lgkmcnt(2)\n\t"
      "buffer_store_dwordx4 v[144:147], v208, s[28:31], s72 offen offset:1024 sc1\n\t"
      "v_mfma_f32_16x16x32_bf16 v[128:131], a[16:19], v[80:83], v[24:27]\n\t"
      "ds_read_b128 v[68:71], v199 offset:512\n\t"
      "v_mfma_f32_16x16x32_bf16 v[128:131], a[40:43], v[84:87], v[128:131]\n\t"
      "v_readlane_b32 s86, v250, s44\n\t"
      "v_mfma_f32_16x16x32_bf16 v[128:131], a[64:67], v[88:91], v[128:131]\n\t"
      "v_cvt_pk_bf16_f32 v138, v132, v133\n\t"
      "v_mfma_f32_16x16x32_bf16 v[128:131], a[88:91], v[92:95], v[128:131]\n\t"
      "v_cvt_pk_bf16_f32 v139, v134, v135\n\t"
      "v_mfma_f32_16x16x32_bf16 v[128:131], a[112:115], v[96:99], v[128:131]\n\t"
      "ds_write_b128 v218, v[136:139] offset:12288\n\t"
      "v_mfma_f32_16x16x32_bf16 v[128:131], a[136:139], v[100:103], v[128:131]\n\t"
      "v_mfma_f32_16x16x32_bf16 v[128:131], a[160:163], v[104:107], v[128:131]\n\t"
      "v_mfma_f32_16x16x32_bf16 v[128:131], a[184:187], v[108:111], v[128:131]\n\t"
      "ds_read_b128 v[148:151], v220 offset:2048\n\t"
      "v_mfma_f32_16x16x32_bf16 v[128:131], a[208:211], v[112:115], v[128:131]\n\t"
      "v_mfma_f32_16x16x32_bf16 v[128:131], a[232:235], v[116:119], v[128:131]\n\t"
      "ds_read_b128 v[72:75], v196 offset:512\n\t"
      "v_mfma_f32_16x16x32_bf16 v[128:131], v[164:167], v[120:123], v[128:131]\n\t"
      "v_mfma_f32_16x16x32_bf16 v[128:131], v[188:191], v[124:127], v[128:131]\n\t"
      "buffer_load_dwordx4 v208, s[20:23], s73 offen offset:1024 lds\n\t"
      "v_mfma_f32_16x16x32_bf16 v[132:135], a[20:23], v[80:83], v[28:31]\n\t"
      "ds_read_b128 v[76:79], v197 offset:512\n\t"
      "v_mfma_f32_16x16x32_bf16 v[132:135], a[44:47], v[84:87], v[132:135]\n\t"
      "v_mfma_f32_16x16x32_bf16 v[132:135], a[68:71], v[88:91], v[132:135]\n\t"
      "v_cvt_pk_bf16_f32 v136, v128, v129\n\t"
      "v_mfma_f32_16x16x32_bf16 v[132:135], a[92:95], v[92:95], v[132:135]\n\t"
      "v_cvt_pk_bf16_f32 v137, v130, v131\n\t"
      "s_sub_u32 s84, s84, 4\n\t"
      "s_max_i32 s84, s84, s86\n\t"
      "v_mfma_f32_16x16x32_bf16 v[132:135], a[116:119], v[96:99], v[132:135]\n\t"
      "s_sub_u32 s83, s82, s80\n\t"
      "v_mfma_f32_16x16x32_bf16 v[132:135], a[140:143], v[100:103], v[132:135]\n\t"
      "s_max_i32 s83, s83, 0\n\t"
      "s_add_u32 s82, s80, s83\n\t"
      "v_mfma_f32_16x16x32_bf16 v[132:135], a[164:167], v[104:107], v[132:135]\n\t"
      "s_mov_b32 s45, s46\n\t"
      "v_mfma_f32_16x16x32_bf16 v[132:135], a[188:191], v[108:111], v[132:135]\n\t"
      "s_mov_b32 s46, s47\n\t"
      "v_mfma_f32_16x16x32_bf16 v[132:135], a[212:215], v[112:115], v[132:135]\n\t"
      "s_mov_b32 s47, s48\n\t"
      "v_mfma_f32_16x16x32_bf16 v[132:135], a[236:239], v[116:119], v[132:135]\n\t"
      "s_add_u32 s82, s82, s84\n\t"
      "v_mfma_f32_16x16x32_bf16 v[132:135], v[168:171], v[120:123], v[132:135]\n\t"
      "s_cmp_lg_u32 s44, s49\n\t"
      "v_mfma_f32_16x16x32_bf16 v[132:135], v[192:195], v[124:127], v[132:135]\n\t"
      "s_waitcnt lgkmcnt(2)\n\t"
      "buffer_store_dwordx4 v[148:151], v208, s[28:31], s72 offen offset:2048 sc1\n\t"
      ".p2align 6\n\t"
      ".Lloop_%=:\n\t"
      "s_waitcnt lgkmcnt(12)\n\t"
      "v_mfma_f32_16x16x32_bf16 v[128:131], a[0:3], v[32:35], v[8:11]\n\t"
      "v_mfma_f32_16x16x32_bf16 v[128:131], a[24:27], v[36:39], v[128:131]\n\t"
      "v_readlane_b32 s72, v249, s44\n\t"
      "v_mfma_f32_16x16x32_bf16 v[128:131], a[48:51], v[40:43], v[128:131]\n\t"
      "v_cvt_pk_bf16_f32 v138, v132, v133\n\t"
      "v_mfma_f32_16x16x32_bf16 v[128:131], a[72:75], v[44:47], v[128:131]\n\t"
      "v_cvt_pk_bf16_f32 v139, v134, v135\n\t"
      "s_waitcnt lgkmcnt(6)\n\t"
      "v_mfma_f32_16x16x32_bf16 v[128:131], a[96:99], v[48:51], v[128:131]\n\t"
      "ds_write_b128 v219, v[136:139] offset:12288\n\t"
      "v_mfma_f32_16x16x32_bf16 v[128:131], a[120:123], v[52:55], v[128:131]\n\t"
      "v_mfma_f32_16x16x32_bf16 v[128:131], a[144:147], v[56:59], v[128:131]\n\t"
      "buffer_load_dwordx4 v208, s[20:23], s73 offen offset:2048 lds\n\t"
      "v_mfma_f32_16x16x32_bf16 v[128:131], a[168:171], v[60:63], v[128:131]\n\t"
      "s_waitcnt lgkmcnt(1)\n\t"
      "v_mfma_f32_16x16x32_bf16 v[128:131], a[192:195], v[64:67], v[128:131]\n\t"
      "v_add_u32_e32 v217, s46, v204\n\t"
      "v_mfma_f32_16x16x32_bf16 v[128:131], a[216:219], v[68:71], v[128:131]\n\t"
      "v_add_u32_e32 v218, s46, v205\n\t"
      "v_mfma_f32_16x16x32_bf16 v[128:131], a[240:243], v[72:75], v[128:131]\n\t"
      "v_add_u32_e32 v219, s46, v206\n\t"
      "v_mfma_f32_16x16x32_bf16 v[128:131], v[172:175], v[76:79], v[128:131]\n\t"
      "s_cmp_gt_i32 s83, 1\n\t"
      "s_cbranch_scc1 .LslpA_%=\n\t"
      ".LretA_%=:\n\t"
      "s_waitcnt vmcnt(18) lgkmcnt(0)\n\t"
      "s_barrier\n\t"
      "v_mfma_f32_16x16x32_bf16 v[132:135], a[4:7], v[32:35], v[12:15]\n\t"
      "ds_read_b128 v[140:143], v220 offset:12288\n\t"
      "v_mfma_f32_16x16x32_bf16 v[132:135], a[28:31], v[36:39], v[132:135]\n\t"
      "v_mfma_f32_16x16x32_bf16 v[132:135], a[52:55], v[40:43], v[132:135]\n\t"
      "v_cvt_pk_bf16_f32 v136, v128, v129\n\t"
      "v_mfma_f32_16x16x32_bf16 v[132:135], a[76:79], v[44:47], v[132:135]\n\t"
      "ds_read_b128 v[80:83], v196 offset:12288\n\t"
      "v_cvt_pk_bf16_f32 v137, v130, v131\n\t"
      "v_mfma_f32_16x16x32_bf16 v[132:135], a[100:103], v[48:51], v[132:135]\n\t"
      "s_add_u32 s48, s47, 0x6000\n\t"
      "v_mfma_f32_16x16x32_bf16 v[132:135], a[124:127], v[52:55], v[132:135]\n\t"
      "s_cmp_eq_u32 s48, s50\n\t"
      "s_cselect_b32 s48, s41, s48\n\t"
      "v_mfma_f32_16x16x32_bf16 v[132:135], a[148:151], v[56:59], v[132:135]\n\t"
      "ds_read_b128 v[84:87], v197 offset:12288\n\t"
      "v_mfma_f32_16x16x32_bf16 v[132:135], a[172:175], v[60:63], v[132:135]\n\t"
      "v_readlane_b32 s73, v249, s85\n\t"
      "v_mfma_f32_16x16x32_bf16 v[132:135], a[196:199], v[64:67], v[132:135]\n\t"
      "v_mfma_f32_16x16x32_bf16 v[132:135], a[220:223], v[68:71], v[132:135]\n\t"
      "ds_read_b128 v[88:91], v198 offset:12288\n\t"
      "v_mfma_f32_16x16x32_bf16 v[132:135], a[244:247], v[72:75], v[132:135]\n\t"
      "v_mfma_f32_16x16x32_bf16 v[132:135], v[176:179], v[76:79], v[132:135]\n\t"
      "s_waitcnt lgkmcnt(3)\n\t"
      "buffer_store_dwordx4 v[140:143], v209, s[28:31], s72 offen sc1\n\t"
      "v_mfma_f32_16x16x32_bf16 v[128:131], a[8:11], v[32:35], v[16:19]\n\t"
      "ds_read_b128 v[92:95], v199 offset:12288\n\t"
      "v_mfma_f32_16x16x32_bf16 v[128:131], a[32:35], v[36:39], v[128:131]\n\t"
      "s_memrealtime s[80:81]\n\t"
      "v_mfma_f32_16x16x32_bf16 v[128:131], a[56:59], v[40:43], v[128:131]\n\t"
      "v_cvt_pk_bf16_f32 v138, v132, v133\n\t"
      "v_mfma_f32_16x16x32_bf16 v[128:131], a[80:83], v[44:47], v[128:131]\n\t"
      "v_cvt_pk_bf16_f32 v139, v134, v135\n\t"
      "v_mfma_f32_16x16x32_bf16 v[128:131], a[104:107], v[48:51], v[128:131]\n\t"
      "ds_write_b128 v217, v[136:139] offset:0\n\t"
      "v_mfma_f32_16x16x32_bf16 v[128:131], a[128:131], v[52:55], v[128:131]\n\t"
      "s_add_u32 m0, s45, 0x3000\n\t"
      "v_mfma_f32_16x16x32_bf16 v[128:131], a[152:155], v[56:59], v[128:131]\n\t"
      "v_mfma_f32_16x16x32_bf16 v[128:131], a[176:179], v[60:63], v[128:131]\n\t"
      "ds_read_b128 v[96:99], v197 offset:12544\n\t"
      "v_mfma_f32_16x16x32_bf16 v[128:131], a[200:203], v[64:67], v[128:131]\n\t"
      "v_mfma_f32_16x16x32_bf16 v[128:131], a[224:227], v[68:71], v[128:131]\n\t"
      "ds_read_b128 v[100:103], v198 offset:12544\n\t"
      "v_mfma_f32_16x16x32_bf16 v[128:131], a[248:251], v[72:75], v[128:131]\n\t"
      "v_mfma_f32_16x16x32_bf16 v[128:131], v[180:183], v[76:79], v[128:131]\n\t"
      "buffer_load_dwordx4 v209, s[20:23], s73 offen lds\n\t"
      "v_mfma_f32_16x16x32_bf16 v[132:135], a[12:15], v[32:35], v[20:23]\n\t"
      "ds_read_b128 v[104:107], v199 offset:12544\n\t"
      "v_mfma_f32_16x16x32_bf16 v[132:135], a[36:39], v[36:39], v[132:135]\n\t"
      "v_mfma_f32_16x16x32_bf16 v[132:135], a[60:63], v[40:43], v[132:135]\n\t"
      "v_cvt_pk_bf16_f32 v136, v128, v129\n\t"
      "v_mfma_f32_16x16x32_bf16 v[132:135], a[84:87], v[44:47], v[132:135]\n\t"
      "v_cvt_pk_bf16_f32 v137, v130, v131\n\t"
      "ds_read_b128 v[144:147], v220 offset:13312\n\t"
      "v_mfma_f32_16x16x32_bf16 v[132:135], a[108:111], v[48:51], v[132:135]\n\t"
      "v_mfma_f32_16x16x32_bf16 v[132:135], a[132:135], v[52:55], v[132:135]\n\t"
      "v_mfma_f32_16x16x32_bf16 v[132:135], a[156:159], v[56:59], v[132:135]\n\t"
      "ds_read_b128 v[108:111], v196 offset:12544\n\t"
      "v_mfma_f32_16x16x32_bf16 v[132:135], a[180:183], v[60:63], v[132:135]\n\t"
      "v_mfma_f32_16x16x32_bf16 v[132:135], a[204:207], v[64:67], v[132:135]\n\t"
      "v_mfma_f32_16x16x32_bf16 v[132:135], a[228:231], v[68:71], v[132:135]\n\t"
      "ds_read_b128 v[112:115], v198 offset:12800\n\t"
      "v_mfma_f32_16x16x32_bf16 v[132:135], a[252:255], v[72:75], v[132:135]\n\t"
      "v_mfma_f32_16x16x32_bf16 v[132:135], v[184:187], v[76:79], v[132:135]\n\t"
      "s_waitcnt lgkmcnt(2)\n\t"
      "buffer_store_dwordx4 v[144:147], v209, s[28:31], s72 offen offset:1024 sc1\n\t"
      "v_mfma_f32_16x16x32_bf16 v[128:131], a[16:19], v[32:35], v[24:27]\n\t"
      "ds_read_b128 v[116:119], v199 offset:12800\n\t"
      "v_mfma_f32_16x16x32_bf16 v[128:131], a[40:43], v[36:39], v[128:131]\n\t"
      "v_mfma_f32_16x16x32_bf16 v[128:131], a[64:67], v[40:43], v[128:131]\n\t"
      "v_cvt_pk_bf16_f32 v138, v132, v133\n\t"
      "v_mfma_f32_16x16x32_bf16 v[128:131], a[88:91], v[44:47], v[128:131]\n\t"
      "v_cvt_pk_bf16_f32 v139, v134, v135\n\t"
      "v_mfma_f32_16x16x32_bf16 v[128:131], a[112:115], v[48:51], v[128:131]\n\t"
      "ds_write_b128 v218, v[136:139] offset:0\n\t"
      "v_mfma_f32_16x16x32_bf16 v[128:131], a[136:139], v[52:55], v[128:131]\n\t"
      "v_mfma_f32_16x16x32_bf16 v[128:131], a[160:163], v[56:59], v[128:131]\n\t"
      "v_mfma_f32_16x16x32_bf16 v[128:131], a[184:187], v[60:63], v[128:131]\n\t"
      "ds_read_b128 v[148:151], v220 offset:14336\n\t"
      "v_mfma_f32_16x16x32_bf16 v[128:131], a[208:211], v[64:67], v[128:131]\n\t"
      "v_mfma_f32_16x16x32_bf16 v[128:131], a[232:235], v[68:71], v[128:131]\n\t"
      "ds_read_b128 v[120:123], v196 offset:12800\n\t"
      "v_mfma_f32_16x16x32_bf16 v[128:131], v[164:167], v[72:75], v[128:131]\n\t"
      "v_mfma_f32_16x16x32_bf16 v[128:131], v[188:191], v[76:79], v[128:131]\n\t"
      "buffer_load_dwordx4 v209, s[20:23], s73 offen offset:1024 lds\n\t"
      "v_mfma_f32_16x16x32_bf16 v[132:135], a[20:23], v[32:35], v[28:31]\n\t"
      "ds_read_b128 v[124:127], v197 offset:12800\n\t"
      "v_mfma_f32_16x16x32_bf16 v[132:135], a[44:47], v[36:39], v[132:135]\n\t"
      "v_mfma_f32_16x16x32_bf16 v[132:135], a[68:71], v[40:43], v[132:135]\n\t"
      "v_cvt_pk_bf16_f32 v136, v128, v129\n\t"
      "v_add_u32_e32 v220, s46, v207\n\t"
      "v_mfma_f32_16x16x32_bf16 v[132:135], a[92:95], v[44:47], v[132:135]\n\t"
      "v_cvt_pk_bf16_f32 v137, v130, v131\n\t"
      "s_sub_u32 s84, s84, 4\n\t"
      "s_max_i32 s84, s84, s86\n\t"
      "v_mfma_f32_16x16x32_bf16 v[132:135], a[116:119], v[48:51], v[132:135]\n\t"
      "s_sub_u32 s83, s82, s80\n\t"
      "v_mfma_f32_16x16x32_bf16 v[132:135], a[140:143], v[52:55], v[132:135]\n\t"
      "s_max_i32 s83, s83, 0\n\t"
      "s_add_u32 s82, s80, s83\n\t"
      "v_mfma_f32_16x16x32_bf16 v[132:135], a[164:167], v[56:59], v[132:135]\n\t"
      "v_mfma_f32_16x16x32_bf16 v[132:135], a[188:191], v[60:63], v[132:135]\n\t"
      "v_mfma_f32_16x16x32_bf16 v[132:135], a[212:215], v[64:67], v[132:135]\n\t"
      "s_add_u32 s44, s44, 1\n\t"
      "v_mfma_f32_16x16x32_bf16 v[132:135], a[236:239], v[68:71], v[132:135]\n\t"
      "s_add_u32 s82, s82, s84\n\t"
      "v_mfma_f32_16x16x32_bf16 v[132:135], v[168:171], v[72:75], v[132:135]\n\t"
      "s_add_u32 s85, s85, 1\n\t"
      "v_mfma_f32_16x16x32_bf16 v[132:135], v[192:195], v[76:79], v[132:135]\n\t"
      "s_waitcnt lgkmcnt(2)\n\t"
      "buffer_store_dwordx4 v[148:151], v209, s[28:31], s72 offen offset:2048 sc1\n\t"
      "s_waitcnt lgkmcnt(12)\n\t"
      "v_mfma_f32_16x16x32_bf16 v[128:131], a[0:3], v[80:83], v[8:11]\n\t"
      "v_mfma_f32_16x16x32_bf16 v[128:131], a[24:27], v[84:87], v[128:131]\n\t"
      "v_readlane_b32 s72, v248, s44\n\t"
      "v_mfma_f32_16x16x32_bf16 v[128:131], a[48:51], v[88:91], v[128:131]\n\t"
      "v_cvt_pk_bf16_f32 v138, v132, v133\n\t"
      "v_mfma_f32_16x16x32_bf16 v[128:131], a[72:75], v[92:95], v[128:131]\n\t"
      "v_cvt_pk_bf16_f32 v139, v134, v135\n\t"
      "s_waitcnt lgkmcnt(6)\n\t"
      "v_mfma_f32_16x16x32_bf16 v[128:131], a[96:99], v[96:99], v[128:131]\n\t"
      "ds_write_b128 v219, v[136:139] offset:0\n\t"
      "v_mfma_f32_16x16x32_bf16 v[128:131], a[120:123], v[100:103], v[128:131]\n\t"
      "v_mfma_f32_16x16x32_bf16 v[128:131], a[144:147], v[104:107], v[128:131]\n\t"
      "buffer_load_dwordx4 v209, s[20:23], s73 offen offset:2048 lds\n\t"
      "v_mfma_f32_16x16x32_bf16 v[128:131], a[168:171], v[108:111], v[128:131]\n\t"
      "v_add_u32_e32 v196, s47, v200\n\t"
      "s_waitcnt lgkmcnt(1)\n\t"
      "v_mfma_f32_16x16x32_bf16 v[128:131], a[192:195], v[112:115], v[128:131]\n\t"
      "v_add_u32_e32 v197, s47, v201\n\t"
      "v_mfma_f32_16x16x32_bf16 v[128:131], a[216:219], v[116:119], v[128:131]\n\t"
      "v_add_u32_e32 v198, s47, v202\n\t"
      "v_mfma_f32_16x16x32_bf16 v[128:131], a[240:243], v[120:123], v[128:131]\n\t"
      "v_add_u32_e32 v199, s47, v203\n\t"
      "v_mfma_f32_16x16x32_bf16 v[128:131], v[172:175], v[124:127], v[128:131]\n\t"
      "s_cmp_gt_i32 s83, 1\n\t"
      "s_cbranch_scc1 .LslpB_%=\n\t"
      ".LretB_%=:\n\t"
      "s_waitcnt vmcnt(18) lgkmcnt(0)\n\t"
      "s_barrier\n\t"
      "v_mfma_f32_16x16x32_bf16 v[132:135], a[4:7], v[80:83], v[12:15]\n\t"
      "ds_read_b128 v[140:143], v220 offset:0\n\t"
      "v_mfma_f32_16x16x32_bf16 v[132:135], a[28:31], v[84:87], v[132:135]\n\t"
      "v_mfma_f32_16x16x32_bf16 v[132:135], a[52:55], v[88:91], v[132:135]\n\t"
      "v_cvt_pk_bf16_f32 v136, v128, v129\n\t"
      "v_mfma_f32_16x16x32_bf16 v[132:135], a[76:79], v[92:95], v[132:135]\n\t"
      "ds_read_b128 v[32:35], v196 offset:0\n\t"
      "v_cvt_pk_bf16_f32 v137, v130, v131\n\t"
      "v_mfma_f32_16x16x32_bf16 v[132:135], a[100:103], v[96:99], v[132:135]\n\t"
      "v_mfma_f32_16x16x32_bf16 v[132:135], a[124:127], v[100:103], v[132:135]\n\t"
      "v_mfma_f32_16x16x32_bf16 v[132:135], a[148:151], v[104:107], v[132:135]\n\t"
      "ds_read_b128 v[36:39], v197 offset:0\n\t"
      "v_mfma_f32_16x16x32_bf16 v[132:135], a[172:175], v[108:111], v[132:135]\n\t"
      "v_readlane_b32 s73, v248, s85\n\t"
      "v_mfma_f32_16x16x32_bf16 v[132:135], a[196:199], v[112:115], v[132:135]\n\t"
      "v_mfma_f32_16x16x32_bf16 v[132:135], a[220:223], v[116:119], v[132:135]\n\t"
      "ds_read_b128 v[40:43], v198 offset:0\n\t"
      "v_mfma_f32_16x16x32_bf16 v[132:135], a[244:247], v[120:123], v[132:135]\n\t"
      "v_mfma_f32_16x16x32_bf16 v[132:135], v[176:179], v[124:127], v[132:135]\n\t"
      "s_waitcnt lgkmcnt(3)\n\t"
      "buffer_store_dwordx4 v[140:143], v208, s[28:31], s72 offen sc1\n\t"
      "v_mfma_f32_16x16x32_bf16 v[128:131], a[8:11], v[80:83], v[16:19]\n\t"
      "ds_read_b128 v[44:47], v199 offset:0\n\t"
      "v_mfma_f32_16x16x32_bf16 v[128:131], a[32:35], v[84:87], v[128:131]\n\t"
      "s_memrealtime s[80:81]\n\t"
      "v_mfma_f32_16x16x32_bf16 v[128:131], a[56:59], v[88:91], v[128:131]\n\t"
      "v_cvt_pk_bf16_f32 v138, v132, v133\n\t"
      "v_mfma_f32_16x16x32_bf16 v[128:131], a[80:83], v[92:95], v[128:131]\n\t"
      "v_cvt_pk_bf16_f32 v139, v134, v135\n\t"
      "v_mfma_f32_16x16x32_bf16 v[128:131], a[104:107], v[96:99], v[128:131]\n\t"
      "ds_write_b128 v217, v[136:139] offset:12288\n\t"
      "v_mfma_f32_16x16x32_bf16 v[128:131], a[128:131], v[100:103], v[128:131]\n\t"
      "s_mov_b32 m0, s46\n\t"
      "v_mfma_f32_16x16x32_bf16 v[128:131], a[152:155], v[104:107], v[128:131]\n\t"
      "v_mfma_f32_16x16x32_bf16 v[128:131], a[176:179], v[108:111], v[128:131]\n\t"
      "ds_read_b128 v[48:51], v197 offset:256\n\t"
      "v_mfma_f32_16x16x32_bf16 v[128:131], a[200:203], v[112:115], v[128:131]\n\t"
      "v_mfma_f32_16x16x32_bf16 v[128:131], a[224:227], v[116:119], v[128:131]\n\t"
      "ds_read_b128 v[52:55], v198 offset:256\n\t"
      "v_mfma_f32_16x16x32_bf16 v[128:131], a[248:251], v[120:123], v[128:131]\n\t"
      "v_mfma_f32_16x16x32_bf16 v[128:131], v[180:183], v[124:127], v[128:131]\n\t"
      "buffer_load_dwordx4 v208, s[20:23], s73 offen lds\n\t"
      "v_mfma_f32_16x16x32_bf16 v[132:135], a[12:15], v[80:83], v[20:23]\n\t"
      "ds_read_b128 v[56:59], v199 offset:256\n\t"
      "v_mfma_f32_16x16x32_bf16 v[132:135], a[36:39], v[84:87], v[132:135]\n\t"
      "v_mfma_f32_16x16x32_bf16 v[132:135], a[60:63], v[88:91], v[132:135]\n\t"
      "v_cvt_pk_bf16_f32 v136, v128, v129\n\t"
      "v_mfma_f32_16x16x32_bf16 v[132:135], a[84:87], v[92:95], v[132:135]\n\t"
      "v_cvt_pk_bf16_f32 v137, v130, v131\n\t"
      "ds_read_b128 v[144:147], v220 offset:1024\n\t"
      "v_mfma_f32_16x16x32_bf16 v[132:135], a[108:111], v[96:99], v[132:135]\n\t"
      "v_mfma_f32_16x16x32_bf16 v[132:135], a[132:135], v[100:103], v[132:135]\n\t"
      "v_mfma_f32_16x16x32_bf16 v[132:135], a[156:159], v[104:107], v[132:135]\n\t"
      "ds_read_b128 v[60:63], v196 offset:256\n\t"
      "v_mfma_f32_16x16x32_bf16 v[132:135], a[180:183], v[108:111], v[132:135]\n\t"
      "v_mfma_f32_16x16x32_bf16 v[132:135], a[204:207], v[112:115], v[132:135]\n\t"
      "v_mfma_f32_16x16x32_bf16 v[132:135], a[228:231], v[116:119], v[132:135]\n\t"
      "ds_read_b128 v[64:67], v198 offset:512\n\t"
      "v_mfma_f32_16x16x32_bf16 v[132:135], a[252:255], v[120:123], v[132:135]\n\t"
      "v_mfma_f32_16x16x32_bf16 v[132:135], v[184:187], v[124:127], v[132:135]\n\t"
      "s_waitcnt lgkmcnt(2)\n\t"
      "buffer_store_dwordx4 v[144:147], v208, s[28:31], s72 offen offset:1024 sc1\n\t"
      "v_mfma_f32_16x16x32_bf16 v[128:131], a[16:19], v[80:83], v[24:27]\n\t"
      "ds_read_b128 v[68:71], v199 offset:512\n\t"
      "v_mfma_f32_16x16x32_bf16 v[128:131], a[40:43], v[84:87], v[128:131]\n\t"
      "v_readlane_b32 s86, v250, s44\n\t"
      "v_mfma_f32_16x16x32_bf16 v[128:131], a[64:67], v[88:91], v[128:131]\n\t"
      "v_cvt_pk_bf16_f32 v138, v132, v133\n\t"
      "v_mfma_f32_16x16x32_bf16 v[128:131], a[88:91], v[92:95], v[128:131]\n\t"
      "v_cvt_pk_bf16_f32 v139, v134, v135\n\t"
      "v_mfma_f32_16x16x32_bf16 v[128:131], a[112:115], v[96:99], v[128:131]\n\t"
      "ds_write_b128 v218, v[136:139] offset:12288\n\t"
      "v_mfma_f32_16x16x32_bf16 v[128:131], a[136:139], v[100:103], v[128:131]\n\t"
      "v_mfma_f32_16x16x32_bf16 v[128:131], a[160:163], v[104:107], v[128:131]\n\t"
      "v_mfma_f32_16x16x32_bf16 v[128:131], a[184:187], v[108:111], v[128:131]\n\t"
      "ds_read_b128 v[148:151], v220 offset:2048\n\t"
      "v_mfma_f32_16x16x32_bf16 v[128:131], a[208:211], v[112:115], v[128:131]\n\t"
      "v_mfma_f32_16x16x32_bf16 v[128:131], a[232:235], v[116:119], v[128:131]\n\t"
      "ds_read_b128 v[72:75], v196 offset:512\n\t"
      "v_mfma_f32_16x16x32_bf16 v[128:131], v[164:167], v[120:123], v[128:131]\n\t"
      "v_mfma_f32_16x16x32_bf16 v[128:131], v[188:191], v[124:127], v[128:131]\n\t"
      "buffer_load_dwordx4 v208, s[20:23], s73 offen offset:1024 lds\n\t"
      "v_mfma_f32_16x16x32_bf16 v[132:135], a[20:23], v[80:83], v[28:31]\n\t"
      "ds_read_b128 v[76:79], v197 offset:512\n\t"
      "v_mfma_f32_16x16x32_bf16 v[132:135], a[44:47], v[84:87], v[132:135]\n\t"
      "v_mfma_f32_16x16x32_bf16 v[132:135], a[68:71], v[88:91], v[132:135]\n\t"
      "v_cvt_pk_bf16_f32 v136, v128, v129\n\t"
      "v_mfma_f32_16x16x32_bf16 v[132:135], a[92:95], v[92:95], v[132:135]\n\t"
      "v_cvt_pk_bf16_f32 v137, v130, v131\n\t"
      "s_sub_u32 s84, s84, 4\n\t"
      "s_max_i32 s84, s84, s86\n\t"
      "v_mfma_f32_16x16x32_bf16 v[132:135], a[116:119], v[96:99], v[132:135]\n\t"
      "s_sub_u32 s83, s82, s80\n\t"
      "v_mfma_f32_16x16x32_bf16 v[132:135], a[140:143], v[100:103], v[132:135]\n\t"
      "s_max_i32 s83, s83, 0\n\t"
      "s_add_u32 s82, s80, s83\n\t"
      "v_mfma_f32_16x16x32_bf16 v[132:135], a[164:167], v[104:107], v[132:135]\n\t"
      "s_mov_b32 s45, s46\n\t"
      "v_mfma_f32_16x16x32_bf16 v[132:135], a[188:191], v[108:111], v[132:135]\n\t"
      "s_mov_b32 s46, s47\n\t"
      "v_mfma_f32_16x16x32_bf16 v[132:135], a[212:215], v[112:115], v[132:135]\n\t"
      "s_mov_b32 s47, s48\n\t"
      "v_mfma_f32_16x16x32_bf16 v[132:135], a[236:239], v[116:119], v[132:135]\n\t"
      "s_add_u32 s82, s82, s84\n\t"
      "v_mfma_f32_16x16x32_bf16 v[132:135], v[168:171], v[120:123], v[132:135]\n\t"
      "s_cmp_lg_u32 s44, s49\n\t"
      "v_mfma_f32_16x16x32_bf16 v[132:135], v[192:195], v[124:127], v[132:135]\n\t"
      "s_waitcnt lgkmcnt(2)\n\t"
      "buffer_store_dwordx4 v[148:151], v208, s[28:31], s72 offen offset:2048 sc1\n\t"
      "s_cbranch_scc1 .Lloop_%=\n\t"
      "s_cmp_lg_u32 s44, 17\n\t"
      "s_cbranch_scc1 .Lstop_%=\n\t"
      "v_readlane_b32 s72, v249, s44\n\t"
      "v_readlane_b32 s76, v254, 1\n\t"
      "v_readlane_b32 s78, v255, 1\n\t"
      "s_nop 1\n\t"
      "v_cvt_pk_bf16_f32 v138, v132, v133\n\t"
      "v_cvt_pk_bf16_f32 v139, v134, v135\n\t"
      "ds_write_b128 v219, v[136:139] offset:12288\n\t"
      "s_cmp_eq_u32 s41, s43\n\t"
      "s_cbranch_scc0 .Lc5_%=\n\t"
      "s_andn2_b32 s76, 4, s76\n\t"
      "s_or_b32 s76, s76, s64\n\t"
      "s_xor_b32 s76, s76, s78\n\t"
      "v_mov_b32_e32 v240, s76\n\t"
      "ds_write_b32 v247, v240\n\t"
      ".Lc5_%=:\n\t"
      "s_waitcnt lgkmcnt(0)\n\t"
      "s_barrier\n\t"
      "ds_read_b128 v[32:35], v220 offset:12288\n\t"
      "ds_read_b128 v[36:39], v220 offset:13312\n\t"
      "ds_read_b128 v[40:43], v220 offset:14336\n\t"
      "ds_read_b32 v241, v247\n\t"
      "s_waitcnt lgkmcnt(3)\n\t"
      "buffer_store_dwordx4 v[32:35], v209, s[28:31], s72 offen sc1\n\t"
      "s_waitcnt lgkmcnt(2)\n\t"
      "buffer_store_dwordx4 v[36:39], v209, s[28:31], s72 offen offset:1024 sc1\n\t"
      "s_waitcnt lgkmcnt(1)\n\t"
      "buffer_store_dwordx4 v[40:43], v209, s[28:31], s72 offen offset:2048 sc1\n\t"
      "s_waitcnt lgkmcnt(0)\n\t"
      "v_cmp_ne_u32_e32 vcc, 0, v241\n\t"
      "s_cmp_lg_u32 s62, 0\n\t"
      "s_cbranch_scc1 .Lfin_%=\n\t"
      "s_cbranch_vccnz .Lsec_%=\n\t"
      ".Lfin_%=:\n\t"
      "s_endpgm\n\t"
      ".Lsec_%=:\n\t"
      "s_mov_b32 s62, 1\n\t"
      "v_lshl_add_u32 v240, v251, 1, s66\n\t"
      "v_sub_u32_e32 v248, v240, v248\n\t"
      "v_sub_u32_e32 v249, v240, v249\n\t"
      "v_cmp_eq_u32_e32 vcc, 0, v252\n\t"
      "v_mov_b32_e32 v240, 0x6c00000\n\t"
      "s_nop 1\n\t"
      "v_cndmask_b32_e32 v248, v248, v240, vcc\n\t"
      "v_cndmask_b32_e32 v249, v249, v240, vcc\n\t"
      "s_add_u32 s44, s94, 3\n\t"
      "s_add_u32 s85, s44, 1\n\t"
      "s_mov_b32 s83, 0\n\t"
      "s_waitcnt vmcnt(0) lgkmcnt(0)\n\t"
      "s_barrier\n\t"
      "v_readlane_b32 s73, v248, s85\n\t"
      "s_mov_b32 m0, s46\n\t"
      "s_nop 4\n\t"
      "buffer_load_dwordx4 v208, s[20:23], s73 offen lds\n\t"
      "buffer_load_dwordx4 v208, s[20:23], s73 offen offset:1024 lds\n\t"
      "buffer_load_dwordx4 v208, s[20:23], s73 offen offset:2048 lds\n\t"
      "v_readlane_b32 s73, v249, s85\n\t"
      "s_add_u32 m0, s46, 0x3000\n\t"
      "s_nop 4\n\t"
      "buffer_load_dwordx4 v209, s[20:23], s73 offen lds\n\t"
      "buffer_load_dwordx4 v209, s[20:23], s73 offen offset:1024 lds\n\t"
      "buffer_load_dwordx4 v209, s[20:23], s73 offen offset:2048 lds\n\t"
      "s_add_u32 s85, s85, 1\n\t"
      "s_nop 4\n\t"
      "v_readlane_b32 s73, v248, s85\n\t"
      "s_mov_b32 m0, s47\n\t"
      "s_nop 4\n\t"
      "buffer_load_dwordx4 v208, s[20:23], s73 offen lds\n\t"
      "buffer_load_dwordx4 v208, s[20:23], s73 offen offset:1024 lds\n\t"
      "buffer_load_dwordx4 v208, s[20:23], s73 offen offset:2048 lds\n\t"
      "v_readlane_b32 s73, v249, s85\n\t"
      "s_add_u32 m0, s47, 0x3000\n\t"
      "s_nop 4\n\t"
      "buffer_load_dwordx4 v209, s[20:23], s73 offen lds\n\t"
      "buffer_load_dwordx4 v209, s[20:23], s73 offen offset:1024 lds\n\t"
      "buffer_load_dwordx4 v209, s[20:23], s73 offen offset:2048 lds\n\t"
      "s_add_u32 s85, s85, 1\n\t"
      "s_nop 4\n\t"
      "v_readlane_b32 s73, v248, s85\n\t"
      "s_mov_b32 m0, s45\n\t"
      "s_nop 4\n\t"
      "buffer_load_dwordx4 v208, s[20:23], s73 offen lds\n\t"
      "buffer_load_dwordx4 v208, s[20:23], s73 offen offset:1024 lds\n\t"
      "buffer_load_dwordx4 v208, s[20:23], s73 offen offset:2048 lds\n\t"
      "s_waitcnt vmcnt(0)\n\t"
      "s_barrier\n\t"
      "ds_read_b128 v[32:35], v196 offset:0\n\t"
      "ds_read_b128 v[36:39], v197 offset:0\n\t"
      "ds_read_b128 v[40:43], v198 offset:0\n\t"
      "ds_read_b128 v[44:47], v199 offset:0\n\t"
      "ds_read_b128 v[48:51], v197 offset:256\n\t"
      "ds_read_b128 v[52:55], v198 offset:256\n\t"
      "ds_read_b128 v[56:59], v199 offset:256\n\t"
      "ds_read_b128 v[60:63], v196 offset:256\n\t"
      "ds_read_b128 v[64:67], v198 offset:512\n\t"
      "ds_read_b128 v[68:71], v199 offset:512\n\t"
      "ds_read_b128 v[72:75], v196 offset:512\n\t"
      "ds_read_b128 v[76:79], v197 offset:512\n\t"
      "s_waitcnt lgkmcnt(0)\n\t"
      "s_branch .Lloop_%=\n\t"
      ".Lstop_%=:\n\t"
      "s_mov_b32 s70, 0\n\t"
      "s_add_u32 s49, s89, 10\n\t"
      "s_cmp_lg_u32 s44, s49\n\t"
      "s_cselect_b32 s49, s49, 17\n\t"
      "s_cbranch_scc0 .Lstop2_%=\n\t"
      "s_cmp_eq_u32 s94, 0\n\t"
      "s_cbranch_scc1 .Llt0_%=\n\t"
      "ds_read_b128 v[226:229], v238 offset:24576\n\t"
      "ds_read_b128 v[230:233], v238 offset:25600\n\t"
      "ds_read_b128 v[234:237], v238 offset:26624\n\t"
      "v_readlane_b32 s68, v248, 0\n\t"
      "s_nop 4\n\t"
      "s_waitcnt lgkmcnt(2)\n\t"
      "buffer_store_dwordx4 v[226:229], v208, s[28:31], s68 offen sc1\n\t"
      "s_waitcnt lgkmcnt(1)\n\t"
      "buffer_store_dwordx4 v[230:233], v208, s[28:31], s68 offen offset:1024 sc1\n\t"
      "s_waitcnt lgkmcnt(0)\n\t"
      "buffer_store_dwordx4 v[234:237], v208, s[28:31], s68 offen offset:2048 sc1\n\t"
      ".Llt0_%=:\n\t"
      "v_readlane_b32 s76, v254, 0\n\t"
      "s_cmp_lg_u32 s41, s43\n\t"
      "s_cbranch_scc1 .Lloop_%=\n\t"
      "s_andn2_b32 s76, 4, s76\n\t"
      "s_or_b32 s76, s76, s64\n\t"
      "v_mov_b32_e32 v240, s76\n\t"
      "s_mov_b64 exec, 1\n\t"
      "buffer_store_dword v240, v253, s[24:27], 0 offen sc1\n\t"
      "s_mov_b64 exec, -1\n\t"
      "s_branch .Lloop_%=\n\t"
      ".Lstop2_%=:\n\t"
      "s_cmp_eq_u32 s94, 0\n\t"
      "s_cbranch_scc1 .Llt1_%=\n\t"
      "ds_read_b128 v[226:229], v238 offset:36864\n\t"
      "ds_read_b128 v[230:233], v238 offset:37888\n\t"
      "ds_read_b128 v[234:237], v238 offset:38912\n\t"
      "v_readlane_b32 s68, v249, 0\n\t"
      "s_nop 4\n\t"
      "s_waitcnt lgkmcnt(2)\n\t"
      "buffer_store_dwordx4 v[226:229], v209, s[28:31], s68 offen sc1\n\t"
      "s_waitcnt lgkmcnt(1)\n\t"
      "buffer_store_dwordx4 v[230:233], v209, s[28:31], s68 offen offset:1024 sc1\n\t"
      "s_waitcnt lgkmcnt(0)\n\t"
      "buffer_store_dwordx4 v[234:237], v209, s[28:31], s68 offen offset:2048 sc1\n\t"
      ".Llt1_%=:\n\t"
      "s_cmp_lg_u32 s41, s43\n\t"
      "s_cbranch_scc1 .Lloop_%=\n\t"
      "buffer_load_dword v255, v253, s[24:27], 0 offen sc1\n\t"
      "s_branch .Lloop_%=\n\t"
      ".LslpP3_%=:\n\t"
      "s_sleep 1\n\t"
      "s_sub_u32 s83, s83, 3\n\t"
      "s_cmp_gt_i32 s83, 1\n\t"
      "s_cbranch_scc1 .LslpP3_%=\n\t"
      "s_branch .LretP3_%=\n\t"
      ".LslpP4_%=:\n\t"
      "s_sleep 1\n\t"
      "s_sub_u32 s83, s83, 3\n\t"
      "s_cmp_gt_i32 s83, 1\n\t"
      "s_cbranch_scc1 .LslpP4_%=\n\t"
      "s_branch .LretP4_%=\n\t"
      ".LslpP5_%=:\n\t"
      "s_sleep 1\n\t"
      "s_sub_u32 s83, s83, 3\n\t"
      "s_cmp_gt_i32 s83, 1\n\t"
      "s_cbranch_scc1 .LslpP5_%=\n\t"
      "s_branch .LretP5_%=\n\t"
      ".LslpA_%=:\n\t"
      "s_sleep 1\n\t"
      "s_sub_u32 s83, s83, 3\n\t"
      "s_cmp_gt_i32 s83, 1\n\t"
      "s_cbranch_scc1 .LslpA_%=\n\t"
      "s_branch .LretA_%=\n\t"
      ".LslpB_%=:\n\t"
      "s_sleep 1\n\t"
      "s_sub_u32 s83, s83, 3\n\t"
      "s_cmp_gt_i32 s83, 1\n\t"
      "s_cbranch_scc1 .LslpB_%=\n\t"
      "s_branch .LretB_%=\n\t"
      ".Lgs_%=:\n\t"
      "s_sleep 8\n\t"
      "buffer_load_dwordx2 v[240:241], v245, s[24:27], 0 offen sc1\n\t"
      "s_memrealtime s[76:77]\n\t"
      "s_waitcnt vmcnt(0) lgkmcnt(0)\n\t"
      "v_subrev_u32_e32 v242, s63, v240\n\t"
      "v_xor_b32_e32 v241, s53, v241\n\t"
      "v_lshrrev_b32_e32 v240, 13, v242\n\t"
      "v_or_b32_e32 v240, v240, v241\n\t"
      "v_cmp_ne_u32_e32 vcc, 0, v240\n\t"
      "v_and_b32_e32 v242, 15, v242\n\t"
      "s_cbranch_vccz .Lgr_%=\n\t"
      "s_sub_u32 s76, s76, s52\n\t"
      "s_cmp_lt_u32 s76, 2000\n\t"
      "s_cbranch_scc1 .Lgs_%=\n\t"
      "v_mov_b32_e32 v242, 64\n\t"
      "s_branch .Lgr_%=\n\t"
      ".Lpb_%=:\n\t"
      "s_mov_b64 vcc, -1\n\t"
      "s_branch .Lp2_%=\n\t"
      ".Lwsb_%=:\n\t"
      "s_mov_b64 vcc, 0\n\t"
      "s_waitcnt vmcnt(12)\n\t"
      "v_lshlrev_b32_e32 v8, 16, v226\n\t"
      "v_and_b32_e32 v9, 0xffff0000, v226\n\t"
      "v_lshlrev_b32_e32 v10, 16, v227\n\t"
      "v_and_b32_e32 v11, 0xffff0000, v227\n\t"
      "v_lshlrev_b32_e32 v12, 16, v228\n\t"
      "v_and_b32_e32 v13, 0xffff0000, v228\n\t"
      "v_lshlrev_b32_e32 v14, 16, v229\n\t"
      "v_and_b32_e32 v15, 0xffff0000, v229\n\t"
      "v_lshlrev_b32_e32 v16, 16, v230\n\t"
      "v_and_b32_e32 v17, 0xffff0000, v230\n\t"
      "v_lshlrev_b32_e32 v18, 16, v231\n\t"
      "v_and_b32_e32 v19, 0xffff0000, v231\n\t"
      "v_lshlrev_b32_e32 v20, 16, v232\n\t"
      "v_and_b32_e32 v21, 0xffff0000, v232\n\t"
      "v_lshlrev_b32_e32 v22, 16, v233\n\t"
      "v_and_b32_e32 v23, 0xffff0000, v233\n\t"
      "v_lshlrev_b32_e32 v24, 16, v234\n\t"
      "v_and_b32_e32 v25, 0xffff0000, v234\n\t"
      "v_lshlrev_b32_e32 v26, 16, v235\n\t"
      "v_and_b32_e32 v27, 0xffff0000, v235\n\t"
      "v_lshlrev_b32_e32 v28, 16, v236\n\t"
      "v_and_b32_e32 v29, 0xffff0000, v236\n\t"
      "v_lshlrev_b32_e32 v30, 16, v237\n\t"
      "v_and_b32_e32 v31, 0xffff0000, v237\n\t"
      "s_add_u32 m0, s41, 0x1e000\n\t"
      "s_mov_b32 s51, 0x0\n\t"
      "buffer_load_dwordx4 v211, s[16:19], s51 offen lds\n\t"
      "buffer_load_dwordx4 v212, s[16:19], s51 offen offset:1024 lds\n\t"
      "buffer_load_dwordx4 v213, s[16:19], s51 offen offset:2048 lds\n\t"
      "s_add_u32 m0, s41, 0x21000\n\t"
      "s_mov_b32 s51, 0xc00\n\t"
      "buffer_load_dwordx4 v211, s[16:19], s51 offen lds\n\t"
      "buffer_load_dwordx4 v212, s[16:19], s51 offen offset:1024 lds\n\t"
      "buffer_load_dwordx4 v213, s[16:19], s51 offen offset:2048 lds\n\t"
      "s_add_u32 m0, s41, 0x24000\n\t"
      "s_mov_b32 s51, 0x6000\n\t"
      "buffer_load_dwordx4 v211, s[16:19], s51 offen lds\n\t"
      "buffer_load_dwordx4 v212, s[16:19], s51 offen offset:1024 lds\n\t"
      "buffer_load_dwordx4 v213, s[16:19], s51 offen offset:2048 lds\n\t"
      "s_add_u32 m0, s41, 0x12000\n\t"
      "s_mov_b32 s51, 0x6c00\n\t"
      "buffer_load_dwordx4 v211, s[16:19], s51 offen lds\n\t"
      "buffer_load_dwordx4 v212, s[16:19], s51 offen offset:1024 lds\n\t"
      "buffer_load_dwordx4 v213, s[16:19], s51 offen offset:2048 lds\n\t"
      "s_add_u32 m0, s41, 0x15000\n\t"
      "s_mov_b32 s51, 0xc000\n\t"
      "buffer_load_dwordx4 v211, s[16:19], s51 offen lds\n\t"
      "buffer_load_dwordx4 v212, s[16:19], s51 offen offset:1024 lds\n\t"
      "buffer_load_dwordx4 v213, s[16:19], s51 offen offset:2048 lds\n\t"
      "s_mov_b32 s44, 1\n\t"
      "s_mov_b32 s85, 4\n\t"
      "s_add_u32 s45, s41, 0x6000\n\t"
      "s_add_u32 s46, s41, 0xc000\n\t"
      "s_mov_b32 s47, s41\n\t"
      "s_waitcnt vmcnt(12)\n\t"
      "s_barrier\n\t"
      "ds_read_b64_tr_b16 a[0:1], v214 offset:49152\n\t"
      "ds_read_b64_tr_b16 a[4:5], v214 offset:49160\n\t"
      "ds_read_b64_tr_b16 a[8:9], v214 offset:49216\n\t"
      "ds_read_b64_tr_b16 a[12:13], v214 offset:49224\n\t"
      "ds_read_b64_tr_b16 a[16:17], v214 offset:49280\n\t"
      "ds_read_b64_tr_b16 a[20:21], v214 offset:49288\n\t"
      "ds_read_b128 v[156:159], v196 offset:0\n\t"
      "ds_read_b128 v[160:163], v196 offset:12288\n\t"
      "ds_read_b128 v[128:131], v196 offset:24576\n\t"
      "s_add_u32 m0, s41, 0x18000\n\t"
      "s_mov_b32 s51, 0xcc00\n\t"
      "buffer_load_dwordx4 v211, s[16:19], s51 offen lds\n\t"
      "buffer_load_dwordx4 v212, s[16:19], s51 offen offset:1024 lds\n\t"
      "buffer_load_dwordx4 v213, s[16:19], s51 offen offset:2048 lds\n\t"
      "s_waitcnt vmcnt(12)\n\t"
      "ds_read_b64_tr_b16 a[2:3], v221 offset:0\n\t"
      "ds_read_b64_tr_b16 a[6:7], v221 offset:8\n\t"
      "ds_read_b64_tr_b16 a[10:11], v221 offset:64\n\t"
      "ds_read_b64_tr_b16 a[14:15], v221 offset:72\n\t"
      "ds_read_b64_tr_b16 a[18:19], v221 offset:128\n\t"
      "ds_read_b64_tr_b16 a[22:23], v221 offset:136\n\t"
      "s_add_u32 m0, s41, 0x1b000\n\t"
      "s_mov_b32 s51, 0x12000\n\t"
      "buffer_load_dwordx4 v211, s[16:19], s51 offen lds\n\t"
      "buffer_load_dwordx4 v212, s[16:19], s51 offen offset:1024 lds\n\t"
      "buffer_load_dwordx4 v213, s[16:19], s51 offen offset:2048 lds\n\t"
      "s_waitcnt lgkmcnt(0)\n\t"
      "v_mfma_f32_16x16x32_bf16 v[32:35], a[0:3], v[156:159], v[8:11]\n\t"
      "v_mfma_f32_16x16x32_bf16 v[36:39], a[4:7], v[156:159], v[12:15]\n\t"
      "s_waitcnt vmcnt(12)\n\t"
      "ds_read_b64_tr_b16 a[24:25], v221 offset:12288\n\t"
      "v_mfma_f32_16x16x32_bf16 v[40:43], a[8:11], v[156:159], v[16:19]\n\t"
      "ds_read_b64_tr_b16 a[28:29], v221 offset:12296\n\t"
      "v_mfma_f32_16x16x32_bf16 v[44:47], a[12:15], v[156:159], v[20:23]\n\t"
      "ds_read_b64_tr_b16 a[32:33], v221 offset:12352\n\t"
      "v_mfma_f32_16x16x32_bf16 v[48:51], a[16:19], v[156:159], v[24:27]\n\t"
      "ds_read_b64_tr_b16 a[36:37], v221 offset:12360\n\t"
      "ds_read_b128 v[136:139], v196 offset:36864\n\t"
      "v_mfma_f32_16x16x32_bf16 v[52:55], a[20:23], v[156:159], v[28:31]\n\t"
      "ds_read_b64_tr_b16 a[40:41], v221 offset:12416\n\t"
      "v_mfma_f32_16x16x32_bf16 v[56:59], a[0:3], v[160:163], v[8:11]\n\t"
      "ds_read_b64_tr_b16 a[44:45], v221 offset:12424\n\t"
      "v_mfma_f32_16x16x32_bf16 v[60:63], a[4:7], v[160:163], v[12:15]\n\t"
      "v_mfma_f32_16x16x32_bf16 v[64:67], a[8:11], v[160:163], v[16:19]\n\t"
      "s_waitcnt vmcnt(9)\n\t"
      "ds_read_b64_tr_b16 a[26:27], v214 offset:0\n\t"
      "v_mfma_f32_16x16x32_bf16 v[68:71], a[12:15], v[160:163], v[20:23]\n\t"
      "ds_read_b64_tr_b16 a[30:31], v214 offset:8\n\t"
      "v_mfma_f32_16x16x32_bf16 v[72:75], a[16:19], v[160:163], v[24:27]\n\t"
      "ds_read_b64_tr_b16 a[34:35], v214 offset:64\n\t"
      "ds_read_b128 v[140:143], v197 offset:0\n\t"
      "v_mfma_f32_16x16x32_bf16 v[76:79], a[20:23], v[160:163], v[28:31]\n\t"
      "ds_read_b64_tr_b16 a[38:39], v214 offset:72\n\t"
      "v_mfma_f32_16x16x32_bf16 v[80:83], a[0:3], v[128:131], v[8:11]\n\t"
      "ds_read_b64_tr_b16 a[42:43], v214 offset:128\n\t"
      "s_add_u32 m0, s41, 0x1e000\n\t"
      "s_mov_b32 s51, 0x12c00\n\t"
      "buffer_load_dwordx4 v211, s[16:19], s51 offen lds\n\t"
      "v_mfma_f32_16x16x32_bf16 v[84:87], a[4:7], v[128:131], v[12:15]\n\t"
      "ds_read_b64_tr_b16 a[46:47], v214 offset:136\n\t"
      "v_mfma_f32_16x16x32_bf16 v[88:91], a[8:11], v[128:131], v[16:19]\n\t"
      "v_mfma_f32_16x16x32_bf16 v[92:95], a[12:15], v[128:131], v[20:23]\n\t"
      "buffer_load_dwordx4 v212, s[16:19], s51 offen offset:1024 lds\n\t"
      "v_mfma_f32_16x16x32_bf16 v[96:99], a[16:19], v[128:131], v[24:27]\n\t"
      "ds_read_b128 v[144:147], v197 offset:12288\n\t"
      "v_mfma_f32_16x16x32_bf16 v[100:103], a[20:23], v[128:131], v[28:31]\n\t"
      "s_waitcnt lgkmcnt(10)\n\t"
      "v_mfma_f32_16x16x32_bf16 v[104:107], a[0:3], v[136:139], v[8:11]\n\t"
      "buffer_load_dwordx4 v213, s[16:19], s51 offen offset:2048 lds\n\t"
      "v_mfma_f32_16x16x32_bf16 v[108:111], a[4:7], v[136:139], v[12:15]\n\t"
      "v_mfma_f32_16x16x32_bf16 v[112:115], a[8:11], v[136:139], v[16:19]\n\t"
      "s_waitcnt vmcnt(9)\n\t"
      "ds_read_b64_tr_b16 a[48:49], v214 offset:12288\n\t"
      "v_mfma_f32_16x16x32_bf16 v[116:119], a[12:15], v[136:139], v[20:23]\n\t"
      "ds_read_b64_tr_b16 a[52:53], v214 offset:12296\n\t"
      "s_add_u32 m0, s41, 0x21000\n\t"
      "s_mov_b32 s51, 0x18000\n\t"
      "buffer_load_dwordx4 v211, s[16:19], s51 offen lds\n\t"
      "v_mfma_f32_16x16x32_bf16 v[120:123], a[16:19], v[136:139], v[24:27]\n\t"
      "ds_read_b64_tr_b16 a[56:57], v214 offset:12352\n\t"
      "ds_read_b128 v[148:151], v197 offset:24576\n\t"
      "v_mfma_f32_16x16x32_bf16 v[132:135], a[20:23], v[136:139], v[28:31]\n\t"
      "ds_read_b64_tr_b16 a[60:61], v214 offset:12360\n\t"
      "s_waitcnt lgkmcnt(6)\n\t"
      "v_mfma_f32_16x16x32_bf16 v[32:35], a[24:27], v[140:143], v[32:35]\n\t"
      "ds_read_b64_tr_b16 a[64:65], v214 offset:12416\n\t"
      "buffer_load_dwordx4 v212, s[16:19], s51 offen offset:1024 lds\n\t"
      "v_mfma_f32_16x16x32_bf16 v[36:39], a[28:31], v[140:143], v[36:39]\n\t"
      "ds_read_b64_tr_b16 a[68:69], v214 offset:12424\n\t"
      "v_mfma_f32_16x16x32_bf16 v[40:43], a[32:35], v[140:143], v[40:43]\n\t"
      "v_mfma_f32_16x16x32_bf16 v[44:47], a[36:39], v[140:143], v[44:47]\n\t"
      "buffer_load_dwordx4 v213, s[16:19], s51 offen offset:2048 lds\n\t"
      "v_mfma_f32_16x16x32_bf16 v[48:51], a[40:43], v[140:143], v[48:51]\n\t"
      "ds_read_b128 v[152:155], v197 offset:36864\n\t"
      "v_mfma_f32_16x16x32_bf16 v[52:55], a[44:47], v[140:143], v[52:55]\n\t"
      "s_waitcnt lgkmcnt(8)\n\t"
      "v_mfma_f32_16x16x32_bf16 v[56:59], a[24:27], v[144:147], v[56:59]\n\t"
      "s_add_u32 m0, s41, 0x24000\n\t"
      "s_mov_b32 s51, 0x18c00\n\t"
      "buffer_load_dwordx4 v211, s[16:19], s51 offen lds\n\t"
      "v_mfma_f32_16x16x32_bf16 v[60:63], a[28:31], v[144:147], v[60:63]\n\t"
      "v_mfma_f32_16x16x32_bf16 v[64:67], a[32:35], v[144:147], v[64:67]\n\t"
      "s_waitcnt vmcnt(10)\n\t"
      "ds_read_b64_tr_b16 a[50:51], v214 offset:24576\n\t"
      "v_mfma_f32_16x16x32_bf16 v[68:71], a[36:39], v[144:147], v[68:71]\n\t"
      "ds_read_b64_tr_b16 a[54:55], v214 offset:24584\n\t"
      "buffer_load_dwordx4 v212, s[16:19], s51 offen offset:1024 lds\n\t"
      "v_mfma_f32_16x16x32_bf16 v[72:75], a[40:43], v[144:147], v[72:75]\n\t"
      "ds_read_b64_tr_b16 a[58:59], v214 offset:24640\n\t"
      "ds_read_b128 v[156:159], v198 offset:0\n\t"
      "v_mfma_f32_16x16x32_bf16 v[76:79], a[44:47], v[144:147], v[76:79]\n\t"
      "ds_read_b64_tr_b16 a[62:63], v214 offset:24648\n\t"
      "s_waitcnt lgkmcnt(9)\n\t"
      "v_mfma_f32_16x16x32_bf16 v[80:83], a[24:27], v[148:151], v[80:83]\n\t"
      "ds_read_b64_tr_b16 a[66:67], v214 offset:24704\n\t"
      "buffer_load_dwordx4 v213, s[16:19], s51 offen offset:2048 lds\n\t"
      "v_mfma_f32_16x16x32_bf16 v[84:87], a[28:31], v[148:151], v[84:87]\n\t"
      "ds_read_b64_tr_b16 a[70:71], v214 offset:24712\n\t"
      "v_mfma_f32_16x16x32_bf16 v[88:91], a[32:35], v[148:151], v[88:91]\n\t"
      "v_mfma_f32_16x16x32_bf16 v[92:95], a[36:39], v[148:151], v[92:95]\n\t"
      "s_add_u32 m0, s41, 0x12000\n\t"
      "s_mov_b32 s51, 0x1e000\n\t"
      "buffer_load_dwordx4 v211, s[16:19], s51 offen lds\n\t"
      "v_mfma_f32_16x16x32_bf16 v[96:99], a[40:43], v[148:151], v[96:99]\n\t"
      "ds_read_b128 v[160:163], v198 offset:12288\n\t"
      "v_mfma_f32_16x16x32_bf16 v[100:103], a[44:47], v[148:151], v[100:103]\n\t"
      "s_waitcnt lgkmcnt(8)\n\t"
      "v_mfma_f32_16x16x32_bf16 v[104:107], a[24:27], v[152:155], v[104:107]\n\t"
      "buffer_load_dwordx4 v212, s[16:19], s51 offen offset:1024 lds\n\t"
      "v_mfma_f32_16x16x32_bf16 v[108:111], a[28:31], v[152:155], v[108:111]\n\t"
      "v_mfma_f32_16x16x32_bf16 v[112:115], a[32:35], v[152:155], v[112:115]\n\t"
      "s_waitcnt vmcnt(11)\n\t"
      "ds_read_b64_tr_b16 a[72:73], v214 offset:36864\n\t"
      "v_mfma_f32_16x16x32_bf16 v[116:119], a[36:39], v[152:155], v[116:119]\n\t"
      "ds_read_b64_tr_b16 a[76:77], v214 offset:36872\n\t"
      "buffer_load_dwordx4 v213, s[16:19], s51 offen offset:2048 lds\n\t"
      "v_mfma_f32_16x16x32_bf16 v[120:123], a[40:43], v[152:155], v[120:123]\n\t"
      "ds_read_b64_tr_b16 a[80:81], v214 offset:36928\n\t"
      "ds_read_b128 v[128:131], v198 offset:24576\n\t"
      "v_mfma_f32_16x16x32_bf16 v[132:135], a[44:47], v[152:155], v[132:135]\n\t"
      "ds_read_b64_tr_b16 a[84:85], v214 offset:36936\n\t"
      "s_waitcnt lgkmcnt(6)\n\t"
      "v_mfma_f32_16x16x32_bf16 v[32:35], a[48:51], v[156:159], v[32:35]\n\t"
      "ds_read_b64_tr_b16 a[88:89], v214 offset:36992\n\t"
      "s_add_u32 m0, s41, 0x15000\n\t"
      "s_mov_b32 s51, 0x1ec00\n\t"
      "buffer_load_dwordx4 v211, s[16:19], s51 offen lds\n\t"
      "v_mfma_f32_16x16x32_bf16 v[36:39], a[52:55], v[156:159], v[36:39]\n\t"
      "ds_read_b64_tr_b16 a[92:93], v214 offset:37000\n\t"
      "v_mfma_f32_16x16x32_bf16 v[40:43], a[56:59], v[156:159], v[40:43]\n\t"
      "v_mfma_f32_16x16x32_bf16 v[44:47], a[60:63], v[156:159], v[44:47]\n\t"
      "buffer_load_dwordx4 v212, s[16:19], s51 offen offset:1024 lds\n\t"
      "v_mfma_f32_16x16x32_bf16 v[48:51], a[64:67], v[156:159], v[48:51]\n\t"
      "ds_read_b128 v[136:139], v198 offset:36864\n\t"
      "v_mfma_f32_16x16x32_bf16 v[52:55], a[68:71], v[156:159], v[52:55]\n\t"
      "s_waitcnt lgkmcnt(8)\n\t"
      "v_mfma_f32_16x16x32_bf16 v[56:59], a[48:51], v[160:163], v[56:59]\n\t"
      "buffer_load_dwordx4 v213, s[16:19], s51 offen offset:2048 lds\n\t"
      "v_mfma_f32_16x16x32_bf16 v[60:63], a[52:55], v[160:163], v[60:63]\n\t"
      "v_mfma_f32_16x16x32_bf16 v[64:67], a[56:59], v[160:163], v[64:67]\n\t"
      "s_waitcnt vmcnt(12)\n\t"
      "ds_read_b64_tr_b16 a[74:75], v214 offset:49152\n\t"
      "v_mfma_f32_16x16x32_bf16 v[68:71], a[60:63], v[160:163], v[68:71]\n\t"
      "ds_read_b64_tr_b16 a[78:79], v214 offset:49160\n\t"
      "s_add_u32 m0, s41, 0x18000\n\t"
      "s_mov_b32 s51, 0x24000\n\t"
      "buffer_load_dwordx4 v211, s[16:19], s51 offen lds\n\t"
      "v_mfma_f32_16x16x32_bf16 v[72:75], a[64:67], v[160:163], v[72:75]\n\t"
      "ds_read_b64_tr_b16 a[82:83], v214 offset:49216\n\t"
      "ds_read_b128 v[140:143], v199 offset:0\n\t"
      "v_mfma_f32_16x16x32_bf16 v[76:79], a[68:71], v[160:163], v[76:79]\n\t"
      "ds_read_b64_tr_b16 a[86:87], v214 offset:49224\n\t"
      "s_waitcnt lgkmcnt(9)\n\t"
      "v_mfma_f32_16x16x32_bf16 v[80:83], a[48:51], v[128:131], v[80:83]\n\t"
      "ds_read_b64_tr_b16 a[90:91], v214 offset:49280\n\t"
      "buffer_load_dwordx4 v212, s[16:19], s51 offen offset:1024 lds\n\t"
      "v_mfma_f32_16x16x32_bf16 v[84:87], a[52:55], v[128:131], v[84:87]\n\t"
      "ds_read_b64_tr_b16 a[94:95], v214 offset:49288\n\t"
      "v_mfma_f32_16x16x32_bf16 v[88:91], a[56:59], v[128:131], v[88:91]\n\t"
      "v_mfma_f32_16x16x32_bf16 v[92:95], a[60:63], v[128:131], v[92:95]\n\t"
      "buffer_load_dwordx4 v213, s[16:19], s51 offen offset:2048 lds\n\t"
      "v_mfma_f32_16x16x32_bf16 v[96:99], a[64:67], v[128:131], v[96:99]\n\t"
      "ds_read_b128 v[144:147], v199 offset:12288\n\t"
      "v_mfma_f32_16x16x32_bf16 v[100:103], a[68:71], v[128:131], v[100:103]\n\t"
      "s_waitcnt lgkmcnt(8)\n\t"
      "v_mfma_f32_16x16x32_bf16 v[104:107], a[48:51], v[136:139], v[104:107]\n\t"
      "s_add_u32 m0, s41, 0x1b000\n\t"
      "s_mov_b32 s51, 0x24c00\n\t"
      "buffer_load_dwordx4 v211, s[16:19], s51 offen lds\n\t"
      "v_mfma_f32_16x16x32_bf16 v[108:111], a[52:55], v[136:139], v[108:111]\n\t"
      "v_mfma_f32_16x16x32_bf16 v[112:115], a[56:59], v[136:139], v[112:115]\n\t"
      "s_waitcnt vmcnt(13)\n\t"
      "ds_read_b64_tr_b16 a[96:97], v221 offset:0\n\t"
      "v_mfma_f32_16x16x32_bf16 v[116:119], a[60:63], v[136:139], v[116:119]\n\t"
      "ds_read_b64_tr_b16 a[100:101], v221 offset:8\n\t"
      "buffer_load_dwordx4 v212, s[16:19], s51 offen offset:1024 lds\n\t"
      "v_mfma_f32_16x16x32_bf16 v[120:123], a[64:67], v[136:139], v[120:123]\n\t"
      "ds_read_b64_tr_b16 a[104:105], v221 offset:64\n\t"
      "ds_read_b128 v[148:151], v199 offset:24576\n\t"
      "v_mfma_f32_16x16x32_bf16 v[132:135], a[68:71], v[136:139], v[132:135]\n\t"
      "ds_read_b64_tr_b16 a[108:109], v221 offset:72\n\t"
      "s_waitcnt lgkmcnt(6)\n\t"
      "v_mfma_f32_16x16x32_bf16 v[32:35], a[72:75], v[140:143], v[32:35]\n\t"
      "ds_read_b64_tr_b16 a[112:113], v221 offset:128\n\t"
      "buffer_load_dwordx4 v213, s[16:19], s51 offen offset:2048 lds\n\t"
      "v_mfma_f32_16x16x32_bf16 v[36:39], a[76:79], v[140:143], v[36:39]\n\t"
      "ds_read_b64_tr_b16 a[116:117], v221 offset:136\n\t"
      "v_mfma_f32_16x16x32_bf16 v[40:43], a[80:83], v[140:143], v[40:43]\n\t"
      "v_mfma_f32_16x16x32_bf16 v[44:47], a[84:87], v[140:143], v[44:47]\n\t"
      "s_add_u32 m0, s41, 0x1e000\n\t"
      "s_mov_b32 s51, 0x2a000\n\t"
      "buffer_load_dwordx4 v211, s[16:19], s51 offen lds\n\t"
      "v_mfma_f32_16x16x32_bf16 v[48:51], a[88:91], v[140:143], v[48:51]\n\t"
      "ds_read_b128 v[152:155], v199 offset:36864\n\t"
      "v_mfma_f32_16x16x32_bf16 v[52:55], a[92:95], v[140:143], v[52:55]\n\t"
      "s_waitcnt lgkmcnt(8)\n\t"
      "v_mfma_f32_16x16x32_bf16 v[56:59], a[72:75], v[144:147], v[56:59]\n\t"
      "buffer_load_dwordx4 v212, s[16:19], s51 offen offset:1024 lds\n\t"
      "v_mfma_f32_16x16x32_bf16 v[60:63], a[76:79], v[144:147], v[60:63]\n\t"
      "v_mfma_f32_16x16x32_bf16 v[64:67], a[80:83], v[144:147], v[64:67]\n\t"
      "s_waitcnt vmcnt(14)\n\t"
      "ds_read_b64_tr_b16 a[98:99], v221 offset:12288\n\t"
      "v_mfma_f32_16x16x32_bf16 v[68:71], a[84:87], v[144:147], v[68:71]\n\t"
      "ds_read_b64_tr_b16 a[102:103], v221 offset:12296\n\t"
      "buffer_load_dwordx4 v213, s[16:19], s51 offen offset:2048 lds\n\t"
      "v_mfma_f32_16x16x32_bf16 v[72:75], a[88:91], v[144:147], v[72:75]\n\t"
      "ds_read_b64_tr_b16 a[106:107], v221 offset:12352\n\t"
      "ds_read_b128 v[156:159], v197 offset:256\n\t"
      "v_mfma_f32_16x16x32_bf16 v[76:79], a[92:95], v[144:147], v[76:79]\n\t"
      "ds_read_b64_tr_b16 a[110:111], v221 offset:12360\n\t"
      "s_waitcnt lgkmcnt(9)\n\t"
      "v_mfma_f32_16x16x32_bf16 v[80:83], a[72:75], v[148:151], v[80:83]\n\t"
      "ds_read_b64_tr_b16 a[114:115], v221 offset:12416\n\t"
      "s_waitcnt lgkmcnt(7)\n\t"
      "s_add_u32 m0, s41, 0x21000\n\t"
      "s_mov_b32 s51, 0x2ac00\n\t"
      "buffer_load_dwordx4 v211, s[16:19], s51 offen lds\n\t"
      "v_mfma_f32_16x16x32_bf16 v[84:87], a[76:79], v[148:151], v[84:87]\n\t"
      "ds_read_b64_tr_b16 a[118:119], v221 offset:12424\n\t"
      "v_mfma_f32_16x16x32_bf16 v[88:91], a[80:83], v[148:151], v[88:91]\n\t"
      "v_mfma_f32_16x16x32_bf16 v[92:95], a[84:87], v[148:151], v[92:95]\n\t"
      "buffer_load_dwordx4 v212, s[16:19], s51 offen offset:1024 lds\n\t"
      "v_mfma_f32_16x16x32_bf16 v[96:99], a[88:91], v[148:151], v[96:99]\n\t"
      "ds_read_b128 v[160:163], v197 offset:12544\n\t"
      "v_mfma_f32_16x16x32_bf16 v[100:103], a[92:95], v[148:151], v[100:103]\n\t"
      "s_waitcnt lgkmcnt(8)\n\t"
      "v_mfma_f32_16x16x32_bf16 v[104:107], a[72:75], v[152:155], v[104:107]\n\t"
      "buffer_load_dwordx4 v213, s[16:19], s51 offen offset:2048 lds\n\t"
      "v_mfma_f32_16x16x32_bf16 v[108:111], a[76:79], v[152:155], v[108:111]\n\t"
      "v_mfma_f32_16x16x32_bf16 v[112:115], a[80:83], v[152:155], v[112:115]\n\t"
      "s_waitcnt vmcnt(15)\n\t"
      "ds_read_b64_tr_b16 a[120:121], v214 offset:0\n\t"
      "v_mfma_f32_16x16x32_bf16 v[116:119], a[84:87], v[152:155], v[116:119]\n\t"
      "ds_read_b64_tr_b16 a[124:125], v214 offset:8\n\t"
      "s_waitcnt lgkmcnt(3)\n\t"
      "s_add_u32 m0, s41, 0x24000\n\t"
      "s_mov_b32 s51, 0x30000\n\t"
      "buffer_load_dwordx4 v211, s[16:19], s51 offen lds\n\t"
      "v_mfma_f32_16x16x32_bf16 v[120:123], a[88:91], v[152:155], v[120:123]\n\t"
      "ds_read_b64_tr_b16 a[128:129], v214 offset:64\n\t"
      "ds_read_b128 v[128:131], v197 offset:24832\n\t"
      "v_mfma_f32_16x16x32_bf16 v[132:135], a[92:95], v[152:155], v[132:135]\n\t"
      "ds_read_b64_tr_b16 a[132:133], v214 offset:72\n\t"
      "v_mfma_f32_16x16x32_bf16 v[32:35], a[96:99], v[156:159], v[32:35]\n\t"
      "ds_read_b64_tr_b16 a[136:137], v214 offset:128\n\t"
      "buffer_load_dwordx4 v212, s[16:19], s51 offen offset:1024 lds\n\t"
      "v_mfma_f32_16x16x32_bf16 v[36:39], a[100:103], v[156:159], v[36:39]\n\t"
      "ds_read_b64_tr_b16 a[140:141], v214 offset:136\n\t"
      "v_mfma_f32_16x16x32_bf16 v[40:43], a[104:107], v[156:159], v[40:43]\n\t"
      "v_mfma_f32_16x16x32_bf16 v[44:47], a[108:111], v[156:159], v[44:47]\n\t"
      "buffer_load_dwordx4 v213, s[16:19], s51 offen offset:2048 lds\n\t"
      "v_mfma_f32_16x16x32_bf16 v[48:51], a[112:115], v[156:159], v[48:51]\n\t"
      "ds_read_b128 v[136:139], v197 offset:37120\n\t"
      "v_mfma_f32_16x16x32_bf16 v[52:55], a[116:119], v[156:159], v[52:55]\n\t"
      "s_waitcnt lgkmcnt(8)\n\t"
      "v_mfma_f32_16x16x32_bf16 v[56:59], a[96:99], v[160:163], v[56:59]\n\t"
      "v_mfma_f32_16x16x32_bf16 v[60:63], a[100:103], v[160:163], v[60:63]\n\t"
      "v_mfma_f32_16x16x32_bf16 v[64:67], a[104:107], v[160:163], v[64:67]\n\t"
      "s_waitcnt vmcnt(15)\n\t"
      "ds_read_b64_tr_b16 a[122:123], v214 offset:12288\n\t"
      "s_waitcnt lgkmcnt(2)\n\t"
      "s_add_u32 m0, s41, 0x12000\n\t"
      "s_mov_b32 s51, 0x30c00\n\t"
      "buffer_load_dwordx4 v211, s[16:19], s51 offen lds\n\t"
      "v_mfma_f32_16x16x32_bf16 v[68:71], a[108:111], v[160:163], v[68:71]\n\t"
      "ds_read_b64_tr_b16 a[126:127], v214 offset:12296\n\t"
      "v_mfma_f32_16x16x32_bf16 v[72:75], a[112:115], v[160:163], v[72:75]\n\t"
      "ds_read_b64_tr_b16 a[130:131], v214 offset:12352\n\t"
      "ds_read_b128 v[140:143], v198 offset:256\n\t"
      "v_mfma_f32_16x16x32_bf16 v[76:79], a[116:119], v[160:163], v[76:79]\n\t"
      "ds_read_b64_tr_b16 a[134:135], v214 offset:12360\n\t"
      "buffer_load_dwordx4 v212, s[16:19], s51 offen offset:1024 lds\n\t"
      "v_mfma_f32_16x16x32_bf16 v[80:83], a[96:99], v[128:131], v[80:83]\n\t"
      "ds_read_b64_tr_b16 a[138:139], v214 offset:12416\n\t"
      "v_mfma_f32_16x16x32_bf16 v[84:87], a[100:103], v[128:131], v[84:87]\n\t"
      "ds_read_b64_tr_b16 a[142:143], v214 offset:12424\n\t"
      "v_mfma_f32_16x16x32_bf16 v[88:91], a[104:107], v[128:131], v[88:91]\n\t"
      "buffer_load_dwordx4 v213, s[16:19], s51 offen offset:2048 lds\n\t"
      "v_mfma_f32_16x16x32_bf16 v[92:95], a[108:111], v[128:131], v[92:95]\n\t"
      "v_mfma_f32_16x16x32_bf16 v[96:99], a[112:115], v[128:131], v[96:99]\n\t"
      "ds_read_b128 v[144:147], v198 offset:12544\n\t"
      "v_mfma_f32_16x16x32_bf16 v[100:103], a[116:119], v[128:131], v[100:103]\n\t"
      "s_waitcnt lgkmcnt(8)\n\t"
      "v_mfma_f32_16x16x32_bf16 v[104:107], a[96:99], v[136:139], v[104:107]\n\t"
      "v_mfma_f32_16x16x32_bf16 v[108:111], a[100:103], v[136:139], v[108:111]\n\t"
      "v_mfma_f32_16x16x32_bf16 v[112:115], a[104:107], v[136:139], v[112:115]\n\t"
      "s_waitcnt vmcnt(15)\n\t"
      "ds_read_b64_tr_b16 a[144:145], v214 offset:24576\n\t"
      "s_waitcnt lgkmcnt(2)\n\t"
      "s_add_u32 m0, s41, 0x15000\n\t"
      "s_mov_b32 s51, 0x36000\n\t"
      "buffer_load_dwordx4 v211, s[16:19], s51 offen lds\n\t"
      "v_mfma_f32_16x16x32_bf16 v[116:119], a[108:111], v[136:139], v[116:119]\n\t"
      "ds_read_b64_tr_b16 a[148:149], v214 offset:24584\n\t"
      "v_mfma_f32_16x16x32_bf16 v[120:123], a[112:115], v[136:139], v[120:123]\n\t"
      "ds_read_b64_tr_b16 a[152:153], v214 offset:24640\n\t"
      "ds_read_b128 v[148:151], v198 offset:24832\n\t"
      "v_mfma_f32_16x16x32_bf16 v[132:135], a[116:119], v[136:139], v[132:135]\n\t"
      "ds_read_b64_tr_b16 a[156:157], v214 offset:24648\n\t"
      "buffer_load_dwordx4 v212, s[16:19], s51 offen offset:1024 lds\n\t"
      "v_mfma_f32_16x16x32_bf16 v[32:35], a[120:123], v[140:143], v[32:35]\n\t"
      "ds_read_b64_tr_b16 a[160:161], v214 offset:24704\n\t"
      "v_mfma_f32_16x16x32_bf16 v[36:39], a[124:127], v[140:143], v[36:39]\n\t"
      "ds_read_b64_tr_b16 a[164:165], v214 offset:24712\n\t"
      "v_mfma_f32_16x16x32_bf16 v[40:43], a[128:131], v[140:143], v[40:43]\n\t"
      "buffer_load_dwordx4 v213, s[16:19], s51 offen offset:2048 lds\n\t"
      "v_mfma_f32_16x16x32_bf16 v[44:47], a[132:135], v[140:143], v[44:47]\n\t"
      "v_mfma_f32_16x16x32_bf16 v[48:51], a[136:139], v[140:143], v[48:51]\n\t"
      "ds_read_b128 v[152:155], v198 offset:37120\n\t"
      "v_mfma_f32_16x16x32_bf16 v[52:55], a[140:143], v[140:143], v[52:55]\n\t"
      "s_waitcnt lgkmcnt(8)\n\t"
      "v_mfma_f32_16x16x32_bf16 v[56:59], a[120:123], v[144:147], v[56:59]\n\t"
      "v_mfma_f32_16x16x32_bf16 v[60:63], a[124:127], v[144:147], v[60:63]\n\t"
      "v_mfma_f32_16x16x32_bf16 v[64:67], a[128:131], v[144:147], v[64:67]\n\t"
      "s_waitcnt vmcnt(15)\n\t"
      "ds_read_b64_tr_b16 a[146:147], v214 offset:36864\n\t"
      "s_waitcnt lgkmcnt(2)\n\t"
      "s_add_u32 m0, s41, 0x18000\n\t"
      "s_mov_b32 s51, 0x36c00\n\t"
      "buffer_load_dwordx4 v211, s[16:19], s51 offen lds\n\t"
      "v_mfma_f32_16x16x32_bf16 v[68:71], a[132:135], v[144:147], v[68:71]\n\t"
      "ds_read_b64_tr_b16 a[150:151], v214 offset:36872\n\t"
      "v_mfma_f32_16x16x32_bf16 v[72:75], a[136:139], v[144:147], v[72:75]\n\t"
      "ds_read_b64_tr_b16 a[154:155], v214 offset:36928\n\t"
      "ds_read_b128 v[156:159], v199 offset:256\n\t"
      "v_mfma_f32_16x16x32_bf16 v[76:79], a[140:143], v[144:147], v[76:79]\n\t"
      "ds_read_b64_tr_b16 a[158:159], v214 offset:36936\n\t"
      "buffer_load_dwordx4 v212, s[16:19], s51 offen offset:1024 lds\n\t"
      "v_mfma_f32_16x16x32_bf16 v[80:83], a[120:123], v[148:151], v[80:83]\n\t"
      "ds_read_b64_tr_b16 a[162:163], v214 offset:36992\n\t"
      "v_mfma_f32_16x16x32_bf16 v[84:87], a[124:127], v[148:151], v[84:87]\n\t"
      "ds_read_b64_tr_b16 a[166:167], v214 offset:37000\n\t"
      "v_mfma_f32_16x16x32_bf16 v[88:91], a[128:131], v[148:151], v[88:91]\n\t"
      "buffer_load_dwordx4 v213, s[16:19], s51 offen offset:2048 lds\n\t"
      "v_mfma_f32_16x16x32_bf16 v[92:95], a[132:135], v[148:151], v[92:95]\n\t"
      "v_mfma_f32_16x16x32_bf16 v[96:99], a[136:139], v[148:151], v[96:99]\n\t"
      "ds_read_b128 v[160:163], v199 offset:12544\n\t"
      "v_mfma_f32_16x16x32_bf16 v[100:103], a[140:143], v[148:151], v[100:103]\n\t"
      "s_waitcnt lgkmcnt(8)\n\t"
      "v_mfma_f32_16x16x32_bf16 v[104:107], a[120:123], v[152:155], v[104:107]\n\t"
      "v_mfma_f32_16x16x32_bf16 v[108:111], a[124:127], v[152:155], v[108:111]\n\t"
      "v_mfma_f32_16x16x32_bf16 v[112:115], a[128:131], v[152:155], v[112:115]\n\t"
      "s_waitcnt vmcnt(15)\n\t"
      "ds_read_b64_tr_b16 a[168:169], v214 offset:49152\n\t"
      "s_waitcnt lgkmcnt(2)\n\t"
      "s_add_u32 m0, s41, 0x1b000\n\t"
      "s_mov_b32 s51, 0x3c000\n\t"
      "buffer_load_dwordx4 v211, s[16:19], s51 offen lds\n\t"
      "v_mfma_f32_16x16x32_bf16 v[116:119], a[132:135], v[152:155], v[116:119]\n\t"
      "ds_read_b64_tr_b16 a[172:173], v214 offset:49160\n\t"
      "v_mfma_f32_16x16x32_bf16 v[120:123], a[136:139], v[152:155], v[120:123]\n\t"
      "ds_read_b64_tr_b16 a[176:177], v214 offset:49216\n\t"
      "ds_read_b128 v[128:131], v199 offset:24832\n\t"
      "v_mfma_f32_16x16x32_bf16 v[132:135], a[140:143], v[152:155], v[132:135]\n\t"
      "ds_read_b64_tr_b16 a[180:181], v214 offset:49224\n\t"
      "buffer_load_dwordx4 v212, s[16:19], s51 offen offset:1024 lds\n\t"
      "v_mfma_f32_16x16x32_bf16 v[32:35], a[144:147], v[156:159], v[32:35]\n\t"
      "ds_read_b64_tr_b16 a[184:185], v214 offset:49280\n\t"
      "v_mfma_f32_16x16x32_bf16 v[36:39], a[148:151], v[156:159], v[36:39]\n\t"
      "ds_read_b64_tr_b16 a[188:189], v214 offset:49288\n\t"
      "v_mfma_f32_16x16x32_bf16 v[40:43], a[152:155], v[156:159], v[40:43]\n\t"
      "buffer_load_dwordx4 v213, s[16:19], s51 offen offset:2048 lds\n\t"
      "v_mfma_f32_16x16x32_bf16 v[44:47], a[156:159], v[156:159], v[44:47]\n\t"
      "v_mfma_f32_16x16x32_bf16 v[48:51], a[160:163], v[156:159], v[48:51]\n\t"
      "ds_read_b128 v[136:139], v199 offset:37120\n\t"
      "v_mfma_f32_16x16x32_bf16 v[52:55], a[164:167], v[156:159], v[52:55]\n\t"
      "s_waitcnt lgkmcnt(8)\n\t"
      "v_mfma_f32_16x16x32_bf16 v[56:59], a[144:147], v[160:163], v[56:59]\n\t"
      "v_mfma_f32_16x16x32_bf16 v[60:63], a[148:151], v[160:163], v[60:63]\n\t"
      "v_mfma_f32_16x16x32_bf16 v[64:67], a[152:155], v[160:163], v[64:67]\n\t"
      "s_waitcnt vmcnt(15)\n\t"
      "ds_read_b64_tr_b16 a[170:171], v221 offset:0\n\t"
      "s_waitcnt lgkmcnt(2)\n\t"
      "s_add_u32 m0, s41, 0x1e000\n\t"
      "s_mov_b32 s51, 0x3cc00\n\t"
      "buffer_load_dwordx4 v211, s[16:19], s51 offen lds\n\t"
      "v_mfma_f32_16x16x32_bf16 v[68:71], a[156:159], v[160:163], v[68:71]\n\t"
      "ds_read_b64_tr_b16 a[174:175], v221 offset:8\n\t"
      "v_mfma_f32_16x16x32_bf16 v[72:75], a[160:163], v[160:163], v[72:75]\n\t"
      "ds_read_b64_tr_b16 a[178:179], v221 offset:64\n\t"
      "ds_read_b128 v[140:143], v196 offset:256\n\t"
      "v_mfma_f32_16x16x32_bf16 v[76:79], a[164:167], v[160:163], v[76:79]\n\t"
      "ds_read_b64_tr_b16 a[182:183], v221 offset:72\n\t"
      "buffer_load_dwordx4 v212, s[16:19], s51 offen offset:1024 lds\n\t"
      "v_mfma_f32_16x16x32_bf16 v[80:83], a[144:147], v[128:131], v[80:83]\n\t"
      "ds_read_b64_tr_b16 a[186:187], v221 offset:128\n\t"
      "v_mfma_f32_16x16x32_bf16 v[84:87], a[148:151], v[128:131], v[84:87]\n\t"
      "ds_read_b64_tr_b16 a[190:191], v221 offset:136\n\t"
      "v_mfma_f32_16x16x32_bf16 v[88:91], a[152:155], v[128:131], v[88:91]\n\t"
      "buffer_load_dwordx4 v213, s[16:19], s51 offen offset:2048 lds\n\t"
      "v_mfma_f32_16x16x32_bf16 v[92:95], a[156:159], v[128:131], v[92:95]\n\t"
      "v_mfma_f32_16x16x32_bf16 v[96:99], a[160:163], v[128:131], v[96:99]\n\t"
      "ds_read_b128 v[144:147], v196 offset:12544\n\t"
      "v_mfma_f32_16x16x32_bf16 v[100:103], a[164:167], v[128:131], v[100:103]\n\t"
      "s_waitcnt lgkmcnt(8)\n\t"
      "v_mfma_f32_16x16x32_bf16 v[104:107], a[144:147], v[136:139], v[104:107]\n\t"
      "v_mfma_f32_16x16x32_bf16 v[108:111], a[148:151], v[136:139], v[108:111]\n\t"
      "v_mfma_f32_16x16x32_bf16 v[112:115], a[152:155], v[136:139], v[112:115]\n\t"
      "s_waitcnt vmcnt(15)\n\t"
      "ds_read_b64_tr_b16 a[192:193], v221 offset:12288\n\t"
      "s_waitcnt lgkmcnt(2)\n\t"
      "s_add_u32 m0, s41, 0x21000\n\t"
      "s_mov_b32 s51, 0x42000\n\t"
      "buffer_load_dwordx4 v211, s[16:19], s51 offen lds\n\t"
      "v_mfma_f32_16x16x32_bf16 v[116:119], a[156:159], v[136:139], v[116:119]\n\t"
      "ds_read_b64_tr_b16 a[196:197], v221 offset:12296\n\t"
      "v_mfma_f32_16x16x32_bf16 v[120:123], a[160:163], v[136:139], v[120:123]\n\t"
      "ds_read_b64_tr_b16 a[200:201], v221 offset:12352\n\t"
      "ds_read_b128 v[148:151], v196 offset:24832\n\t"
      "v_mfma_f32_16x16x32_bf16 v[132:135], a[164:167], v[136:139], v[132:135]\n\t"
      "ds_read_b64_tr_b16 a[204:205], v221 offset:12360\n\t"
      "buffer_load_dwordx4 v212, s[16:19], s51 offen offset:1024 lds\n\t"
      "v_mfma_f32_16x16x32_bf16 v[32:35], a[168:171], v[140:143], v[32:35]\n\t"
      "ds_read_b64_tr_b16 a[208:209], v221 offset:12416\n\t"
      "v_mfma_f32_16x16x32_bf16 v[36:39], a[172:175], v[140:143], v[36:39]\n\t"
      "ds_read_b64_tr_b16 a[212:213], v221 offset:12424\n\t"
      "v_mfma_f32_16x16x32_bf16 v[40:43], a[176:179], v[140:143], v[40:43]\n\t"
      "buffer_load_dwordx4 v213, s[16:19], s51 offen offset:2048 lds\n\t"
      "v_mfma_f32_16x16x32_bf16 v[44:47], a[180:183], v[140:143], v[44:47]\n\t"
      "v_mfma_f32_16x16x32_bf16 v[48:51], a[184:187], v[140:143], v[48:51]\n\t"
      "ds_read_b128 v[152:155], v196 offset:37120\n\t"
      "v_mfma_f32_16x16x32_bf16 v[52:55], a[188:191], v[140:143], v[52:55]\n\t"
      "s_waitcnt lgkmcnt(8)\n\t"
      "v_mfma_f32_16x16x32_bf16 v[56:59], a[168:171], v[144:147], v[56:59]\n\t"
      "v_mfma_f32_16x16x32_bf16 v[60:63], a[172:175], v[144:147], v[60:63]\n\t"
      "v_mfma_f32_16x16x32_bf16 v[64:67], a[176:179], v[144:147], v[64:67]\n\t"
      "s_waitcnt vmcnt(15)\n\t"
      "ds_read_b64_tr_b16 a[194:195], v214 offset:0\n\t"
      "s_waitcnt lgkmcnt(2)\n\t"
      "s_add_u32 m0, s41, 0x24000\n\t"
      "s_mov_b32 s51, 0x42c00\n\t"
      "buffer_load_dwordx4 v211, s[16:19], s51 offen lds\n\t"
      "v_mfma_f32_16x16x32_bf16 v[68:71], a[180:183], v[144:147], v[68:71]\n\t"
      "ds_read_b64_tr_b16 a[198:199], v214 offset:8\n\t"
      "v_mfma_f32_16x16x32_bf16 v[72:75], a[184:187], v[144:147], v[72:75]\n\t"
      "ds_read_b64_tr_b16 a[202:203], v214 offset:64\n\t"
      "ds_read_b128 v[156:159], v198 offset:512\n\t"
      "v_mfma_f32_16x16x32_bf16 v[76:79], a[188:191], v[144:147], v[76:79]\n\t"
      "ds_read_b64_tr_b16 a[206:207], v214 offset:72\n\t"
      "buffer_load_dwordx4 v212, s[16:19], s51 offen offset:1024 lds\n\t"
      "v_mfma_f32_16x16x32_bf16 v[80:83], a[168:171], v[148:151], v[80:83]\n\t"
      "ds_read_b64_tr_b16 a[210:211], v214 offset:128\n\t"
      "v_mfma_f32_16x16x32_bf16 v[84:87], a[172:175], v[148:151], v[84:87]\n\t"
      "ds_read_b64_tr_b16 a[214:215], v214 offset:136\n\t"
      "v_mfma_f32_16x16x32_bf16 v[88:91], a[176:179], v[148:151], v[88:91]\n\t"
      "buffer_load_dwordx4 v213, s[16:19], s51 offen offset:2048 lds\n\t"
      "v_mfma_f32_16x16x32_bf16 v[92:95], a[180:183], v[148:151], v[92:95]\n\t"
      "v_mfma_f32_16x16x32_bf16 v[96:99], a[184:187], v[148:151], v[96:99]\n\t"
      "ds_read_b128 v[160:163], v198 offset:12800\n\t"
      "v_mfma_f32_16x16x32_bf16 v[100:103], a[188:191], v[148:151], v[100:103]\n\t"
      "s_waitcnt lgkmcnt(8)\n\t"
      "v_mfma_f32_16x16x32_bf16 v[104:107], a[168:171], v[152:155], v[104:107]\n\t"
      "s_add_u32 m0, s41, 0xc000\n\t"
      "s_add_u32 s51, s40, 0xc00000\n\t"
      "buffer_load_dwordx4 v208, s[20:23], s51 offen lds\n\t"
      "v_mfma_f32_16x16x32_bf16 v[108:111], a[172:175], v[152:155], v[108:111]\n\t"
      "v_mfma_f32_16x16x32_bf16 v[112:115], a[176:179], v[152:155], v[112:115]\n\t"
      "s_waitcnt vmcnt(16)\n\t"
      "ds_read_b64_tr_b16 a[216:217], v214 offset:12288\n\t"
      "buffer_load_dwordx4 v208, s[20:23], s51 offen offset:1024 lds\n\t"
      "v_mfma_f32_16x16x32_bf16 v[116:119], a[180:183], v[152:155], v[116:119]\n\t"
      "ds_read_b64_tr_b16 a[220:221], v214 offset:12296\n\t"
      "v_mfma_f32_16x16x32_bf16 v[120:123], a[184:187], v[152:155], v[120:123]\n\t"
      "ds_read_b64_tr_b16 a[224:225], v214 offset:12352\n\t"
      "ds_read_b128 v[128:131], v198 offset:25088\n\t"
      "buffer_load_dwordx4 v208, s[20:23], s51 offen offset:2048 lds\n\t"
      "v_mfma_f32_16x16x32_bf16 v[132:135], a[188:191], v[152:155], v[132:135]\n\t"
      "ds_read_b64_tr_b16 a[228:229], v214 offset:12360\n\t"
      "s_waitcnt lgkmcnt(6)\n\t"
      "v_mfma_f32_16x16x32_bf16 v[32:35], a[192:195], v[156:159], v[32:35]\n\t"
      "ds_read_b64_tr_b16 a[232:233], v214 offset:12416\n\t"
      "s_add_u32 m0, s41, 0xf000\n\t"
      "s_add_u32 s51, s40, 0xc00000\n\t"
      "buffer_load_dwordx4 v209, s[20:23], s51 offen lds\n\t"
      "v_mfma_f32_16x16x32_bf16 v[36:39], a[196:199], v[156:159], v[36:39]\n\t"
      "ds_read_b64_tr_b16 a[236:237], v214 offset:12424\n\t"
      "v_mfma_f32_16x16x32_bf16 v[40:43], a[200:203], v[156:159], v[40:43]\n\t"
      "buffer_load_dwordx4 v209, s[20:23], s51 offen offset:1024 lds\n\t"
      "v_mfma_f32_16x16x32_bf16 v[44:47], a[204:207], v[156:159], v[44:47]\n\t"
      "v_mfma_f32_16x16x32_bf16 v[48:51], a[208:211], v[156:159], v[48:51]\n\t"
      "ds_read_b128 v[136:139], v198 offset:37376\n\t"
      "buffer_load_dwordx4 v209, s[20:23], s51 offen offset:2048 lds\n\t"
      "v_mfma_f32_16x16x32_bf16 v[52:55], a[212:215], v[156:159], v[52:55]\n\t"
      "s_waitcnt lgkmcnt(8)\n\t"
      "v_mfma_f32_16x16x32_bf16 v[56:59], a[192:195], v[160:163], v[56:59]\n\t"
      "v_mfma_f32_16x16x32_bf16 v[60:63], a[196:199], v[160:163], v[60:63]\n\t"
      "v_mfma_f32_16x16x32_bf16 v[64:67], a[200:203], v[160:163], v[64:67]\n\t"
      "s_waitcnt vmcnt(18)\n\t"
      "ds_read_b64_tr_b16 a[218:219], v214 offset:24576\n\t"
      "v_mfma_f32_16x16x32_bf16 v[68:71], a[204:207], v[160:163], v[68:71]\n\t"
      "ds_read_b64_tr_b16 a[222:223], v214 offset:24584\n\t"
      "v_mfma_f32_16x16x32_bf16 v[72:75], a[208:211], v[160:163], v[72:75]\n\t"
      "ds_read_b64_tr_b16 a[226:227], v214 offset:24640\n\t"
      "ds_read_b128 v[140:143], v199 offset:512\n\t"
      "v_mfma_f32_16x16x32_bf16 v[76:79], a[212:215], v[160:163], v[76:79]\n\t"
      "ds_read_b64_tr_b16 a[230:231], v214 offset:24648\n\t"
      "s_waitcnt lgkmcnt(9)\n\t"
      "v_mfma_f32_16x16x32_bf16 v[80:83], a[192:195], v[128:131], v[80:83]\n\t"
      "ds_read_b64_tr_b16 a[234:235], v214 offset:24704\n\t"
      "v_mfma_f32_16x16x32_bf16 v[84:87], a[196:199], v[128:131], v[84:87]\n\t"
      "ds_read_b64_tr_b16 a[238:239], v214 offset:24712\n\t"
      "v_mfma_f32_16x16x32_bf16 v[88:91], a[200:203], v[128:131], v[88:91]\n\t"
      "v_mfma_f32_16x16x32_bf16 v[92:95], a[204:207], v[128:131], v[92:95]\n\t"
      "v_mfma_f32_16x16x32_bf16 v[96:99], a[208:211], v[128:131], v[96:99]\n\t"
      "ds_read_b128 v[144:147], v199 offset:12800\n\t"
      "v_mfma_f32_16x16x32_bf16 v[100:103], a[212:215], v[128:131], v[100:103]\n\t"
      "s_waitcnt lgkmcnt(8)\n\t"
      "v_mfma_f32_16x16x32_bf16 v[104:107], a[192:195], v[136:139], v[104:107]\n\t"
      "v_mfma_f32_16x16x32_bf16 v[108:111], a[196:199], v[136:139], v[108:111]\n\t"
      "v_mfma_f32_16x16x32_bf16 v[112:115], a[200:203], v[136:139], v[112:115]\n\t"
      "s_waitcnt vmcnt(15)\n\t"
      "ds_read_b64_tr_b16 a[240:241], v214 offset:36864\n\t"
      "v_mfma_f32_16x16x32_bf16 v[116:119], a[204:207], v[136:139], v[116:119]\n\t"
      "ds_read_b64_tr_b16 a[244:245], v214 offset:36872\n\t"
      "v_mfma_f32_16x16x32_bf16 v[120:123], a[208:211], v[136:139], v[120:123]\n\t"
      "ds_read_b64_tr_b16 a[248:249], v214 offset:36928\n\t"
      "ds_read_b128 v[148:151], v199 offset:25088\n\t"
      "v_mfma_f32_16x16x32_bf16 v[132:135], a[212:215], v[136:139], v[132:135]\n\t"
      "ds_read_b64_tr_b16 a[252:253], v214 offset:36936\n\t"
      "s_waitcnt lgkmcnt(6)\n\t"
      "v_mfma_f32_16x16x32_bf16 v[32:35], a[216:219], v[140:143], v[32:35]\n\t"
      "ds_read_b64_tr_b16 v[164:165], v214 offset:36992\n\t"
      "v_mfma_f32_16x16x32_bf16 v[36:39], a[220:223], v[140:143], v[36:39]\n\t"
      "ds_read_b64_tr_b16 v[168:169], v214 offset:37000\n\t"
      "v_mfma_f32_16x16x32_bf16 v[40:43], a[224:227], v[140:143], v[40:43]\n\t"
      "s_waitcnt vmcnt(12)\n\t"
      "ds_read_b64_tr_b16 a[242:243], v214 offset:49152\n\t"
      "v_mfma_f32_16x16x32_bf16 v[44:47], a[228:231], v[140:143], v[44:47]\n\t"
      "ds_read_b64_tr_b16 a[246:247], v214 offset:49160\n\t"
      "v_mfma_f32_16x16x32_bf16 v[48:51], a[232:235], v[140:143], v[48:51]\n\t"
      "ds_read_b64_tr_b16 a[250:251], v214 offset:49216\n\t"
      "ds_read_b128 v[152:155], v199 offset:37376\n\t"
      "v_mfma_f32_16x16x32_bf16 v[52:55], a[236:239], v[140:143], v[52:55]\n\t"
      "ds_read_b64_tr_b16 a[254:255], v214 offset:49224\n\t"
      "s_waitcnt lgkmcnt(12)\n\t"
      "v_mfma_f32_16x16x32_bf16 v[56:59], a[216:219], v[144:147], v[56:59]\n\t"
      "ds_read_b64_tr_b16 v[166:167], v214 offset:49280\n\t"
      "v_mfma_f32_16x16x32_bf16 v[60:63], a[220:223], v[144:147], v[60:63]\n\t"
      "ds_read_b64_tr_b16 v[170:171], v214 offset:49288\n\t"
      "v_mfma_f32_16x16x32_bf16 v[64:67], a[224:227], v[144:147], v[64:67]\n\t"
      "s_waitcnt vmcnt(9)\n\t"
      "ds_read_b64_tr_b16 v[172:173], v221 offset:0\n\t"
      "v_mfma_f32_16x16x32_bf16 v[68:71], a[228:231], v[144:147], v[68:71]\n\t"
      "ds_read_b64_tr_b16 v[176:177], v221 offset:8\n\t"
      "v_mfma_f32_16x16x32_bf16 v[72:75], a[232:235], v[144:147], v[72:75]\n\t"
      "ds_read_b64_tr_b16 v[180:181], v221 offset:64\n\t"
      "ds_read_b128 v[156:159], v196 offset:512\n\t"
      "v_mfma_f32_16x16x32_bf16 v[76:79], a[236:239], v[144:147], v[76:79]\n\t"
      "ds_read_b64_tr_b16 v[184:185], v221 offset:72\n\t"
      "s_waitcnt lgkmcnt(14)\n\t"
      "v_mfma_f32_16x16x32_bf16 v[80:83], a[216:219], v[148:151], v[80:83]\n\t"
      "ds_read_b64_tr_b16 v[188:189], v221 offset:128\n\t"
      "v_mfma_f32_16x16x32_bf16 v[84:87], a[220:223], v[148:151], v[84:87]\n\t"
      "ds_read_b64_tr_b16 v[192:193], v221 offset:136\n\t"
      "v_mfma_f32_16x16x32_bf16 v[88:91], a[224:227], v[148:151], v[88:91]\n\t"
      "v_mfma_f32_16x16x32_bf16 v[92:95], a[228:231], v[148:151], v[92:95]\n\t"
      "v_mfma_f32_16x16x32_bf16 v[96:99], a[232:235], v[148:151], v[96:99]\n\t"
      "ds_read_b128 v[160:163], v196 offset:12800\n\t"
      "v_mfma_f32_16x16x32_bf16 v[100:103], a[236:239], v[148:151], v[100:103]\n\t"
      "s_waitcnt lgkmcnt(11)\n\t"
      "v_mfma_f32_16x16x32_bf16 v[104:107], a[216:219], v[152:155], v[104:107]\n\t"
      "v_mfma_f32_16x16x32_bf16 v[108:111], a[220:223], v[152:155], v[108:111]\n\t"
      "v_mfma_f32_16x16x32_bf16 v[112:115], a[224:227], v[152:155], v[112:115]\n\t"
      "s_waitcnt vmcnt(6)\n\t"
      "ds_read_b64_tr_b16 v[174:175], v221 offset:12288\n\t"
      "buffer_load_dwordx2 v[240:241], v245, s[24:27], 0 offen sc1\n\t"
      "v_mfma_f32_16x16x32_bf16 v[116:119], a[228:231], v[152:155], v[116:119]\n\t"
      "ds_read_b64_tr_b16 v[178:179], v221 offset:12296\n\t"
      "s_cmp_eq_u32 s41, s43\n\t"
      "s_cbranch_scc0 .Lc4_%=\n\t"
      "buffer_load_dword v254, v253, s[24:27], 0 offen sc1\n\t"
      ".Lc4_%=:\n\t"
      "v_mfma_f32_16x16x32_bf16 v[120:123], a[232:235], v[152:155], v[120:123]\n\t"
      "ds_read_b64_tr_b16 v[182:183], v221 offset:12352\n\t"
      "ds_read_b128 v[140:143], v197 offset:512\n\t"
      "v_mfma_f32_16x16x32_bf16 v[132:135], a[236:239], v[152:155], v[132:135]\n\t"
      "ds_read_b64_tr_b16 v[186:187], v221 offset:12360\n\t"
      "s_waitcnt lgkmcnt(9)\n\t"
      "v_mfma_f32_16x16x32_bf16 v[32:35], a[240:243], v[156:159], v[32:35]\n\t"
      "ds_read_b64_tr_b16 v[190:191], v221 offset:12416\n\t"
      "v_mfma_f32_16x16x32_bf16 v[36:39], a[244:247], v[156:159], v[36:39]\n\t"
      "ds_read_b64_tr_b16 v[194:195], v221 offset:12424\n\t"
      "v_mfma_f32_16x16x32_bf16 v[40:43], a[248:251], v[156:159], v[40:43]\n\t"
      "v_mfma_f32_16x16x32_bf16 v[44:47], a[252:255], v[156:159], v[44:47]\n\t"
      "v_mfma_f32_16x16x32_bf16 v[48:51], v[164:167], v[156:159], v[48:51]\n\t"
      "ds_read_b128 v[144:147], v197 offset:12800\n\t"
      "v_mfma_f32_16x16x32_bf16 v[52:55], v[168:171], v[156:159], v[52:55]\n\t"
      "s_waitcnt lgkmcnt(8)\n\t"
      "v_mfma_f32_16x16x32_bf16 v[56:59], a[240:243], v[160:163], v[56:59]\n\t"
      "v_mfma_f32_16x16x32_bf16 v[60:63], a[244:247], v[160:163], v[60:63]\n\t"
      "v_mfma_f32_16x16x32_bf16 v[64:67], a[248:251], v[160:163], v[64:67]\n\t"
      "v_mfma_f32_16x16x32_bf16 v[68:71], a[252:255], v[160:163], v[68:71]\n\t"
      "v_mfma_f32_16x16x32_bf16 v[72:75], v[164:167], v[160:163], v[72:75]\n\t"
      "ds_read_b128 v[128:131], v196 offset:25088\n\t"
      "v_mfma_f32_16x16x32_bf16 v[76:79], v[168:171], v[160:163], v[76:79]\n\t"
      "s_waitcnt lgkmcnt(2)\n\t"
      "v_mfma_f32_16x16x32_bf16 v[32:35], v[172:175], v[140:143], v[32:35]\n\t"
      "v_mfma_f32_16x16x32_bf16 v[36:39], v[176:179], v[140:143], v[36:39]\n\t"
      "v_mfma_f32_16x16x32_bf16 v[40:43], v[180:183], v[140:143], v[40:43]\n\t"
      "v_mfma_f32_16x16x32_bf16 v[44:47], v[184:187], v[140:143], v[44:47]\n\t"
      "v_mfma_f32_16x16x32_bf16 v[48:51], v[188:191], v[140:143], v[48:51]\n\t"
      "ds_read_b128 v[136:139], v196 offset:37376\n\t"
      "v_mfma_f32_16x16x32_bf16 v[52:55], v[192:195], v[140:143], v[52:55]\n\t"
      "s_waitcnt lgkmcnt(2)\n\t"
      "v_mfma_f32_16x16x32_bf16 v[56:59], v[172:175], v[144:147], v[56:59]\n\t"
      "v_mfma_f32_16x16x32_bf16 v[60:63], v[176:179], v[144:147], v[60:63]\n\t"
      "s_waitcnt vmcnt(4) lgkmcnt(2)\n\t"
      "s_barrier\n\t"
      "v_mfma_f32_16x16x32_bf16 v[64:67], v[180:183], v[144:147], v[64:67]\n\t"
      "v_cvt_pk_bf16_f32 v32, v32, v33\n\t"
      "v_cvt_pk_bf16_f32 v33, v34, v35\n\t"
      "v_mfma_f32_16x16x32_bf16 v[68:71], v[184:187], v[144:147], v[68:71]\n\t"
      "v_cvt_pk_bf16_f32 v34, v36, v37\n\t"
      "v_cvt_pk_bf16_f32 v35, v38, v39\n\t"
      "v_mfma_f32_16x16x32_bf16 v[72:75], v[188:191], v[144:147], v[72:75]\n\t"
      "ds_read_b128 v[148:151], v197 offset:25088\n\t"
      "ds_write_b128 v217, v[32:35] offset:24576\n\t"
      "v_cvt_pk_bf16_f32 v40, v40, v41\n\t"
      "v_mfma_f32_16x16x32_bf16 v[76:79], v[192:195], v[144:147], v[76:79]\n\t"
      "v_cvt_pk_bf16_f32 v41, v42, v43\n\t"
      "v_cvt_pk_bf16_f32 v42, v44, v45\n\t"
      "s_mov_b32 m0, s41\n\t"
      "s_add_u32 s51, s40, 0x1200000\n\t"
      "buffer_load_dwordx4 v208, s[20:23], s51 offen lds\n\t"
      "s_waitcnt lgkmcnt(3)\n\t"
      "v_mfma_f32_16x16x32_bf16 v[80:83], a[240:243], v[128:131], v[80:83]\n\t"
      "v_cvt_pk_bf16_f32 v43, v46, v47\n\t"
      "ds_write_b128 v218, v[40:43] offset:24576\n\t"
      "ds_read_b128 v[32:35], v196 offset:49152\n\t"
      "v_mfma_f32_16x16x32_bf16 v[84:87], a[244:247], v[128:131], v[84:87]\n\t"
      "v_cvt_pk_bf16_f32 v48, v48, v49\n\t"
      "v_cvt_pk_bf16_f32 v49, v50, v51\n\t"
      "v_mfma_f32_16x16x32_bf16 v[88:91], a[248:251], v[128:131], v[88:91]\n\t"
      "v_cvt_pk_bf16_f32 v50, v52, v53\n\t"
      "v_cvt_pk_bf16_f32 v51, v54, v55\n\t"
      "buffer_load_dwordx4 v208, s[20:23], s51 offen offset:1024 lds\n\t"
      "v_mfma_f32_16x16x32_bf16 v[92:95], a[252:255], v[128:131], v[92:95]\n\t"
      "ds_write_b128 v219, v[48:51] offset:24576\n\t"
      "ds_read_b128 v[36:39], v197 offset:49152\n\t"
      "v_cvt_pk_bf16_f32 v56, v56, v57\n\t"
      "v_mfma_f32_16x16x32_bf16 v[96:99], v[164:167], v[128:131], v[96:99]\n\t"
      "ds_read_b128 v[152:155], v197 offset:37376\n\t"
      "v_cvt_pk_bf16_f32 v57, v58, v59\n\t"
      "v_cvt_pk_bf16_f32 v58, v60, v61\n\t"
      "v_mfma_f32_16x16x32_bf16 v[100:103], v[168:171], v[128:131], v[100:103]\n\t"
      "v_cvt_pk_bf16_f32 v59, v62, v63\n\t"
      "ds_write_b128 v217, v[56:59] offset:36864\n\t"
      "ds_read_b128 v[40:43], v198 offset:49152\n\t"
      "s_waitcnt lgkmcnt(9)\n\t"
      "v_mfma_f32_16x16x32_bf16 v[104:107], a[240:243], v[136:139], v[104:107]\n\t"
      "v_cvt_pk_bf16_f32 v64, v64, v65\n\t"
      "v_cvt_pk_bf16_f32 v65, v66, v67\n\t"
      "buffer_load_dwordx4 v208, s[20:23], s51 offen offset:2048 lds\n\t"
      "v_mfma_f32_16x16x32_bf16 v[108:111], a[244:247], v[136:139], v[108:111]\n\t"
      "v_cvt_pk_bf16_f32 v66, v68, v69\n\t"
      "v_cvt_pk_bf16_f32 v67, v70, v71\n\t"
      "v_mfma_f32_16x16x32_bf16 v[112:115], a[248:251], v[136:139], v[112:115]\n\t"
      "ds_write_b128 v218, v[64:67] offset:36864\n\t"
      "ds_read_b128 v[44:47], v199 offset:49152\n\t"
      "v_cvt_pk_bf16_f32 v72, v72, v73\n\t"
      "v_mfma_f32_16x16x32_bf16 v[116:119], a[252:255], v[136:139], v[116:119]\n\t"
      "v_cvt_pk_bf16_f32 v73, v74, v75\n\t"
      "v_cvt_pk_bf16_f32 v74, v76, v77\n\t"
      "v_mfma_f32_16x16x32_bf16 v[120:123], v[164:167], v[136:139], v[120:123]\n\t"
      "v_cvt_pk_bf16_f32 v75, v78, v79\n\t"
      "ds_write_b128 v219, v[72:75] offset:36864\n\t"
      "ds_read_b128 v[48:51], v197 offset:49408\n\t"
      "v_mfma_f32_16x16x32_bf16 v[132:135], v[168:171], v[136:139], v[132:135]\n\t"
      "s_add_u32 m0, s41, 0x3000\n\t"
      "s_add_u32 s51, s40, 0x1200000\n\t"
      "buffer_load_dwordx4 v209, s[20:23], s51 offen lds\n\t"
      "ds_read_b128 v[52:55], v198 offset:49408\n\t"
      "s_waitcnt lgkmcnt(13)\n\t"
      "v_mfma_f32_16x16x32_bf16 v[80:83], v[172:175], v[148:151], v[80:83]\n\t"
      "ds_read_b128 v[56:59], v199 offset:49408\n\t"
      "ds_read_b128 v[60:63], v196 offset:49408\n\t"
      "v_mfma_f32_16x16x32_bf16 v[84:87], v[176:179], v[148:151], v[84:87]\n\t"
      "ds_read_b128 v[64:67], v198 offset:49664\n\t"
      "s_memrealtime s[80:81]\n\t"
      "v_mfma_f32_16x16x32_bf16 v[88:91], v[180:183], v[148:151], v[88:91]\n\t"
      "buffer_load_dwordx4 v209, s[20:23], s51 offen offset:1024 lds\n\t"
      "ds_read_b128 v[68:71], v199 offset:49664\n\t"
      "v_mfma_f32_16x16x32_bf16 v[92:95], v[184:187], v[148:151], v[92:95]\n\t"
      "ds_read_b128 v[72:75], v196 offset:49664\n\t"
      "ds_read_b128 v[76:79], v197 offset:49664\n\t"
      "v_mfma_f32_16x16x32_bf16 v[96:99], v[188:191], v[148:151], v[96:99]\n\t"
      "v_mfma_f32_16x16x32_bf16 v[100:103], v[192:195], v[148:151], v[100:103]\n\t"
      "buffer_load_dwordx4 v209, s[20:23], s51 offen offset:2048 lds\n\t"
      "s_waitcnt lgkmcnt(13)\n\t"
      "v_mfma_f32_16x16x32_bf16 v[104:107], v[172:175], v[152:155], v[104:107]\n\t"
      "v_mfma_f32_16x16x32_bf16 v[108:111], v[176:179], v[152:155], v[108:111]\n\t"
      "v_mfma_f32_16x16x32_bf16 v[112:115], v[180:183], v[152:155], v[112:115]\n\t"
      "v_mfma_f32_16x16x32_bf16 v[116:119], v[184:187], v[152:155], v[116:119]\n\t"
      "v_mfma_f32_16x16x32_bf16 v[120:123], v[188:191], v[152:155], v[120:123]\n\t"
      "v_mfma_f32_16x16x32_bf16 v[132:135], v[192:195], v[152:155], v[132:135]\n\t"
      "v_add_u32_e32 v196, s46, v200\n\t"
      "v_add_u32_e32 v197, s46, v201\n\t"
      "v_add_u32_e32 v198, s46, v202\n\t"
      "v_add_u32_e32 v199, s46, v203\n\t"
      "s_mov_b32 s87, 76\n\t"
      "s_mov_b32 s86, 80\n\t"
      "s_mov_b32 s84, 96\n\t"
      "s_branch .Lp2_%=\n\t"
      ".Lend_%=:\n\t"
      ".Lconv_end:\n\t"
      :
      : "{s[16:31]}"(d16), "{s[36:39]}"(db), "{s[52:53]}"(rs), "{s40}"(t0), "{s41}"(p0), "{s43}"(lds0), "{s94}"(ty), "{s95}"(tres), "{s[60:61]}"(pres),
      "{s69}"(pre1), "{s74}"(plm), "{s75}"(plb), "{s66}"(sst ? 24576u : 0u - 24576u), "{s89}"((blk >> 4) % 5u), "{v208}"(ga), "{v209}"(gb),
      "{v211}"(woff[0]), "{v212}"(woff[1]), "{v213}"(woff[2]), "{v248}"(ta), "{v249}"(ta), "{v250}"(tp), "{v251}"(t1), "{v252}"(mk), "{v245}"(vgv),
      "{v246}"(vxw), "{v247}"(vxr), "{v253}"(vcell), "{v200}"(cfa[0]), "{v201}"(cfa[1]), "{v202}"(cfa[2]), "{v203}"(cfa[3]), "{v204}"(csw[0]),
      "{v205}"(csw[1]), "{v206}"(csw[2]), "{v207}"(csr), "{v214}"(tbase + 73728u), "{v221}"(tbase + 135168u), "{v[226:229]}"(br0), "{v[230:233]}"(br1),
      "{v[234:237]}"(br2), "{v6}"(cpo)
      : "memory", "scc", "vcc",
      "s42", "s44", "s45", "s46", "s47", "s48", "s49", "s50", "s51", "s54", "s55", "s56", "s57", "s58", "s59", "s62", "s63", "s64", "s65", "s67",
      "s68", "s70", "s71", "s72", "s73", "s76", "s77", "s78", "s79", "s80", "s81", "s82", "s83", "s84", "s85", "s86", "s87", "s88", "s90", "s91",
      "s92", "s93", "s96", "s97", "s98", "s99",
      "v1", "v2", "v3", "v4", "v5", "v7", "v8", "v9", "v10", "v11", "v12", "v13", "v14", "v15", "v16", "v17", "v18", "v19", "v20", "v21", "v22", "v23",
      "v24", "v25", "v26", "v27", "v28", "v29", "v30", "v31", "v32", "v33", "v34", "v35", "v36", "v37", "v38", "v39", "v40", "v41", "v42", "v43",
      "v44", "v45", "v46", "v47", "v48", "v49", "v50", "v51", "v52", "v53", "v54", "v55", "v56", "v57", "v58", "v59", "v60", "v61", "v62", "v63",
      "v64", "v65", "v66", "v67", "v68", "v69", "v70", "v71", "v72", "v73", "v74", "v75", "v76", "v77", "v78", "v79", "v80", "v81", "v82", "v83",
      "v84", "v85", "v86", "v87", "v88", "v89", "v90", "v91", "v92", "v93", "v94", "v95", "v96", "v97", "v98", "v99", "v100", "v101", "v102", "v103",
      "v104", "v105", "v106", "v107", "v108", "v109", "v110", "v111", "v112", "v113", "v114", "v115", "v116", "v117", "v118", "v119", "v120", "v121",
      "v122", "v123", "v124", "v125", "v126", "v127", "v128", "v129", "v130", "v131", "v132", "v133", "v134", "v135", "v136", "v137", "v138", "v139",
      "v140", "v141", "v142", "v143", "v144", "v145", "v146", "v147", "v148", "v149", "v150", "v151", "v152", "v153", "v154", "v155", "v156", "v157",
      "v158", "v159", "v160", "v161", "v162", "v163", "v164", "v165", "v166", "v167", "v168", "v169", "v170", "v171", "v172", "v173", "v174", "v175",
      "v176", "v177", "v178", "v179", "v180", "v181", "v182", "v183", "v184", "v185", "v186", "v187", "v188", "v189", "v190", "v191", "v192", "v193",
      "v194", "v195", "v196", "v197", "v198", "v199", "v210", "v215", "v216", "v217", "v218", "v219", "v220", "v222", "v223", "v224", "v225", "v238",
      "v239", "v240", "v241", "v242", "v243", "v244", "v254", "v255",
      "a0", "a1", "a2", "a3", "a4", "a5", "a6", "a7", "a8", "a9", "a10", "a11", "a12", "a13", "a14", "a15", "a16", "a17", "a18", "a19", "a20", "a21",
      "a22", "a23", "a24", "a25", "a26", "a27", "a28", "a29", "a30", "a31", "a32", "a33", "a34", "a35", "a36", "a37", "a38", "a39", "a40", "a41",
      "a42", "a43", "a44", "a45", "a46", "a47", "a48", "a49", "a50", "a51", "a52", "a53", "a54", "a55", "a56", "a57", "a58", "a59", "a60", "a61",
      "a62", "a63", "a64", "a65", "a66", "a67", "a68", "a69", "a70", "a71", "a72", "a73", "a74", "a75", "a76", "a77", "a78", "a79", "a80", "a81",
      "a82", "a83", "a84", "a85", "a86", "a87", "a88", "a89", "a90", "a91", "a92", "a93", "a94", "a95", "a96", "a97", "a98", "a99", "a100", "a101",
      "a102", "a103", "a104", "a105", "a106", "a107", "a108", "a109", "a110", "a111", "a112", "a113", "a114", "a115", "a116", "a117", "a118", "a119",
      "a120", "a121", "a122", "a123", "a124", "a125", "a126", "a127", "a128", "a129", "a130", "a131", "a132", "a133", "a134", "a135", "a136", "a137",
      "a138", "a139", "a140", "a141", "a142", "a143", "a144", "a145", "a146", "a147", "a148", "a149", "a150", "a151", "a152", "a153", "a154", "a155",
      "a156", "a157", "a158", "a159", "a160", "a161", "a162", "a163", "a164", "a165", "a166", "a167", "a168", "a169", "a170", "a171", "a172", "a173",
      "a174", "a175", "a176", "a177", "a178", "a179", "a180", "a181", "a182", "a183", "a184", "a185", "a186", "a187", "a188", "a189", "a190", "a191",
      "a192", "a193", "a194", "a195", "a196", "a197", "a198", "a199", "a200", "a201", "a202", "a203", "a204", "a205", "a206", "a207", "a208", "a209",
      "a210", "a211", "a212", "a213", "a214", "a215", "a216", "a217", "a218", "a219", "a220", "a221", "a222", "a223", "a224", "a225", "a226", "a227",
      "a228", "a229", "a230", "a231", "a232", "a233", "a234", "a235", "a236", "a237", "a238", "a239", "a240", "a241", "a242", "a243", "a244", "a245",
      "a246", "a247", "a248", "a249", "a250", "a251", "a252", "a253", "a254", "a255");
}

extern "C" void kernel_launch(void* const* d_in, const int* in_sizes, int n_in,
                              void* d_out, int out_size, void* d_ws, size_t ws_size,
                              hipStream_t stream) {
  conv1x1_kernel<<<256, 256, 0, stream>>>(d_in[1], 0x0002000000048000ull, d_in[0], 0x0002000006c00000ull, d_ws, 0x0002000000000c00ull, d_out, 0x0002000006c00000ull,
                                          d_in[2], 0x0002000000000300ull);
}
```

```cpp
#include <hip/hip_runtime.h>
#include <stdint.h>

#define LAS __attribute__((address_space(3)))
typedef __attribute__((ext_vector_type(16))) unsigned u32x16;
typedef __attribute__((ext_vector_type(4))) unsigned u32x4;
typedef unsigned long long u64;

__global__ __launch_bounds__(256, 1) void conv1x1_kernel(const void* w, u64 cw, const void* x, u64 cx, void* ws, u64 cs, void* out, u64 co,
                                                         const void* bias, u64 cb) {
  __shared__ __attribute__((aligned(1024))) unsigned char lds[160768];

  u32x16 d16;
  u32x4 db;
  u64 rs;
  asm volatile(
      "s_load_dwordx16 s[16:31], %3, 0x0\n\t"
      "s_load_dwordx4 s[36:39], %3, 0x40\n\t"
      "s_memrealtime s[52:53]\n\t"
      : "={s[16:31]}"(d16), "={s[36:39]}"(db), "={s[52:53]}"(rs)
      : "s"(__builtin_amdgcn_kernarg_segment_ptr())
      : "memory");
  unsigned cpo = ((blockIdx.x >> 3) * 256u + threadIdx.x) * 4u;
  asm volatile(
      "s_getpc_b64 s[92:93]\n\t"
      ".Lconv_cp:\n\t"
      "v_min_u32_e32 v6, .Lconv_end-.Lconv_cp-4, v6\n\t"
      "global_load_dword v6, v6, s[92:93]\n\t"
      : "+{v6}"(cpo)
      :
      : "memory", "s92", "s93");
  __builtin_amdgcn_sched_barrier(0);
  unsigned tid  = threadIdx.x;
  unsigned lane = tid & 63;
  unsigned wv   = __builtin_amdgcn_readfirstlane(tid >> 6);
  const unsigned lds0 = (unsigned)(size_t)(LAS unsigned char*)lds;
  unsigned blk  = blockIdx.x;
  const unsigned p0   = lds0 + 3072u * wv;
  const unsigned lq = lane >> 4, ph = (lane >> 2) & 3, pl = lane & 3;
  const unsigned gs = 256u * lq + 64u * ((ph - lq) & 3) + 16u * (pl ^ ((4u - wv) & 3));
  const unsigned ga = gs + 3072u * (wv < 3u ? wv : 5u);
  const unsigned gb = gs + 3072u * (wv < 2u ? wv + 6u : wv + 1u);
  const unsigned boff = 192u * wv + 16u * lq;
  const unsigned role = (blk >> 2) & 1u;
  unsigned pk = (blk & 3u) * 32u + (blk >> 3);
  const unsigned sst  = (role ^ (blk >> 4)) & 1u;
  const unsigned t0   = pk * 49152u + sst * 24576u;
  const unsigned pv = blk >> 7, ps = ((blk & 7u) | ((blk >> 1) & 56u)) ^ (4u * pv), pgr = (ps >> 2) & 7u, pi = (ps & 3u) * 2u + pv;
  const unsigned ppg = min(pgr * 8u, 52u) + (pi & (0x13777777u >> (4u * pgr)) & 7u);
  const unsigned pop = (ppg << 21) + ((ps & 3u) * 4u + pv) * 0x1c080u + 0x24080u;
  unsigned pfire = ((role ^ ppg) & 1u) * 2u, psel = ps >> 5;
  const unsigned vpub = lane == 0u ? 0u + (pv * 64u + ps) * 8u : 0x40000000u;
  const unsigned pfo = (blk >> 4) * 18432u;
  const unsigned pfv = tid * 16u;
  const unsigned ty = (blk >> 3) & 1u;
  const unsigned tidx = (blk >> 3) * 4u + wv, tsel = tidx >> 6;
  const unsigned toff = (tidx & 63u) < 54u ? (tidx & 63u) << 21 : 113246204u;
  unsigned tres, pre1;
  u64 pres;
  u32x4 br0, br1, br2;
  asm volatile(
      "s_add_u32 m0, s43, 0x27000\n\t"
      "s_waitcnt lgkmcnt(0)\n\t"
      "s_cmp_eq_u32 s94, 1\n\t"
      "s_cbranch_scc0 .Lc1_%=\n\t"
      "s_cmp_eq_u32 s41, s43\n\t"
      "s_cbranch_scc0 .Lc0_%=\n\t"
      "s_cmp_eq_u32 s78, 0\n\t"
      "s_cselect_b64 s[96:97], s[20:21], s[28:29]\n\t"
      "s_load_dword s69, s[96:97], s76 offset:0x0\n\t"
      "s_load_dwordx2 s[60:61], s[96:97], s76 offset:0x3300\n\t"
      ".Lc0_%=:\n\t"
      "buffer_load_dwordx4 v216, s[16:19], s42 offen lds\n\t"
      "s_add_u32 s51, s42, 0x1000\n\t"
      "buffer_load_dwordx4 v216, s[16:19], s51 offen lds\n\t"
      "s_add_u32 s51, s42, 0x2000\n\t"
      "buffer_load_dwordx4 v216, s[16:19], s51 offen lds\n\t"
      "s_add_u32 s51, s42, 0x3000\n\t"
      "buffer_load_dwordx4 v216, s[16:19], s51 offen lds\n\t"
      "s_add_u32 s51, s42, 0x4000\n\t"
      "buffer_load_dwordx4 v216, s[16:19], s51 offen lds\n\t"
      ".Lc1_%=:\n\t"
      "buffer_load_dwordx4 v[226:229], v215, s[36:39], 0 offen offset:0\n\t"
      "buffer_load_dwordx4 v[230:233], v215, s[36:39], 0 offen offset:64\n\t"
      "buffer_load_dwordx4 v[234:237], v215, s[36:39], 0 offen offset:128\n\t"
      "s_mov_b32 m0, s41\n\t"
      "s_nop 0\n\t"
      "buffer_load_dwordx4 v208, s[20:23], s40 offen lds\n\t"
      "buffer_load_dwordx4 v208, s[20:23], s40 offen offset:1024 lds\n\t"
      "buffer_load_dwordx4 v208, s[20:23], s40 offen offset:2048 lds\n\t"
      "s_cmp_eq_u32 s94, 0\n\t"
      "s_cbranch_scc0 .Lvb0_%=\n\t"
      "s_add_u32 m0, s41, 0x3000\n\t"
      "s_nop 0\n\t"
      "buffer_load_dwordx4 v209, s[20:23], s40 offen lds\n\t"
      "buffer_load_dwordx4 v209, s[20:23], s40 offen offset:1024 lds\n\t"
      "buffer_load_dwordx4 v209, s[20:23], s40 offen offset:2048 lds\n\t"
      "s_cmp_eq_u32 s99, 0\n\t"
      "s_cselect_b64 s[96:97], s[20:21], s[28:29]\n\t"
      "s_load_dword s95, s[96:97], s98\n\t"
      "s_branch .Lvb1_%=\n\t"
      ".Lvb0_%=:\n\t"
      "s_cmp_eq_u32 s41, s43\n\t"
      "s_cbranch_scc1 .Lev0_%=\n\t"
      "s_cmp_eq_u32 s99, 0\n\t"
      "s_cselect_b64 s[96:97], s[20:21], s[28:29]\n\t"
      "s_load_dword s95, s[96:97], s98\n\t"
      "s_cmp_eq_u32 s94, 0\n\t"
      "s_cbranch_scc1 .Lev1_%=\n\t"
      "s_sleep 8\n\t"
      "s_branch .Lev1_%=\n\t"
      ".Lev0_%=:\n\t"
      "s_andn2_b32 s79, s52, 15\n\t"
      "v_mov_b32_e32 v241, s53\n\t"
      "s_waitcnt lgkmcnt(2)\n\t"
      "s_nop 7\n\t"
      "s_getreg_b32 s78, hwreg(HW_REG_IB_STS, 8, 4)\n\t"
      "s_cmp_eq_u32 s78, 1\n\t"
      "s_cselect_b32 s51, 2, 0\n\t"
      "s_xor_b32 s77, s77, s51\n\t"
      "s_cmp_lg_u32 s78, 0\n\t"
      "s_cselect_b32 s77, s77, 1\n\t"
      "s_or_b32 s79, s79, s77\n\t"
      "v_mov_b32_e32 v240, s79\n\t"
      "buffer_store_dwordx2 v[240:241], v244, s[24:27], 0 offen sc1\n\t"
      "s_cmp_eq_u32 s99, 0\n\t"
      "s_cselect_b64 s[96:97], s[20:21], s[28:29]\n\t"
      "s_load_dword s95, s[96:97], s98\n\t"
      ".Lev1_%=:\n\t"
      "s_add_u32 m0, s41, 0x3000\n\t"
      "s_nop 0\n\t"
      "buffer_load_dwordx4 v209, s[20:23], s40 offen lds\n\t"
      "buffer_load_dwordx4 v209, s[20:23], s40 offen offset:1024 lds\n\t"
      "buffer_load_dwordx4 v209, s[20:23], s40 offen offset:2048 lds\n\t"
      "s_add_u32 m0, s41, 0x6000\n\t"
      "s_add_u32 s51, s40, 0x600000\n\t"
      "buffer_load_dwordx4 v208, s[20:23], s51 offen lds\n\t"
      "buffer_load_dwordx4 v208, s[20:23], s51 offen offset:1024 lds\n\t"
      "buffer_load_dwordx4 v208, s[20:23], s51 offen offset:2048 lds\n\t"
      "s_add_u32 m0, s41, 0x9000\n\t"
      "s_add_u32 s51, s40, 0x600000\n\t"
      "buffer_load_dwordx4 v209, s[20:23], s51 offen lds\n\t"
      "buffer_load_dwordx4 v209, s[20:23], s51 offen offset:1024 lds\n\t"
      "buffer_load_dwordx4 v209, s[20:23], s51 offen offset:2048 lds\n\t"
      ".Lvb1_%=:\n\t"
      : "={s95}"(tres), "={s[60:61]}"(pres), "={s69}"(pre1), "={v[226:229]}"(br0), "={v[230:233]}"(br1), "={v[234:237]}"(br2),
        "+v"(lane), "+{s77}"(pfire), "+{s78}"(psel), "+{v6}"(cpo)
      : "{s[16:31]}"(d16), "{s[36:39]}"(db), "{s40}"(t0), "{s41}"(p0), "{s43}"(lds0), "{s94}"(ty),
        "{v208}"(ga), "{v209}"(gb), "{v215}"(boff), "{v216}"(pfv),
        "{s42}"(pfo), "{s98}"(toff), "{s99}"(tsel), "{s76}"(pop), "{s[52:53]}"(rs), "{v244}"(vpub)
      : "memory", "scc", "s51", "s92", "s93", "s96", "s97", "s79", "v240", "v241");
  asm volatile("" : "+s"(wv), "+s"(pk));
  __builtin_amdgcn_sched_barrier(0);
  const unsigned vcell = lane < 2u ? 2048u + pk * 8u + 4u * (role ^ lane) : 0x40000000u;
  const unsigned ob = (unsigned)(((lane < 12u ? 0x41ccd62ca418820ull >> (5u * lane) : 0x22a4c1eeull >> (5u * ((lane - 12u) & 7u)))) & 31u) * 6291456u + pk * 49152u;
  const unsigned pg = (ob + 24576u) >> 21;
  const unsigned ta = lane < 18u ? ob + 24576u * sst : 113246208u, t1 = ob + 24576u * (sst ^ 1u);
  const unsigned tq = (lane >= 9u ? 83u : 80u), tp = lane >= 14u ? 0u : tq;
  const unsigned mk = lane >= 4u + ty && lane < 18u ? 1u << (pg < 52u ? (pg >> 3) * 4u : 28u) : 0u;
  const unsigned plm = (unsigned)__builtin_amdgcn_ballot_w64(((pg ^ role ^ sst) & 1u) != 0u), plb = wv < 2u ? plm : ~plm;
  const unsigned vgv = 0u + (wv & 1u) * 512u + lane * 8u;
  const unsigned vxr = lds0 + 159744u + lane * 16u, vxw = vxr + wv * 4u;
  const unsigned li = lane & 15, lg = lane >> 4, rl = li & 3, hi = li >> 2;
  const unsigned rb = 768u * li + 16u * (lg ^ ((4u - hi) & 3)) - 3072u * wv;
  unsigned cfa[4], csw[3];
#pragma unroll
  for (unsigned u = 0; u < 4; ++u) cfa[u] = rb + 64u * ((u - rl) & 3);
#pragma unroll
  for (unsigned j = 0; j < 3; ++j) {
    const unsigned e = 3u * wv + j;
    csw[j] = rb + 256u * (e >> 2) + 64u * (((e & 3) + (e >> 2) - rl) & 3);
  }
  const unsigned csr = 16u * lane;
  const unsigned tbase = p0 + (4u * lg + (li >> 2)) * 192u + 16u * (li & 3);
  unsigned woff[3];
#pragma unroll
  for (unsigned j = 0; j < 3; ++j) {
    const unsigned q = 64u * j + lane, rr = q / 12u;
    woff[j] = (8u * (rr >> 2) + (rr & 3)) * 768u + 192u * wv + 16u * (q % 12u) - 1024u * j;
  }
  asm volatile(
      "s_cmp_eq_u32 s94, 0\n\t"
      "s_cbranch_scc0 .Lc2_%=\n\t"
      "s_add_u32 m0, s41, 0x1e000\n\t"
      "s_mov_b32 s51, 0x0\n\t"
      "buffer_load_dwordx4 v211, s[16:19], s51 offen lds\n\t"
      "buffer_load_dwordx4 v212, s[16:19], s51 offen offset:1024 lds\n\t"
      "buffer_load_dwordx4 v213, s[16:19], s51 offen offset:2048 lds\n\t"
      ".Lc2_%=:\n\t"
      "s_add_u32 s49, s89, 5\n\t"
      "s_mov_b32 s62, 0\n\t"
      "s_mov_b32 s70, 0\n\t"
      "s_add_u32 s50, s41, 0x12000\n\t"
      "s_add_u32 s51, s41, 0x12000\n\t"
      "s_sub_u32 s63, s52, 800\n\t"
      "s_andn2_b32 s63, s63, 15\n\t"
      "v_add_u32_e32 v196, s41, v200\n\t"
      "v_add_u32_e32 v197, s41, v201\n\t"
      "v_add_u32_e32 v198, s41, v202\n\t"
      "v_add_u32_e32 v199, s41, v203\n\t"
      "v_add_u32_e32 v217, s51, v204\n\t"
      "v_add_u32_e32 v218, s51, v205\n\t"
      "v_add_u32_e32 v219, s51, v206\n\t"
      "v_add_u32_e32 v220, s51, v207\n\t"
      "v_add_u32_e32 v238, s51, v207\n\t"
      "s_cmp_eq_u32 s94, 0\n\t"
      "s_cbranch_scc0 .Lpb_%=\n\t"
      "s_add_u32 m0, s41, 0x21000\n\t"
      "s_mov_b32 s51, 0xc00\n\t"
      "buffer_load_dwordx4 v211, s[16:19], s51 offen lds\n\t"
      "buffer_load_dwordx4 v212, s[16:19], s51 offen offset:1024 lds\n\t"
      "buffer_load_dwordx4 v213, s[16:19], s51 offen offset:2048 lds\n\t"
      "s_add_u32 m0, s41, 0x24000\n\t"
      "s_mov_b32 s51, 0x6000\n\t"
      "buffer_load_dwordx4 v211, s[16:19], s51 offen lds\n\t"
      "buffer_load_dwordx4 v212, s[16:19], s51 offen offset:1024 lds\n\t"
      "buffer_load_dwordx4 v213, s[16:19], s51 offen offset:2048 lds\n\t"
      "s_waitcnt vmcnt(15)\n\t"
      "v_lshlrev_b32_e32 v8, 16, v226\n\t"
      "v_and_b32_e32 v9, 0xffff0000, v226\n\t"
      "v_lshlrev_b32_e32 v10, 16, v227\n\t"
      "v_and_b32_e32 v11, 0xffff0000, v227\n\t"
      "v_lshlrev_b32_e32 v12, 16, v228\n\t"
      "v_and_b32_e32 v13, 0xffff0000, v228\n\t"
      "v_lshlrev_b32_e32 v14, 16, v229\n\t"
      "v_and_b32_e32 v15, 0xffff0000, v229\n\t"
      "v_lshlrev_b32_e32 v16, 16, v230\n\t"
      "v_and_b32_e32 v17, 0xffff0000, v230\n\t"
      "v_lshlrev_b32_e32 v18, 16, v231\n\t"
      "v_and_b32_e32 v19, 0xffff0000, v231\n\t"
      "v_lshlrev_b32_e32 v20, 16, v232\n\t"
      "v_and_b32_e32 v21, 0xffff0000, v232\n\t"
      "v_lshlrev_b32_e32 v22, 16, v233\n\t"
      "v_and_b32_e32 v23, 0xffff0000, v233\n\t"
      "v_lshlrev_b32_e32 v24, 16, v234\n\t"
      "v_and_b32_e32 v25, 0xffff0000, v234\n\t"
      "v_lshlrev_b32_e32 v26, 16, v235\n\t"
      "v_and_b32_e32 v27, 0xffff0000, v235\n\t"
      "v_lshlrev_b32_e32 v28, 16, v236\n\t"
      "v_and_b32_e32 v29, 0xffff0000, v236\n\t"
      "v_lshlrev_b32_e32 v30, 16, v237\n\t"
      "v_and_b32_e32 v31, 0xffff0000, v237\n\t"
      "s_add_u32 m0, s41, 0x12000\n\t"
      "s_mov_b32 s51, 0x6c00\n\t"
      "buffer_load_dwordx4 v211, s[16:19], s51 offen lds\n\t"
      "buffer_load_dwordx4 v212, s[16:19], s51 offen offset:1024 lds\n\t"
      "buffer_load_dwordx4 v213, s[16:19], s51 offen offset:2048 lds\n\t"
      "s_add_u32 m0, s41, 0x15000\n\t"
      "s_mov_b32 s51, 0xc000\n\t"
      "buffer_load_dwordx4 v211, s[16:19], s51 offen lds\n\t"
      "buffer_load_dwordx4 v212, s[16:19], s51 offen offset:1024 lds\n\t"
      "buffer_load_dwordx4 v213, s[16:19], s51 offen offset:2048 lds\n\t"
      "s_add_u32 m0, s41, 0x18000\n\t"
      "s_mov_b32 s51, 0xcc00\n\t"
      "buffer_load_dwordx4 v211, s[16:19], s51 offen lds\n\t"
      "buffer_load_dwordx4 v212, s[16:19], s51 offen offset:1024 lds\n\t"
      "buffer_load_dwordx4 v213, s[16:19], s51 offen offset:2048 lds\n\t"
      "s_add_u32 m0, s41, 0x1b000\n\t"
      "s_mov_b32 s51, 0x12000\n\t"
      "buffer_load_dwordx4 v211, s[16:19], s51 offen lds\n\t"
      "buffer_load_dwordx4 v212, s[16:19], s51 offen offset:1024 lds\n\t"
      "buffer_load_dwordx4 v213, s[16:19], s51 offen offset:2048 lds\n\t"
      "s_mov_b32 s44, 0\n\t"
      "s_mov_b32 s85, 3\n\t"
      "s_mov_b32 s45, s41\n\t"
      "s_add_u32 s46, s41, 0x6000\n\t"
      "s_add_u32 s47, s41, 0xc000\n\t"
      "s_mov_b64 vcc, 0\n\t"
      "s_waitcnt vmcnt(18)\n\t"
      "s_barrier\n\t"
      "ds_read_b64_tr_b16 a[0:1], v214 offset:49152\n\t"
      "ds_read_b64_tr_b16 a[4:5], v214 offset:49160\n\t"
      "ds_read_b64_tr_b16 a[8:9], v214 offset:49216\n\t"
      "ds_read_b64_tr_b16 a[12:13], v214 offset:49224\n\t"
      "ds_read_b64_tr_b16 a[16:17], v214 offset:49280\n\t"
      "ds_read_b64_tr_b16 a[20:21], v214 offset:49288\n\t"
      "ds_read_b128 v[140:143], v196 offset:0\n\t"
      "s_waitcnt vmcnt(15)\n\t"
      "ds_read_b64_tr_b16 a[2:3], v221 offset:0\n\t"
      "ds_read_b64_tr_b16 a[6:7], v221 offset:8\n\t"
      "ds_read_b64_tr_b16 a[10:11], v221 offset:64\n\t"
      "ds_read_b64_tr_b16 a[14:15], v221 offset:72\n\t"
      "ds_read_b64_tr_b16 a[18:19], v221 offset:128\n\t"
      "ds_read_b64_tr_b16 a[22:23], v221 offset:136\n\t"
      "ds_read_b128 v[152:155], v196 offset:12288\n\t"
      "s_waitcnt lgkmcnt(8)\n\t"
      "s_add_u32 m0, s41, 0x1e000\n\t"
      "s_mov_b32 s51, 0x12c00\n\t"
      "buffer_load_dwordx4 v211, s[16:19], s51 offen lds\n\t"
      "buffer_load_dwordx4 v212, s[16:19], s51 offen offset:1024 lds\n\t"
      "buffer_load_dwordx4 v213, s[16:19], s51 offen offset:2048 lds\n\t"
      "s_waitcnt vmcnt(15)\n\t"
      "ds_read_b64_tr_b16 a[24:25], v221 offset:12288\n\t"
      "ds_read_b64_tr_b16 a[28:29], v221 offset:12296\n\t"
      "ds_read_b64_tr_b16 a[32:33], v221 offset:12352\n\t"
      "ds_read_b64_tr_b16 a[36:37], v221 offset:12360\n\t"
      "ds_read_b64_tr_b16 a[40:41], v221 offset:12416\n\t"
      "ds_read_b64_tr_b16 a[44:45], v221 offset:12424\n\t"
      "ds_read_b128 v[144:147], v197 offset:0\n\t"
      "s_waitcnt lgkmcnt(8)\n\t"
      "v_mfma_f32_16x16x32_bf16 v[80:83], a[0:3], v[140:143], v[8:11]\n\t"
      "s_add_u32 m0, s41, 0x21000\n\t"
      "s_mov_b32 s51, 0x18000\n\t"
      "buffer_load_dwordx4 v211, s[16:19], s51 offen lds\n\t"
      "v_mfma_f32_16x16x32_bf16 v[84:87], a[4:7], v[140:143], v[12:15]\n\t"
      "buffer_load_dwordx4 v212, s[16:19], s51 offen offset:1024 lds\n\t"
      "v_mfma_f32_16x16x32_bf16 v[88:91], a[8:11], v[140:143], v[16:19]\n\t"
      "buffer_load_dwordx4 v213, s[16:19], s51 offen offset:2048 lds\n\t"
      "s_waitcnt vmcnt(15)\n\t"
      "ds_read_b64_tr_b16 a[26:27], v214 offset:0\n\t"
      "v_mfma_f32_16x16x32_bf16 v[92:95], a[12:15], v[140:143], v[20:23]\n\t"
      "ds_read_b64_tr_b16 a[30:31], v214 offset:8\n\t"
      "ds_read_b64_tr_b16 a[34:35], v214 offset:64\n\t"
      "v_mfma_f32_16x16x32_bf16 v[96:99], a[16:19], v[140:143], v[24:27]\n\t"
      "ds_read_b64_tr_b16 a[38:39], v214 offset:72\n\t"
      "ds_read_b64_tr_b16 a[42:43], v214 offset:128\n\t"
      "v_mfma_f32_16x16x32_bf16 v[100:103], a[20:23], v[140:143], v[28:31]\n\t"
      "ds_read_b64_tr_b16 a[46:47], v214 offset:136\n\t"
      "ds_read_b128 v[156:159], v197 offset:12288\n\t"
      "s_waitcnt lgkmcnt(8)\n\t"
      "v_mfma_f32_16x16x32_bf16 v[104:107], a[0:3], v[152:155], v[8:11]\n\t"
      "s_add_u32 m0, s41, 0x24000\n\t"
      "s_mov_b32 s51, 0x18c00\n\t"
      "buffer_load_dwordx4 v211, s[16:19], s51 offen lds\n\t"
      "v_mfma_f32_16x16x32_bf16 v[108:111], a[4:7], v[152:155], v[12:15]\n\t"
      "buffer_load_dwordx4 v212, s[16:19], s51 offen offset:1024 lds\n\t"
      "v_mfma_f32_16x16x32_bf16 v[112:115], a[8:11], v[152:155], v[16:19]\n\t"
      "buffer_load_dwordx4 v213, s[16:19], s51 offen offset:2048 lds\n\t"
      "s_waitcnt vmcnt(15)\n\t"
      "ds_read_b64_tr_b16 a[48:49], v214 offset:12288\n\t"
      "v_mfma_f32_16x16x32_bf16 v[116:119], a[12:15], v[152:155], v[20:23]\n\t"
      "ds_read_b64_tr_b16 a[52:53], v214 offset:12296\n\t"
      "ds_read_b64_tr_b16 a[56:57], v214 offset:12352\n\t"
      "v_mfma_f32_16x16x32_bf16 v[120:123], a[16:19], v[152:155], v[24:27]\n\t"
      "ds_read_b64_tr_b16 a[60:61], v214 offset:12360\n\t"
      "ds_read_b64_tr_b16 a[64:65], v214 offset:12416\n\t"
      "v_mfma_f32_16x16x32_bf16 v[132:135], a[20:23], v[152:155], v[28:31]\n\t"
      "ds_read_b64_tr_b16 a[68:69], v214 offset:12424\n\t"
      "ds_read_b128 v[148:151], v198 offset:0\n\t"
      "s_waitcnt lgkmcnt(8)\n\t"
      "v_mfma_f32_16x16x32_bf16 v[80:83], a[24:27], v[144:147], v[80:83]\n\t"
      "s_add_u32 m0, s41, 0x12000\n\t"
      "s_mov_b32 s51, 0x1e000\n\t"
      "buffer_load_dwordx4 v211, s[16:19], s51 offen lds\n\t"
      "v_mfma_f32_16x16x32_bf16 v[84:87], a[28:31], v[144:147], v[84:87]\n\t"
      "buffer_load_dwordx4 v212, s[16:19], s51 offen offset:1024 lds\n\t"
      "v_mfma_f32_16x16x32_bf16 v[88:91], a[32:35], v[144:147], v[88:91]\n\t"
      "buffer_load_dwordx4 v213, s[16:19], s51 offen offset:2048 lds\n\t"
      "s_waitcnt vmcnt(15)\n\t"
      "ds_read_b64_tr_b16 a[50:51], v214 offset:24576\n\t"
      "v_mfma_f32_16x16x32_bf16 v[92:95], a[36:39], v[144:147], v[92:95]\n\t"
      "ds_read_b64_tr_b16 a[54:55], v214 offset:24584\n\t"
      "ds_read_b64_tr_b16 a[58:59], v214 offset:24640\n\t"
      "v_mfma_f32_16x16x32_bf16 v[96:99], a[40:43], v[144:147], v[96:99]\n\t"
      "ds_read_b64_tr_b16 a[62:63], v214 offset:24648\n\t"
      "ds_read_b64_tr_b16 a[66:67], v214 offset:24704\n\t"
      "v_mfma_f32_16x16x32_bf16 v[100:103], a[44:47], v[144:147], v[100:103]\n\t"
      "ds_read_b64_tr_b16 a[70:71], v214 offset:24712\n\t"
      "ds_read_b128 v[160:163], v198 offset:12288\n\t"
      "s_waitcnt lgkmcnt(8)\n\t"
      "v_mfma_f32_16x16x32_bf16 v[104:107], a[24:27], v[156:159], v[104:107]\n\t"
      "s_add_u32 m0, s41, 0x15000\n\t"
      "s_mov_b32 s51, 0x1ec00\n\t"
      "buffer_load_dwordx4 v211, s[16:19], s51 offen lds\n\t"
      "v_mfma_f32_16x16x32_bf16 v[108:111], a[28:31], v[156:159], v[108:111]\n\t"
      "buffer_load_dwordx4 v212, s[16:19], s51 offen offset:1024 lds\n\t"
      "v_mfma_f32_16x16x32_bf16 v[112:115], a[32:35], v[156:159], v[112:115]\n\t"
      "buffer_load_dwordx4 v213, s[16:19], s51 offen offset:2048 lds\n\t"
      "s_waitcnt vmcnt(15)\n\t"
      "ds_read_b64_tr_b16 a[72:73], v214 offset:36864\n\t"
      "v_mfma_f32_16x16x32_bf16 v[116:119], a[36:39], v[156:159], v[116:119]\n\t"
      "ds_read_b64_tr_b16 a[76:77], v214 offset:36872\n\t"
      "ds_read_b64_tr_b16 a[80:81], v214 offset:36928\n\t"
      "v_mfma_f32_16x16x32_bf16 v[120:123], a[40:43], v[156:159], v[120:123]\n\t"
      "ds_read_b64_tr_b16 a[84:85], v214 offset:36936\n\t"
      "ds_read_b64_tr_b16 a[88:89], v214 offset:36992\n\t"
      "v_mfma_f32_16x16x32_bf16 v[132:135], a[44:47], v[156:159], v[132:135]\n\t"
      "ds_read_b64_tr_b16 a[92:93], v214 offset:37000\n\t"
      "ds_read_b128 v[140:143], v199 offset:0\n\t"
      "s_waitcnt lgkmcnt(8)\n\t"
      "v_mfma_f32_16x16x32_bf16 v[80:83], a[48:51], v[148:151], v[80:83]\n\t"
      "s_add_u32 m0, s41, 0x18000\n\t"
      "s_mov_b32 s51, 0x24000\n\t"
      "buffer_load_dwordx4 v211, s[16:19], s51 offen lds\n\t"
      "v_mfma_f32_16x16x32_bf16 v[84:87], a[52:55], v[148:151], v[84:87]\n\t"
      "buffer_load_dwordx4 v212, s[16:19], s51 offen offset:1024 lds\n\t"
      "v_mfma_f32_16x16x32_bf16 v[88:91], a[56:59], v[148:151], v[88:91]\n\t"
      "buffer_load_dwordx4 v213, s[16:19], s51 offen offset:2048 lds\n\t"
      "s_waitcnt vmcnt(15)\n\t"
      "ds_read_b64_tr_b16 a[74:75], v214 offset:49152\n\t"
      "v_mfma_f32_16x16x32_bf16 v[92:95], a[60:63], v[148:151], v[92:95]\n\t"
      "ds_read_b64_tr_b16 a[78:79], v214 offset:49160\n\t"
      "ds_read_b64_tr_b16 a[82:83], v214 offset:49216\n\t"
      "v_mfma_f32_16x16x32_bf16 v[96:99], a[64:67], v[148:151], v[96:99]\n\t"
      "ds_read_b64_tr_b16 a[86:87], v214 offset:49224\n\t"
      "ds_read_b64_tr_b16 a[90:91], v214 offset:49280\n\t"
      "v_mfma_f32_16x16x32_bf16 v[100:103], a[68:71], v[148:151], v[100:103]\n\t"
      "ds_read_b64_tr_b16 a[94:95], v214 offset:49288\n\t"
      "ds_read_b128 v[152:155], v199 offset:12288\n\t"
      "s_waitcnt lgkmcnt(8)\n\t"
      "v_mfma_f32_16x16x32_bf16 v[104:107], a[48:51], v[160:163], v[104:107]\n\t"
      "s_add_u32 m0, s41, 0x1b000\n\t"
      "s_mov_b32 s51, 0x24c00\n\t"
      "buffer_load_dwordx4 v211, s[16:19], s51 offen lds\n\t"
      "v_mfma_f32_16x16x32_bf16 v[108:111], a[52:55], v[160:163], v[108:111]\n\t"
      "buffer_load_dwordx4 v212, s[16:19], s51 offen offset:1024 lds\n\t"
      "v_mfma_f32_16x16x32_bf16 v[112:115], a[56:59], v[160:163], v[112:115]\n\t"
      "buffer_load_dwordx4 v213, s[16:19], s51 offen offset:2048 lds\n\t"
      "s_waitcnt vmcnt(15)\n\t"
      "ds_read_b64_tr_b16 a[96:97], v221 offset:0\n\t"
      "v_mfma_f32_16x16x32_bf16 v[116:119], a[60:63], v[160:163], v[116:119]\n\t"
      "ds_read_b64_tr_b16 a[100:101], v221 offset:8\n\t"
      "ds_read_b64_tr_b16 a[104:105], v221 offset:64\n\t"
      "v_mfma_f32_16x16x32_bf16 v[120:123], a[64:67], v[160:163], v[120:123]\n\t"
      "ds_read_b64_tr_b16 a[108:109], v221 offset:72\n\t"
      "ds_read_b64_tr_b16 a[112:113], v221 offset:128\n\t"
      "v_mfma_f32_16x16x32_bf16 v[132:135], a[68:71], v[160:163], v[132:135]\n\t"
      "ds_read_b64_tr_b16 a[116:117], v221 offset:136\n\t"
      "ds_read_b128 v[144:147], v197 offset:256\n\t"
      "s_waitcnt lgkmcnt(8)\n\t"
      "v_mfma_f32_16x16x32_bf16 v[80:83], a[72:75], v[140:143], v[80:83]\n\t"
      "s_add_u32 m0, s41, 0x1e000\n\t"
      "s_mov_b32 s51, 0x2a000\n\t"
      "buffer_load_dwordx4 v211, s[16:19], s51 offen lds\n\t"
      "v_mfma_f32_16x16x32_bf16 v[84:87], a[76:79], v[140:143], v[84:87]\n\t"
      "buffer_load_dwordx4 v212, s[16:19], s51 offen offset:1024 lds\n\t"
      "v_mfma_f32_16x16x32_bf16 v[88:91], a[80:83], v[140:143], v[88:91]\n\t"
      "buffer_load_dwordx4 v213, s[16:19], s51 offen offset:2048 lds\n\t"
      "s_waitcnt vmcnt(15)\n\t"
      "ds_read_b64_tr_b16 a[98:99], v221 offset:12288\n\t"
      "v_mfma_f32_16x16x32_bf16 v[92:95], a[84:87], v[140:143], v[92:95]\n\t"
      "ds_read_b64_tr_b16 a[102:103], v221 offset:12296\n\t"
      "ds_read_b64_tr_b16 a[106:107], v221 offset:12352\n\t"
      "v_mfma_f32_16x16x32_bf16 v[96:99], a[88:91], v[140:143], v[96:99]\n\t"
      "ds_read_b64_tr_b16 a[110:111], v221 offset:12360\n\t"
      "ds_read_b64_tr_b16 a[114:115], v221 offset:12416\n\t"
      "v_mfma_f32_16x16x32_bf16 v[100:103], a[92:95], v[140:143], v[100:103]\n\t"
      "ds_read_b64_tr_b16 a[118:119], v221 offset:12424\n\t"
      "ds_read_b128 v[156:159], v197 offset:12544\n\t"
      "s_waitcnt lgkmcnt(8)\n\t"
      "v_mfma_f32_16x16x32_bf16 v[104:107], a[72:75], v[152:155], v[104:107]\n\t"
      "s_add_u32 m0, s41, 0x21000\n\t"
      "s_mov_b32 s51, 0x2ac00\n\t"
      "buffer_load_dwordx4 v211, s[16:19], s51 offen lds\n\t"
      "v_mfma_f32_16x16x32_bf16 v[108:111], a[76:79], v[152:155], v[108:111]\n\t"
      "buffer_load_dwordx4 v212, s[16:19], s51 offen offset:1024 lds\n\t"
      "v_mfma_f32_16x16x32_bf16 v[112:115], a[80:83], v[152:155], v[112:115]\n\t"
      "buffer_load_dwordx4 v213, s[16:19], s51 offen offset:2048 lds\n\t"
      "s_waitcnt vmcnt(15)\n\t"
      "ds_read_b64_tr_b16 a[120:121], v214 offset:0\n\t"
      "v_mfma_f32_16x16x32_bf16 v[116:119], a[84:87], v[152:155], v[116:119]\n\t"
      "ds_read_b64_tr_b16 a[124:125], v214 offset:8\n\t"
      "ds_read_b64_tr_b16 a[128:129], v214 offset:64\n\t"
      "v_mfma_f32_16x16x32_bf16 v[120:123], a[88:91], v[152:155], v[120:123]\n\t"
      "ds_read_b64_tr_b16 a[132:133], v214 offset:72\n\t"
      "ds_read_b64_tr_b16 a[136:137], v214 offset:128\n\t"
      "v_mfma_f32_16x16x32_bf16 v[132:135], a[92:95], v[152:155], v[132:135]\n\t"
      "ds_read_b64_tr_b16 a[140:141], v214 offset:136\n\t"
      "ds_read_b128 v[148:151], v198 offset:256\n\t"
      "s_waitcnt lgkmcnt(8)\n\t"
      "v_mfma_f32_16x16x32_bf16 v[80:83], a[96:99], v[144:147], v[80:83]\n\t"
      "s_add_u32 m0, s41, 0x24000\n\t"
      "s_mov_b32 s51, 0x30000\n\t"
      "buffer_load_dwordx4 v211, s[16:19], s51 offen lds\n\t"
      "v_mfma_f32_16x16x32_bf16 v[84:87], a[100:103], v[144:147], v[84:87]\n\t"
      "buffer_load_dwordx4 v212, s[16:19], s51 offen offset:1024 lds\n\t"
      "v_mfma_f32_16x16x32_bf16 v[88:91], a[104:107], v[144:147], v[88:91]\n\t"
      "buffer_load_dwordx4 v213, s[16:19], s51 offen offset:2048 lds\n\t"
      "s_waitcnt vmcnt(15)\n\t"
      "ds_read_b64_tr_b16 a[122:123], v214 offset:12288\n\t"
      "v_mfma_f32_16x16x32_bf16 v[92:95], a[108:111], v[144:147], v[92:95]\n\t"
      "ds_read_b64_tr_b16 a[126:127], v214 offset:12296\n\t"
      "ds_read_b64_tr_b16 a[130:131], v214 offset:12352\n\t"
      "v_mfma_f32_16x16x32_bf16 v[96:99], a[112:115], v[144:147], v[96:99]\n\t"
      "ds_read_b64_tr_b16 a[134:135], v214 offset:12360\n\t"
      "ds_read_b64_tr_b16 a[138:139], v214 offset:12416\n\t"
      "v_mfma_f32_16x16x32_bf16 v[100:103], a[116:119], v[144:147], v[100:103]\n\t"
      "ds_read_b64_tr_b16 a[142:143], v214 offset:12424\n\t"
      "ds_read_b128 v[160:163], v198 offset:12544\n\t"
      "s_waitcnt lgkmcnt(8)\n\t"
      "v_mfma_f32_16x16x32_bf16 v[104:107], a[96:99], v[156:159], v[104:107]\n\t"
      "s_add_u32 m0, s41, 0x12000\n\t"
      "s_mov_b32 s51, 0x30c00\n\t"
      "buffer_load_dwordx4 v211, s[16:19], s51 offen lds\n\t"
      "v_mfma_f32_16x16x32_bf16 v[108:111], a[100:103], v[156:159], v[108:111]\n\t"
      "buffer_load_dwordx4 v212, s[16:19], s51 offen offset:1024 lds\n\t"
      "v_mfma_f32_16x16x32_bf16 v[112:115], a[104:107], v[156:159], v[112:115]\n\t"
      "buffer_load_dwordx4 v213, s[16:19], s51 offen offset:2048 lds\n\t"
      "s_waitcnt vmcnt(15)\n\t"
      "ds_read_b64_tr_b16 a[144:145], v214 offset:24576\n\t"
      "v_mfma_f32_16x16x32_bf16 v[116:119], a[108:111], v[156:159], v[116:119]\n\t"
      "ds_read_b64_tr_b16 a[148:149], v214 offset:24584\n\t"
      "ds_read_b64_tr_b16 a[152:153], v214 offset:24640\n\t"
      "v_mfma_f32_16x16x32_bf16 v[120:123], a[112:115], v[156:159], v[120:123]\n\t"
      "ds_read_b64_tr_b16 a[156:157], v214 offset:24648\n\t"
      "ds_read_b64_tr_b16 a[160:161], v214 offset:24704\n\t"
      "v_mfma_f32_16x16x32_bf16 v[132:135], a[116:119], v[156:159], v[132:135]\n\t"
      "ds_read_b64_tr_b16 a[164:165], v214 offset:24712\n\t"
      "ds_read_b128 v[140:143], v199 offset:256\n\t"
      "s_waitcnt lgkmcnt(8)\n\t"
      "v_mfma_f32_16x16x32_bf16 v[80:83], a[120:123], v[148:151], v[80:83]\n\t"
      "s_add_u32 m0, s41, 0x15000\n\t"
      "s_mov_b32 s51, 0x36000\n\t"
      "buffer_load_dwordx4 v211, s[16:19], s51 offen lds\n\t"
      "v_mfma_f32_16x16x32_bf16 v[84:87], a[124:127], v[148:151], v[84:87]\n\t"
      "buffer_load_dwordx4 v212, s[16:19], s51 offen offset:1024 lds\n\t"
      "v_mfma_f32_16x16x32_bf16 v[88:91], a[128:131], v[148:151], v[88:91]\n\t"
      "buffer_load_dwordx4 v213, s[16:19], s51 offen offset:2048 lds\n\t"
      "s_waitcnt vmcnt(15)\n\t"
      "ds_read_b64_tr_b16 a[146:147], v214 offset:36864\n\t"
      "v_mfma_f32_16x16x32_bf16 v[92:95], a[132:135], v[148:151], v[92:95]\n\t"
      "ds_read_b64_tr_b16 a[150:151], v214 offset:36872\n\t"
      "ds_read_b64_tr_b16 a[154:155], v214 offset:36928\n\t"
      "v_mfma_f32_16x16x32_bf16 v[96:99], a[136:139], v[148:151], v[96:99]\n\t"
      "ds_read_b64_tr_b16 a[158:159], v214 offset:36936\n\t"
      "ds_read_b64_tr_b16 a[162:163], v214 offset:36992\n\t"
      "v_mfma_f32_16x16x32_bf16 v[100:103], a[140:143], v[148:151], v[100:103]\n\t"
      "ds_read_b64_tr_b16 a[166:167], v214 offset:37000\n\t"
      "ds_read_b128 v[152:155], v199 offset:12544\n\t"
      "s_waitcnt lgkmcnt(8)\n\t"
      "v_mfma_f32_16x16x32_bf16 v[104:107], a[120:123], v[160:163], v[104:107]\n\t"
      "s_add_u32 m0, s41, 0x18000\n\t"
      "s_mov_b32 s51, 0x36c00\n\t"
      "buffer_load_dwordx4 v211, s[16:19], s51 offen lds\n\t"
      "v_mfma_f32_16x16x32_bf16 v[108:111], a[124:127], v[160:163], v[108:111]\n\t"
      "buffer_load_dwordx4 v212, s[16:19], s51 offen offset:1024 lds\n\t"
      "v_mfma_f32_16x16x32_bf16 v[112:115], a[128:131], v[160:163], v[112:115]\n\t"
      "buffer_load_dwordx4 v213, s[16:19], s51 offen offset:2048 lds\n\t"
      "s_waitcnt vmcnt(15)\n\t"
      "ds_read_b64_tr_b16 a[168:169], v214 offset:49152\n\t"
      "v_mfma_f32_16x16x32_bf16 v[116:119], a[132:135], v[160:163], v[116:119]\n\t"
      "ds_read_b64_tr_b16 a[172:173], v214 offset:49160\n\t"
      "ds_read_b64_tr_b16 a[176:177], v214 offset:49216\n\t"
      "v_mfma_f32_16x16x32_bf16 v[120:123], a[136:139], v[160:163], v[120:123]\n\t"
      "ds_read_b64_tr_b16 a[180:181], v214 offset:49224\n\t"
      "ds_read_b64_tr_b16 a[184:185], v214 offset:49280\n\t"
      "v_mfma_f32_16x16x32_bf16 v[132:135], a[140:143], v[160:163], v[132:135]\n\t"
      "ds_read_b64_tr_b16 a[188:189], v214 offset:49288\n\t"
      "ds_read_b128 v[144:147], v196 offset:256\n\t"
      "s_waitcnt lgkmcnt(8)\n\t"
      "v_mfma_f32_16x16x32_bf16 v[80:83], a[144:147], v[140:143], v[80:83]\n\t"
      "s_add_u32 m0, s41, 0x1b000\n\t"
      "s_mov_b32 s51, 0x3c000\n\t"
      "buffer_load_dwordx4 v211, s[16:19], s51 offen lds\n\t"
      "v_mfma_f32_16x16x32_bf16 v[84:87], a[148:151], v[140:143], v[84:87]\n\t"
      "buffer_load_dwordx4 v212, s[16:19], s51 offen offset:1024 lds\n\t"
      "v_mfma_f32_16x16x32_bf16 v[88:91], a[152:155], v[140:143], v[88:91]\n\t"
      "buffer_load_dwordx4 v213, s[16:19], s51 offen offset:2048 lds\n\t"
      "s_waitcnt vmcnt(15)\n\t"
      "ds_read_b64_tr_b16 a[170:171], v221 offset:0\n\t"
      "v_mfma_f32_16x16x32_bf16 v[92:95], a[156:159], v[140:143], v[92:95]\n\t"
      "ds_read_b64_tr_b16 a[174:175], v221 offset:8\n\t"
      "ds_read_b64_tr_b16 a[178:179], v221 offset:64\n\t"
      "v_mfma_f32_16x16x32_bf16 v[96:99], a[160:163], v[140:143], v[96:99]\n\t"
      "ds_read_b64_tr_b16 a[182:183], v221 offset:72\n\t"
      "ds_read_b64_tr_b16 a[186:187], v221 offset:128\n\t"
      "v_mfma_f32_16x16x32_bf16 v[100:103], a[164:167], v[140:143], v[100:103]\n\t"
      "ds_read_b64_tr_b16 a[190:191], v221 offset:136\n\t"
      "ds_read_b128 v[156:159], v196 offset:12544\n\t"
      "s_waitcnt lgkmcnt(8)\n\t"
      "v_mfma_f32_16x16x32_bf16 v[104:107], a[144:147], v[152:155], v[104:107]\n\t"
      "s_add_u32 m0, s41, 0x1e000\n\t"
      "s_mov_b32 s51, 0x3cc00\n\t"
      "buffer_load_dwordx4 v211, s[16:19], s51 offen lds\n\t"
      "v_mfma_f32_16x16x32_bf16 v[108:111], a[148:151], v[152:155], v[108:111]\n\t"
      "buffer_load_dwordx4 v212, s[16:19], s51 offen offset:1024 lds\n\t"
      "v_mfma_f32_16x16x32_bf16 v[112:115], a[152:155], v[152:155], v[112:115]\n\t"
      "buffer_load_dwordx4 v213, s[16:19], s51 offen offset:2048 lds\n\t"
      "s_waitcnt vmcnt(15)\n\t"
      "ds_read_b64_tr_b16 a[192:193], v221 offset:12288\n\t"
      "v_mfma_f32_16x16x32_bf16 v[116:119], a[156:159], v[152:155], v[116:119]\n\t"
      "ds_read_b64_tr_b16 a[196:197], v221 offset:12296\n\t"
      "ds_read_b64_tr_b16 a[200:201], v221 offset:12352\n\t"
      "v_mfma_f32_16x16x32_bf16 v[120:123], a[160:163], v[152:155], v[120:123]\n\t"
      "ds_read_b64_tr_b16 a[204:205], v221 offset:12360\n\t"
      "ds_read_b64_tr_b16 a[208:209], v221 offset:12416\n\t"
      "v_mfma_f32_16x16x32_bf16 v[132:135], a[164:167], v[152:155], v[132:135]\n\t"
      "ds_read_b64_tr_b16 a[212:213], v221 offset:12424\n\t"
      "ds_read_b128 v[148:151], v198 offset:512\n\t"
      "s_waitcnt lgkmcnt(8)\n\t"
      "v_mfma_f32_16x16x32_bf16 v[80:83], a[168:171], v[144:147], v[80:83]\n\t"
      "s_add_u32 m0, s41, 0x21000\n\t"
      "s_mov_b32 s51, 0x42000\n\t"
      "buffer_load_dwordx4 v211, s[16:19], s51 offen lds\n\t"
      "v_mfma_f32_16x16x32_bf16 v[84:87], a[172:175], v[144:147], v[84:87]\n\t"
      "buffer_load_dwordx4 v212, s[16:19], s51 offen offset:1024 lds\n\t"
      "v_mfma_f32_16x16x32_bf16 v[88:91], a[176:179], v[144:147], v[88:91]\n\t"
      "buffer_load_dwordx4 v213, s[16:19], s51 offen offset:2048 lds\n\t"
      "s_waitcnt vmcnt(15)\n\t"
      "ds_read_b64_tr_b16 a[194:195], v214 offset:0\n\t"
      "v_mfma_f32_16x16x32_bf16 v[92:95], a[180:183], v[144:147], v[92:95]\n\t"
      "ds_read_b64_tr_b16 a[198:199], v214 offset:8\n\t"
      "ds_read_b64_tr_b16 a[202:203], v214 offset:64\n\t"
      "v_mfma_f32_16x16x32_bf16 v[96:99], a[184:187], v[144:147], v[96:99]\n\t"
      "ds_read_b64_tr_b16 a[206:207], v214 offset:72\n\t"
      "ds_read_b64_tr_b16 a[210:211], v214 offset:128\n\t"
      "v_mfma_f32_16x16x32_bf16 v[100:103], a[188:191], v[144:147], v[100:103]\n\t"
      "ds_read_b64_tr_b16 a[214:215], v214 offset:136\n\t"
      "ds_read_b128 v[160:163], v198 offset:12800\n\t"
      "s_waitcnt lgkmcnt(8)\n\t"
      "v_mfma_f32_16x16x32_bf16 v[104:107], a[168:171], v[156:159], v[104:107]\n\t"
      "s_add_u32 m0, s41, 0x24000\n\t"
      "s_mov_b32 s51, 0x42c00\n\t"
      "buffer_load_dwordx4 v211, s[16:19], s51 offen lds\n\t"
      "v_mfma_f32_16x16x32_bf16 v[108:111], a[172:175], v[156:159], v[108:111]\n\t"
      "buffer_load_dwordx4 v212, s[16:19], s51 offen offset:1024 lds\n\t"
      "v_mfma_f32_16x16x32_bf16 v[112:115], a[176:179], v[156:159], v[112:115]\n\t"
      "buffer_load_dwordx4 v213, s[16:19], s51 offen offset:2048 lds\n\t"
      "s_waitcnt vmcnt(15)\n\t"
      "ds_read_b64_tr_b16 a[216:217], v214 offset:12288\n\t"
      "v_mfma_f32_16x16x32_bf16 v[116:119], a[180:183], v[156:159], v[116:119]\n\t"
      "ds_read_b64_tr_b16 a[220:221], v214 offset:12296\n\t"
      "ds_read_b64_tr_b16 a[224:225], v214 offset:12352\n\t"
      "v_mfma_f32_16x16x32_bf16 v[120:123], a[184:187], v[156:159], v[120:123]\n\t"
      "ds_read_b64_tr_b16 a[228:229], v214 offset:12360\n\t"
      "ds_read_b64_tr_b16 a[232:233], v214 offset:12416\n\t"
      "v_mfma_f32_16x16x32_bf16 v[132:135], a[188:191], v[156:159], v[132:135]\n\t"
      "ds_read_b64_tr_b16 a[236:237], v214 offset:12424\n\t"
      "ds_read_b128 v[140:143], v199 offset:512\n\t"
      "s_waitcnt lgkmcnt(8)\n\t"
      "v_mfma_f32_16x16x32_bf16 v[80:83], a[192:195], v[148:151], v[80:83]\n\t"
      "s_add_u32 m0, s41, 0x6000\n\t"
      "s_add_u32 s51, s40, 0x600000\n\t"
      "buffer_load_dwordx4 v208, s[20:23], s51 offen lds\n\t"
      "v_mfma_f32_16x16x32_bf16 v[84:87], a[196:199], v[148:151], v[84:87]\n\t"
      "buffer_load_dwordx4 v208, s[20:23], s51 offen offset:1024 lds\n\t"
      "v_mfma_f32_16x16x32_bf16 v[88:91], a[200:203], v[148:151], v[88:91]\n\t"
      "buffer_load_dwordx4 v208, s[20:23], s51 offen offset:2048 lds\n\t"
      "s_waitcnt vmcnt(15)\n\t"
      "ds_read_b64_tr_b16 a[218:219], v214 offset:24576\n\t"
      "v_mfma_f32_16x16x32_bf16 v[92:95], a[204:207], v[148:151], v[92:95]\n\t"
      "ds_read_b64_tr_b16 a[222:223], v214 offset:24584\n\t"
      "ds_read_b64_tr_b16 a[226:227], v214 offset:24640\n\t"
      "v_mfma_f32_16x16x32_bf16 v[96:99], a[208:211], v[148:151], v[96:99]\n\t"
      "ds_read_b64_tr_b16 a[230:231], v214 offset:24648\n\t"
      "ds_read_b64_tr_b16 a[234:235], v214 offset:24704\n\t"
      "v_mfma_f32_16x16x32_bf16 v[100:103], a[212:215], v[148:151], v[100:103]\n\t"
      "ds_read_b64_tr_b16 a[238:239], v214 offset:24712\n\t"
      "ds_read_b128 v[152:155], v199 offset:12800\n\t"
      "s_waitcnt lgkmcnt(14)\n\t"
      "v_mfma_f32_16x16x32_bf16 v[104:107], a[192:195], v[160:163], v[104:107]\n\t"
      "s_add_u32 m0, s41, 0x9000\n\t"
      "s_add_u32 s51, s40, 0x600000\n\t"
      "buffer_load_dwordx4 v209, s[20:23], s51 offen lds\n\t"
      "v_mfma_f32_16x16x32_bf16 v[108:111], a[196:199], v[160:163], v[108:111]\n\t"
      "buffer_load_dwordx4 v209, s[20:23], s51 offen offset:1024 lds\n\t"
      "v_mfma_f32_16x16x32_bf16 v[112:115], a[200:203], v[160:163], v[112:115]\n\t"
      "buffer_load_dwordx4 v209, s[20:23], s51 offen offset:2048 lds\n\t"
      "s_waitcnt vmcnt(15)\n\t"
      "ds_read_b64_tr_b16 a[240:241], v214 offset:36864\n\t"
      "v_mfma_f32_16x16x32_bf16 v[116:119], a[204:207], v[160:163], v[116:119]\n\t"
      "ds_read_b64_tr_b16 a[244:245], v214 offset:36872\n\t"
      "ds_read_b64_tr_b16 a[248:249], v214 offset:36928\n\t"
      "v_mfma_f32_16x16x32_bf16 v[120:123], a[208:211], v[160:163], v[120:123]\n\t"
      "ds_read_b64_tr_b16 a[252:253], v214 offset:36936\n\t"
      "ds_read_b64_tr_b16 v[164:165], v214 offset:36992\n\t"
      "v_mfma_f32_16x16x32_bf16 v[132:135], a[212:215], v[160:163], v[132:135]\n\t"
      "ds_read_b64_tr_b16 v[168:169], v214 offset:37000\n\t"
      "ds_read_b128 v[144:147], v196 offset:512\n\t"
      "s_waitcnt lgkmcnt(8)\n\t"
      "v_mfma_f32_16x16x32_bf16 v[80:83], a[216:219], v[140:143], v[80:83]\n\t"
      "s_add_u32 m0, s41, 0xc000\n\t"
      "s_add_u32 s51, s40, 0xc00000\n\t"
      "buffer_load_dwordx4 v208, s[20:23], s51 offen lds\n\t"
      "v_mfma_f32_16x16x32_bf16 v[84:87], a[220:223], v[140:143], v[84:87]\n\t"
      "buffer_load_dwordx4 v208, s[20:23], s51 offen offset:1024 lds\n\t"
      "v_mfma_f32_16x16x32_bf16 v[88:91], a[224:227], v[140:143], v[88:91]\n\t"
      "buffer_load_dwordx4 v208, s[20:23], s51 offen offset:2048 lds\n\t"
      "s_waitcnt vmcnt(15)\n\t"
      "ds_read_b64_tr_b16 a[242:243], v214 offset:49152\n\t"
      "v_mfma_f32_16x16x32_bf16 v[92:95], a[228:231], v[140:143], v[92:95]\n\t"
      "ds_read_b64_tr_b16 a[246:247], v214 offset:49160\n\t"
      "ds_read_b64_tr_b16 a[250:251], v214 offset:49216\n\t"
      "v_mfma_f32_16x16x32_bf16 v[96:99], a[232:235], v[140:143], v[96:99]\n\t"
      "ds_read_b64_tr_b16 a[254:255], v214 offset:49224\n\t"
      "ds_read_b64_tr_b16 v[166:167], v214 offset:49280\n\t"
      "v_mfma_f32_16x16x32_bf16 v[100:103], a[236:239], v[140:143], v[100:103]\n\t"
      "ds_read_b64_tr_b16 v[170:171], v214 offset:49288\n\t"
      "ds_read_b128 v[156:159], v196 offset:12800\n\t"
      "s_waitcnt lgkmcnt(14)\n\t"
      "v_mfma_f32_16x16x32_bf16 v[104:107], a[216:219], v[152:155], v[104:107]\n\t"
      "s_add_u32 m0, s41, 0xf000\n\t"
      "s_add_u32 s51, s40, 0xc00000\n\t"
      "buffer_load_dwordx4 v209, s[20:23], s51 offen lds\n\t"
      "v_mfma_f32_16x16x32_bf16 v[108:111], a[220:223], v[152:155], v[108:111]\n\t"
      "buffer_load_dwordx4 v209, s[20:23], s51 offen offset:1024 lds\n\t"
      "v_mfma_f32_16x16x32_bf16 v[112:115], a[224:227], v[152:155], v[112:115]\n\t"
      "buffer_load_dwordx4 v209, s[20:23], s51 offen offset:2048 lds\n\t"
      "s_waitcnt vmcnt(15)\n\t"
      "ds_read_b64_tr_b16 v[172:173], v221 offset:0\n\t"
      "v_mfma_f32_16x16x32_bf16 v[116:119], a[228:231], v[152:155], v[116:119]\n\t"
      "ds_read_b64_tr_b16 v[176:177], v221 offset:8\n\t"
      "ds_read_b64_tr_b16 v[180:181], v221 offset:64\n\t"
      "ds_read_b64_tr_b16 v[184:185], v221 offset:72\n\t"
      "v_mfma_f32_16x16x32_bf16 v[120:123], a[232:235], v[152:155], v[120:123]\n\t"
      "ds_read_b64_tr_b16 v[188:189], v221 offset:128\n\t"
      "ds_read_b64_tr_b16 v[192:193], v221 offset:136\n\t"
      "v_mfma_f32_16x16x32_bf16 v[132:135], a[236:239], v[152:155], v[132:135]\n\t"
      "ds_read_b128 v[148:151], v197 offset:512\n\t"
      "ds_read_b128 v[160:163], v197 offset:12800\n\t"
      "s_waitcnt lgkmcnt(9)\n\t"
      "v_mfma_f32_16x16x32_bf16 v[80:83], a[240:243], v[144:147], v[80:83]\n\t"
      "buffer_load_dwordx2 v[240:241], v245, s[24:27], 0 offen sc1\n\t"
      "v_mfma_f32_16x16x32_bf16 v[84:87], a[244:247], v[144:147], v[84:87]\n\t"
      "s_cmp_eq_u32 s41, s43\n\t"
      "s_cbranch_scc0 .Lc3_%=\n\t"
      "buffer_load_dword v254, v253, s[24:27], 0 offen sc1\n\t"
      ".Lc3_%=:\n\t"
      "v_mfma_f32_16x16x32_bf16 v[88:91], a[248:251], v[144:147], v[88:91]\n\t"
      "s_waitcnt vmcnt(13)\n\t"
      "ds_read_b64_tr_b16 v[174:175], v221 offset:12288\n\t"
      "v_mfma_f32_16x16x32_bf16 v[92:95], a[252:255], v[144:147], v[92:95]\n\t"
      "ds_read_b64_tr_b16 v[178:179], v221 offset:12296\n\t"
      "ds_read_b64_tr_b16 v[182:183], v221 offset:12352\n\t"
      "v_mfma_f32_16x16x32_bf16 v[96:99], v[164:167], v[144:147], v[96:99]\n\t"
      "ds_read_b64_tr_b16 v[186:187], v221 offset:12360\n\t"
      "ds_read_b64_tr_b16 v[190:191], v221 offset:12416\n\t"
      "v_mfma_f32_16x16x32_bf16 v[100:103], v[168:171], v[144:147], v[100:103]\n\t"
      "ds_read_b64_tr_b16 v[194:195], v221 offset:12424\n\t"
      "s_memrealtime s[80:81]\n\t"
      "v_add_u32_e32 v196, s46, v200\n\t"
      "v_add_u32_e32 v197, s46, v201\n\t"
      "v_add_u32_e32 v198, s46, v202\n\t"
      "v_add_u32_e32 v199, s46, v203\n\t"
      "s_waitcnt vmcnt(10)\n\t"
      "s_waitcnt lgkmcnt(6)\n\t"
      "s_barrier\n\t"
      "v_mfma_f32_16x16x32_bf16 v[104:107], a[240:243], v[156:159], v[104:107]\n\t"
      "ds_read_b128 v[32:35], v196 offset:0\n\t"
      "v_mfma_f32_16x16x32_bf16 v[108:111], a[244:247], v[156:159], v[108:111]\n\t"
      "ds_read_b128 v[36:39], v197 offset:0\n\t"
      "v_mfma_f32_16x16x32_bf16 v[112:115], a[248:251], v[156:159], v[112:115]\n\t"
      "ds_read_b128 v[40:43], v198 offset:0\n\t"
      "v_mfma_f32_16x16x32_bf16 v[116:119], a[252:255], v[156:159], v[116:119]\n\t"
      "ds_read_b128 v[44:47], v199 offset:0\n\t"
      "v_mfma_f32_16x16x32_bf16 v[120:123], v[164:167], v[156:159], v[120:123]\n\t"
      "ds_read_b128 v[48:51], v197 offset:256\n\t"
      "v_mfma_f32_16x16x32_bf16 v[132:135], v[168:171], v[156:159], v[132:135]\n\t"
      "s_waitcnt lgkmcnt(5)\n\t"
      "s_mov_b32 s87, 75\n\t"
      "s_mov_b32 s86, 80\n\t"
      "s_mov_b32 s84, 100\n\t"
      "v_mfma_f32_16x16x32_bf16 v[80:83], v[172:175], v[148:151], v[80:83]\n\t"
      "ds_read_b128 v[52:55], v198 offset:256\n\t"
      "v_mfma_f32_16x16x32_bf16 v[84:87], v[176:179], v[148:151], v[84:87]\n\t"
      "ds_read_b128 v[56:59], v199 offset:256\n\t"
      "v_mfma_f32_16x16x32_bf16 v[88:91], v[180:183], v[148:151], v[88:91]\n\t"
      "ds_read_b128 v[60:63], v196 offset:256\n\t"
      "v_mfma_f32_16x16x32_bf16 v[92:95], v[184:187], v[148:151], v[92:95]\n\t"
      "ds_read_b128 v[64:67], v198 offset:512\n\t"
      "v_mfma_f32_16x16x32_bf16 v[96:99], v[188:191], v[148:151], v[96:99]\n\t"
      "ds_read_b128 v[68:71], v199 offset:512\n\t"
      "v_mfma_f32_16x16x32_bf16 v[100:103], v[192:195], v[148:151], v[100:103]\n\t"
      "ds_read_b128 v[72:75], v196 offset:512\n\t"
      "v_mfma_f32_16x16x32_bf16 v[104:107], v[172:175], v[160:163], v[104:107]\n\t"
      "ds_read_b128 v[76:79], v197 offset:512\n\t"
      "v_mfma_f32_16x16x32_bf16 v[108:111], v[176:179], v[160:163], v[108:111]\n\t"
      "v_mfma_f32_16x16x32_bf16 v[112:115], v[180:183], v[160:163], v[112:115]\n\t"
      "v_mfma_f32_16x16x32_bf16 v[116:119], v[184:187], v[160:163], v[116:119]\n\t"
      "v_mfma_f32_16x16x32_bf16 v[120:123], v[188:191], v[160:163], v[120:123]\n\t"
      "v_mfma_f32_16x16x32_bf16 v[132:135], v[192:195], v[160:163], v[132:135]\n\t"
      ".Lp2_%=:\n\t"
      "s_cbranch_vccnz .Lwsb_%=\n\t"
      "s_waitcnt lgkmcnt(8)\n\t"
      "v_mfma_f32_16x16x32_bf16 v[128:131], a[0:3], v[32:35], v[8:11]\n\t"
      "v_cvt_pk_bf16_f32 v80, v80, v81\n\t"
      "v_cvt_pk_bf16_f32 v81, v82, v83\n\t"
      "v_mfma_f32_16x16x32_bf16 v[128:131], a[24:27], v[36:39], v[128:131]\n\t"
      "v_cvt_pk_bf16_f32 v136, v120, v121\n\t"
      "v_cvt_pk_bf16_f32 v137, v122, v123\n\t"
      "v_mfma_f32_16x16x32_bf16 v[128:131], a[48:51], v[40:43], v[128:131]\n\t"
      "v_cvt_pk_bf16_f32 v138, v132, v133\n\t"
      "v_mfma_f32_16x16x32_bf16 v[128:131], a[72:75], v[44:47], v[128:131]\n\t"
      "v_cvt_pk_bf16_f32 v139, v134, v135\n\t"
      "s_waitcnt lgkmcnt(4)\n\t"
      "v_mfma_f32_16x16x32_bf16 v[128:131], a[96:99], v[48:51], v[128:131]\n\t"
      "ds_write_b128 v219, v[136:139] offset:12288\n\t"
      "v_mfma_f32_16x16x32_bf16 v[128:131], a[120:123], v[52:55], v[128:131]\n\t"
      "v_cvt_pk_bf16_f32 v82, v84, v85\n\t"
      "v_cvt_pk_bf16_f32 v83, v86, v87\n\t"
      "v_mfma_f32_16x16x32_bf16 v[128:131], a[144:147], v[56:59], v[128:131]\n\t"
      "v_cvt_pk_bf16_f32 v88, v88, v89\n\t"
      "v_cvt_pk_bf16_f32 v89, v90, v91\n\t"
      "v_mfma_f32_16x16x32_bf16 v[128:131], a[168:171], v[60:63], v[128:131]\n\t"
      "ds_write_b128 v217, v[80:83] offset:0\n\t"
      "v_cvt_pk_bf16_f32 v90, v92, v93\n\t"
      "s_waitcnt lgkmcnt(2)\n\t"
      "v_mfma_f32_16x16x32_bf16 v[128:131], a[192:195], v[64:67], v[128:131]\n\t"
      "v_cvt_pk_bf16_f32 v91, v94, v95\n\t"
      "v_cvt_pk_bf16_f32 v96, v96, v97\n\t"
      "v_mfma_f32_16x16x32_bf16 v[128:131], a[216:219], v[68:71], v[128:131]\n\t"
      "ds_write_b128 v218, v[88:91] offset:0\n\t"
      "v_cvt_pk_bf16_f32 v97, v98, v99\n\t"
      "v_mfma_f32_16x16x32_bf16 v[128:131], a[240:243], v[72:75], v[128:131]\n\t"
      "v_cvt_pk_bf16_f32 v98, v100, v101\n\t"
      "v_cvt_pk_bf16_f32 v99, v102, v103\n\t"
      "v_mfma_f32_16x16x32_bf16 v[128:131], v[172:175], v[76:79], v[128:131]\n\t"
      "v_cvt_pk_bf16_f32 v104, v104, v105\n\t"
      "v_cvt_pk_bf16_f32 v105, v106, v107\n\t"
      "v_mfma_f32_16x16x32_bf16 v[132:135], a[4:7], v[32:35], v[12:15]\n\t"
      "ds_write_b128 v219, v[96:99] offset:0\n\t"
      "v_cvt_pk_bf16_f32 v106, v108, v109\n\t"
      "v_mfma_f32_16x16x32_bf16 v[132:135], a[28:31], v[36:39], v[132:135]\n\t"
      "v_cvt_pk_bf16_f32 v107, v110, v111\n\t"
      "v_cvt_pk_bf16_f32 v112, v112, v113\n\t"
      "v_mfma_f32_16x16x32_bf16 v[132:135], a[52:55], v[40:43], v[132:135]\n\t"
      "v_cvt_pk_bf16_f32 v136, v128, v129\n\t"
      "v_mfma_f32_16x16x32_bf16 v[132:135], a[76:79], v[44:47], v[132:135]\n\t"
      "v_cvt_pk_bf16_f32 v137, v130, v131\n\t"
      "v_mfma_f32_16x16x32_bf16 v[132:135], a[100:103], v[48:51], v[132:135]\n\t"
      "ds_write_b128 v217, v[104:107] offset:12288\n\t"
      "v_cvt_pk_bf16_f32 v113, v114, v115\n\t"
      "s_add_u32 s48, s47, 0x6000\n\t"
      "v_mfma_f32_16x16x32_bf16 v[132:135], a[124:127], v[52:55], v[132:135]\n\t"
      "v_cvt_pk_bf16_f32 v114, v116, v117\n\t"
      "v_cvt_pk_bf16_f32 v115, v118, v119\n\t"
      "s_cmp_eq_u32 s48, s50\n\t"
      "s_cselect_b32 s48, s41, s48\n\t"
      "v_mfma_f32_16x16x32_bf16 v[132:135], a[148:151], v[56:59], v[132:135]\n\t"
      "ds_write_b128 v218, v[112:115] offset:12288\n\t"
      "v_mfma_f32_16x16x32_bf16 v[132:135], a[172:175], v[60:63], v[132:135]\n\t"
      "v_add_u32_e32 v217, s46, v204\n\t"
      "v_mfma_f32_16x16x32_bf16 v[132:135], a[196:199], v[64:67], v[132:135]\n\t"
      "v_add_u32_e32 v218, s46, v205\n\t"
      "v_mfma_f32_16x16x32_bf16 v[132:135], a[220:223], v[68:71], v[132:135]\n\t"
      "v_readlane_b32 s73, v248, s85\n\t"
      "v_add_u32_e32 v219, s46, v206\n\t"
      "v_mfma_f32_16x16x32_bf16 v[132:135], a[244:247], v[72:75], v[132:135]\n\t"
      "s_mov_b32 m0, s45\n\t"
      "v_mfma_f32_16x16x32_bf16 v[132:135], v[176:179], v[76:79], v[132:135]\n\t"
      "s_waitcnt lgkmcnt(0)\n\t"
      "s_barrier\n\t"
      "v_mfma_f32_16x16x32_bf16 v[128:131], a[8:11], v[32:35], v[16:19]\n\t"
      "s_mov_b32 s82, s80\n\t"
      "v_mfma_f32_16x16x32_bf16 v[128:131], a[32:35], v[36:39], v[128:131]\n\t"
      "s_memrealtime s[80:81]\n\t"
      "v_mfma_f32_16x16x32_bf16 v[128:131], a[56:59], v[40:43], v[128:131]\n\t"
      "v_cvt_pk_bf16_f32 v138, v132, v133\n\t"
      "v_mfma_f32_16x16x32_bf16 v[128:131], a[80:83], v[44:47], v[128:131]\n\t"
      "v_cvt_pk_bf16_f32 v139, v134, v135\n\t"
      "v_mfma_f32_16x16x32_bf16 v[128:131], a[104:107], v[48:51], v[128:131]\n\t"
      "ds_write_b128 v217, v[136:139] offset:0\n\t"
      "v_mfma_f32_16x16x32_bf16 v[128:131], a[128:131], v[52:55], v[128:131]\n\t"
      "buffer_load_dwordx4 v208, s[20:23], s73 offen lds\n\t"
      "v_mfma_f32_16x16x32_bf16 v[128:131], a[152:155], v[56:59], v[128:131]\n\t"
      "v_mfma_f32_16x16x32_bf16 v[128:131], a[176:179], v[60:63], v[128:131]\n\t"
      "v_mfma_f32_16x16x32_bf16 v[128:131], a[200:203], v[64:67], v[128:131]\n\t"
      "v_mfma_f32_16x16x32_bf16 v[128:131], a[224:227], v[68:71], v[128:131]\n\t"
      "v_mfma_f32_16x16x32_bf16 v[128:131], a[248:251], v[72:75], v[128:131]\n\t"
      "v_mfma_f32_16x16x32_bf16 v[128:131], v[180:183], v[76:79], v[128:131]\n\t"
      "v_mfma_f32_16x16x32_bf16 v[132:135], a[12:15], v[32:35], v[20:23]\n\t"
      "v_mfma_f32_16x16x32_bf16 v[132:135], a[36:39], v[36:39], v[132:135]\n\t"
      "buffer_load_dwordx4 v208, s[20:23], s73 offen offset:1024 lds\n\t"
      "v_mfma_f32_16x16x32_bf16 v[132:135], a[60:63], v[40:43], v[132:135]\n\t"
      "v_cvt_pk_bf16_f32 v136, v128, v129\n\t"
      "v_mfma_f32_16x16x32_bf16 v[132:135], a[84:87], v[44:47], v[132:135]\n\t"
      "v_cvt_pk_bf16_f32 v137, v130, v131\n\t"
      "v_mfma_f32_16x16x32_bf16 v[132:135], a[108:111], v[48:51], v[132:135]\n\t"
      "v_mfma_f32_16x16x32_bf16 v[132:135], a[132:135], v[52:55], v[132:135]\n\t"
      "v_mfma_f32_16x16x32_bf16 v[132:135], a[156:159], v[56:59], v[132:135]\n\t"
      "v_mfma_f32_16x16x32_bf16 v[132:135], a[180:183], v[60:63], v[132:135]\n\t"
      "v_mfma_f32_16x16x32_bf16 v[132:135], a[204:207], v[64:67], v[132:135]\n\t"
      "v_mfma_f32_16x16x32_bf16 v[132:135], a[228:231], v[68:71], v[132:135]\n\t"
      "buffer_load_dwordx4 v208, s[20:23], s73 offen offset:2048 lds\n\t"
      "v_mfma_f32_16x16x32_bf16 v[132:135], a[252:255], v[72:75], v[132:135]\n\t"
      "v_readlane_b32 s73, v249, s85\n\t"
      "s_add_u32 m0, s45, 0x3000\n\t"
      "v_mfma_f32_16x16x32_bf16 v[132:135], v[184:187], v[76:79], v[132:135]\n\t"
      "s_waitcnt vmcnt(10)\n\t"
      "s_barrier\n\t"
      "v_mfma_f32_16x16x32_bf16 v[128:131], a[16:19], v[32:35], v[24:27]\n\t"
      "ds_read_b128 v[80:83], v196 offset:12288\n\t"
      "v_mfma_f32_16x16x32_bf16 v[128:131], a[40:43], v[36:39], v[128:131]\n\t"
      "v_mfma_f32_16x16x32_bf16 v[128:131], a[64:67], v[40:43], v[128:131]\n\t"
      "ds_read_b128 v[84:87], v197 offset:12288\n\t"
      "v_cvt_pk_bf16_f32 v138, v132, v133\n\t"
      "v_mfma_f32_16x16x32_bf16 v[128:131], a[88:91], v[44:47], v[128:131]\n\t"
      "ds_read_b128 v[88:91], v198 offset:12288\n\t"
      "v_cvt_pk_bf16_f32 v139, v134, v135\n\t"
      "v_mfma_f32_16x16x32_bf16 v[128:131], a[112:115], v[48:51], v[128:131]\n\t"
      "ds_write_b128 v218, v[136:139] offset:0\n\t"
      "v_mfma_f32_16x16x32_bf16 v[128:131], a[136:139], v[52:55], v[128:131]\n\t"
      "buffer_load_dwordx4 v209, s[20:23], s73 offen lds\n\t"
      "s_waitcnt vmcnt(10)\n\t"
      "s_cmp_eq_u32 s94, 0\n\t"
      "s_cbranch_scc0 .Lwgvw_0_%=\n\t"
      "s_waitcnt vmcnt(4)\n\t"
      ".Lwgvw_0_%=:\n\t"
      "v_subrev_u32_e32 v242, s63, v240\n\t"
      "v_xor_b32_e32 v241, s53, v241\n\t"
      "v_mfma_f32_16x16x32_bf16 v[128:131], a[160:163], v[56:59], v[128:131]\n\t"
      "ds_read_b128 v[92:95], v199 offset:12288\n\t"
      "v_lshrrev_b32_e32 v240, 13, v242\n\t"
      "v_or_b32_e32 v240, v240, v241\n\t"
      "v_mfma_f32_16x16x32_bf16 v[128:131], a[184:187], v[60:63], v[128:131]\n\t"
      "ds_read_b128 v[96:99], v197 offset:12544\n\t"
      "v_mfma_f32_16x16x32_bf16 v[128:131], a[208:211], v[64:67], v[128:131]\n\t"
      "v_cmp_ne_u32_e32 vcc, 0, v240\n\t"
      "v_and_b32_e32 v242, 15, v242\n\t"
      "v_mfma_f32_16x16x32_bf16 v[128:131], a[232:235], v[68:71], v[128:131]\n\t"
      "v_mfma_f32_16x16x32_bf16 v[128:131], v[164:167], v[72:75], v[128:131]\n\t"
      "ds_read_b128 v[100:103], v198 offset:12544\n\t"
      "s_cbranch_vccnz .Lgs_%=\n\t"
      ".Lgr_%=:\n\t"
      "ds_write_b32 v246, v242\n\t"
      "v_mfma_f32_16x16x32_bf16 v[128:131], v[188:191], v[76:79], v[128:131]\n\t"
      "ds_read_b128 v[104:107], v199 offset:12544\n\t"
      "v_mfma_f32_16x16x32_bf16 v[132:135], a[20:23], v[32:35], v[28:31]\n\t"
      "ds_read_b128 v[108:111], v196 offset:12544\n\t"
      "v_mfma_f32_16x16x32_bf16 v[132:135], a[44:47], v[36:39], v[132:135]\n\t"
      "buffer_load_dwordx4 v209, s[20:23], s73 offen offset:1024 lds\n\t"
      "v_mfma_f32_16x16x32_bf16 v[132:135], a[68:71], v[40:43], v[132:135]\n\t"
      "ds_read_b128 v[112:115], v198 offset:12800\n\t"
      "v_cvt_pk_bf16_f32 v136, v128, v129\n\t"
      "v_add_u32_e32 v220, s46, v207\n\t"
      "v_mfma_f32_16x16x32_bf16 v[132:135], a[92:95], v[44:47], v[132:135]\n\t"
      "ds_read_b128 v[116:119], v199 offset:12800\n\t"
      "v_cvt_pk_bf16_f32 v137, v130, v131\n\t"
      "s_sub_u32 s84, s84, 4\n\t"
      "s_max_i32 s84, s84, s86\n\t"
      "v_mfma_f32_16x16x32_bf16 v[132:135], a[116:119], v[48:51], v[132:135]\n\t"
      "s_sub_u32 s83, s82, s80\n\t"
      "v_mfma_f32_16x16x32_bf16 v[132:135], a[140:143], v[52:55], v[132:135]\n\t"
      "s_max_i32 s83, s83, 0\n\t"
      "s_add_u32 s82, s80, s83\n\t"
      "v_mfma_f32_16x16x32_bf16 v[132:135], a[164:167], v[56:59], v[132:135]\n\t"
      "ds_read_b128 v[120:123], v196 offset:12800\n\t"
      "v_mfma_f32_16x16x32_bf16 v[132:135], a[188:191], v[60:63], v[132:135]\n\t"
      "ds_read_b128 v[124:127], v197 offset:12800\n\t"
      "v_mfma_f32_16x16x32_bf16 v[132:135], a[212:215], v[64:67], v[132:135]\n\t"
      "v_mfma_f32_16x16x32_bf16 v[132:135], a[236:239], v[68:71], v[132:135]\n\t"
      "s_add_u32 s82, s82, s84\n\t"
      "v_mfma_f32_16x16x32_bf16 v[132:135], v[168:171], v[72:75], v[132:135]\n\t"
      "s_add_u32 s44, s44, 1\n\t"
      "s_add_u32 s85, s85, 1\n\t"
      "v_mfma_f32_16x16x32_bf16 v[132:135], v[192:195], v[76:79], v[132:135]\n\t"
      "s_waitcnt lgkmcnt(9)\n\t"
      "v_mfma_f32_16x16x32_bf16 v[128:131], a[0:3], v[80:83], v[8:11]\n\t"
      "v_mfma_f32_16x16x32_bf16 v[128:131], a[24:27], v[84:87], v[128:131]\n\t"
      "v_readlane_b32 s72, v248, s44\n\t"
      "v_mfma_f32_16x16x32_bf16 v[128:131], a[48:51], v[88:91], v[128:131]\n\t"
      "v_cvt_pk_bf16_f32 v138, v132, v133\n\t"
      "v_mfma_f32_16x16x32_bf16 v[128:131], a[72:75], v[92:95], v[128:131]\n\t"
      "v_cvt_pk_bf16_f32 v139, v134, v135\n\t"
      "s_waitcnt lgkmcnt(4)\n\t"
      "v_mfma_f32_16x16x32_bf16 v[128:131], a[96:99], v[96:99], v[128:131]\n\t"
      "ds_write_b128 v219, v[136:139] offset:0\n\t"
      "v_mfma_f32_16x16x32_bf16 v[128:131], a[120:123], v[100:103], v[128:131]\n\t"
      "v_mfma_f32_16x16x32_bf16 v[128:131], a[144:147], v[104:107], v[128:131]\n\t"
      "buffer_load_dwordx4 v209, s[20:23], s73 offen offset:2048 lds\n\t"
      "v_mfma_f32_16x16x32_bf16 v[128:131], a[168:171], v[108:111], v[128:131]\n\t"
      "v_add_u32_e32 v196, s47, v200\n\t"
      "s_waitcnt lgkmcnt(1)\n\t"
      "v_mfma_f32_16x16x32_bf16 v[128:131], a[192:195], v[112:115], v[128:131]\n\t"
      "v_add_u32_e32 v197, s47, v201\n\t"
      "v_mfma_f32_16x16x32_bf16 v[128:131], a[216:219], v[116:119], v[128:131]\n\t"
      "v_add_u32_e32 v198, s47, v202\n\t"
      "v_mfma_f32_16x16x32_bf16 v[128:131], a[240:243], v[120:123], v[128:131]\n\t"
      "v_add_u32_e32 v199, s47, v203\n\t"
      "v_mfma_f32_16x16x32_bf16 v[128:131], v[172:175], v[124:127], v[128:131]\n\t"
      "s_cmp_gt_i32 s83, 1\n\t"
      "s_cbranch_scc1 .LslpP3_%=\n\t"
      ".LretP3_%=:\n\t"
      "s_cmp_eq_u32 s94, 1\n\t"
      "s_cbranch_scc0 .LwP3barv_1_%=\n\t"
      "s_waitcnt vmcnt(9)\n\t"
      ".LwP3barv_1_%=:\n\t"
      "s_waitcnt lgkmcnt(0)\n\t"
      "s_barrier\n\t"
      "v_mfma_f32_16x16x32_bf16 v[132:135], a[4:7], v[80:83], v[12:15]\n\t"
      "ds_read_b128 v[140:143], v220 offset:0\n\t"
      "v_mfma_f32_16x16x32_bf16 v[132:135], a[28:31], v[84:87], v[132:135]\n\t"
      "ds_read_b128 v[240:243], v247 offset:0\n\t"
      "v_mfma_f32_16x16x32_bf16 v[132:135], a[52:55], v[88:91], v[132:135]\n\t"
      "v_cvt_pk_bf16_f32 v136, v128, v129\n\t"
      "v_mfma_f32_16x16x32_bf16 v[132:135], a[76:79], v[92:95], v[132:135]\n\t"
      "ds_read_b128 v[32:35], v196 offset:0\n\t"
      "v_cvt_pk_bf16_f32 v137, v130, v131\n\t"
      "v_mfma_f32_16x16x32_bf16 v[132:135], a[100:103], v[96:99], v[132:135]\n\t"
      "v_readlane_b32 s68, v248, s94\n\t"
      "v_mfma_f32_16x16x32_bf16 v[132:135], a[124:127], v[100:103], v[132:135]\n\t"
      "ds_read_b128 v[152:155], v238 offset:0\n\t"
      "v_mfma_f32_16x16x32_bf16 v[132:135], a[148:151], v[104:107], v[132:135]\n\t"
      "ds_read_b128 v[36:39], v197 offset:0\n\t"
      "v_mfma_f32_16x16x32_bf16 v[132:135], a[172:175], v[108:111], v[132:135]\n\t"
      "v_mfma_f32_16x16x32_bf16 v[132:135], a[196:199], v[112:115], v[132:135]\n\t"
      "s_waitcnt lgkmcnt(3)\n\t"
      "v_add3_u32 v240, v240, v241, v242\n\t"
      "v_mfma_f32_16x16x32_bf16 v[132:135], a[220:223], v[116:119], v[132:135]\n\t"
      "ds_read_b128 v[40:43], v198 offset:0\n\t"
      "v_mfma_f32_16x16x32_bf16 v[132:135], a[244:247], v[120:123], v[132:135]\n\t"
      "v_add_u32_e32 v240, v240, v243\n\t"
      "v_mfma_f32_16x16x32_bf16 v[132:135], v[176:179], v[124:127], v[132:135]\n\t"
      "buffer_store_dwordx4 v[140:143], v208, s[28:31], s72 offen sc1\n\t"
      "v_mfma_f32_16x16x32_bf16 v[128:131], a[8:11], v[80:83], v[16:19]\n\t"
      "ds_read_b128 v[44:47], v199 offset:0\n\t"
      "v_mfma_f32_16x16x32_bf16 v[128:131], a[32:35], v[84:87], v[128:131]\n\t"
      "v_add_u32_dpp v241, v240, v240 quad_perm:[1,0,3,2] row_mask:0xf bank_mask:0xf\n\t"
      "s_memrealtime s[80:81]\n\t"
      "v_mfma_f32_16x16x32_bf16 v[128:131], a[56:59], v[88:91], v[128:131]\n\t"
      "v_cvt_pk_bf16_f32 v138, v132, v133\n\t"
      "v_mfma_f32_16x16x32_bf16 v[128:131], a[80:83], v[92:95], v[128:131]\n\t"
      "v_cvt_pk_bf16_f32 v139, v134, v135\n\t"
      "v_mfma_f32_16x16x32_bf16 v[128:131], a[104:107], v[96:99], v[128:131]\n\t"
      "ds_write_b128 v217, v[136:139] offset:12288\n\t"
      "v_add_u32_dpp v240, v241, v241 quad_perm:[2,3,0,1] row_mask:0xf bank_mask:0xf\n\t"
      "v_mfma_f32_16x16x32_bf16 v[128:131], a[128:131], v[100:103], v[128:131]\n\t"
      "s_mov_b32 m0, s46\n\t"
      "v_mfma_f32_16x16x32_bf16 v[128:131], a[152:155], v[104:107], v[128:131]\n\t"
      "s_waitcnt lgkmcnt(4)\n\t"
      "buffer_store_dwordx4 v[152:155], v208, s[28:31], s68 offen sc1\n\t"
      "v_cmp_le_u32_e64 s[76:77], 18, v240\n\t"
      "v_cmp_ge_u32_e64 s[78:79], 14, v240\n\t"
      "v_mfma_f32_16x16x32_bf16 v[128:131], a[176:179], v[108:111], v[128:131]\n\t"
      "ds_read_b128 v[48:51], v197 offset:256\n\t"
      "v_mfma_f32_16x16x32_bf16 v[128:131], a[200:203], v[112:115], v[128:131]\n\t"
      "v_cmp_lt_u32_e32 vcc, 63, v240\n\t"
      "s_and_b32 s65, s76, s77\n\t"
      "v_mfma_f32_16x16x32_bf16 v[128:131], a[224:227], v[116:119], v[128:131]\n\t"
      "ds_read_b128 v[52:55], v198 offset:256\n\t"
      "v_mfma_f32_16x16x32_bf16 v[128:131], a[248:251], v[120:123], v[128:131]\n\t"
      "ds_read_b128 v[156:159], v238 offset:1024\n\t"
      "s_and_b32 s78, s78, s79\n\t"
      "s_and_b32 s67, s65, 0x11111111\n\t"
      "v_mfma_f32_16x16x32_bf16 v[128:131], v[180:183], v[124:127], v[128:131]\n\t"
      "v_mfma_f32_16x16x32_bf16 v[132:135], a[12:15], v[80:83], v[20:23]\n\t"
      "ds_read_b128 v[56:59], v199 offset:256\n\t"
      "s_or_b32 s65, s65, s78\n\t"
      "s_cmp_lg_u64 vcc, 0\n\t"
      "s_cselect_b32 s65, 0, s65\n\t"
      "s_cselect_b32 s67, 0, s67\n\t"
      "v_mfma_f32_16x16x32_bf16 v[132:135], a[36:39], v[84:87], v[132:135]\n\t"
      "v_and_b32_e32 v240, s67, v252\n\t"
      "v_and_b32_e32 v241, s65, v252\n\t"
      "v_mfma_f32_16x16x32_bf16 v[132:135], a[60:63], v[88:91], v[132:135]\n\t"
      "v_cvt_pk_bf16_f32 v136, v128, v129\n\t"
      "v_mfma_f32_16x16x32_bf16 v[132:135], a[84:87], v[92:95], v[132:135]\n\t"
      "v_cvt_pk_bf16_f32 v137, v130, v131\n\t"
      "ds_read_b128 v[144:147], v220 offset:1024\n\t"
      "v_mfma_f32_16x16x32_bf16 v[132:135], a[108:111], v[96:99], v[132:135]\n\t"
      "v_cmp_ne_u32_e64 s[76:77], 0, v240\n\t"
      "v_cmp_ne_u32_e64 s[78:79], 0, v241\n\t"
      "v_mfma_f32_16x16x32_bf16 v[132:135], a[132:135], v[100:103], v[132:135]\n\t"
      "s_waitcnt lgkmcnt(2)\n\t"
      "buffer_store_dwordx4 v[156:159], v208, s[28:31], s68 offen offset:1024 sc1\n\t"
      "v_subrev_u32_e32 v243, 5, v250\n\t"
      "s_and_b32 s64, s65, 0x22222222\n\t"
      "v_mfma_f32_16x16x32_bf16 v[132:135], a[156:159], v[104:107], v[132:135]\n\t"
      "ds_read_b128 v[60:63], v196 offset:256\n\t"
      "v_subrev_u32_e32 v242, 3, v250\n\t"
      "v_mfma_f32_16x16x32_bf16 v[132:135], a[180:183], v[108:111], v[132:135]\n\t"
      "s_xor_b32 s75, s75, s76\n\t"
      "s_xor_b32 s74, s74, s76\n\t"
      "v_mfma_f32_16x16x32_bf16 v[132:135], a[204:207], v[112:115], v[132:135]\n\t"
      "s_and_b32 s76, s75, s78\n\t"
      "v_cndmask_b32_e64 v249, v248, v251, s[76:77]\n\t"
      "v_mfma_f32_16x16x32_bf16 v[132:135], a[228:231], v[116:119], v[132:135]\n\t"
      "ds_read_b128 v[64:67], v198 offset:512\n\t"
      "s_bcnt1_i32_b32 s51, s78\n\t"
      "s_cmp_lt_u32 s51, 4\n\t"
      "s_cselect_b32 s51, 2, 0\n\t"
      "v_mfma_f32_16x16x32_bf16 v[132:135], a[252:255], v[120:123], v[132:135]\n\t"
      "s_and_b32 s76, s74, s78\n\t"
      "v_cndmask_b32_e64 v248, v248, v251, s[76:77]\n\t"
      "v_mfma_f32_16x16x32_bf16 v[132:135], v[184:187], v[124:127], v[132:135]\n\t"
      "s_waitcnt lgkmcnt(2)\n\t"
      "buffer_store_dwordx4 v[144:147], v208, s[28:31], s72 offen offset:1024 sc1\n\t"
      "v_mfma_f32_16x16x32_bf16 v[128:131], a[16:19], v[80:83], v[24:27]\n\t"
      "ds_read_b128 v[68:71], v199 offset:512\n\t"
      "v_add_u32_e32 v250, s51, v250\n\t"
      "v_mfma_f32_16x16x32_bf16 v[128:131], a[40:43], v[84:87], v[128:131]\n\t"
      "s_lshr_b32 s74, s78, 3\n\t"
      "s_or_b32 s76, s74, s78\n\t"
      "v_mfma_f32_16x16x32_bf16 v[128:131], a[64:67], v[88:91], v[128:131]\n\t"
      "v_cvt_pk_bf16_f32 v138, v132, v133\n\t"
      "v_readlane_b32 s73, v248, s85\n\t"
      "v_mfma_f32_16x16x32_bf16 v[128:131], a[88:91], v[92:95], v[128:131]\n\t"
      "v_cvt_pk_bf16_f32 v139, v134, v135\n\t"
      "ds_read_b128 v[160:163], v238 offset:2048\n\t"
      "v_mfma_f32_16x16x32_bf16 v[128:131], a[112:115], v[96:99], v[128:131]\n\t"
      "ds_write_b128 v218, v[136:139] offset:12288\n\t"
      "v_mfma_f32_16x16x32_bf16 v[128:131], a[136:139], v[100:103], v[128:131]\n\t"
      "v_cndmask_b32_e64 v250, v250, v242, s[76:77]\n\t"
      "s_and_b32 s78, s74, s78\n\t"
      "v_mfma_f32_16x16x32_bf16 v[128:131], a[160:163], v[104:107], v[128:131]\n\t"
      "s_and_b32 s78, s78, 0x3fff\n\t"
      "v_cndmask_b32_e64 v250, v250, v243, s[78:79]\n\t"
      "v_mfma_f32_16x16x32_bf16 v[128:131], a[184:187], v[108:111], v[128:131]\n\t"
      "ds_read_b128 v[148:151], v220 offset:2048\n\t"
      "v_mfma_f32_16x16x32_bf16 v[128:131], a[208:211], v[112:115], v[128:131]\n\t"
      "buffer_load_dwordx4 v208, s[20:23], s73 offen lds\n\t"
      "v_mfma_f32_16x16x32_bf16 v[128:131], a[232:235], v[116:119], v[128:131]\n\t"
      "ds_read_b128 v[72:75], v196 offset:512\n\t"
      "s_or_b32 s64, s64, s67\n\t"
      "v_mfma_f32_16x16x32_bf16 v[128:131], v[164:167], v[120:123], v[128:131]\n\t"
      "v_readlane_b32 s86, v250, s44\n\t"
      "v_mfma_f32_16x16x32_bf16 v[128:131], v[188:191], v[124:127], v[128:131]\n\t"
      "v_mfma_f32_16x16x32_bf16 v[132:135], a[20:23], v[80:83], v[28:31]\n\t"
      "ds_read_b128 v[76:79], v197 offset:512\n\t"
      "v_mfma_f32_16x16x32_bf16 v[132:135], a[44:47], v[84:87], v[132:135]\n\t"
      "buffer_load_dwordx4 v208, s[20:23], s73 offen offset:1024 lds\n\t"
      "v_mfma_f32_16x16x32_bf16 v[132:135], a[68:71], v[88:91], v[132:135]\n\t"
      "v_cvt_pk_bf16_f32 v136, v128, v129\n\t"
      "v_mfma_f32_16x16x32_bf16 v[132:135], a[92:95], v[92:95], v[132:135]\n\t"
      "v_cvt_pk_bf16_f32 v137, v130, v131\n\t"
      "s_sub_u32 s84, s84, 4\n\t"
      "s_max_i32 s84, s84, s86\n\t"
      "v_mfma_f32_16x16x32_bf16 v[132:135], a[116:119], v[96:99], v[132:135]\n\t"
      "s_sub_u32 s83, s82, s80\n\t"
      "v_mfma_f32_16x16x32_bf16 v[132:135], a[140:143], v[100:103], v[132:135]\n\t"
      "s_waitcnt lgkmcnt(4)\n\t"
      "buffer_store_dwordx4 v[160:163], v208, s[28:31], s68 offen offset:2048 sc1\n\t"
      "s_max_i32 s83, s83, 0\n\t"
      "s_add_u32 s82, s80, s83\n\t"
      "v_mfma_f32_16x16x32_bf16 v[132:135], a[164:167], v[104:107], v[132:135]\n\t"
      "s_mov_b32 s45, s46\n\t"
      "v_mfma_f32_16x16x32_bf16 v[132:135], a[188:191], v[108:111], v[132:135]\n\t"
      "s_mov_b32 s46, s47\n\t"
      "v_mfma_f32_16x16x32_bf16 v[132:135], a[212:215], v[112:115], v[132:135]\n\t"
      "s_mov_b32 s47, s48\n\t"
      "v_mfma_f32_16x16x32_bf16 v[132:135], a[236:239], v[116:119], v[132:135]\n\t"
      "s_add_u32 s82, s82, s84\n\t"
      "v_mfma_f32_16x16x32_bf16 v[132:135], v[168:171], v[120:123], v[132:135]\n\t"
      "s_cmp_lg_u32 s44, s49\n\t"
      "v_mfma_f32_16x16x32_bf16 v[132:135], v[192:195], v[124:127], v[132:135]\n\t"
      "s_waitcnt lgkmcnt(2)\n\t"
      "buffer_store_dwordx4 v[148:151], v208, s[28:31], s72 offen offset:2048 sc1\n\t"
      "v_mfma_f32_16x16x32_bf16 v[128:131], a[0:3], v[32:35], v[8:11]\n\t"
      "v_mfma_f32_16x16x32_bf16 v[128:131], a[24:27], v[36:39], v[128:131]\n\t"
      "v_readlane_b32 s72, v249, s44\n\t"
      "v_mfma_f32_16x16x32_bf16 v[128:131], a[48:51], v[40:43], v[128:131]\n\t"
      "v_cvt_pk_bf16_f32 v138, v132, v133\n\t"
      "v_mfma_f32_16x16x32_bf16 v[128:131], a[72:75], v[44:47], v[128:131]\n\t"
      "v_cvt_pk_bf16_f32 v139, v134, v135\n\t"
      "v_mfma_f32_16x16x32_bf16 v[128:131], a[96:99], v[48:51], v[128:131]\n\t"
      "ds_write_b128 v219, v[136:139] offset:12288\n\t"
      "v_mfma_f32_16x16x32_bf16 v[128:131], a[120:123], v[52:55], v[128:131]\n\t"
      "v_mfma_f32_16x16x32_bf16 v[128:131], a[144:147], v[56:59], v[128:131]\n\t"
      "buffer_load_dwordx4 v208, s[20:23], s73 offen offset:2048 lds\n\t"
      "v_mfma_f32_16x16x32_bf16 v[128:131], a[168:171], v[60:63], v[128:131]\n\t"
      "s_waitcnt lgkmcnt(1)\n\t"
      "v_mfma_f32_16x16x32_bf16 v[128:131], a[192:195], v[64:67], v[128:131]\n\t"
      "v_add_u32_e32 v217, s46, v204\n\t"
      "v_mfma_f32_16x16x32_bf16 v[128:131], a[216:219], v[68:71], v[128:131]\n\t"
      "v_add_u32_e32 v218, s46, v205\n\t"
      "v_mfma_f32_16x16x32_bf16 v[128:131], a[240:243], v[72:75], v[128:131]\n\t"
      "v_add_u32_e32 v219, s46, v206\n\t"
      "v_mfma_f32_16x16x32_bf16 v[128:131], v[172:175], v[76:79], v[128:131]\n\t"
      "s_cmp_gt_i32 s83, 1\n\t"
      "s_cbranch_scc1 .LslpP4_%=\n\t"
      ".LretP4_%=:\n\t"
      "s_waitcnt vmcnt(15) lgkmcnt(0)\n\t"
      "s_barrier\n\t"
      "v_mfma_f32_16x16x32_bf16 v[132:135], a[4:7], v[32:35], v[12:15]\n\t"
      "ds_read_b128 v[140:143], v220 offset:12288\n\t"
      "v_mfma_f32_16x16x32_bf16 v[132:135], a[28:31], v[36:39], v[132:135]\n\t"
      "v_mfma_f32_16x16x32_bf16 v[132:135], a[52:55], v[40:43], v[132:135]\n\t"
      "v_cvt_pk_bf16_f32 v136, v128, v129\n\t"
      "v_mfma_f32_16x16x32_bf16 v[132:135], a[76:79], v[44:47], v[132:135]\n\t"
      "ds_read_b128 v[80:83], v196 offset:12288\n\t"
      "v_cvt_pk_bf16_f32 v137, v130, v131\n\t"
      "v_mfma_f32_16x16x32_bf16 v[132:135], a[100:103], v[48:51], v[132:135]\n\t"
      "v_readlane_b32 s68, v248, s94\n\t"
      "s_add_u32 s48, s47, 0x6000\n\t"
      "v_mfma_f32_16x16x32_bf16 v[132:135], a[124:127], v[52:55], v[132:135]\n\t"
      "ds_read_b128 v[152:155], v238 offset:12288\n\t"
      "s_cmp_eq_u32 s48, s50\n\t"
      "s_cselect_b32 s48, s41, s48\n\t"
      "v_mfma_f32_16x16x32_bf16 v[132:135], a[148:151], v[56:59], v[132:135]\n\t"
      "ds_read_b128 v[84:87], v197 offset:12288\n\t"
      "v_mfma_f32_16x16x32_bf16 v[132:135], a[172:175], v[60:63], v[132:135]\n\t"
      "v_readlane_b32 s73, v249, s85\n\t"
      "v_mfma_f32_16x16x32_bf16 v[132:135], a[196:199], v[64:67], v[132:135]\n\t"
      "v_mfma_f32_16x16x32_bf16 v[132:135], a[220:223], v[68:71], v[132:135]\n\t"
      "ds_read_b128 v[88:91], v198 offset:12288\n\t"
      "v_mfma_f32_16x16x32_bf16 v[132:135], a[244:247], v[72:75], v[132:135]\n\t"
      "v_mfma_f32_16x16x32_bf16 v[132:135], v[176:179], v[76:79], v[132:135]\n\t"
      "s_waitcnt lgkmcnt(4)\n\t"
      "buffer_store_dwordx4 v[140:143], v209, s[28:31], s72 offen sc1\n\t"
      "v_mfma_f32_16x16x32_bf16 v[128:131], a[8:11], v[32:35], v[16:19]\n\t"
      "ds_read_b128 v[92:95], v199 offset:12288\n\t"
      "v_mfma_f32_16x16x32_bf16 v[128:131], a[32:35], v[36:39], v[128:131]\n\t"
      "s_memrealtime s[80:81]\n\t"
      "v_mfma_f32_16x16x32_bf16 v[128:131], a[56:59], v[40:43], v[128:131]\n\t"
      "v_cvt_pk_bf16_f32 v138, v132, v133\n\t"
      "v_mfma_f32_16x16x32_bf16 v[128:131], a[80:83], v[44:47], v[128:131]\n\t"
      "v_cvt_pk_bf16_f32 v139, v134, v135\n\t"
      "v_mfma_f32_16x16x32_bf16 v[128:131], a[104:107], v[48:51], v[128:131]\n\t"
      "ds_write_b128 v217, v[136:139] offset:0\n\t"
      "v_mfma_f32_16x16x32_bf16 v[128:131], a[128:131], v[52:55], v[128:131]\n\t"
      "s_add_u32 m0, s45, 0x3000\n\t"
      "v_mfma_f32_16x16x32_bf16 v[128:131], a[152:155], v[56:59], v[128:131]\n\t"
      "s_waitcnt lgkmcnt(4)\n\t"
      "buffer_store_dwordx4 v[152:155], v209, s[28:31], s68 offen sc1\n\t"
      "v_mfma_f32_16x16x32_bf16 v[128:131], a[176:179], v[60:63], v[128:131]\n\t"
      "ds_read_b128 v[96:99], v197 offset:12544\n\t"
      "v_mfma_f32_16x16x32_bf16 v[128:131], a[200:203], v[64:67], v[128:131]\n\t"
      "v_mfma_f32_16x16x32_bf16 v[128:131], a[224:227], v[68:71], v[128:131]\n\t"
      "ds_read_b128 v[100:103], v198 offset:12544\n\t"
      "v_mfma_f32_16x16x32_bf16 v[128:131], a[248:251], v[72:75], v[128:131]\n\t"
      "ds_read_b128 v[156:159], v238 offset:13312\n\t"
      "v_mfma_f32_16x16x32_bf16 v[128:131], v[180:183], v[76:79], v[128:131]\n\t"
      "buffer_load_dwordx4 v209, s[20:23], s73 offen lds\n\t"
      "v_mfma_f32_16x16x32_bf16 v[132:135], a[12:15], v[32:35], v[20:23]\n\t"
      "ds_read_b128 v[104:107], v199 offset:12544\n\t"
      "v_mfma_f32_16x16x32_bf16 v[132:135], a[36:39], v[36:39], v[132:135]\n\t"
      "v_mfma_f32_16x16x32_bf16 v[132:135], a[60:63], v[40:43], v[132:135]\n\t"
      "v_cvt_pk_bf16_f32 v136, v128, v129\n\t"
      "v_mfma_f32_16x16x32_bf16 v[132:135], a[84:87], v[44:47], v[132:135]\n\t"
      "v_cvt_pk_bf16_f32 v137, v130, v131\n\t"
      "ds_read_b128 v[144:147], v220 offset:13312\n\t"
      "v_mfma_f32_16x16x32_bf16 v[132:135], a[108:111], v[48:51], v[132:135]\n\t"
      "v_mfma_f32_16x16x32_bf16 v[132:135], a[132:135], v[52:55], v[132:135]\n\t"
      "s_waitcnt lgkmcnt(2)\n\t"
      "buffer_store_dwordx4 v[156:159], v209, s[28:31], s68 offen offset:1024 sc1\n\t"
      "v_mfma_f32_16x16x32_bf16 v[132:135], a[156:159], v[56:59], v[132:135]\n\t"
      "ds_read_b128 v[108:111], v196 offset:12544\n\t"
      "v_mfma_f32_16x16x32_bf16 v[132:135], a[180:183], v[60:63], v[132:135]\n\t"
      "v_mfma_f32_16x16x32_bf16 v[132:135], a[204:207], v[64:67], v[132:135]\n\t"
      "v_mfma_f32_16x16x32_bf16 v[132:135], a[228:231], v[68:71], v[132:135]\n\t"
      "ds_read_b128 v[112:115], v198 offset:12800\n\t"
      "v_mfma_f32_16x16x32_bf16 v[132:135], a[252:255], v[72:75], v[132:135]\n\t"
      "v_mfma_f32_16x16x32_bf16 v[132:135], v[184:187], v[76:79], v[132:135]\n\t"
      "s_waitcnt lgkmcnt(2)\n\t"
      "buffer_store_dwordx4 v[144:147], v209, s[28:31], s72 offen offset:1024 sc1\n\t"
      "v_mfma_f32_16x16x32_bf16 v[128:131], a[16:19], v[32:35], v[24:27]\n\t"
      "ds_read_b128 v[116:119], v199 offset:12800\n\t"
      "v_mfma_f32_16x16x32_bf16 v[128:131], a[40:43], v[36:39], v[128:131]\n\t"
      "v_mfma_f32_16x16x32_bf16 v[128:131], a[64:67], v[40:43], v[128:131]\n\t"
      "v_cvt_pk_bf16_f32 v138, v132, v133\n\t"
      "v_mfma_f32_16x16x32_bf16 v[128:131], a[88:91], v[44:47], v[128:131]\n\t"
      "v_cvt_pk_bf16_f32 v139, v134, v135\n\t"
      "ds_read_b128 v[160:163], v238 offset:14336\n\t"
      "v_mfma_f32_16x16x32_bf16 v[128:131], a[112:115], v[48:51], v[128:131]\n\t"
      "ds_write_b128 v218, v[136:139] offset:0\n\t"
      "v_mfma_f32_16x16x32_bf16 v[128:131], a[136:139], v[52:55], v[128:131]\n\t"
      "v_mfma_f32_16x16x32_bf16 v[128:131], a[160:163], v[56:59], v[128:131]\n\t"
      "v_mfma_f32_16x16x32_bf16 v[128:131], a[184:187], v[60:63], v[128:131]\n\t"
      "ds_read_b128 v[148:151], v220 offset:14336\n\t"
      "v_mfma_f32_16x16x32_bf16 v[128:131], a[208:211], v[64:67], v[128:131]\n\t"
      "v_mfma_f32_16x16x32_bf16 v[128:131], a[232:235], v[68:71], v[128:131]\n\t"
      "ds_read_b128 v[120:123], v196 offset:12800\n\t"
      "v_mfma_f32_16x16x32_bf16 v[128:131], v[164:167], v[72:75], v[128:131]\n\t"
      "v_mfma_f32_16x16x32_bf16 v[128:131], v[188:191], v[76:79], v[128:131]\n\t"
      "buffer_load_dwordx4 v209, s[20:23], s73 offen offset:1024 lds\n\t"
      "v_mfma_f32_16x16x32_bf16 v[132:135], a[20:23], v[32:35], v[28:31]\n\t"
      "ds_read_b128 v[124:127], v197 offset:12800\n\t"
      "v_mfma_f32_16x16x32_bf16 v[132:135], a[44:47], v[36:39], v[132:135]\n\t"
      "v_mfma_f32_16x16x32_bf16 v[132:135], a[68:71], v[40:43], v[132:135]\n\t"
      "v_cvt_pk_bf16_f32 v136, v128, v129\n\t"
      "v_add_u32_e32 v220, s46, v207\n\t"
      "v_mfma_f32_16x16x32_bf16 v[132:135], a[92:95], v[44:47], v[132:135]\n\t"
      "v_cvt_pk_bf16_f32 v137, v130, v131\n\t"
      "s_sub_u32 s84, s84, 4\n\t"
      "s_max_i32 s84, s84, s86\n\t"
      "v_mfma_f32_16x16x32_bf16 v[132:135], a[116:119], v[48:51], v[132:135]\n\t"
      "s_sub_u32 s83, s82, s80\n\t"
      "v_mfma_f32_16x16x32_bf16 v[132:135], a[140:143], v[52:55], v[132:135]\n\t"
      "s_waitcnt lgkmcnt(4)\n\t"
      "buffer_store_dwordx4 v[160:163], v209, s[28:31], s68 offen offset:2048 sc1\n\t"
      "s_max_i32 s83, s83, 0\n\t"
      "s_add_u32 s82, s80, s83\n\t"
      "v_mfma_f32_16x16x32_bf16 v[132:135], a[164:167], v[56:59], v[132:135]\n\t"
      "v_mfma_f32_16x16x32_bf16 v[132:135], a[188:191], v[60:63], v[132:135]\n\t"
      "v_mfma_f32_16x16x32_bf16 v[132:135], a[212:215], v[64:67], v[132:135]\n\t"
      "s_add_u32 s44, s44, 1\n\t"
      "v_mfma_f32_16x16x32_bf16 v[132:135], a[236:239], v[68:71], v[132:135]\n\t"
      "s_add_u32 s82, s82, s84\n\t"
      "v_mfma_f32_16x16x32_bf16 v[132:135], v[168:171], v[72:75], v[132:135]\n\t"
      "s_add_u32 s85, s85, 1\n\t"
      "v_mfma_f32_16x16x32_bf16 v[132:135], v[192:195], v[76:79], v[132:135]\n\t"
      "s_waitcnt lgkmcnt(2)\n\t"
      "buffer_store_dwordx4 v[148:151], v209, s[28:31], s72 offen offset:2048 sc1\n\t"
      "v_mfma_f32_16x16x32_bf16 v[128:131], a[0:3], v[80:83], v[8:11]\n\t"
      "v_mfma_f32_16x16x32_bf16 v[128:131], a[24:27], v[84:87], v[128:131]\n\t"
      "v_readlane_b32 s72, v248, s44\n\t"
      "v_mfma_f32_16x16x32_bf16 v[128:131], a[48:51], v[88:91], v[128:131]\n\t"
      "v_cvt_pk_bf16_f32 v138, v132, v133\n\t"
      "v_mfma_f32_16x16x32_bf16 v[128:131], a[72:75], v[92:95], v[128:131]\n\t"
      "v_cvt_pk_bf16_f32 v139, v134, v135\n\t"
      "v_mfma_f32_16x16x32_bf16 v[128:131], a[96:99], v[96:99], v[128:131]\n\t"
      "ds_write_b128 v219, v[136:139] offset:0\n\t"
      "v_mfma_f32_16x16x32_bf16 v[128:131], a[120:123], v[100:103], v[128:131]\n\t"
      "v_mfma_f32_16x16x32_bf16 v[128:131], a[144:147], v[104:107], v[128:131]\n\t"
      "buffer_load_dwordx4 v209, s[20:23], s73 offen offset:2048 lds\n\t"
      "v_mfma_f32_16x16x32_bf16 v[128:131], a[168:171], v[108:111], v[128:131]\n\t"
      "v_add_u32_e32 v196, s47, v200\n\t"
      "s_waitcnt lgkmcnt(1)\n\t"
      "v_mfma_f32_16x16x32_bf16 v[128:131], a[192:195], v[112:115], v[128:131]\n\t"
      "v_add_u32_e32 v197, s47, v201\n\t"
      "v_mfma_f32_16x16x32_bf16 v[128:131], a[216:219], v[116:119], v[128:131]\n\t"
      "v_add_u32_e32 v198, s47, v202\n\t"
      "v_mfma_f32_16x16x32_bf16 v[128:131], a[240:243], v[120:123], v[128:131]\n\t"
      "v_add_u32_e32 v199, s47, v203\n\t"
      "v_mfma_f32_16x16x32_bf16 v[128:131], v[172:175], v[124:127], v[128:131]\n\t"
      "s_cmp_gt_i32 s83, 1\n\t"
      "s_cbranch_scc1 .LslpP5_%=\n\t"
      ".LretP5_%=:\n\t"
      "s_waitcnt vmcnt(21) lgkmcnt(0)\n\t"
      "s_barrier\n\t"
      "v_mfma_f32_16x16x32_bf16 v[132:135], a[4:7], v[80:83], v[12:15]\n\t"
      "ds_read_b128 v[140:143], v220 offset:0\n\t"
      "v_mfma_f32_16x16x32_bf16 v[132:135], a[28:31], v[84:87], v[132:135]\n\t"
      "v_mfma_f32_16x16x32_bf16 v[132:135], a[52:55], v[88:91], v[132:135]\n\t"
      "v_cvt_pk_bf16_f32 v136, v128, v129\n\t"
      "v_mfma_f32_16x16x32_bf16 v[132:135], a[76:79], v[92:95], v[132:135]\n\t"
      "ds_read_b128 v[32:35], v196 offset:0\n\t"
      "v_cvt_pk_bf16_f32 v137, v130, v131\n\t"
      "v_mfma_f32_16x16x32_bf16 v[132:135], a[100:103], v[96:99], v[132:135]\n\t"
      "v_mfma_f32_16x16x32_bf16 v[132:135], a[124:127], v[100:103], v[132:135]\n\t"
      "v_mfma_f32_16x16x32_bf16 v[132:135], a[148:151], v[104:107], v[132:135]\n\t"
      "ds_read_b128 v[36:39], v197 offset:0\n\t"
      "v_mfma_f32_16x16x32_bf16 v[132:135], a[172:175], v[108:111], v[132:135]\n\t"
      "v_readlane_b32 s73, v248, s85\n\t"
      "v_mfma_f32_16x16x32_bf16 v[132:135], a[196:199], v[112:115], v[132:135]\n\t"
      "v_mfma_f32_16x16x32_bf16 v[132:135], a[220:223], v[116:119], v[132:135]\n\t"
      "ds_read_b128 v[40:43], v198 offset:0\n\t"
      "v_mfma_f32_16x16x32_bf16 v[132:135], a[244:247], v[120:123], v[132:135]\n\t"
      "v_mfma_f32_16x16x32_bf16 v[132:135], v[176:179], v[124:127], v[132:135]\n\t"
      "s_waitcnt lgkmcnt(3)\n\t"
      "buffer_store_dwordx4 v[140:143], v208, s[28:31], s72 offen sc1\n\t"
      "v_mfma_f32_16x16x32_bf16 v[128:131], a[8:11], v[80:83], v[16:19]\n\t"
      "ds_read_b128 v[44:47], v199 offset:0\n\t"
      "v_mfma_f32_16x16x32_bf16 v[128:131], a[32:35], v[84:87], v[128:131]\n\t"
      "s_memrealtime s[80:81]\n\t"
      "v_mfma_f32_16x16x32_bf16 v[128:131], a[56:59], v[88:91], v[128:131]\n\t"
      "v_cvt_pk_bf16_f32 v138, v132, v133\n\t"
      "v_mfma_f32_16x16x32_bf16 v[128:131], a[80:83], v[92:95], v[128:131]\n\t"
      "v_cvt_pk_bf16_f32 v139, v134, v135\n\t"
      "v_mfma_f32_16x16x32_bf16 v[128:131], a[104:107], v[96:99], v[128:131]\n\t"
      "ds_write_b128 v217, v[136:139] offset:12288\n\t"
      "v_mfma_f32_16x16x32_bf16 v[128:131], a[128:131], v[100:103], v[128:131]\n\t"
      "s_mov_b32 m0, s46\n\t"
      "v_mfma_f32_16x16x32_bf16 v[128:131], a[152:155], v[104:107], v[128:131]\n\t"
      "v_mfma_f32_16x16x32_bf16 v[128:131], a[176:179], v[108:111], v[128:131]\n\t"
      "ds_read_b128 v[48:51], v197 offset:256\n\t"
      "v_mfma_f32_16x16x32_bf16 v[128:131], a[200:203], v[112:115], v[128:131]\n\t"
      "v_mfma_f32_16x16x32_bf16 v[128:131], a[224:227], v[116:119], v[128:131]\n\t"
      "ds_read_b128 v[52:55], v198 offset:256\n\t"
      "v_mfma_f32_16x16x32_bf16 v[128:131], a[248:251], v[120:123], v[128:131]\n\t"
      "v_mfma_f32_16x16x32_bf16 v[128:131], v[180:183], v[124:127], v[128:131]\n\t"
      "buffer_load_dwordx4 v208, s[20:23], s73 offen lds\n\t"
      "v_mfma_f32_16x16x32_bf16 v[132:135], a[12:15], v[80:83], v[20:23]\n\t"
      "ds_read_b128 v[56:59], v199 offset:256\n\t"
      "v_mfma_f32_16x16x32_bf16 v[132:135], a[36:39], v[84:87], v[132:135]\n\t"
      "v_mfma_f32_16x16x32_bf16 v[132:135], a[60:63], v[88:91], v[132:135]\n\t"
      "v_cvt_pk_bf16_f32 v136, v128, v129\n\t"
      "v_mfma_f32_16x16x32_bf16 v[132:135], a[84:87], v[92:95], v[132:135]\n\t"
      "v_cvt_pk_bf16_f32 v137, v130, v131\n\t"
      "ds_read_b128 v[144:147], v220 offset:1024\n\t"
      "v_mfma_f32_16x16x32_bf16 v[132:135], a[108:111], v[96:99], v[132:135]\n\t"
      "v_mfma_f32_16x16x32_bf16 v[132:135], a[132:135], v[100:103], v[132:135]\n\t"
      "v_mfma_f32_16x16x32_bf16 v[132:135], a[156:159], v[104:107], v[132:135]\n\t"
      "ds_read_b128 v[60:63], v196 offset:256\n\t"
      "v_mfma_f32_16x16x32_bf16 v[132:135], a[180:183], v[108:111], v[132:135]\n\t"
      "v_mfma_f32_16x16x32_bf16 v[132:135], a[204:207], v[112:115], v[132:135]\n\t"
      "v_mfma_f32_16x16x32_bf16 v[132:135], a[228:231], v[116:119], v[132:135]\n\t"
      "ds_read_b128 v[64:67], v198 offset:512\n\t"
      "v_mfma_f32_16x16x32_bf16 v[132:135], a[252:255], v[120:123], v[132:135]\n\t"
      "v_mfma_f32_16x16x32_bf16 v[132:135], v[184:187], v[124:127], v[132:135]\n\t"
      "s_waitcnt lgkmcnt(2)\n\t"
      "buffer_store_dwordx4 v[144:147], v208, s[28:31], s72 offen offset:1024 sc1\n\t"
      "v_mfma_f32_16x16x32_bf16 v[128:131], a[16:19], v[80:83], v[24:27]\n\t"
      "ds_read_b128 v[68:71], v199 offset:512\n\t"
      "v_mfma_f32_16x16x32_bf16 v[128:131], a[40:43], v[84:87], v[128:131]\n\t"
      "v_readlane_b32 s86, v250, s44\n\t"
      "v_mfma_f32_16x16x32_bf16 v[128:131], a[64:67], v[88:91], v[128:131]\n\t"
      "v_cvt_pk_bf16_f32 v138, v132, v133\n\t"
      "v_mfma_f32_16x16x32_bf16 v[128:131], a[88:91], v[92:95], v[128:131]\n\t"
      "v_cvt_pk_bf16_f32 v139, v134, v135\n\t"
      "v_mfma_f32_16x16x32_bf16 v[128:131], a[112:115], v[96:99], v[128:131]\n\t"
      "ds_write_b128 v218, v[136:139] offset:12288\n\t"
      "v_mfma_f32_16x16x32_bf16 v[128:131], a[136:139], v[100:103], v[128:131]\n\t"
      "v_mfma_f32_16x16x32_bf16 v[128:131], a[160:163], v[104:107], v[128:131]\n\t"
      "v_mfma_f32_16x16x32_bf16 v[128:131], a[184:187], v[108:111], v[128:131]\n\t"
      "ds_read_b128 v[148:151], v220 offset:2048\n\t"
      "v_mfma_f32_16x16x32_bf16 v[128:131], a[208:211], v[112:115], v[128:131]\n\t"
      "v_mfma_f32_16x16x32_bf16 v[128:131], a[232:235], v[116:119], v[128:131]\n\t"
      "ds_read_b128 v[72:75], v196 offset:512\n\t"
      "v_mfma_f32_16x16x32_bf16 v[128:131], v[164:167], v[120:123], v[128:131]\n\t"
      "v_mfma_f32_16x16x32_bf16 v[128:131], v[188:191], v[124:127], v[128:131]\n\t"
      "buffer_load_dwordx4 v208, s[20:23], s73 offen offset:1024 lds\n\t"
      "v_mfma_f32_16x16x32_bf16 v[132:135], a[20:23], v[80:83], v[28:31]\n\t"
      "ds_read_b128 v[76:79], v197 offset:512\n\t"
      "v_mfma_f32_16x16x32_bf16 v[132:135], a[44:47], v[84:87], v[132:135]\n\t"
      "v_mfma_f32_16x16x32_bf16 v[132:135], a[68:71], v[88:91], v[132:135]\n\t"
      "v_cvt_pk_bf16_f32 v136, v128, v129\n\t"
      "v_mfma_f32_16x16x32_bf16 v[132:135], a[92:95], v[92:95], v[132:135]\n\t"
      "v_cvt_pk_bf16_f32 v137, v130, v131\n\t"
      "s_sub_u32 s84, s84, 4\n\t"
      "s_max_i32 s84, s84, s86\n\t"
      "v_mfma_f32_16x16x32_bf16 v[132:135], a[116:119], v[96:99], v[132:135]\n\t"
      "s_sub_u32 s83, s82, s80\n\t"
      "v_mfma_f32_16x16x32_bf16 v[132:135], a[140:143], v[100:103], v[132:135]\n\t"
      "s_max_i32 s83, s83, 0\n\t"
      "s_add_u32 s82, s80, s83\n\t"
      "v_mfma_f32_16x16x32_bf16 v[132:135], a[164:167], v[104:107], v[132:135]\n\t"
      "s_mov_b32 s45, s46\n\t"
      "v_mfma_f32_16x16x32_bf16 v[132:135], a[188:191], v[108:111], v[132:135]\n\t"
      "s_mov_b32 s46, s47\n\t"
      "v_mfma_f32_16x16x32_bf16 v[132:135], a[212:215], v[112:115], v[132:135]\n\t"
      "s_mov_b32 s47, s48\n\t"
      "v_mfma_f32_16x16x32_bf16 v[132:135], a[236:239], v[116:119], v[132:135]\n\t"
      "s_add_u32 s82, s82, s84\n\t"
      "v_mfma_f32_16x16x32_bf16 v[132:135], v[168:171], v[120:123], v[132:135]\n\t"
      "s_cmp_lg_u32 s44, s49\n\t"
      "v_mfma_f32_16x16x32_bf16 v[132:135], v[192:195], v[124:127], v[132:135]\n\t"
      "s_waitcnt lgkmcnt(2)\n\t"
      "buffer_store_dwordx4 v[148:151], v208, s[28:31], s72 offen offset:2048 sc1\n\t"
      ".p2align 6\n\t"
      ".Lloop_%=:\n\t"
      "s_waitcnt lgkmcnt(12)\n\t"
      "v_mfma_f32_16x16x32_bf16 v[128:131], a[0:3], v[32:35], v[8:11]\n\t"
      "v_mfma_f32_16x16x32_bf16 v[128:131], a[24:27], v[36:39], v[128:131]\n\t"
      "v_readlane_b32 s72, v249, s44\n\t"
      "v_mfma_f32_16x16x32_bf16 v[128:131], a[48:51], v[40:43], v[128:131]\n\t"
      "v_cvt_pk_bf16_f32 v138, v132, v133\n\t"
      "v_mfma_f32_16x16x32_bf16 v[128:131], a[72:75], v[44:47], v[128:131]\n\t"
      "v_cvt_pk_bf16_f32 v139, v134, v135\n\t"
      "s_waitcnt lgkmcnt(6)\n\t"
      "v_mfma_f32_16x16x32_bf16 v[128:131], a[96:99], v[48:51], v[128:131]\n\t"
      "ds_write_b128 v219, v[136:139] offset:12288\n\t"
      "v_mfma_f32_16x16x32_bf16 v[128:131], a[120:123], v[52:55], v[128:131]\n\t"
      "v_mfma_f32_16x16x32_bf16 v[128:131], a[144:147], v[56:59], v[128:131]\n\t"
      "buffer_load_dwordx4 v208, s[20:23], s73 offen offset:2048 lds\n\t"
      "v_mfma_f32_16x16x32_bf16 v[128:131], a[168:171], v[60:63], v[128:131]\n\t"
      "s_waitcnt lgkmcnt(1)\n\t"
      "v_mfma_f32_16x16x32_bf16 v[128:131], a[192:195], v[64:67], v[128:131]\n\t"
      "v_add_u32_e32 v217, s46, v204\n\t"
      "v_mfma_f32_16x16x32_bf16 v[128:131], a[216:219], v[68:71], v[128:131]\n\t"
      "v_add_u32_e32 v218, s46, v205\n\t"
      "v_mfma_f32_16x16x32_bf16 v[128:131], a[240:243], v[72:75], v[128:131]\n\t"
      "v_add_u32_e32 v219, s46, v206\n\t"
      "v_mfma_f32_16x16x32_bf16 v[128:131], v[172:175], v[76:79], v[128:131]\n\t"
      "s_cmp_gt_i32 s83, 1\n\t"
      "s_cbranch_scc1 .LslpA_%=\n\t"
      ".LretA_%=:\n\t"
      "s_waitcnt vmcnt(18) lgkmcnt(0)\n\t"
      "s_barrier\n\t"
      "v_mfma_f32_16x16x32_bf16 v[132:135], a[4:7], v[32:35], v[12:15]\n\t"
      "ds_read_b128 v[140:143], v220 offset:12288\n\t"
      "v_mfma_f32_16x16x32_bf16 v[132:135], a[28:31], v[36:39], v[132:135]\n\t"
      "v_mfma_f32_16x16x32_bf16 v[132:135], a[52:55], v[40:43], v[132:135]\n\t"
      "v_cvt_pk_bf16_f32 v136, v128, v129\n\t"
      "v_mfma_f32_16x16x32_bf16 v[132:135], a[76:79], v[44:47], v[132:135]\n\t"
      "ds_read_b128 v[80:83], v196 offset:12288\n\t"
      "v_cvt_pk_bf16_f32 v137, v130, v131\n\t"
      "v_mfma_f32_16x16x32_bf16 v[132:135], a[100:103], v[48:51], v[132:135]\n\t"
      "s_add_u32 s48, s47, 0x6000\n\t"
      "v_mfma_f32_16x16x32_bf16 v[132:135], a[124:127], v[52:55], v[132:135]\n\t"
      "s_cmp_eq_u32 s48, s50\n\t"
      "s_cselect_b32 s48, s41, s48\n\t"
      "v_mfma_f32_16x16x32_bf16 v[132:135], a[148:151], v[56:59], v[132:135]\n\t"
      "ds_read_b128 v[84:87], v197 offset:12288\n\t"
      "v_mfma_f32_16x16x32_bf16 v[132:135], a[172:175], v[60:63], v[132:135]\n\t"
      "v_readlane_b32 s73, v249, s85\n\t"
      "v_mfma_f32_16x16x32_bf16 v[132:135], a[196:199], v[64:67], v[132:135]\n\t"
      "v_mfma_f32_16x16x32_bf16 v[132:135], a[220:223], v[68:71], v[132:135]\n\t"
      "ds_read_b128 v[88:91], v198 offset:12288\n\t"
      "v_mfma_f32_16x16x32_bf16 v[132:135], a[244:247], v[72:75], v[132:135]\n\t"
      "v_mfma_f32_16x16x32_bf16 v[132:135], v[176:179], v[76:79], v[132:135]\n\t"
      "s_waitcnt lgkmcnt(3)\n\t"
      "buffer_store_dwordx4 v[140:143], v209, s[28:31], s72 offen sc1\n\t"
      "v_mfma_f32_16x16x32_bf16 v[128:131], a[8:11], v[32:35], v[16:19]\n\t"
      "ds_read_b128 v[92:95], v199 offset:12288\n\t"
      "v_mfma_f32_16x16x32_bf16 v[128:131], a[32:35], v[36:39], v[128:131]\n\t"
      "s_memrealtime s[80:81]\n\t"
      "v_mfma_f32_16x16x32_bf16 v[128:131], a[56:59], v[40:43], v[128:131]\n\t"
      "v_cvt_pk_bf16_f32 v138, v132, v133\n\t"
      "v_mfma_f32_16x16x32_bf16 v[128:131], a[80:83], v[44:47], v[128:131]\n\t"
      "v_cvt_pk_bf16_f32 v139, v134, v135\n\t"
      "v_mfma_f32_16x16x32_bf16 v[128:131], a[104:107], v[48:51], v[128:131]\n\t"
      "ds_write_b128 v217, v[136:139] offset:0\n\t"
      "v_mfma_f32_16x16x32_bf16 v[128:131], a[128:131], v[52:55], v[128:131]\n\t"
      "s_add_u32 m0, s45, 0x3000\n\t"
      "v_mfma_f32_16x16x32_bf16 v[128:131], a[152:155], v[56:59], v[128:131]\n\t"
      "v_mfma_f32_16x16x32_bf16 v[128:131], a[176:179], v[60:63], v[128:131]\n\t"
      "ds_read_b128 v[96:99], v197 offset:12544\n\t"
      "v_mfma_f32_16x16x32_bf16 v[128:131], a[200:203], v[64:67], v[128:131]\n\t"
      "v_mfma_f32_16x16x32_bf16 v[128:131], a[224:227], v[68:71], v[128:131]\n\t"
      "ds_read_b128 v[100:103], v198 offset:12544\n\t"
      "v_mfma_f32_16x16x32_bf16 v[128:131], a[248:251], v[72:75], v[128:131]\n\t"
      "v_mfma_f32_16x16x32_bf16 v[128:131], v[180:183], v[76:79], v[128:131]\n\t"
      "buffer_load_dwordx4 v209, s[20:23], s73 offen lds\n\t"
      "v_mfma_f32_16x16x32_bf16 v[132:135], a[12:15], v[32:35], v[20:23]\n\t"
      "ds_read_b128 v[104:107], v199 offset:12544\n\t"
      "v_mfma_f32_16x16x32_bf16 v[132:135], a[36:39], v[36:39], v[132:135]\n\t"
      "v_mfma_f32_16x16x32_bf16 v[132:135], a[60:63], v[40:43], v[132:135]\n\t"
      "v_cvt_pk_bf16_f32 v136, v128, v129\n\t"
      "v_mfma_f32_16x16x32_bf16 v[132:135], a[84:87], v[44:47], v[132:135]\n\t"
      "v_cvt_pk_bf16_f32 v137, v130, v131\n\t"
      "ds_read_b128 v[144:147], v220 offset:13312\n\t"
      "v_mfma_f32_16x16x32_bf16 v[132:135], a[108:111], v[48:51], v[132:135]\n\t"
      "v_mfma_f32_16x16x32_bf16 v[132:135], a[132:135], v[52:55], v[132:135]\n\t"
      "v_mfma_f32_16x16x32_bf16 v[132:135], a[156:159], v[56:59], v[132:135]\n\t"
      "ds_read_b128 v[108:111], v196 offset:12544\n\t"
      "v_mfma_f32_16x16x32_bf16 v[132:135], a[180:183], v[60:63], v[132:135]\n\t"
      "v_mfma_f32_16x16x32_bf16 v[132:135], a[204:207], v[64:67], v[132:135]\n\t"
      "v_mfma_f32_16x16x32_bf16 v[132:135], a[228:231], v[68:71], v[132:135]\n\t"
      "ds_read_b128 v[112:115], v198 offset:12800\n\t"
      "v_mfma_f32_16x16x32_bf16 v[132:135], a[252:255], v[72:75], v[132:135]\n\t"
      "v_mfma_f32_16x16x32_bf16 v[132:135], v[184:187], v[76:79], v[132:135]\n\t"
      "s_waitcnt lgkmcnt(2)\n\t"
      "buffer_store_dwordx4 v[144:147], v209, s[28:31], s72 offen offset:1024 sc1\n\t"
      "v_mfma_f32_16x16x32_bf16 v[128:131], a[16:19], v[32:35], v[24:27]\n\t"
      "ds_read_b128 v[116:119], v199 offset:12800\n\t"
      "v_mfma_f32_16x16x32_bf16 v[128:131], a[40:43], v[36:39], v[128:131]\n\t"
      "v_mfma_f32_16x16x32_bf16 v[128:131], a[64:67], v[40:43], v[128:131]\n\t"
      "v_cvt_pk_bf16_f32 v138, v132, v133\n\t"
      "v_mfma_f32_16x16x32_bf16 v[128:131], a[88:91], v[44:47], v[128:131]\n\t"
      "v_cvt_pk_bf16_f32 v139, v134, v135\n\t"
      "v_mfma_f32_16x16x32_bf16 v[128:131], a[112:115], v[48:51], v[128:131]\n\t"
      "ds_write_b128 v218, v[136:139] offset:0\n\t"
      "v_mfma_f32_16x16x32_bf16 v[128:131], a[136:139], v[52:55], v[128:131]\n\t"
      "v_mfma_f32_16x16x32_bf16 v[128:131], a[160:163], v[56:59], v[128:131]\n\t"
      "v_mfma_f32_16x16x32_bf16 v[128:131], a[184:187], v[60:63], v[128:131]\n\t"
      "ds_read_b128 v[148:151], v220 offset:14336\n\t"
      "v_mfma_f32_16x16x32_bf16 v[128:131], a[208:211], v[64:67], v[128:131]\n\t"
      "v_mfma_f32_16x16x32_bf16 v[128:131], a[232:235], v[68:71], v[128:131]\n\t"
      "ds_read_b128 v[120:123], v196 offset:12800\n\t"
      "v_mfma_f32_16x16x32_bf16 v[128:131], v[164:167], v[72:75], v[128:131]\n\t"
      "v_mfma_f32_16x16x32_bf16 v[128:131], v[188:191], v[76:79], v[128:131]\n\t"
      "buffer_load_dwordx4 v209, s[20:23], s73 offen offset:1024 lds\n\t"
      "v_mfma_f32_16x16x32_bf16 v[132:135], a[20:23], v[32:35], v[28:31]\n\t"
      "ds_read_b128 v[124:127], v197 offset:12800\n\t"
      "v_mfma_f32_16x16x32_bf16 v[132:135], a[44:47], v[36:39], v[132:135]\n\t"
      "v_mfma_f32_16x16x32_bf16 v[132:135], a[68:71], v[40:43], v[132:135]\n\t"
      "v_cvt_pk_bf16_f32 v136, v128, v129\n\t"
      "v_add_u32_e32 v220, s46, v207\n\t"
      "v_mfma_f32_16x16x32_bf16 v[132:135], a[92:95], v[44:47], v[132:135]\n\t"
      "v_cvt_pk_bf16_f32 v137, v130, v131\n\t"
      "s_sub_u32 s84, s84, 4\n\t"
      "s_max_i32 s84, s84, s86\n\t"
      "v_mfma_f32_16x16x32_bf16 v[132:135], a[116:119], v[48:51], v[132:135]\n\t"
      "s_sub_u32 s83, s82, s80\n\t"
      "v_mfma_f32_16x16x32_bf16 v[132:135], a[140:143], v[52:55], v[132:135]\n\t"
      "s_max_i32 s83, s83, 0\n\t"
      "s_add_u32 s82, s80, s83\n\t"
      "v_mfma_f32_16x16x32_bf16 v[132:135], a[164:167], v[56:59], v[132:135]\n\t"
      "v_mfma_f32_16x16x32_bf16 v[132:135], a[188:191], v[60:63], v[132:135]\n\t"
      "v_mfma_f32_16x16x32_bf16 v[132:135], a[212:215], v[64:67], v[132:135]\n\t"
      "s_add_u32 s44, s44, 1\n\t"
      "v_mfma_f32_16x16x32_bf16 v[132:135], a[236:239], v[68:71], v[132:135]\n\t"
      "s_add_u32 s82, s82, s84\n\t"
      "v_mfma_f32_16x16x32_bf16 v[132:135], v[168:171], v[72:75], v[132:135]\n\t"
      "s_add_u32 s85, s85, 1\n\t"
      "v_mfma_f32_16x16x32_bf16 v[132:135], v[192:195], v[76:79], v[132:135]\n\t"
      "s_waitcnt lgkmcnt(2)\n\t"
      "buffer_store_dwordx4 v[148:151], v209, s[28:31], s72 offen offset:2048 sc1\n\t"
      "s_waitcnt lgkmcnt(12)\n\t"
      "v_mfma_f32_16x16x32_bf16 v[128:131], a[0:3], v[80:83], v[8:11]\n\t"
      "v_mfma_f32_16x16x32_bf16 v[128:131], a[24:27], v[84:87], v[128:131]\n\t"
      "v_readlane_b32 s72, v248, s44\n\t"
      "v_mfma_f32_16x16x32_bf16 v[128:131], a[48:51], v[88:91], v[128:131]\n\t"
      "v_cvt_pk_bf16_f32 v138, v132, v133\n\t"
      "v_mfma_f32_16x16x32_bf16 v[128:131], a[72:75], v[92:95], v[128:131]\n\t"
      "v_cvt_pk_bf16_f32 v139, v134, v135\n\t"
      "s_waitcnt lgkmcnt(6)\n\t"
      "v_mfma_f32_16x16x32_bf16 v[128:131], a[96:99], v[96:99], v[128:131]\n\t"
      "ds_write_b128 v219, v[136:139] offset:0\n\t"
      "v_mfma_f32_16x16x32_bf16 v[128:131], a[120:123], v[100:103], v[128:131]\n\t"
      "v_mfma_f32_16x16x32_bf16 v[128:131], a[144:147], v[104:107], v[128:131]\n\t"
      "buffer_load_dwordx4 v209, s[20:23], s73 offen offset:2048 lds\n\t"
      "v_mfma_f32_16x16x32_bf16 v[128:131], a[168:171], v[108:111], v[128:131]\n\t"
      "v_add_u32_e32 v196, s47, v200\n\t"
      "s_waitcnt lgkmcnt(1)\n\t"
      "v_mfma_f32_16x16x32_bf16 v[128:131], a[192:195], v[112:115], v[128:131]\n\t"
      "v_add_u32_e32 v197, s47, v201\n\t"
      "v_mfma_f32_16x16x32_bf16 v[128:131], a[216:219], v[116:119], v[128:131]\n\t"
      "v_add_u32_e32 v198, s47, v202\n\t"
      "v_mfma_f32_16x16x32_bf16 v[128:131], a[240:243], v[120:123], v[128:131]\n\t"
      "v_add_u32_e32 v199, s47, v203\n\t"
      "v_mfma_f32_16x16x32_bf16 v[128:131], v[172:175], v[124:127], v[128:131]\n\t"
      "s_cmp_gt_i32 s83, 1\n\t"
      "s_cbranch_scc1 .LslpB_%=\n\t"
      ".LretB_%=:\n\t"
      "s_waitcnt vmcnt(18) lgkmcnt(0)\n\t"
      "s_barrier\n\t"
      "v_mfma_f32_16x16x32_bf16 v[132:135], a[4:7], v[80:83], v[12:15]\n\t"
      "ds_read_b128 v[140:143], v220 offset:0\n\t"
      "v_mfma_f32_16x16x32_bf16 v[132:135], a[28:31], v[84:87], v[132:135]\n\t"
      "v_mfma_f32_16x16x32_bf16 v[132:135], a[52:55], v[88:91], v[132:135]\n\t"
      "v_cvt_pk_bf16_f32 v136, v128, v129\n\t"
      "v_mfma_f32_16x16x32_bf16 v[132:135], a[76:79], v[92:95], v[132:135]\n\t"
      "ds_read_b128 v[32:35], v196 offset:0\n\t"
      "v_cvt_pk_bf16_f32 v137, v130, v131\n\t"
      "v_mfma_f32_16x16x32_bf16 v[132:135], a[100:103], v[96:99], v[132:135]\n\t"
      "v_mfma_f32_16x16x32_bf16 v[132:135], a[124:127], v[100:103], v[132:135]\n\t"
      "v_mfma_f32_16x16x32_bf16 v[132:135], a[148:151], v[104:107], v[132:135]\n\t"
      "ds_read_b128 v[36:39], v197 offset:0\n\t"
      "v_mfma_f32_16x16x32_bf16 v[132:135], a[172:175], v[108:111], v[132:135]\n\t"
      "v_readlane_b32 s73, v248, s85\n\t"
      "v_mfma_f32_16x16x32_bf16 v[132:135], a[196:199], v[112:115], v[132:135]\n\t"
      "v_mfma_f32_16x16x32_bf16 v[132:135], a[220:223], v[116:119], v[132:135]\n\t"
      "ds_read_b128 v[40:43], v198 offset:0\n\t"
      "v_mfma_f32_16x16x32_bf16 v[132:135], a[244:247], v[120:123], v[132:135]\n\t"
      "v_mfma_f32_16x16x32_bf16 v[132:135], v[176:179], v[124:127], v[132:135]\n\t"
      "s_waitcnt lgkmcnt(3)\n\t"
      "buffer_store_dwordx4 v[140:143], v208, s[28:31], s72 offen sc1\n\t"
      "v_mfma_f32_16x16x32_bf16 v[128:131], a[8:11], v[80:83], v[16:19]\n\t"
      "ds_read_b128 v[44:47], v199 offset:0\n\t"
      "v_mfma_f32_16x16x32_bf16 v[128:131], a[32:35], v[84:87], v[128:131]\n\t"
      "s_memrealtime s[80:81]\n\t"
      "v_mfma_f32_16x16x32_bf16 v[128:131], a[56:59], v[88:91], v[128:131]\n\t"
      "v_cvt_pk_bf16_f32 v138, v132, v133\n\t"
      "v_mfma_f32_16x16x32_bf16 v[128:131], a[80:83], v[92:95], v[128:131]\n\t"
      "v_cvt_pk_bf16_f32 v139, v134, v135\n\t"
      "v_mfma_f32_16x16x32_bf16 v[128:131], a[104:107], v[96:99], v[128:131]\n\t"
      "ds_write_b128 v217, v[136:139] offset:12288\n\t"
      "v_mfma_f32_16x16x32_bf16 v[128:131], a[128:131], v[100:103], v[128:131]\n\t"
      "s_mov_b32 m0, s46\n\t"
      "v_mfma_f32_16x16x32_bf16 v[128:131], a[152:155], v[104:107], v[128:131]\n\t"
      "v_mfma_f32_16x16x32_bf16 v[128:131], a[176:179], v[108:111], v[128:131]\n\t"
      "ds_read_b128 v[48:51], v197 offset:256\n\t"
      "v_mfma_f32_16x16x32_bf16 v[128:131], a[200:203], v[112:115], v[128:131]\n\t"
      "v_mfma_f32_16x16x32_bf16 v[128:131], a[224:227], v[116:119], v[128:131]\n\t"
      "ds_read_b128 v[52:55], v198 offset:256\n\t"
      "v_mfma_f32_16x16x32_bf16 v[128:131], a[248:251], v[120:123], v[128:131]\n\t"
      "v_mfma_f32_16x16x32_bf16 v[128:131], v[180:183], v[124:127], v[128:131]\n\t"
      "buffer_load_dwordx4 v208, s[20:23], s73 offen lds\n\t"
      "v_mfma_f32_16x16x32_bf16 v[132:135], a[12:15], v[80:83], v[20:23]\n\t"
      "ds_read_b128 v[56:59], v199 offset:256\n\t"
      "v_mfma_f32_16x16x32_bf16 v[132:135], a[36:39], v[84:87], v[132:135]\n\t"
      "v_mfma_f32_16x16x32_bf16 v[132:135], a[60:63], v[88:91], v[132:135]\n\t"
      "v_cvt_pk_bf16_f32 v136, v128, v129\n\t"
      "v_mfma_f32_16x16x32_bf16 v[132:135], a[84:87], v[92:95], v[132:135]\n\t"
      "v_cvt_pk_bf16_f32 v137, v130, v131\n\t"
      "ds_read_b128 v[144:147], v220 offset:1024\n\t"
      "v_mfma_f32_16x16x32_bf16 v[132:135], a[108:111], v[96:99], v[132:135]\n\t"
      "v_mfma_f32_16x16x32_bf16 v[132:135], a[132:135], v[100:103], v[132:135]\n\t"
      "v_mfma_f32_16x16x32_bf16 v[132:135], a[156:159], v[104:107], v[132:135]\n\t"
      "ds_read_b128 v[60:63], v196 offset:256\n\t"
      "v_mfma_f32_16x16x32_bf16 v[132:135], a[180:183], v[108:111], v[132:135]\n\t"
      "v_mfma_f32_16x16x32_bf16 v[132:135], a[204:207], v[112:115], v[132:135]\n\t"
      "v_mfma_f32_16x16x32_bf16 v[132:135], a[228:231], v[116:119], v[132:135]\n\t"
      "ds_read_b128 v[64:67], v198 offset:512\n\t"
      "v_mfma_f32_16x16x32_bf16 v[132:135], a[252:255], v[120:123], v[132:135]\n\t"
      "v_mfma_f32_16x16x32_bf16 v[132:135], v[184:187], v[124:127], v[132:135]\n\t"
      "s_waitcnt lgkmcnt(2)\n\t"
      "buffer_store_dwordx4 v[144:147], v208, s[28:31], s72 offen offset:1024 sc1\n\t"
      "v_mfma_f32_16x16x32_bf16 v[128:131], a[16:19], v[80:83], v[24:27]\n\t"
      "ds_read_b128 v[68:71], v199 offset:512\n\t"
      "v_mfma_f32_16x16x32_bf16 v[128:131], a[40:43], v[84:87], v[128:131]\n\t"
      "v_readlane_b32 s86, v250, s44\n\t"
      "v_mfma_f32_16x16x32_bf16 v[128:131], a[64:67], v[88:91], v[128:131]\n\t"
      "v_cvt_pk_bf16_f32 v138, v132, v133\n\t"
      "v_mfma_f32_16x16x32_bf16 v[128:131], a[88:91], v[92:95], v[128:131]\n\t"
      "v_cvt_pk_bf16_f32 v139, v134, v135\n\t"
      "v_mfma_f32_16x16x32_bf16 v[128:131], a[112:115], v[96:99], v[128:131]\n\t"
      "ds_write_b128 v218, v[136:139] offset:12288\n\t"
      "v_mfma_f32_16x16x32_bf16 v[128:131], a[136:139], v[100:103], v[128:131]\n\t"
      "v_mfma_f32_16x16x32_bf16 v[128:131], a[160:163], v[104:107], v[128:131]\n\t"
      "v_mfma_f32_16x16x32_bf16 v[128:131], a[184:187], v[108:111], v[128:131]\n\t"
      "ds_read_b128 v[148:151], v220 offset:2048\n\t"
      "v_mfma_f32_16x16x32_bf16 v[128:131], a[208:211], v[112:115], v[128:131]\n\t"
      "v_mfma_f32_16x16x32_bf16 v[128:131], a[232:235], v[116:119], v[128:131]\n\t"
      "ds_read_b128 v[72:75], v196 offset:512\n\t"
      "v_mfma_f32_16x16x32_bf16 v[128:131], v[164:167], v[120:123], v[128:131]\n\t"
      "v_mfma_f32_16x16x32_bf16 v[128:131], v[188:191], v[124:127], v[128:131]\n\t"
      "buffer_load_dwordx4 v208, s[20:23], s73 offen offset:1024 lds\n\t"
      "v_mfma_f32_16x16x32_bf16 v[132:135], a[20:23], v[80:83], v[28:31]\n\t"
      "ds_read_b128 v[76:79], v197 offset:512\n\t"
      "v_mfma_f32_16x16x32_bf16 v[132:135], a[44:47], v[84:87], v[132:135]\n\t"
      "v_mfma_f32_16x16x32_bf16 v[132:135], a[68:71], v[88:91], v[132:135]\n\t"
      "v_cvt_pk_bf16_f32 v136, v128, v129\n\t"
      "v_mfma_f32_16x16x32_bf16 v[132:135], a[92:95], v[92:95], v[132:135]\n\t"
      "v_cvt_pk_bf16_f32 v137, v130, v131\n\t"
      "s_sub_u32 s84, s84, 4\n\t"
      "s_max_i32 s84, s84, s86\n\t"
      "v_mfma_f32_16x16x32_bf16 v[132:135], a[116:119], v[96:99], v[132:135]\n\t"
      "s_sub_u32 s83, s82, s80\n\t"
      "v_mfma_f32_16x16x32_bf16 v[132:135], a[140:143], v[100:103], v[132:135]\n\t"
      "s_max_i32 s83, s83, 0\n\t"
      "s_add_u32 s82, s80, s83\n\t"
      "v_mfma_f32_16x16x32_bf16 v[132:135], a[164:167], v[104:107], v[132:135]\n\t"
      "s_mov_b32 s45, s46\n\t"
      "v_mfma_f32_16x16x32_bf16 v[132:135], a[188:191], v[108:111], v[132:135]\n\t"
      "s_mov_b32 s46, s47\n\t"
      "v_mfma_f32_16x16x32_bf16 v[132:135], a[212:215], v[112:115], v[132:135]\n\t"
      "s_mov_b32 s47, s48\n\t"
      "v_mfma_f32_16x16x32_bf16 v[132:135], a[236:239], v[116:119], v[132:135]\n\t"
      "s_add_u32 s82, s82, s84\n\t"
      "v_mfma_f32_16x16x32_bf16 v[132:135], v[168:171], v[120:123], v[132:135]\n\t"
      "s_cmp_lg_u32 s44, s49\n\t"
      "v_mfma_f32_16x16x32_bf16 v[132:135], v[192:195], v[124:127], v[132:135]\n\t"
      "s_waitcnt lgkmcnt(2)\n\t"
      "buffer_store_dwordx4 v[148:151], v208, s[28:31], s72 offen offset:2048 sc1\n\t"
      "s_cbranch_scc1 .Lloop_%=\n\t"
      "s_cmp_lg_u32 s44, 17\n\t"
      "s_cbranch_scc1 .Lstop_%=\n\t"
      "v_readlane_b32 s72, v249, s44\n\t"
      "s_nop 3\n\t"
      "v_cvt_pk_bf16_f32 v138, v132, v133\n\t"
      "v_cvt_pk_bf16_f32 v139, v134, v135\n\t"
      "ds_write_b128 v219, v[136:139] offset:12288\n\t"
      "s_waitcnt lgkmcnt(0)\n\t"
      "s_barrier\n\t"
      "ds_read_b128 v[32:35], v220 offset:12288\n\t"
      "ds_read_b128 v[36:39], v220 offset:13312\n\t"
      "ds_read_b128 v[40:43], v220 offset:14336\n\t"
      "s_waitcnt lgkmcnt(2)\n\t"
      "buffer_store_dwordx4 v[32:35], v209, s[28:31], s72 offen sc1\n\t"
      "s_waitcnt lgkmcnt(1)\n\t"
      "buffer_store_dwordx4 v[36:39], v209, s[28:31], s72 offen offset:1024 sc1\n\t"
      "s_waitcnt lgkmcnt(0)\n\t"
      "buffer_store_dwordx4 v[40:43], v209, s[28:31], s72 offen offset:2048 sc1\n\t"
      "v_readlane_b32 s76, v254, 1\n\t"
      "v_readlane_b32 s78, v255, 1\n\t"
      "s_cmp_eq_u32 s41, s43\n\t"
      "s_cbranch_scc0 .Lc5_%=\n\t"
      "s_andn2_b32 s76, 4, s76\n\t"
      "s_or_b32 s76, s76, s64\n\t"
      "s_xor_b32 s76, s76, s78\n\t"
      "v_mov_b32_e32 v240, s76\n\t"
      "ds_write_b32 v247, v240\n\t"
      ".Lc5_%=:\n\t"
      "s_waitcnt lgkmcnt(0)\n\t"
      "s_barrier\n\t"
      "ds_read_b32 v241, v247\n\t"
      "s_waitcnt lgkmcnt(0)\n\t"
      "v_cmp_ne_u32_e32 vcc, 0, v241\n\t"
      "s_cmp_lg_u32 s62, 0\n\t"
      "s_cbranch_scc1 .Lfin_%=\n\t"
      "s_cbranch_vccnz .Lsec_%=\n\t"
      ".Lfin_%=:\n\t"
      "s_endpgm\n\t"
      ".Lstop_%=:\n\t"
      "s_mov_b32 s70, 0\n\t"
      "s_add_u32 s49, s89, 10\n\t"
      "s_cmp_lg_u32 s44, s49\n\t"
      "s_cselect_b32 s49, s49, 17\n\t"
      "s_cbranch_scc0 .Lstop2_%=\n\t"
      "s_cmp_eq_u32 s94, 0\n\t"
      "s_cbranch_scc1 .Llt0_%=\n\t"
      "ds_read_b128 v[226:229], v238 offset:24576\n\t"
      "ds_read_b128 v[230:233], v238 offset:25600\n\t"
      "ds_read_b128 v[234:237], v238 offset:26624\n\t"
      "v_readlane_b32 s68, v248, 0\n\t"
      "s_nop 4\n\t"
      "s_waitcnt lgkmcnt(2)\n\t"
      "buffer_store_dwordx4 v[226:229], v208, s[28:31], s68 offen sc1\n\t"
      "s_waitcnt lgkmcnt(1)\n\t"
      "buffer_store_dwordx4 v[230:233], v208, s[28:31], s68 offen offset:1024 sc1\n\t"
      "s_waitcnt lgkmcnt(0)\n\t"
      "buffer_store_dwordx4 v[234:237], v208, s[28:31], s68 offen offset:2048 sc1\n\t"
      ".Llt0_%=:\n\t"
      "v_readlane_b32 s76, v254, 0\n\t"
      "s_cmp_lg_u32 s41, s43\n\t"
      "s_cbranch_scc1 .Lloop_%=\n\t"
      "s_andn2_b32 s76, 4, s76\n\t"
      "s_or_b32 s76, s76, s64\n\t"
      "v_mov_b32_e32 v240, s76\n\t"
      "s_mov_b64 exec, 1\n\t"
      "buffer_store_dword v240, v253, s[24:27], 0 offen sc1\n\t"
      "s_mov_b64 exec, -1\n\t"
      "s_branch .Lloop_%=\n\t"
      ".Lstop2_%=:\n\t"
      "s_cmp_eq_u32 s94, 0\n\t"
      "s_cbranch_scc1 .Llt1_%=\n\t"
      "ds_read_b128 v[226:229], v238 offset:36864\n\t"
      "ds_read_b128 v[230:233], v238 offset:37888\n\t"
      "ds_read_b128 v[234:237], v238 offset:38912\n\t"
      "v_readlane_b32 s68, v249, 0\n\t"
      "s_nop 4\n\t"
      "s_waitcnt lgkmcnt(2)\n\t"
      "buffer_store_dwordx4 v[226:229], v209, s[28:31], s68 offen sc1\n\t"
      "s_waitcnt lgkmcnt(1)\n\t"
      "buffer_store_dwordx4 v[230:233], v209, s[28:31], s68 offen offset:1024 sc1\n\t"
      "s_waitcnt lgkmcnt(0)\n\t"
      "buffer_store_dwordx4 v[234:237], v209, s[28:31], s68 offen offset:2048 sc1\n\t"
      ".Llt1_%=:\n\t"
      "s_cmp_lg_u32 s41, s43\n\t"
      "s_cbranch_scc1 .Lloop_%=\n\t"
      "buffer_load_dword v255, v253, s[24:27], 0 offen sc1\n\t"
      "s_branch .Lloop_%=\n\t"
      ".LslpP3_%=:\n\t"
      "s_sleep 1\n\t"
      "s_sub_u32 s83, s83, 3\n\t"
      "s_cmp_gt_i32 s83, 1\n\t"
      "s_cbranch_scc1 .LslpP3_%=\n\t"
      "s_branch .LretP3_%=\n\t"
      ".LslpP4_%=:\n\t"
      "s_sleep 1\n\t"
      "s_sub_u32 s83, s83, 3\n\t"
      "s_cmp_gt_i32 s83, 1\n\t"
      "s_cbranch_scc1 .LslpP4_%=\n\t"
      "s_branch .LretP4_%=\n\t"
      ".LslpP5_%=:\n\t"
      "s_sleep 1\n\t"
      "s_sub_u32 s83, s83, 3\n\t"
      "s_cmp_gt_i32 s83, 1\n\t"
      "s_cbranch_scc1 .LslpP5_%=\n\t"
      "s_branch .LretP5_%=\n\t"
      ".LslpA_%=:\n\t"
      "s_sleep 1\n\t"
      "s_sub_u32 s83, s83, 3\n\t"
      "s_cmp_gt_i32 s83, 1\n\t"
      "s_cbranch_scc1 .LslpA_%=\n\t"
      "s_branch .LretA_%=\n\t"
      ".LslpB_%=:\n\t"
      "s_sleep 1\n\t"
      "s_sub_u32 s83, s83, 3\n\t"
      "s_cmp_gt_i32 s83, 1\n\t"
      "s_cbranch_scc1 .LslpB_%=\n\t"
      "s_branch .LretB_%=\n\t"
      ".Lsec_%=:\n\t"
      "s_mov_b32 s62, 1\n\t"
      "v_lshl_add_u32 v240, v251, 1, s66\n\t"
      "v_sub_u32_e32 v248, v240, v248\n\t"
      "v_sub_u32_e32 v249, v240, v249\n\t"
      "v_cmp_eq_u32_e32 vcc, 0, v252\n\t"
      "v_mov_b32_e32 v240, 0x6c00000\n\t"
      "s_nop 1\n\t"
      "v_cndmask_b32_e32 v248, v248, v240, vcc\n\t"
      "v_cndmask_b32_e32 v249, v249, v240, vcc\n\t"
      "s_add_u32 s44, s94, 3\n\t"
      "s_add_u32 s85, s44, 1\n\t"
      "s_mov_b32 s83, 0\n\t"
      "s_waitcnt vmcnt(0) lgkmcnt(0)\n\t"
      "s_barrier\n\t"
      "v_readlane_b32 s73, v248, s85\n\t"
      "s_mov_b32 m0, s46\n\t"
      "s_nop 4\n\t"
      "buffer_load_dwordx4 v208, s[20:23], s73 offen lds\n\t"
      "buffer_load_dwordx4 v208, s[20:23], s73 offen offset:1024 lds\n\t"
      "buffer_load_dwordx4 v208, s[20:23], s73 offen offset:2048 lds\n\t"
      "v_readlane_b32 s73, v249, s85\n\t"
      "s_add_u32 m0, s46, 0x3000\n\t"
      "s_nop 4\n\t"
      "buffer_load_dwordx4 v209, s[20:23], s73 offen lds\n\t"
      "buffer_load_dwordx4 v209, s[20:23], s73 offen offset:1024 lds\n\t"
      "buffer_load_dwordx4 v209, s[20:23], s73 offen offset:2048 lds\n\t"
      "s_add_u32 s85, s85, 1\n\t"
      "s_nop 4\n\t"
      "v_readlane_b32 s73, v248, s85\n\t"
      "s_mov_b32 m0, s47\n\t"
      "s_nop 4\n\t"
      "buffer_load_dwordx4 v208, s[20:23], s73 offen lds\n\t"
      "buffer_load_dwordx4 v208, s[20:23], s73 offen offset:1024 lds\n\t"
      "buffer_load_dwordx4 v208, s[20:23], s73 offen offset:2048 lds\n\t"
      "v_readlane_b32 s73, v249, s85\n\t"
      "s_add_u32 m0, s47, 0x3000\n\t"
      "s_nop 4\n\t"
      "buffer_load_dwordx4 v209, s[20:23], s73 offen lds\n\t"
      "buffer_load_dwordx4 v209, s[20:23], s73 offen offset:1024 lds\n\t"
      "buffer_load_dwordx4 v209, s[20:23], s73 offen offset:2048 lds\n\t"
      "s_add_u32 s85, s85, 1\n\t"
      "s_nop 4\n\t"
      "v_readlane_b32 s73, v248, s85\n\t"
      "s_mov_b32 m0, s45\n\t"
      "s_nop 4\n\t"
      "buffer_load_dwordx4 v208, s[20:23], s73 offen lds\n\t"
      "buffer_load_dwordx4 v208, s[20:23], s73 offen offset:1024 lds\n\t"
      "buffer_load_dwordx4 v208, s[20:23], s73 offen offset:2048 lds\n\t"
      "s_waitcnt vmcnt(0)\n\t"
      "s_barrier\n\t"
      "ds_read_b128 v[32:35], v196 offset:0\n\t"
      "ds_read_b128 v[36:39], v197 offset:0\n\t"
      "ds_read_b128 v[40:43], v198 offset:0\n\t"
      "ds_read_b128 v[44:47], v199 offset:0\n\t"
      "ds_read_b128 v[48:51], v197 offset:256\n\t"
      "ds_read_b128 v[52:55], v198 offset:256\n\t"
      "ds_read_b128 v[56:59], v199 offset:256\n\t"
      "ds_read_b128 v[60:63], v196 offset:256\n\t"
      "ds_read_b128 v[64:67], v198 offset:512\n\t"
      "ds_read_b128 v[68:71], v199 offset:512\n\t"
      "ds_read_b128 v[72:75], v196 offset:512\n\t"
      "ds_read_b128 v[76:79], v197 offset:512\n\t"
      "s_waitcnt lgkmcnt(0)\n\t"
      "s_branch .Lloop_%=\n\t"
      ".Lgs_%=:\n\t"
      "s_sleep 8\n\t"
      "buffer_load_dwordx2 v[240:241], v245, s[24:27], 0 offen sc1\n\t"
      "s_memrealtime s[76:77]\n\t"
      "s_waitcnt vmcnt(0) lgkmcnt(0)\n\t"
      "v_subrev_u32_e32 v242, s63, v240\n\t"
      "v_xor_b32_e32 v241, s53, v241\n\t"
      "v_lshrrev_b32_e32 v240, 13, v242\n\t"
      "v_or_b32_e32 v240, v240, v241\n\t"
      "v_cmp_ne_u32_e32 vcc, 0, v240\n\t"
      "v_and_b32_e32 v242, 15, v242\n\t"
      "s_cbranch_vccz .Lgr_%=\n\t"
      "s_sub_u32 s76, s76, s52\n\t"
      "s_cmp_lt_u32 s76, 2000\n\t"
      "s_cbranch_scc1 .Lgs_%=\n\t"
      "v_mov_b32_e32 v242, 64\n\t"
      "s_branch .Lgr_%=\n\t"
      ".Lpb_%=:\n\t"
      "s_mov_b64 vcc, -1\n\t"
      "s_branch .Lp2_%=\n\t"
      ".Lwsb_%=:\n\t"
      "s_mov_b64 vcc, 0\n\t"
      "s_waitcnt vmcnt(12)\n\t"
      "v_lshlrev_b32_e32 v8, 16, v226\n\t"
      "v_and_b32_e32 v9, 0xffff0000, v226\n\t"
      "v_lshlrev_b32_e32 v10, 16, v227\n\t"
      "v_and_b32_e32 v11, 0xffff0000, v227\n\t"
      "v_lshlrev_b32_e32 v12, 16, v228\n\t"
      "v_and_b32_e32 v13, 0xffff0000, v228\n\t"
      "v_lshlrev_b32_e32 v14, 16, v229\n\t"
      "v_and_b32_e32 v15, 0xffff0000, v229\n\t"
      "v_lshlrev_b32_e32 v16, 16, v230\n\t"
      "v_and_b32_e32 v17, 0xffff0000, v230\n\t"
      "v_lshlrev_b32_e32 v18, 16, v231\n\t"
      "v_and_b32_e32 v19, 0xffff0000, v231\n\t"
      "v_lshlrev_b32_e32 v20, 16, v232\n\t"
      "v_and_b32_e32 v21, 0xffff0000, v232\n\t"
      "v_lshlrev_b32_e32 v22, 16, v233\n\t"
      "v_and_b32_e32 v23, 0xffff0000, v233\n\t"
      "v_lshlrev_b32_e32 v24, 16, v234\n\t"
      "v_and_b32_e32 v25, 0xffff0000, v234\n\t"
      "v_lshlrev_b32_e32 v26, 16, v235\n\t"
      "v_and_b32_e32 v27, 0xffff0000, v235\n\t"
      "v_lshlrev_b32_e32 v28, 16, v236\n\t"
      "v_and_b32_e32 v29, 0xffff0000, v236\n\t"
      "v_lshlrev_b32_e32 v30, 16, v237\n\t"
      "v_and_b32_e32 v31, 0xffff0000, v237\n\t"
      "s_add_u32 m0, s41, 0x1e000\n\t"
      "s_mov_b32 s51, 0x0\n\t"
      "buffer_load_dwordx4 v211, s[16:19], s51 offen lds\n\t"
      "buffer_load_dwordx4 v212, s[16:19], s51 offen offset:1024 lds\n\t"
      "buffer_load_dwordx4 v213, s[16:19], s51 offen offset:2048 lds\n\t"
      "s_add_u32 m0, s41, 0x21000\n\t"
      "s_mov_b32 s51, 0xc00\n\t"
      "buffer_load_dwordx4 v211, s[16:19], s51 offen lds\n\t"
      "buffer_load_dwordx4 v212, s[16:19], s51 offen offset:1024 lds\n\t"
      "buffer_load_dwordx4 v213, s[16:19], s51 offen offset:2048 lds\n\t"
      "s_add_u32 m0, s41, 0x24000\n\t"
      "s_mov_b32 s51, 0x6000\n\t"
      "buffer_load_dwordx4 v211, s[16:19], s51 offen lds\n\t"
      "buffer_load_dwordx4 v212, s[16:19], s51 offen offset:1024 lds\n\t"
      "buffer_load_dwordx4 v213, s[16:19], s51 offen offset:2048 lds\n\t"
      "s_add_u32 m0, s41, 0x12000\n\t"
      "s_mov_b32 s51, 0x6c00\n\t"
      "buffer_load_dwordx4 v211, s[16:19], s51 offen lds\n\t"
      "buffer_load_dwordx4 v212, s[16:19], s51 offen offset:1024 lds\n\t"
      "buffer_load_dwordx4 v213, s[16:19], s51 offen offset:2048 lds\n\t"
      "s_add_u32 m0, s41, 0x15000\n\t"
      "s_mov_b32 s51, 0xc000\n\t"
      "buffer_load_dwordx4 v211, s[16:19], s51 offen lds\n\t"
      "buffer_load_dwordx4 v212, s[16:19], s51 offen offset:1024 lds\n\t"
      "buffer_load_dwordx4 v213, s[16:19], s51 offen offset:2048 lds\n\t"
      "s_mov_b32 s44, 1\n\t"
      "s_mov_b32 s85, 4\n\t"
      "s_add_u32 s45, s41, 0x6000\n\t"
      "s_add_u32 s46, s41, 0xc000\n\t"
      "s_mov_b32 s47, s41\n\t"
      "s_waitcnt vmcnt(12)\n\t"
      "s_barrier\n\t"
      "ds_read_b64_tr_b16 a[0:1], v214 offset:49152\n\t"
      "ds_read_b64_tr_b16 a[4:5], v214 offset:49160\n\t"
      "ds_read_b64_tr_b16 a[8:9], v214 offset:49216\n\t"
      "ds_read_b64_tr_b16 a[12:13], v214 offset:49224\n\t"
      "ds_read_b64_tr_b16 a[16:17], v214 offset:49280\n\t"
      "ds_read_b64_tr_b16 a[20:21], v214 offset:49288\n\t"
      "ds_read_b128 v[156:159], v196 offset:0\n\t"
      "ds_read_b128 v[160:163], v196 offset:12288\n\t"
      "ds_read_b128 v[128:131], v196 offset:24576\n\t"
      "s_add_u32 m0, s41, 0x18000\n\t"
      "s_mov_b32 s51, 0xcc00\n\t"
      "buffer_load_dwordx4 v211, s[16:19], s51 offen lds\n\t"
      "buffer_load_dwordx4 v212, s[16:19], s51 offen offset:1024 lds\n\t"
      "buffer_load_dwordx4 v213, s[16:19], s51 offen offset:2048 lds\n\t"
      "s_waitcnt vmcnt(12)\n\t"
      "ds_read_b64_tr_b16 a[2:3], v221 offset:0\n\t"
      "ds_read_b64_tr_b16 a[6:7], v221 offset:8\n\t"
      "ds_read_b64_tr_b16 a[10:11], v221 offset:64\n\t"
      "ds_read_b64_tr_b16 a[14:15], v221 offset:72\n\t"
      "ds_read_b64_tr_b16 a[18:19], v221 offset:128\n\t"
      "ds_read_b64_tr_b16 a[22:23], v221 offset:136\n\t"
      "s_add_u32 m0, s41, 0x1b000\n\t"
      "s_mov_b32 s51, 0x12000\n\t"
      "buffer_load_dwordx4 v211, s[16:19], s51 offen lds\n\t"
      "buffer_load_dwordx4 v212, s[16:19], s51 offen offset:1024 lds\n\t"
      "buffer_load_dwordx4 v213, s[16:19], s51 offen offset:2048 lds\n\t"
      "s_waitcnt lgkmcnt(0)\n\t"
      "v_mfma_f32_16x16x32_bf16 v[32:35], a[0:3], v[156:159], v[8:11]\n\t"
      "v_mfma_f32_16x16x32_bf16 v[36:39], a[4:7], v[156:159], v[12:15]\n\t"
      "s_waitcnt vmcnt(12)\n\t"
      "ds_read_b64_tr_b16 a[24:25], v221 offset:12288\n\t"
      "v_mfma_f32_16x16x32_bf16 v[40:43], a[8:11], v[156:159], v[16:19]\n\t"
      "ds_read_b64_tr_b16 a[28:29], v221 offset:12296\n\t"
      "v_mfma_f32_16x16x32_bf16 v[44:47], a[12:15], v[156:159], v[20:23]\n\t"
      "ds_read_b64_tr_b16 a[32:33], v221 offset:12352\n\t"
      "v_mfma_f32_16x16x32_bf16 v[48:51], a[16:19], v[156:159], v[24:27]\n\t"
      "ds_read_b64_tr_b16 a[36:37], v221 offset:12360\n\t"
      "ds_read_b128 v[136:139], v196 offset:36864\n\t"
      "v_mfma_f32_16x16x32_bf16 v[52:55], a[20:23], v[156:159], v[28:31]\n\t"
      "ds_read_b64_tr_b16 a[40:41], v221 offset:12416\n\t"
      "v_mfma_f32_16x16x32_bf16 v[56:59], a[0:3], v[160:163], v[8:11]\n\t"
      "ds_read_b64_tr_b16 a[44:45], v221 offset:12424\n\t"
      "v_mfma_f32_16x16x32_bf16 v[60:63], a[4:7], v[160:163], v[12:15]\n\t"
      "v_mfma_f32_16x16x32_bf16 v[64:67], a[8:11], v[160:163], v[16:19]\n\t"
      "s_waitcnt vmcnt(9)\n\t"
      "ds_read_b64_tr_b16 a[26:27], v214 offset:0\n\t"
      "v_mfma_f32_16x16x32_bf16 v[68:71], a[12:15], v[160:163], v[20:23]\n\t"
      "ds_read_b64_tr_b16 a[30:31], v214 offset:8\n\t"
      "v_mfma_f32_16x16x32_bf16 v[72:75], a[16:19], v[160:163], v[24:27]\n\t"
      "ds_read_b64_tr_b16 a[34:35], v214 offset:64\n\t"
      "ds_read_b128 v[140:143], v197 offset:0\n\t"
      "v_mfma_f32_16x16x32_bf16 v[76:79], a[20:23], v[160:163], v[28:31]\n\t"
      "ds_read_b64_tr_b16 a[38:39], v214 offset:72\n\t"
      "v_mfma_f32_16x16x32_bf16 v[80:83], a[0:3], v[128:131], v[8:11]\n\t"
      "ds_read_b64_tr_b16 a[42:43], v214 offset:128\n\t"
      "s_add_u32 m0, s41, 0x1e000\n\t"
      "s_mov_b32 s51, 0x12c00\n\t"
      "buffer_load_dwordx4 v211, s[16:19], s51 offen lds\n\t"
      "v_mfma_f32_16x16x32_bf16 v[84:87], a[4:7], v[128:131], v[12:15]\n\t"
      "ds_read_b64_tr_b16 a[46:47], v214 offset:136\n\t"
      "v_mfma_f32_16x16x32_bf16 v[88:91], a[8:11], v[128:131], v[16:19]\n\t"
      "v_mfma_f32_16x16x32_bf16 v[92:95], a[12:15], v[128:131], v[20:23]\n\t"
      "buffer_load_dwordx4 v212, s[16:19], s51 offen offset:1024 lds\n\t"
      "v_mfma_f32_16x16x32_bf16 v[96:99], a[16:19], v[128:131], v[24:27]\n\t"
      "ds_read_b128 v[144:147], v197 offset:12288\n\t"
      "v_mfma_f32_16x16x32_bf16 v[100:103], a[20:23], v[128:131], v[28:31]\n\t"
      "s_waitcnt lgkmcnt(10)\n\t"
      "v_mfma_f32_16x16x32_bf16 v[104:107], a[0:3], v[136:139], v[8:11]\n\t"
      "buffer_load_dwordx4 v213, s[16:19], s51 offen offset:2048 lds\n\t"
      "v_mfma_f32_16x16x32_bf16 v[108:111], a[4:7], v[136:139], v[12:15]\n\t"
      "v_mfma_f32_16x16x32_bf16 v[112:115], a[8:11], v[136:139], v[16:19]\n\t"
      "s_waitcnt vmcnt(9)\n\t"
      "ds_read_b64_tr_b16 a[48:49], v214 offset:12288\n\t"
      "v_mfma_f32_16x16x32_bf16 v[116:119], a[12:15], v[136:139], v[20:23]\n\t"
      "ds_read_b64_tr_b16 a[52:53], v214 offset:12296\n\t"
      "s_add_u32 m0, s41, 0x21000\n\t"
      "s_mov_b32 s51, 0x18000\n\t"
      "buffer_load_dwordx4 v211, s[16:19], s51 offen lds\n\t"
      "v_mfma_f32_16x16x32_bf16 v[120:123], a[16:19], v[136:139], v[24:27]\n\t"
      "ds_read_b64_tr_b16 a[56:57], v214 offset:12352\n\t"
      "ds_read_b128 v[148:151], v197 offset:24576\n\t"
      "v_mfma_f32_16x16x32_bf16 v[132:135], a[20:23], v[136:139], v[28:31]\n\t"
      "ds_read_b64_tr_b16 a[60:61], v214 offset:12360\n\t"
      "s_waitcnt lgkmcnt(6)\n\t"
      "v_mfma_f32_16x16x32_bf16 v[32:35], a[24:27], v[140:143], v[32:35]\n\t"
      "ds_read_b64_tr_b16 a[64:65], v214 offset:12416\n\t"
      "buffer_load_dwordx4 v212, s[16:19], s51 offen offset:1024 lds\n\t"
      "v_mfma_f32_16x16x32_bf16 v[36:39], a[28:31], v[140:143], v[36:39]\n\t"
      "ds_read_b64_tr_b16 a[68:69], v214 offset:12424\n\t"
      "v_mfma_f32_16x16x32_bf16 v[40:43], a[32:35], v[140:143], v[40:43]\n\t"
      "v_mfma_f32_16x16x32_bf16 v[44:47], a[36:39], v[140:143], v[44:47]\n\t"
      "buffer_load_dwordx4 v213, s[16:19], s51 offen offset:2048 lds\n\t"
      "v_mfma_f32_16x16x32_bf16 v[48:51], a[40:43], v[140:143], v[48:51]\n\t"
      "ds_read_b128 v[152:155], v197 offset:36864\n\t"
      "v_mfma_f32_16x16x32_bf16 v[52:55], a[44:47], v[140:143], v[52:55]\n\t"
      "s_waitcnt lgkmcnt(8)\n\t"
      "v_mfma_f32_16x16x32_bf16 v[56:59], a[24:27], v[144:147], v[56:59]\n\t"
      "s_add_u32 m0, s41, 0x24000\n\t"
      "s_mov_b32 s51, 0x18c00\n\t"
      "buffer_load_dwordx4 v211, s[16:19], s51 offen lds\n\t"
      "v_mfma_f32_16x16x32_bf16 v[60:63], a[28:31], v[144:147], v[60:63]\n\t"
      "v_mfma_f32_16x16x32_bf16 v[64:67], a[32:35], v[144:147], v[64:67]\n\t"
      "s_waitcnt vmcnt(10)\n\t"
      "ds_read_b64_tr_b16 a[50:51], v214 offset:24576\n\t"
      "v_mfma_f32_16x16x32_bf16 v[68:71], a[36:39], v[144:147], v[68:71]\n\t"
      "ds_read_b64_tr_b16 a[54:55], v214 offset:24584\n\t"
      "buffer_load_dwordx4 v212, s[16:19], s51 offen offset:1024 lds\n\t"
      "v_mfma_f32_16x16x32_bf16 v[72:75], a[40:43], v[144:147], v[72:75]\n\t"
      "ds_read_b64_tr_b16 a[58:59], v214 offset:24640\n\t"
      "ds_read_b128 v[156:159], v198 offset:0\n\t"
      "v_mfma_f32_16x16x32_bf16 v[76:79], a[44:47], v[144:147], v[76:79]\n\t"
      "ds_read_b64_tr_b16 a[62:63], v214 offset:24648\n\t"
      "s_waitcnt lgkmcnt(9)\n\t"
      "v_mfma_f32_16x16x32_bf16 v[80:83], a[24:27], v[148:151], v[80:83]\n\t"
      "ds_read_b64_tr_b16 a[66:67], v214 offset:24704\n\t"
      "buffer_load_dwordx4 v213, s[16:19], s51 offen offset:2048 lds\n\t"
      "v_mfma_f32_16x16x32_bf16 v[84:87], a[28:31], v[148:151], v[84:87]\n\t"
      "ds_read_b64_tr_b16 a[70:71], v214 offset:24712\n\t"
      "v_mfma_f32_16x16x32_bf16 v[88:91], a[32:35], v[148:151], v[88:91]\n\t"
      "v_mfma_f32_16x16x32_bf16 v[92:95], a[36:39], v[148:151], v[92:95]\n\t"
      "s_add_u32 m0, s41, 0x12000\n\t"
      "s_mov_b32 s51, 0x1e000\n\t"
      "buffer_load_dwordx4 v211, s[16:19], s51 offen lds\n\t"
      "v_mfma_f32_16x16x32_bf16 v[96:99], a[40:43], v[148:151], v[96:99]\n\t"
      "ds_read_b128 v[160:163], v198 offset:12288\n\t"
      "v_mfma_f32_16x16x32_bf16 v[100:103], a[44:47], v[148:151], v[100:103]\n\t"
      "s_waitcnt lgkmcnt(8)\n\t"
      "v_mfma_f32_16x16x32_bf16 v[104:107], a[24:27], v[152:155], v[104:107]\n\t"
      "buffer_load_dwordx4 v212, s[16:19], s51 offen offset:1024 lds\n\t"
      "v_mfma_f32_16x16x32_bf16 v[108:111], a[28:31], v[152:155], v[108:111]\n\t"
      "v_mfma_f32_16x16x32_bf16 v[112:115], a[32:35], v[152:155], v[112:115]\n\t"
      "s_waitcnt vmcnt(11)\n\t"
      "ds_read_b64_tr_b16 a[72:73], v214 offset:36864\n\t"
      "v_mfma_f32_16x16x32_bf16 v[116:119], a[36:39], v[152:155], v[116:119]\n\t"
      "ds_read_b64_tr_b16 a[76:77], v214 offset:36872\n\t"
      "buffer_load_dwordx4 v213, s[16:19], s51 offen offset:2048 lds\n\t"
      "v_mfma_f32_16x16x32_bf16 v[120:123], a[40:43], v[152:155], v[120:123]\n\t"
      "ds_read_b64_tr_b16 a[80:81], v214 offset:36928\n\t"
      "ds_read_b128 v[128:131], v198 offset:24576\n\t"
      "v_mfma_f32_16x16x32_bf16 v[132:135], a[44:47], v[152:155], v[132:135]\n\t"
      "ds_read_b64_tr_b16 a[84:85], v214 offset:36936\n\t"
      "s_waitcnt lgkmcnt(6)\n\t"
      "v_mfma_f32_16x16x32_bf16 v[32:35], a[48:51], v[156:159], v[32:35]\n\t"
      "ds_read_b64_tr_b16 a[88:89], v214 offset:36992\n\t"
      "s_add_u32 m0, s41, 0x15000\n\t"
      "s_mov_b32 s51, 0x1ec00\n\t"
      "buffer_load_dwordx4 v211, s[16:19], s51 offen lds\n\t"
      "v_mfma_f32_16x16x32_bf16 v[36:39], a[52:55], v[156:159], v[36:39]\n\t"
      "ds_read_b64_tr_b16 a[92:93], v214 offset:37000\n\t"
      "v_mfma_f32_16x16x32_bf16 v[40:43], a[56:59], v[156:159], v[40:43]\n\t"
      "v_mfma_f32_16x16x32_bf16 v[44:47], a[60:63], v[156:159], v[44:47]\n\t"
      "buffer_load_dwordx4 v212, s[16:19], s51 offen offset:1024 lds\n\t"
      "v_mfma_f32_16x16x32_bf16 v[48:51], a[64:67], v[156:159], v[48:51]\n\t"
      "ds_read_b128 v[136:139], v198 offset:36864\n\t"
      "v_mfma_f32_16x16x32_bf16 v[52:55], a[68:71], v[156:159], v[52:55]\n\t"
      "s_waitcnt lgkmcnt(8)\n\t"
      "v_mfma_f32_16x16x32_bf16 v[56:59], a[48:51], v[160:163], v[56:59]\n\t"
      "buffer_load_dwordx4 v213, s[16:19], s51 offen offset:2048 lds\n\t"
      "v_mfma_f32_16x16x32_bf16 v[60:63], a[52:55], v[160:163], v[60:63]\n\t"
      "v_mfma_f32_16x16x32_bf16 v[64:67], a[56:59], v[160:163], v[64:67]\n\t"
      "s_waitcnt vmcnt(12)\n\t"
      "ds_read_b64_tr_b16 a[74:75], v214 offset:49152\n\t"
      "v_mfma_f32_16x16x32_bf16 v[68:71], a[60:63], v[160:163], v[68:71]\n\t"
      "ds_read_b64_tr_b16 a[78:79], v214 offset:49160\n\t"
      "s_add_u32 m0, s41, 0x18000\n\t"
      "s_mov_b32 s51, 0x24000\n\t"
      "buffer_load_dwordx4 v211, s[16:19], s51 offen lds\n\t"
      "v_mfma_f32_16x16x32_bf16 v[72:75], a[64:67], v[160:163], v[72:75]\n\t"
      "ds_read_b64_tr_b16 a[82:83], v214 offset:49216\n\t"
      "ds_read_b128 v[140:143], v199 offset:0\n\t"
      "v_mfma_f32_16x16x32_bf16 v[76:79], a[68:71], v[160:163], v[76:79]\n\t"
      "ds_read_b64_tr_b16 a[86:87], v214 offset:49224\n\t"
      "s_waitcnt lgkmcnt(9)\n\t"
      "v_mfma_f32_16x16x32_bf16 v[80:83], a[48:51], v[128:131], v[80:83]\n\t"
      "ds_read_b64_tr_b16 a[90:91], v214 offset:49280\n\t"
      "buffer_load_dwordx4 v212, s[16:19], s51 offen offset:1024 lds\n\t"
      "v_mfma_f32_16x16x32_bf16 v[84:87], a[52:55], v[128:131], v[84:87]\n\t"
      "ds_read_b64_tr_b16 a[94:95], v214 offset:49288\n\t"
      "v_mfma_f32_16x16x32_bf16 v[88:91], a[56:59], v[128:131], v[88:91]\n\t"
      "v_mfma_f32_16x16x32_bf16 v[92:95], a[60:63], v[128:131], v[92:95]\n\t"
      "buffer_load_dwordx4 v213, s[16:19], s51 offen offset:2048 lds\n\t"
      "v_mfma_f32_16x16x32_bf16 v[96:99], a[64:67], v[128:131], v[96:99]\n\t"
      "ds_read_b128 v[144:147], v199 offset:12288\n\t"
      "v_mfma_f32_16x16x32_bf16 v[100:103], a[68:71], v[128:131], v[100:103]\n\t"
      "s_waitcnt lgkmcnt(8)\n\t"
      "v_mfma_f32_16x16x32_bf16 v[104:107], a[48:51], v[136:139], v[104:107]\n\t"
      "s_add_u32 m0, s41, 0x1b000\n\t"
      "s_mov_b32 s51, 0x24c00\n\t"
      "buffer_load_dwordx4 v211, s[16:19], s51 offen lds\n\t"
      "v_mfma_f32_16x16x32_bf16 v[108:111], a[52:55], v[136:139], v[108:111]\n\t"
      "v_mfma_f32_16x16x32_bf16 v[112:115], a[56:59], v[136:139], v[112:115]\n\t"
      "s_waitcnt vmcnt(13)\n\t"
      "ds_read_b64_tr_b16 a[96:97], v221 offset:0\n\t"
      "v_mfma_f32_16x16x32_bf16 v[116:119], a[60:63], v[136:139], v[116:119]\n\t"
      "ds_read_b64_tr_b16 a[100:101], v221 offset:8\n\t"
      "buffer_load_dwordx4 v212, s[16:19], s51 offen offset:1024 lds\n\t"
      "v_mfma_f32_16x16x32_bf16 v[120:123], a[64:67], v[136:139], v[120:123]\n\t"
      "ds_read_b64_tr_b16 a[104:105], v221 offset:64\n\t"
      "ds_read_b128 v[148:151], v199 offset:24576\n\t"
      "v_mfma_f32_16x16x32_bf16 v[132:135], a[68:71], v[136:139], v[132:135]\n\t"
      "ds_read_b64_tr_b16 a[108:109], v221 offset:72\n\t"
      "s_waitcnt lgkmcnt(6)\n\t"
      "v_mfma_f32_16x16x32_bf16 v[32:35], a[72:75], v[140:143], v[32:35]\n\t"
      "ds_read_b64_tr_b16 a[112:113], v221 offset:128\n\t"
      "buffer_load_dwordx4 v213, s[16:19], s51 offen offset:2048 lds\n\t"
      "v_mfma_f32_16x16x32_bf16 v[36:39], a[76:79], v[140:143], v[36:39]\n\t"
      "ds_read_b64_tr_b16 a[116:117], v221 offset:136\n\t"
      "v_mfma_f32_16x16x32_bf16 v[40:43], a[80:83], v[140:143], v[40:43]\n\t"
      "v_mfma_f32_16x16x32_bf16 v[44:47], a[84:87], v[140:143], v[44:47]\n\t"
      "s_add_u32 m0, s41, 0x1e000\n\t"
      "s_mov_b32 s51, 0x2a000\n\t"
      "buffer_load_dwordx4 v211, s[16:19], s51 offen lds\n\t"
      "v_mfma_f32_16x16x32_bf16 v[48:51], a[88:91], v[140:143], v[48:51]\n\t"
      "ds_read_b128 v[152:155], v199 offset:36864\n\t"
      "v_mfma_f32_16x16x32_bf16 v[52:55], a[92:95], v[140:143], v[52:55]\n\t"
      "s_waitcnt lgkmcnt(8)\n\t"
      "v_mfma_f32_16x16x32_bf16 v[56:59], a[72:75], v[144:147], v[56:59]\n\t"
      "buffer_load_dwordx4 v212, s[16:19], s51 offen offset:1024 lds\n\t"
      "v_mfma_f32_16x16x32_bf16 v[60:63], a[76:79], v[144:147], v[60:63]\n\t"
      "v_mfma_f32_16x16x32_bf16 v[64:67], a[80:83], v[144:147], v[64:67]\n\t"
      "s_waitcnt vmcnt(14)\n\t"
      "ds_read_b64_tr_b16 a[98:99], v221 offset:12288\n\t"
      "v_mfma_f32_16x16x32_bf16 v[68:71], a[84:87], v[144:147], v[68:71]\n\t"
      "ds_read_b64_tr_b16 a[102:103], v221 offset:12296\n\t"
      "buffer_load_dwordx4 v213, s[16:19], s51 offen offset:2048 lds\n\t"
      "v_mfma_f32_16x16x32_bf16 v[72:75], a[88:91], v[144:147], v[72:75]\n\t"
      "ds_read_b64_tr_b16 a[106:107], v221 offset:12352\n\t"
      "ds_read_b128 v[156:159], v197 offset:256\n\t"
      "v_mfma_f32_16x16x32_bf16 v[76:79], a[92:95], v[144:147], v[76:79]\n\t"
      "ds_read_b64_tr_b16 a[110:111], v221 offset:12360\n\t"
      "s_waitcnt lgkmcnt(9)\n\t"
      "v_mfma_f32_16x16x32_bf16 v[80:83], a[72:75], v[148:151], v[80:83]\n\t"
      "ds_read_b64_tr_b16 a[114:115], v221 offset:12416\n\t"
      "s_waitcnt lgkmcnt(7)\n\t"
      "s_add_u32 m0, s41, 0x21000\n\t"
      "s_mov_b32 s51, 0x2ac00\n\t"
      "buffer_load_dwordx4 v211, s[16:19], s51 offen lds\n\t"
      "v_mfma_f32_16x16x32_bf16 v[84:87], a[76:79], v[148:151], v[84:87]\n\t"
      "ds_read_b64_tr_b16 a[118:119], v221 offset:12424\n\t"
      "v_mfma_f32_16x16x32_bf16 v[88:91], a[80:83], v[148:151], v[88:91]\n\t"
      "v_mfma_f32_16x16x32_bf16 v[92:95], a[84:87], v[148:151], v[92:95]\n\t"
      "buffer_load_dwordx4 v212, s[16:19], s51 offen offset:1024 lds\n\t"
      "v_mfma_f32_16x16x32_bf16 v[96:99], a[88:91], v[148:151], v[96:99]\n\t"
      "ds_read_b128 v[160:163], v197 offset:12544\n\t"
      "v_mfma_f32_16x16x32_bf16 v[100:103], a[92:95], v[148:151], v[100:103]\n\t"
      "s_waitcnt lgkmcnt(8)\n\t"
      "v_mfma_f32_16x16x32_bf16 v[104:107], a[72:75], v[152:155], v[104:107]\n\t"
      "buffer_load_dwordx4 v213, s[16:19], s51 offen offset:2048 lds\n\t"
      "v_mfma_f32_16x16x32_bf16 v[108:111], a[76:79], v[152:155], v[108:111]\n\t"
      "v_mfma_f32_16x16x32_bf16 v[112:115], a[80:83], v[152:155], v[112:115]\n\t"
      "s_waitcnt vmcnt(15)\n\t"
      "ds_read_b64_tr_b16 a[120:121], v214 offset:0\n\t"
      "v_mfma_f32_16x16x32_bf16 v[116:119], a[84:87], v[152:155], v[116:119]\n\t"
      "ds_read_b64_tr_b16 a[124:125], v214 offset:8\n\t"
      "s_waitcnt lgkmcnt(3)\n\t"
      "s_add_u32 m0, s41, 0x24000\n\t"
      "s_mov_b32 s51, 0x30000\n\t"
      "buffer_load_dwordx4 v211, s[16:19], s51 offen lds\n\t"
      "v_mfma_f32_16x16x32_bf16 v[120:123], a[88:91], v[152:155], v[120:123]\n\t"
      "ds_read_b64_tr_b16 a[128:129], v214 offset:64\n\t"
      "ds_read_b128 v[128:131], v197 offset:24832\n\t"
      "v_mfma_f32_16x16x32_bf16 v[132:135], a[92:95], v[152:155], v[132:135]\n\t"
      "ds_read_b64_tr_b16 a[132:133], v214 offset:72\n\t"
      "v_mfma_f32_16x16x32_bf16 v[32:35], a[96:99], v[156:159], v[32:35]\n\t"
      "ds_read_b64_tr_b16 a[136:137], v214 offset:128\n\t"
      "buffer_load_dwordx4 v212, s[16:19], s51 offen offset:1024 lds\n\t"
      "v_mfma_f32_16x16x32_bf16 v[36:39], a[100:103], v[156:159], v[36:39]\n\t"
      "ds_read_b64_tr_b16 a[140:141], v214 offset:136\n\t"
      "v_mfma_f32_16x16x32_bf16 v[40:43], a[104:107], v[156:159], v[40:43]\n\t"
      "v_mfma_f32_16x16x32_bf16 v[44:47], a[108:111], v[156:159], v[44:47]\n\t"
      "buffer_load_dwordx4 v213, s[16:19], s51 offen offset:2048 lds\n\t"
      "v_mfma_f32_16x16x32_bf16 v[48:51], a[112:115], v[156:159], v[48:51]\n\t"
      "ds_read_b128 v[136:139], v197 offset:37120\n\t"
      "v_mfma_f32_16x16x32_bf16 v[52:55], a[116:119], v[156:159], v[52:55]\n\t"
      "s_waitcnt lgkmcnt(8)\n\t"
      "v_mfma_f32_16x16x32_bf16 v[56:59], a[96:99], v[160:163], v[56:59]\n\t"
      "v_mfma_f32_16x16x32_bf16 v[60:63], a[100:103], v[160:163], v[60:63]\n\t"
      "v_mfma_f32_16x16x32_bf16 v[64:67], a[104:107], v[160:163], v[64:67]\n\t"
      "s_waitcnt vmcnt(15)\n\t"
      "ds_read_b64_tr_b16 a[122:123], v214 offset:12288\n\t"
      "s_waitcnt lgkmcnt(2)\n\t"
      "s_add_u32 m0, s41, 0x12000\n\t"
      "s_mov_b32 s51, 0x30c00\n\t"
      "buffer_load_dwordx4 v211, s[16:19], s51 offen lds\n\t"
      "v_mfma_f32_16x16x32_bf16 v[68:71], a[108:111], v[160:163], v[68:71]\n\t"
      "ds_read_b64_tr_b16 a[126:127], v214 offset:12296\n\t"
      "v_mfma_f32_16x16x32_bf16 v[72:75], a[112:115], v[160:163], v[72:75]\n\t"
      "ds_read_b64_tr_b16 a[130:131], v214 offset:12352\n\t"
      "ds_read_b128 v[140:143], v198 offset:256\n\t"
      "v_mfma_f32_16x16x32_bf16 v[76:79], a[116:119], v[160:163], v[76:79]\n\t"
      "ds_read_b64_tr_b16 a[134:135], v214 offset:12360\n\t"
      "buffer_load_dwordx4 v212, s[16:19], s51 offen offset:1024 lds\n\t"
      "v_mfma_f32_16x16x32_bf16 v[80:83], a[96:99], v[128:131], v[80:83]\n\t"
      "ds_read_b64_tr_b16 a[138:139], v214 offset:12416\n\t"
      "v_mfma_f32_16x16x32_bf16 v[84:87], a[100:103], v[128:131], v[84:87]\n\t"
      "ds_read_b64_tr_b16 a[142:143], v214 offset:12424\n\t"
      "v_mfma_f32_16x16x32_bf16 v[88:91], a[104:107], v[128:131], v[88:91]\n\t"
      "buffer_load_dwordx4 v213, s[16:19], s51 offen offset:2048 lds\n\t"
      "v_mfma_f32_16x16x32_bf16 v[92:95], a[108:111], v[128:131], v[92:95]\n\t"
      "v_mfma_f32_16x16x32_bf16 v[96:99], a[112:115], v[128:131], v[96:99]\n\t"
      "ds_read_b128 v[144:147], v198 offset:12544\n\t"
      "v_mfma_f32_16x16x32_bf16 v[100:103], a[116:119], v[128:131], v[100:103]\n\t"
      "s_waitcnt lgkmcnt(8)\n\t"
      "v_mfma_f32_16x16x32_bf16 v[104:107], a[96:99], v[136:139], v[104:107]\n\t"
      "v_mfma_f32_16x16x32_bf16 v[108:111], a[100:103], v[136:139], v[108:111]\n\t"
      "v_mfma_f32_16x16x32_bf16 v[112:115], a[104:107], v[136:139], v[112:115]\n\t"
      "s_waitcnt vmcnt(15)\n\t"
      "ds_read_b64_tr_b16 a[144:145], v214 offset:24576\n\t"
      "s_waitcnt lgkmcnt(2)\n\t"
      "s_add_u32 m0, s41, 0x15000\n\t"
      "s_mov_b32 s51, 0x36000\n\t"
      "buffer_load_dwordx4 v211, s[16:19], s51 offen lds\n\t"
      "v_mfma_f32_16x16x32_bf16 v[116:119], a[108:111], v[136:139], v[116:119]\n\t"
      "ds_read_b64_tr_b16 a[148:149], v214 offset:24584\n\t"
      "v_mfma_f32_16x16x32_bf16 v[120:123], a[112:115], v[136:139], v[120:123]\n\t"
      "ds_read_b64_tr_b16 a[152:153], v214 offset:24640\n\t"
      "ds_read_b128 v[148:151], v198 offset:24832\n\t"
      "v_mfma_f32_16x16x32_bf16 v[132:135], a[116:119], v[136:139], v[132:135]\n\t"
      "ds_read_b64_tr_b16 a[156:157], v214 offset:24648\n\t"
      "buffer_load_dwordx4 v212, s[16:19], s51 offen offset:1024 lds\n\t"
      "v_mfma_f32_16x16x32_bf16 v[32:35], a[120:123], v[140:143], v[32:35]\n\t"
      "ds_read_b64_tr_b16 a[160:161], v214 offset:24704\n\t"
      "v_mfma_f32_16x16x32_bf16 v[36:39], a[124:127], v[140:143], v[36:39]\n\t"
      "ds_read_b64_tr_b16 a[164:165], v214 offset:24712\n\t"
      "v_mfma_f32_16x16x32_bf16 v[40:43], a[128:131], v[140:143], v[40:43]\n\t"
      "buffer_load_dwordx4 v213, s[16:19], s51 offen offset:2048 lds\n\t"
      "v_mfma_f32_16x16x32_bf16 v[44:47], a[132:135], v[140:143], v[44:47]\n\t"
      "v_mfma_f32_16x16x32_bf16 v[48:51], a[136:139], v[140:143], v[48:51]\n\t"
      "ds_read_b128 v[152:155], v198 offset:37120\n\t"
      "v_mfma_f32_16x16x32_bf16 v[52:55], a[140:143], v[140:143], v[52:55]\n\t"
      "s_waitcnt lgkmcnt(8)\n\t"
      "v_mfma_f32_16x16x32_bf16 v[56:59], a[120:123], v[144:147], v[56:59]\n\t"
      "v_mfma_f32_16x16x32_bf16 v[60:63], a[124:127], v[144:147], v[60:63]\n\t"
      "v_mfma_f32_16x16x32_bf16 v[64:67], a[128:131], v[144:147], v[64:67]\n\t"
      "s_waitcnt vmcnt(15)\n\t"
      "ds_read_b64_tr_b16 a[146:147], v214 offset:36864\n\t"
      "s_waitcnt lgkmcnt(2)\n\t"
      "s_add_u32 m0, s41, 0x18000\n\t"
      "s_mov_b32 s51, 0x36c00\n\t"
      "buffer_load_dwordx4 v211, s[16:19], s51 offen lds\n\t"
      "v_mfma_f32_16x16x32_bf16 v[68:71], a[132:135], v[144:147], v[68:71]\n\t"
      "ds_read_b64_tr_b16 a[150:151], v214 offset:36872\n\t"
      "v_mfma_f32_16x16x32_bf16 v[72:75], a[136:139], v[144:147], v[72:75]\n\t"
      "ds_read_b64_tr_b16 a[154:155], v214 offset:36928\n\t"
      "ds_read_b128 v[156:159], v199 offset:256\n\t"
      "v_mfma_f32_16x16x32_bf16 v[76:79], a[140:143], v[144:147], v[76:79]\n\t"
      "ds_read_b64_tr_b16 a[158:159], v214 offset:36936\n\t"
      "buffer_load_dwordx4 v212, s[16:19], s51 offen offset:1024 lds\n\t"
      "v_mfma_f32_16x16x32_bf16 v[80:83], a[120:123], v[148:151], v[80:83]\n\t"
      "ds_read_b64_tr_b16 a[162:163], v214 offset:36992\n\t"
      "v_mfma_f32_16x16x32_bf16 v[84:87], a[124:127], v[148:151], v[84:87]\n\t"
      "ds_read_b64_tr_b16 a[166:167], v214 offset:37000\n\t"
      "v_mfma_f32_16x16x32_bf16 v[88:91], a[128:131], v[148:151], v[88:91]\n\t"
      "buffer_load_dwordx4 v213, s[16:19], s51 offen offset:2048 lds\n\t"
      "v_mfma_f32_16x16x32_bf16 v[92:95], a[132:135], v[148:151], v[92:95]\n\t"
      "v_mfma_f32_16x16x32_bf16 v[96:99], a[136:139], v[148:151], v[96:99]\n\t"
      "ds_read_b128 v[160:163], v199 offset:12544\n\t"
      "v_mfma_f32_16x16x32_bf16 v[100:103], a[140:143], v[148:151], v[100:103]\n\t"
      "s_waitcnt lgkmcnt(8)\n\t"
      "v_mfma_f32_16x16x32_bf16 v[104:107], a[120:123], v[152:155], v[104:107]\n\t"
      "v_mfma_f32_16x16x32_bf16 v[108:111], a[124:127], v[152:155], v[108:111]\n\t"
      "v_mfma_f32_16x16x32_bf16 v[112:115], a[128:131], v[152:155], v[112:115]\n\t"
      "s_waitcnt vmcnt(15)\n\t"
      "ds_read_b64_tr_b16 a[168:169], v214 offset:49152\n\t"
      "s_waitcnt lgkmcnt(2)\n\t"
      "s_add_u32 m0, s41, 0x1b000\n\t"
      "s_mov_b32 s51, 0x3c000\n\t"
      "buffer_load_dwordx4 v211, s[16:19], s51 offen lds\n\t"
      "v_mfma_f32_16x16x32_bf16 v[116:119], a[132:135], v[152:155], v[116:119]\n\t"
      "ds_read_b64_tr_b16 a[172:173], v214 offset:49160\n\t"
      "v_mfma_f32_16x16x32_bf16 v[120:123], a[136:139], v[152:155], v[120:123]\n\t"
      "ds_read_b64_tr_b16 a[176:177], v214 offset:49216\n\t"
      "ds_read_b128 v[128:131], v199 offset:24832\n\t"
      "v_mfma_f32_16x16x32_bf16 v[132:135], a[140:143], v[152:155], v[132:135]\n\t"
      "ds_read_b64_tr_b16 a[180:181], v214 offset:49224\n\t"
      "buffer_load_dwordx4 v212, s[16:19], s51 offen offset:1024 lds\n\t"
      "v_mfma_f32_16x16x32_bf16 v[32:35], a[144:147], v[156:159], v[32:35]\n\t"
      "ds_read_b64_tr_b16 a[184:185], v214 offset:49280\n\t"
      "v_mfma_f32_16x16x32_bf16 v[36:39], a[148:151], v[156:159], v[36:39]\n\t"
      "ds_read_b64_tr_b16 a[188:189], v214 offset:49288\n\t"
      "v_mfma_f32_16x16x32_bf16 v[40:43], a[152:155], v[156:159], v[40:43]\n\t"
      "buffer_load_dwordx4 v213, s[16:19], s51 offen offset:2048 lds\n\t"
      "v_mfma_f32_16x16x32_bf16 v[44:47], a[156:159], v[156:159], v[44:47]\n\t"
      "v_mfma_f32_16x16x32_bf16 v[48:51], a[160:163], v[156:159], v[48:51]\n\t"
      "ds_read_b128 v[136:139], v199 offset:37120\n\t"
      "v_mfma_f32_16x16x32_bf16 v[52:55], a[164:167], v[156:159], v[52:55]\n\t"
      "s_waitcnt lgkmcnt(8)\n\t"
      "v_mfma_f32_16x16x32_bf16 v[56:59], a[144:147], v[160:163], v[56:59]\n\t"
      "v_mfma_f32_16x16x32_bf16 v[60:63], a[148:151], v[160:163], v[60:63]\n\t"
      "v_mfma_f32_16x16x32_bf16 v[64:67], a[152:155], v[160:163], v[64:67]\n\t"
      "s_waitcnt vmcnt(15)\n\t"
      "ds_read_b64_tr_b16 a[170:171], v221 offset:0\n\t"
      "s_waitcnt lgkmcnt(2)\n\t"
      "s_add_u32 m0, s41, 0x1e000\n\t"
      "s_mov_b32 s51, 0x3cc00\n\t"
      "buffer_load_dwordx4 v211, s[16:19], s51 offen lds\n\t"
      "v_mfma_f32_16x16x32_bf16 v[68:71], a[156:159], v[160:163], v[68:71]\n\t"
      "ds_read_b64_tr_b16 a[174:175], v221 offset:8\n\t"
      "v_mfma_f32_16x16x32_bf16 v[72:75], a[160:163], v[160:163], v[72:75]\n\t"
      "ds_read_b64_tr_b16 a[178:179], v221 offset:64\n\t"
      "ds_read_b128 v[140:143], v196 offset:256\n\t"
      "v_mfma_f32_16x16x32_bf16 v[76:79], a[164:167], v[160:163], v[76:79]\n\t"
      "ds_read_b64_tr_b16 a[182:183], v221 offset:72\n\t"
      "buffer_load_dwordx4 v212, s[16:19], s51 offen offset:1024 lds\n\t"
      "v_mfma_f32_16x16x32_bf16 v[80:83], a[144:147], v[128:131], v[80:83]\n\t"
      "ds_read_b64_tr_b16 a[186:187], v221 offset:128\n\t"
      "v_mfma_f32_16x16x32_bf16 v[84:87], a[148:151], v[128:131], v[84:87]\n\t"
      "ds_read_b64_tr_b16 a[190:191], v221 offset:136\n\t"
      "v_mfma_f32_16x16x32_bf16 v[88:91], a[152:155], v[128:131], v[88:91]\n\t"
      "buffer_load_dwordx4 v213, s[16:19], s51 offen offset:2048 lds\n\t"
      "v_mfma_f32_16x16x32_bf16 v[92:95], a[156:159], v[128:131], v[92:95]\n\t"
      "v_mfma_f32_16x16x32_bf16 v[96:99], a[160:163], v[128:131], v[96:99]\n\t"
      "ds_read_b128 v[144:147], v196 offset:12544\n\t"
      "v_mfma_f32_16x16x32_bf16 v[100:103], a[164:167], v[128:131], v[100:103]\n\t"
      "s_waitcnt lgkmcnt(8)\n\t"
      "v_mfma_f32_16x16x32_bf16 v[104:107], a[144:147], v[136:139], v[104:107]\n\t"
      "v_mfma_f32_16x16x32_bf16 v[108:111], a[148:151], v[136:139], v[108:111]\n\t"
      "v_mfma_f32_16x16x32_bf16 v[112:115], a[152:155], v[136:139], v[112:115]\n\t"
      "s_waitcnt vmcnt(15)\n\t"
      "ds_read_b64_tr_b16 a[192:193], v221 offset:12288\n\t"
      "s_waitcnt lgkmcnt(2)\n\t"
      "s_add_u32 m0, s41, 0x21000\n\t"
      "s_mov_b32 s51, 0x42000\n\t"
      "buffer_load_dwordx4 v211, s[16:19], s51 offen lds\n\t"
      "v_mfma_f32_16x16x32_bf16 v[116:119], a[156:159], v[136:139], v[116:119]\n\t"
      "ds_read_b64_tr_b16 a[196:197], v221 offset:12296\n\t"
      "v_mfma_f32_16x16x32_bf16 v[120:123], a[160:163], v[136:139], v[120:123]\n\t"
      "ds_read_b64_tr_b16 a[200:201], v221 offset:12352\n\t"
      "ds_read_b128 v[148:151], v196 offset:24832\n\t"
      "v_mfma_f32_16x16x32_bf16 v[132:135], a[164:167], v[136:139], v[132:135]\n\t"
      "ds_read_b64_tr_b16 a[204:205], v221 offset:12360\n\t"
      "buffer_load_dwordx4 v212, s[16:19], s51 offen offset:1024 lds\n\t"
      "v_mfma_f32_16x16x32_bf16 v[32:35], a[168:171], v[140:143], v[32:35]\n\t"
      "ds_read_b64_tr_b16 a[208:209], v221 offset:12416\n\t"
      "v_mfma_f32_16x16x32_bf16 v[36:39], a[172:175], v[140:143], v[36:39]\n\t"
      "ds_read_b64_tr_b16 a[212:213], v221 offset:12424\n\t"
      "v_mfma_f32_16x16x32_bf16 v[40:43], a[176:179], v[140:143], v[40:43]\n\t"
      "buffer_load_dwordx4 v213, s[16:19], s51 offen offset:2048 lds\n\t"
      "v_mfma_f32_16x16x32_bf16 v[44:47], a[180:183], v[140:143], v[44:47]\n\t"
      "v_mfma_f32_16x16x32_bf16 v[48:51], a[184:187], v[140:143], v[48:51]\n\t"
      "ds_read_b128 v[152:155], v196 offset:37120\n\t"
      "v_mfma_f32_16x16x32_bf16 v[52:55], a[188:191], v[140:143], v[52:55]\n\t"
      "s_waitcnt lgkmcnt(8)\n\t"
      "v_mfma_f32_16x16x32_bf16 v[56:59], a[168:171], v[144:147], v[56:59]\n\t"
      "v_mfma_f32_16x16x32_bf16 v[60:63], a[172:175], v[144:147], v[60:63]\n\t"
      "v_mfma_f32_16x16x32_bf16 v[64:67], a[176:179], v[144:147], v[64:67]\n\t"
      "s_waitcnt vmcnt(15)\n\t"
      "ds_read_b64_tr_b16 a[194:195], v214 offset:0\n\t"
      "s_waitcnt lgkmcnt(2)\n\t"
      "s_add_u32 m0, s41, 0x24000\n\t"
      "s_mov_b32 s51, 0x42c00\n\t"
      "buffer_load_dwordx4 v211, s[16:19], s51 offen lds\n\t"
      "v_mfma_f32_16x16x32_bf16 v[68:71], a[180:183], v[144:147], v[68:71]\n\t"
      "ds_read_b64_tr_b16 a[198:199], v214 offset:8\n\t"
      "v_mfma_f32_16x16x32_bf16 v[72:75], a[184:187], v[144:147], v[72:75]\n\t"
      "ds_read_b64_tr_b16 a[202:203], v214 offset:64\n\t"
      "ds_read_b128 v[156:159], v198 offset:512\n\t"
      "v_mfma_f32_16x16x32_bf16 v[76:79], a[188:191], v[144:147], v[76:79]\n\t"
      "ds_read_b64_tr_b16 a[206:207], v214 offset:72\n\t"
      "buffer_load_dwordx4 v212, s[16:19], s51 offen offset:1024 lds\n\t"
      "v_mfma_f32_16x16x32_bf16 v[80:83], a[168:171], v[148:151], v[80:83]\n\t"
      "ds_read_b64_tr_b16 a[210:211], v214 offset:128\n\t"
      "v_mfma_f32_16x16x32_bf16 v[84:87], a[172:175], v[148:151], v[84:87]\n\t"
      "ds_read_b64_tr_b16 a[214:215], v214 offset:136\n\t"
      "v_mfma_f32_16x16x32_bf16 v[88:91], a[176:179], v[148:151], v[88:91]\n\t"
      "buffer_load_dwordx4 v213, s[16:19], s51 offen offset:2048 lds\n\t"
      "v_mfma_f32_16x16x32_bf16 v[92:95], a[180:183], v[148:151], v[92:95]\n\t"
      "v_mfma_f32_16x16x32_bf16 v[96:99], a[184:187], v[148:151], v[96:99]\n\t"
      "ds_read_b128 v[160:163], v198 offset:12800\n\t"
      "v_mfma_f32_16x16x32_bf16 v[100:103], a[188:191], v[148:151], v[100:103]\n\t"
      "s_waitcnt lgkmcnt(8)\n\t"
      "v_mfma_f32_16x16x32_bf16 v[104:107], a[168:171], v[152:155], v[104:107]\n\t"
      "s_add_u32 m0, s41, 0xc000\n\t"
      "s_add_u32 s51, s40, 0xc00000\n\t"
      "buffer_load_dwordx4 v208, s[20:23], s51 offen lds\n\t"
      "v_mfma_f32_16x16x32_bf16 v[108:111], a[172:175], v[152:155], v[108:111]\n\t"
      "v_mfma_f32_16x16x32_bf16 v[112:115], a[176:179], v[152:155], v[112:115]\n\t"
      "s_waitcnt vmcnt(16)\n\t"
      "ds_read_b64_tr_b16 a[216:217], v214 offset:12288\n\t"
      "buffer_load_dwordx4 v208, s[20:23], s51 offen offset:1024 lds\n\t"
      "v_mfma_f32_16x16x32_bf16 v[116:119], a[180:183], v[152:155], v[116:119]\n\t"
      "ds_read_b64_tr_b16 a[220:221], v214 offset:12296\n\t"
      "v_mfma_f32_16x16x32_bf16 v[120:123], a[184:187], v[152:155], v[120:123]\n\t"
      "ds_read_b64_tr_b16 a[224:225], v214 offset:12352\n\t"
      "ds_read_b128 v[128:131], v198 offset:25088\n\t"
      "buffer_load_dwordx4 v208, s[20:23], s51 offen offset:2048 lds\n\t"
      "v_mfma_f32_16x16x32_bf16 v[132:135], a[188:191], v[152:155], v[132:135]\n\t"
      "ds_read_b64_tr_b16 a[228:229], v214 offset:12360\n\t"
      "s_waitcnt lgkmcnt(6)\n\t"
      "v_mfma_f32_16x16x32_bf16 v[32:35], a[192:195], v[156:159], v[32:35]\n\t"
      "ds_read_b64_tr_b16 a[232:233], v214 offset:12416\n\t"
      "s_add_u32 m0, s41, 0xf000\n\t"
      "s_add_u32 s51, s40, 0xc00000\n\t"
      "buffer_load_dwordx4 v209, s[20:23], s51 offen lds\n\t"
      "v_mfma_f32_16x16x32_bf16 v[36:39], a[196:199], v[156:159], v[36:39]\n\t"
      "ds_read_b64_tr_b16 a[236:237], v214 offset:12424\n\t"
      "v_mfma_f32_16x16x32_bf16 v[40:43], a[200:203], v[156:159], v[40:43]\n\t"
      "buffer_load_dwordx4 v209, s[20:23], s51 offen offset:1024 lds\n\t"
      "v_mfma_f32_16x16x32_bf16 v[44:47], a[204:207], v[156:159], v[44:47]\n\t"
      "v_mfma_f32_16x16x32_bf16 v[48:51], a[208:211], v[156:159], v[48:51]\n\t"
      "ds_read_b128 v[136:139], v198 offset:37376\n\t"
      "buffer_load_dwordx4 v209, s[20:23], s51 offen offset:2048 lds\n\t"
      "v_mfma_f32_16x16x32_bf16 v[52:55], a[212:215], v[156:159], v[52:55]\n\t"
      "s_waitcnt lgkmcnt(8)\n\t"
      "v_mfma_f32_16x16x32_bf16 v[56:59], a[192:195], v[160:163], v[56:59]\n\t"
      "v_mfma_f32_16x16x32_bf16 v[60:63], a[196:199], v[160:163], v[60:63]\n\t"
      "v_mfma_f32_16x16x32_bf16 v[64:67], a[200:203], v[160:163], v[64:67]\n\t"
      "s_waitcnt vmcnt(18)\n\t"
      "ds_read_b64_tr_b16 a[218:219], v214 offset:24576\n\t"
      "v_mfma_f32_16x16x32_bf16 v[68:71], a[204:207], v[160:163], v[68:71]\n\t"
      "ds_read_b64_tr_b16 a[222:223], v214 offset:24584\n\t"
      "v_mfma_f32_16x16x32_bf16 v[72:75], a[208:211], v[160:163], v[72:75]\n\t"
      "ds_read_b64_tr_b16 a[226:227], v214 offset:24640\n\t"
      "ds_read_b128 v[140:143], v199 offset:512\n\t"
      "v_mfma_f32_16x16x32_bf16 v[76:79], a[212:215], v[160:163], v[76:79]\n\t"
      "ds_read_b64_tr_b16 a[230:231], v214 offset:24648\n\t"
      "s_waitcnt lgkmcnt(9)\n\t"
      "v_mfma_f32_16x16x32_bf16 v[80:83], a[192:195], v[128:131], v[80:83]\n\t"
      "ds_read_b64_tr_b16 a[234:235], v214 offset:24704\n\t"
      "v_mfma_f32_16x16x32_bf16 v[84:87], a[196:199], v[128:131], v[84:87]\n\t"
      "ds_read_b64_tr_b16 a[238:239], v214 offset:24712\n\t"
      "v_mfma_f32_16x16x32_bf16 v[88:91], a[200:203], v[128:131], v[88:91]\n\t"
      "v_mfma_f32_16x16x32_bf16 v[92:95], a[204:207], v[128:131], v[92:95]\n\t"
      "v_mfma_f32_16x16x32_bf16 v[96:99], a[208:211], v[128:131], v[96:99]\n\t"
      "ds_read_b128 v[144:147], v199 offset:12800\n\t"
      "v_mfma_f32_16x16x32_bf16 v[100:103], a[212:215], v[128:131], v[100:103]\n\t"
      "s_waitcnt lgkmcnt(8)\n\t"
      "v_mfma_f32_16x16x32_bf16 v[104:107], a[192:195], v[136:139], v[104:107]\n\t"
      "v_mfma_f32_16x16x32_bf16 v[108:111], a[196:199], v[136:139], v[108:111]\n\t"
      "v_mfma_f32_16x16x32_bf16 v[112:115], a[200:203], v[136:139], v[112:115]\n\t"
      "s_waitcnt vmcnt(15)\n\t"
      "ds_read_b64_tr_b16 a[240:241], v214 offset:36864\n\t"
      "v_mfma_f32_16x16x32_bf16 v[116:119], a[204:207], v[136:139], v[116:119]\n\t"
      "ds_read_b64_tr_b16 a[244:245], v214 offset:36872\n\t"
      "v_mfma_f32_16x16x32_bf16 v[120:123], a[208:211], v[136:139], v[120:123]\n\t"
      "ds_read_b64_tr_b16 a[248:249], v214 offset:36928\n\t"
      "ds_read_b128 v[148:151], v199 offset:25088\n\t"
      "v_mfma_f32_16x16x32_bf16 v[132:135], a[212:215], v[136:139], v[132:135]\n\t"
      "ds_read_b64_tr_b16 a[252:253], v214 offset:36936\n\t"
      "s_waitcnt lgkmcnt(6)\n\t"
      "v_mfma_f32_16x16x32_bf16 v[32:35], a[216:219], v[140:143], v[32:35]\n\t"
      "ds_read_b64_tr_b16 v[164:165], v214 offset:36992\n\t"
      "v_mfma_f32_16x16x32_bf16 v[36:39], a[220:223], v[140:143], v[36:39]\n\t"
      "ds_read_b64_tr_b16 v[168:169], v214 offset:37000\n\t"
      "v_mfma_f32_16x16x32_bf16 v[40:43], a[224:227], v[140:143], v[40:43]\n\t"
      "s_waitcnt vmcnt(12)\n\t"
      "ds_read_b64_tr_b16 a[242:243], v214 offset:49152\n\t"
      "v_mfma_f32_16x16x32_bf16 v[44:47], a[228:231], v[140:143], v[44:47]\n\t"
      "ds_read_b64_tr_b16 a[246:247], v214 offset:49160\n\t"
      "v_mfma_f32_16x16x32_bf16 v[48:51], a[232:235], v[140:143], v[48:51]\n\t"
      "ds_read_b64_tr_b16 a[250:251], v214 offset:49216\n\t"
      "ds_read_b128 v[152:155], v199 offset:37376\n\t"
      "v_mfma_f32_16x16x32_bf16 v[52:55], a[236:239], v[140:143], v[52:55]\n\t"
      "ds_read_b64_tr_b16 a[254:255], v214 offset:49224\n\t"
      "s_waitcnt lgkmcnt(12)\n\t"
      "v_mfma_f32_16x16x32_bf16 v[56:59], a[216:219], v[144:147], v[56:59]\n\t"
      "ds_read_b64_tr_b16 v[166:167], v214 offset:49280\n\t"
      "v_mfma_f32_16x16x32_bf16 v[60:63], a[220:223], v[144:147], v[60:63]\n\t"
      "ds_read_b64_tr_b16 v[170:171], v214 offset:49288\n\t"
      "v_mfma_f32_16x16x32_bf16 v[64:67], a[224:227], v[144:147], v[64:67]\n\t"
      "s_waitcnt vmcnt(9)\n\t"
      "ds_read_b64_tr_b16 v[172:173], v221 offset:0\n\t"
      "v_mfma_f32_16x16x32_bf16 v[68:71], a[228:231], v[144:147], v[68:71]\n\t"
      "ds_read_b64_tr_b16 v[176:177], v221 offset:8\n\t"
      "v_mfma_f32_16x16x32_bf16 v[72:75], a[232:235], v[144:147], v[72:75]\n\t"
      "ds_read_b64_tr_b16 v[180:181], v221 offset:64\n\t"
      "ds_read_b128 v[156:159], v196 offset:512\n\t"
      "v_mfma_f32_16x16x32_bf16 v[76:79], a[236:239], v[144:147], v[76:79]\n\t"
      "ds_read_b64_tr_b16 v[184:185], v221 offset:72\n\t"
      "s_waitcnt lgkmcnt(14)\n\t"
      "v_mfma_f32_16x16x32_bf16 v[80:83], a[216:219], v[148:151], v[80:83]\n\t"
      "ds_read_b64_tr_b16 v[188:189], v221 offset:128\n\t"
      "v_mfma_f32_16x16x32_bf16 v[84:87], a[220:223], v[148:151], v[84:87]\n\t"
      "ds_read_b64_tr_b16 v[192:193], v221 offset:136\n\t"
      "v_mfma_f32_16x16x32_bf16 v[88:91], a[224:227], v[148:151], v[88:91]\n\t"
      "v_mfma_f32_16x16x32_bf16 v[92:95], a[228:231], v[148:151], v[92:95]\n\t"
      "v_mfma_f32_16x16x32_bf16 v[96:99], a[232:235], v[148:151], v[96:99]\n\t"
      "ds_read_b128 v[160:163], v196 offset:12800\n\t"
      "v_mfma_f32_16x16x32_bf16 v[100:103], a[236:239], v[148:151], v[100:103]\n\t"
      "s_waitcnt lgkmcnt(11)\n\t"
      "v_mfma_f32_16x16x32_bf16 v[104:107], a[216:219], v[152:155], v[104:107]\n\t"
      "v_mfma_f32_16x16x32_bf16 v[108:111], a[220:223], v[152:155], v[108:111]\n\t"
      "v_mfma_f32_16x16x32_bf16 v[112:115], a[224:227], v[152:155], v[112:115]\n\t"
      "s_waitcnt vmcnt(6)\n\t"
      "ds_read_b64_tr_b16 v[174:175], v221 offset:12288\n\t"
      "buffer_load_dwordx2 v[240:241], v245, s[24:27], 0 offen sc1\n\t"
      "v_mfma_f32_16x16x32_bf16 v[116:119], a[228:231], v[152:155], v[116:119]\n\t"
      "ds_read_b64_tr_b16 v[178:179], v221 offset:12296\n\t"
      "s_cmp_eq_u32 s41, s43\n\t"
      "s_cbranch_scc0 .Lc4_%=\n\t"
      "buffer_load_dword v254, v253, s[24:27], 0 offen sc1\n\t"
      ".Lc4_%=:\n\t"
      "v_mfma_f32_16x16x32_bf16 v[120:123], a[232:235], v[152:155], v[120:123]\n\t"
      "ds_read_b64_tr_b16 v[182:183], v221 offset:12352\n\t"
      "ds_read_b128 v[140:143], v197 offset:512\n\t"
      "v_mfma_f32_16x16x32_bf16 v[132:135], a[236:239], v[152:155], v[132:135]\n\t"
      "ds_read_b64_tr_b16 v[186:187], v221 offset:12360\n\t"
      "s_waitcnt lgkmcnt(9)\n\t"
      "v_mfma_f32_16x16x32_bf16 v[32:35], a[240:243], v[156:159], v[32:35]\n\t"
      "ds_read_b64_tr_b16 v[190:191], v221 offset:12416\n\t"
      "v_mfma_f32_16x16x32_bf16 v[36:39], a[244:247], v[156:159], v[36:39]\n\t"
      "ds_read_b64_tr_b16 v[194:195], v221 offset:12424\n\t"
      "v_mfma_f32_16x16x32_bf16 v[40:43], a[248:251], v[156:159], v[40:43]\n\t"
      "v_mfma_f32_16x16x32_bf16 v[44:47], a[252:255], v[156:159], v[44:47]\n\t"
      "v_mfma_f32_16x16x32_bf16 v[48:51], v[164:167], v[156:159], v[48:51]\n\t"
      "ds_read_b128 v[144:147], v197 offset:12800\n\t"
      "v_mfma_f32_16x16x32_bf16 v[52:55], v[168:171], v[156:159], v[52:55]\n\t"
      "s_waitcnt lgkmcnt(8)\n\t"
      "v_mfma_f32_16x16x32_bf16 v[56:59], a[240:243], v[160:163], v[56:59]\n\t"
      "v_mfma_f32_16x16x32_bf16 v[60:63], a[244:247], v[160:163], v[60:63]\n\t"
      "v_mfma_f32_16x16x32_bf16 v[64:67], a[248:251], v[160:163], v[64:67]\n\t"
      "v_mfma_f32_16x16x32_bf16 v[68:71], a[252:255], v[160:163], v[68:71]\n\t"
      "v_mfma_f32_16x16x32_bf16 v[72:75], v[164:167], v[160:163], v[72:75]\n\t"
      "ds_read_b128 v[128:131], v196 offset:25088\n\t"
      "v_mfma_f32_16x16x32_bf16 v[76:79], v[168:171], v[160:163], v[76:79]\n\t"
      "s_waitcnt lgkmcnt(2)\n\t"
      "v_mfma_f32_16x16x32_bf16 v[32:35], v[172:175], v[140:143], v[32:35]\n\t"
      "v_mfma_f32_16x16x32_bf16 v[36:39], v[176:179], v[140:143], v[36:39]\n\t"
      "v_mfma_f32_16x16x32_bf16 v[40:43], v[180:183], v[140:143], v[40:43]\n\t"
      "v_mfma_f32_16x16x32_bf16 v[44:47], v[184:187], v[140:143], v[44:47]\n\t"
      "v_mfma_f32_16x16x32_bf16 v[48:51], v[188:191], v[140:143], v[48:51]\n\t"
      "ds_read_b128 v[136:139], v196 offset:37376\n\t"
      "v_mfma_f32_16x16x32_bf16 v[52:55], v[192:195], v[140:143], v[52:55]\n\t"
      "s_waitcnt lgkmcnt(2)\n\t"
      "v_mfma_f32_16x16x32_bf16 v[56:59], v[172:175], v[144:147], v[56:59]\n\t"
      "v_mfma_f32_16x16x32_bf16 v[60:63], v[176:179], v[144:147], v[60:63]\n\t"
      "s_waitcnt vmcnt(4) lgkmcnt(2)\n\t"
      "s_barrier\n\t"
      "v_mfma_f32_16x16x32_bf16 v[64:67], v[180:183], v[144:147], v[64:67]\n\t"
      "v_cvt_pk_bf16_f32 v32, v32, v33\n\t"
      "v_cvt_pk_bf16_f32 v33, v34, v35\n\t"
      "v_mfma_f32_16x16x32_bf16 v[68:71], v[184:187], v[144:147], v[68:71]\n\t"
      "v_cvt_pk_bf16_f32 v34, v36, v37\n\t"
      "v_cvt_pk_bf16_f32 v35, v38, v39\n\t"
      "v_mfma_f32_16x16x32_bf16 v[72:75], v[188:191], v[144:147], v[72:75]\n\t"
      "ds_read_b128 v[148:151], v197 offset:25088\n\t"
      "ds_write_b128 v217, v[32:35] offset:24576\n\t"
      "v_cvt_pk_bf16_f32 v40, v40, v41\n\t"
      "v_mfma_f32_16x16x32_bf16 v[76:79], v[192:195], v[144:147], v[76:79]\n\t"
      "v_cvt_pk_bf16_f32 v41, v42, v43\n\t"
      "v_cvt_pk_bf16_f32 v42, v44, v45\n\t"
      "s_mov_b32 m0, s41\n\t"
      "s_add_u32 s51, s40, 0x1200000\n\t"
      "buffer_load_dwordx4 v208, s[20:23], s51 offen lds\n\t"
      "s_waitcnt lgkmcnt(3)\n\t"
      "v_mfma_f32_16x16x32_bf16 v[80:83], a[240:243], v[128:131], v[80:83]\n\t"
      "v_cvt_pk_bf16_f32 v43, v46, v47\n\t"
      "ds_write_b128 v218, v[40:43] offset:24576\n\t"
      "ds_read_b128 v[32:35], v196 offset:49152\n\t"
      "v_mfma_f32_16x16x32_bf16 v[84:87], a[244:247], v[128:131], v[84:87]\n\t"
      "v_cvt_pk_bf16_f32 v48, v48, v49\n\t"
      "v_cvt_pk_bf16_f32 v49, v50, v51\n\t"
      "v_mfma_f32_16x16x32_bf16 v[88:91], a[248:251], v[128:131], v[88:91]\n\t"
      "v_cvt_pk_bf16_f32 v50, v52, v53\n\t"
      "v_cvt_pk_bf16_f32 v51, v54, v55\n\t"
      "buffer_load_dwordx4 v208, s[20:23], s51 offen offset:1024 lds\n\t"
      "v_mfma_f32_16x16x32_bf16 v[92:95], a[252:255], v[128:131], v[92:95]\n\t"
      "ds_write_b128 v219, v[48:51] offset:24576\n\t"
      "ds_read_b128 v[36:39], v197 offset:49152\n\t"
      "v_cvt_pk_bf16_f32 v56, v56, v57\n\t"
      "v_mfma_f32_16x16x32_bf16 v[96:99], v[164:167], v[128:131], v[96:99]\n\t"
      "ds_read_b128 v[152:155], v197 offset:37376\n\t"
      "v_cvt_pk_bf16_f32 v57, v58, v59\n\t"
      "v_cvt_pk_bf16_f32 v58, v60, v61\n\t"
      "v_mfma_f32_16x16x32_bf16 v[100:103], v[168:171], v[128:131], v[100:103]\n\t"
      "v_cvt_pk_bf16_f32 v59, v62, v63\n\t"
      "ds_write_b128 v217, v[56:59] offset:36864\n\t"
      "ds_read_b128 v[40:43], v198 offset:49152\n\t"
      "s_waitcnt lgkmcnt(9)\n\t"
      "v_mfma_f32_16x16x32_bf16 v[104:107], a[240:243], v[136:139], v[104:107]\n\t"
      "v_cvt_pk_bf16_f32 v64, v64, v65\n\t"
      "v_cvt_pk_bf16_f32 v65, v66, v67\n\t"
      "buffer_load_dwordx4 v208, s[20:23], s51 offen offset:2048 lds\n\t"
      "v_mfma_f32_16x16x32_bf16 v[108:111], a[244:247], v[136:139], v[108:111]\n\t"
      "v_cvt_pk_bf16_f32 v66, v68, v69\n\t"
      "v_cvt_pk_bf16_f32 v67, v70, v71\n\t"
      "v_mfma_f32_16x16x32_bf16 v[112:115], a[248:251], v[136:139], v[112:115]\n\t"
      "ds_write_b128 v218, v[64:67] offset:36864\n\t"
      "ds_read_b128 v[44:47], v199 offset:49152\n\t"
      "v_cvt_pk_bf16_f32 v72, v72, v73\n\t"
      "v_mfma_f32_16x16x32_bf16 v[116:119], a[252:255], v[136:139], v[116:119]\n\t"
      "v_cvt_pk_bf16_f32 v73, v74, v75\n\t"
      "v_cvt_pk_bf16_f32 v74, v76, v77\n\t"
      "v_mfma_f32_16x16x32_bf16 v[120:123], v[164:167], v[136:139], v[120:123]\n\t"
      "v_cvt_pk_bf16_f32 v75, v78, v79\n\t"
      "ds_write_b128 v219, v[72:75] offset:36864\n\t"
      "ds_read_b128 v[48:51], v197 offset:49408\n\t"
      "v_mfma_f32_16x16x32_bf16 v[132:135], v[168:171], v[136:139], v[132:135]\n\t"
      "s_add_u32 m0, s41, 0x3000\n\t"
      "s_add_u32 s51, s40, 0x1200000\n\t"
      "buffer_load_dwordx4 v209, s[20:23], s51 offen lds\n\t"
      "ds_read_b128 v[52:55], v198 offset:49408\n\t"
      "s_waitcnt lgkmcnt(13)\n\t"
      "v_mfma_f32_16x16x32_bf16 v[80:83], v[172:175], v[148:151], v[80:83]\n\t"
      "ds_read_b128 v[56:59], v199 offset:49408\n\t"
      "ds_read_b128 v[60:63], v196 offset:49408\n\t"
      "v_mfma_f32_16x16x32_bf16 v[84:87], v[176:179], v[148:151], v[84:87]\n\t"
      "ds_read_b128 v[64:67], v198 offset:49664\n\t"
      "s_memrealtime s[80:81]\n\t"
      "v_mfma_f32_16x16x32_bf16 v[88:91], v[180:183], v[148:151], v[88:91]\n\t"
      "buffer_load_dwordx4 v209, s[20:23], s51 offen offset:1024 lds\n\t"
      "ds_read_b128 v[68:71], v199 offset:49664\n\t"
      "v_mfma_f32_16x16x32_bf16 v[92:95], v[184:187], v[148:151], v[92:95]\n\t"
      "ds_read_b128 v[72:75], v196 offset:49664\n\t"
      "ds_read_b128 v[76:79], v197 offset:49664\n\t"
      "v_mfma_f32_16x16x32_bf16 v[96:99], v[188:191], v[148:151], v[96:99]\n\t"
      "v_mfma_f32_16x16x32_bf16 v[100:103], v[192:195], v[148:151], v[100:103]\n\t"
      "buffer_load_dwordx4 v209, s[20:23], s51 offen offset:2048 lds\n\t"
      "s_waitcnt lgkmcnt(13)\n\t"
      "v_mfma_f32_16x16x32_bf16 v[104:107], v[172:175], v[152:155], v[104:107]\n\t"
      "v_mfma_f32_16x16x32_bf16 v[108:111], v[176:179], v[152:155], v[108:111]\n\t"
      "v_mfma_f32_16x16x32_bf16 v[112:115], v[180:183], v[152:155], v[112:115]\n\t"
      "v_mfma_f32_16x16x32_bf16 v[116:119], v[184:187], v[152:155], v[116:119]\n\t"
      "v_mfma_f32_16x16x32_bf16 v[120:123], v[188:191], v[152:155], v[120:123]\n\t"
      "v_mfma_f32_16x16x32_bf16 v[132:135], v[192:195], v[152:155], v[132:135]\n\t"
      "v_add_u32_e32 v196, s46, v200\n\t"
      "v_add_u32_e32 v197, s46, v201\n\t"
      "v_add_u32_e32 v198, s46, v202\n\t"
      "v_add_u32_e32 v199, s46, v203\n\t"
      "s_mov_b32 s87, 75\n\t"
      "s_mov_b32 s86, 80\n\t"
      "s_mov_b32 s84, 96\n\t"
      "s_branch .Lp2_%=\n\t"
      ".Lend_%=:\n\t"
      ".Lconv_end:\n\t"
      :
      : "{s[16:31]}"(d16), "{s[36:39]}"(db), "{s[52:53]}"(rs), "{s40}"(t0), "{s41}"(p0), "{s43}"(lds0), "{s94}"(ty), "{s95}"(tres), "{s[60:61]}"(pres),
      "{s69}"(pre1), "{s74}"(plm), "{s75}"(plb), "{s66}"(sst ? 24576u : 0u - 24576u), "{s89}"((blk >> 4) % 5u), "{v208}"(ga), "{v209}"(gb),
      "{v211}"(woff[0]), "{v212}"(woff[1]), "{v213}"(woff[2]), "{v248}"(ta), "{v249}"(ta), "{v250}"(tp), "{v251}"(t1), "{v252}"(mk), "{v245}"(vgv),
      "{v246}"(vxw), "{v247}"(vxr), "{v253}"(vcell), "{v200}"(cfa[0]), "{v201}"(cfa[1]), "{v202}"(cfa[2]), "{v203}"(cfa[3]), "{v204}"(csw[0]),
      "{v205}"(csw[1]), "{v206}"(csw[2]), "{v207}"(csr), "{v214}"(tbase + 73728u), "{v221}"(tbase + 135168u), "{v[226:229]}"(br0), "{v[230:233]}"(br1),
      "{v[234:237]}"(br2), "{v6}"(cpo)
      : "memory", "scc", "vcc",
      "s42", "s44", "s45", "s46", "s47", "s48", "s49", "s50", "s51", "s54", "s55", "s56", "s57", "s58", "s59", "s62", "s63", "s64", "s65", "s67",
      "s68", "s70", "s71", "s72", "s73", "s76", "s77", "s78", "s79", "s80", "s81", "s82", "s83", "s84", "s85", "s86", "s87", "s88", "s90", "s91",
      "s92", "s93", "s96", "s97", "s98", "s99",
      "v1", "v2", "v3", "v4", "v5", "v7", "v8", "v9", "v10", "v11", "v12", "v13", "v14", "v15", "v16", "v17", "v18", "v19", "v20", "v21", "v22", "v23",
      "v24", "v25", "v26", "v27", "v28", "v29", "v30", "v31", "v32", "v33", "v34", "v35", "v36", "v37", "v38", "v39", "v40", "v41", "v42", "v43",
      "v44", "v45", "v46", "v47", "v48", "v49", "v50", "v51", "v52", "v53", "v54", "v55", "v56", "v57", "v58", "v59", "v60", "v61", "v62", "v63",
      "v64", "v65", "v66", "v67", "v68", "v69", "v70", "v71", "v72", "v73", "v74", "v75", "v76", "v77", "v78", "v79", "v80", "v81", "v82", "v83",
      "v84", "v85", "v86", "v87", "v88", "v89", "v90", "v91", "v92", "v93", "v94", "v95", "v96", "v97", "v98", "v99", "v100", "v101", "v102", "v103",
      "v104", "v105", "v106", "v107", "v108", "v109", "v110", "v111", "v112", "v113", "v114", "v115", "v116", "v117", "v118", "v119", "v120", "v121",
      "v122", "v123", "v124", "v125", "v126", "v127", "v128", "v129", "v130", "v131", "v132", "v133", "v134", "v135", "v136", "v137", "v138", "v139",
      "v140", "v141", "v142", "v143", "v144", "v145", "v146", "v147", "v148", "v149", "v150", "v151", "v152", "v153", "v154", "v155", "v156", "v157",
      "v158", "v159", "v160", "v161", "v162", "v163", "v164", "v165", "v166", "v167", "v168", "v169", "v170", "v171", "v172", "v173", "v174", "v175",
      "v176", "v177", "v178", "v179", "v180", "v181", "v182", "v183", "v184", "v185", "v186", "v187", "v188", "v189", "v190", "v191", "v192", "v193",
      "v194", "v195", "v196", "v197", "v198", "v199", "v210", "v215", "v216", "v217", "v218", "v219", "v220", "v222", "v223", "v224", "v225", "v238",
      "v239", "v240", "v241", "v242", "v243", "v244", "v254", "v255",
      "a0", "a1", "a2", "a3", "a4", "a5", "a6", "a7", "a8", "a9", "a10", "a11", "a12", "a13", "a14", "a15", "a16", "a17", "a18", "a19", "a20", "a21",
      "a22", "a23", "a24", "a25", "a26", "a27", "a28", "a29", "a30", "a31", "a32", "a33", "a34", "a35", "a36", "a37", "a38", "a39", "a40", "a41",
      "a42", "a43", "a44", "a45", "a46", "a47", "a48", "a49", "a50", "a51", "a52", "a53", "a54", "a55", "a56", "a57", "a58", "a59", "a60", "a61",
      "a62", "a63", "a64", "a65", "a66", "a67", "a68", "a69", "a70", "a71", "a72", "a73", "a74", "a75", "a76", "a77", "a78", "a79", "a80", "a81",
      "a82", "a83", "a84", "a85", "a86", "a87", "a88", "a89", "a90", "a91", "a92", "a93", "a94", "a95", "a96", "a97", "a98", "a99", "a100", "a101",
      "a102", "a103", "a104", "a105", "a106", "a107", "a108", "a109", "a110", "a111", "a112", "a113", "a114", "a115", "a116", "a117", "a118", "a119",
      "a120", "a121", "a122", "a123", "a124", "a125", "a126", "a127", "a128", "a129", "a130", "a131", "a132", "a133", "a134", "a135", "a136", "a137",
      "a138", "a139", "a140", "a141", "a142", "a143", "a144", "a145", "a146", "a147", "a148", "a149", "a150", "a151", "a152", "a153", "a154", "a155",
      "a156", "a157", "a158", "a159", "a160", "a161", "a162", "a163", "a164", "a165", "a166", "a167", "a168", "a169", "a170", "a171", "a172", "a173",
      "a174", "a175", "a176", "a177", "a178", "a179", "a180", "a181", "a182", "a183", "a184", "a185", "a186", "a187", "a188", "a189", "a190", "a191",
      "a192", "a193", "a194", "a195", "a196", "a197", "a198", "a199", "a200", "a201", "a202", "a203", "a204", "a205", "a206", "a207", "a208", "a209",
      "a210", "a211", "a212", "a213", "a214", "a215", "a216", "a217", "a218", "a219", "a220", "a221", "a222", "a223", "a224", "a225", "a226", "a227",
      "a228", "a229", "a230", "a231", "a232", "a233", "a234", "a235", "a236", "a237", "a238", "a239", "a240", "a241", "a242", "a243", "a244", "a245",
      "a246", "a247", "a248", "a249", "a250", "a251", "a252", "a253", "a254", "a255");
}

extern "C" void kernel_launch(void* const* d_in, const int* in_sizes, int n_in,
                              void* d_out, int out_size, void* d_ws, size_t ws_size,
                              hipStream_t stream) {
  conv1x1_kernel<<<256, 256, 0, stream>>>(d_in[1], 0x0002000000048000ull, d_in[0], 0x0002000006c00000ull, d_ws, 0x0002000000000c00ull, d_out, 0x0002000006c00000ull,
                                          d_in[2], 0x0002000000000300ull);
}
```

```cpp
#include <hip/hip_runtime.h>
#include <stdint.h>

#define LAS __attribute__((address_space(3)))
typedef __attribute__((ext_vector_type(16))) unsigned u32x16;
typedef __attribute__((ext_vector_type(4))) unsigned u32x4;
typedef unsigned long long u64;

__global__ __launch_bounds__(256, 1) void conv1x1_kernel(const void* w, u64 cw, const void* x, u64 cx, void* ws, u64 cs, void* out, u64 co,
                                                         const void* bias, u64 cb) {
  __shared__ __attribute__((aligned(1024))) unsigned char lds[160768];

  u32x16 d16;
  u32x4 db;
  u64 rs;
  asm volatile(
      "s_load_dwordx16 s[16:31], %3, 0x0\n\t"
      "s_load_dwordx4 s[36:39], %3, 0x40\n\t"
      "s_memrealtime s[52:53]\n\t"
      : "={s[16:31]}"(d16), "={s[36:39]}"(db), "={s[52:53]}"(rs)
      : "s"(__builtin_amdgcn_kernarg_segment_ptr())
      : "memory");
  unsigned cpo = ((blockIdx.x >> 3) * 256u + threadIdx.x) * 4u;
  asm volatile(
      "s_getpc_b64 s[92:93]\n\t"
      ".Lconv_cp:\n\t"
      "v_min_u32_e32 v6, .Lconv_end-.Lconv_cp-4, v6\n\t"
      "global_load_dword v6, v6, s[92:93]\n\t"
      : "+{v6}"(cpo)
      :
      : "memory", "s92", "s93");
  __builtin_amdgcn_sched_barrier(0);
  unsigned tid  = threadIdx.x;
  unsigned lane = tid & 63;
  unsigned wv   = __builtin_amdgcn_readfirstlane(tid >> 6);
  const unsigned lds0 = (unsigned)(size_t)(LAS unsigned char*)lds;
  unsigned blk  = blockIdx.x;
  const unsigned p0   = lds0 + 3072u * wv;
  const unsigned lq = lane >> 4, ph = (lane >> 2) & 3, pl = lane & 3;
  const unsigned gs = 256u * lq + 64u * ((ph - lq) & 3) + 16u * (pl ^ ((4u - wv) & 3));
  const unsigned ga = gs + 3072u * (wv < 3u ? wv : 5u);
  const unsigned gb = gs + 3072u * (wv < 2u ? wv + 6u : wv + 1u);
  const unsigned boff = 192u * wv + 16u * lq;
  const unsigned role = (blk >> 2) & 1u;
  unsigned pk = (blk & 3u) * 32u + (blk >> 3);
  const unsigned sst  = (role ^ (blk >> 4)) & 1u;
  const unsigned t0   = pk * 49152u + sst * 24576u;
  const unsigned pv = blk >> 7, ps = ((blk & 7u) | ((blk >> 1) & 56u)) ^ (4u * pv), pgr = (ps >> 2) & 7u, pi = (ps & 3u) * 2u + pv;
  const unsigned ppg = min(pgr * 8u, 52u) + (pi & (0x13777777u >> (4u * pgr)) & 7u);
  const unsigned pop = (ppg << 21) + ((ps & 3u) * 4u + pv) * 0x1c080u + 0x24080u;
  unsigned pfire = ((role ^ ppg) & 1u) * 2u, psel = ps >> 5;
  const unsigned vpub = lane == 0u ? 0u + (pv * 64u + ps) * 8u : 0x40000000u;
  const unsigned pfo = (blk >> 4) * 18432u;
  const unsigned pfv = tid * 16u;
  const unsigned ty = (blk >> 3) & 1u;
  const unsigned tidx = (blk >> 3) * 4u + wv, tsel = tidx >> 6;
  const unsigned toff = (tidx & 63u) < 54u ? (tidx & 63u) << 21 : 113246204u;
  unsigned tres, pre1;
  u64 pres;
  u32x4 br0, br1, br2;
  asm volatile(
      "s_add_u32 m0, s43, 0x27000\n\t"
      "s_waitcnt lgkmcnt(0)\n\t"
      "s_cmp_eq_u32 s94, 1\n\t"
      "s_cbranch_scc0 .Lc1_%=\n\t"
      "s_cmp_eq_u32 s41, s43\n\t"
      "s_cbranch_scc0 .Lc0_%=\n\t"
      "s_cmp_eq_u32 s78, 0\n\t"
      "s_cselect_b64 s[96:97], s[20:21], s[28:29]\n\t"
      "s_load_dword s69, s[96:97], s76 offset:0x0\n\t"
      "s_load_dwordx2 s[60:61], s[96:97], s76 offset:0x3300\n\t"
      ".Lc0_%=:\n\t"
      "buffer_load_dwordx4 v216, s[16:19], s42 offen lds\n\t"
      "s_add_u32 s51, s42, 0x1000\n\t"
      "buffer_load_dwordx4 v216, s[16:19], s51 offen lds\n\t"
      "s_add_u32 s51, s42, 0x2000\n\t"
      "buffer_load_dwordx4 v216, s[16:19], s51 offen lds\n\t"
      "s_add_u32 s51, s42, 0x3000\n\t"
      "buffer_load_dwordx4 v216, s[16:19], s51 offen lds\n\t"
      "s_add_u32 s51, s42, 0x4000\n\t"
      "buffer_load_dwordx4 v216, s[16:19], s51 offen lds\n\t"
      ".Lc1_%=:\n\t"
      "buffer_load_dwordx4 v[226:229], v215, s[36:39], 0 offen offset:0\n\t"
      "buffer_load_dwordx4 v[230:233], v215, s[36:39], 0 offen offset:64\n\t"
      "buffer_load_dwordx4 v[234:237], v215, s[36:39], 0 offen offset:128\n\t"
      "s_mov_b32 m0, s41\n\t"
      "s_nop 0\n\t"
      "buffer_load_dwordx4 v208, s[20:23], s40 offen lds\n\t"
      "buffer_load_dwordx4 v208, s[20:23], s40 offen offset:1024 lds\n\t"
      "buffer_load_dwordx4 v208, s[20:23], s40 offen offset:2048 lds\n\t"
      "s_cmp_eq_u32 s94, 0\n\t"
      "s_cbranch_scc0 .Lvb0_%=\n\t"
      "s_add_u32 m0, s41, 0x3000\n\t"
      "s_nop 0\n\t"
      "buffer_load_dwordx4 v209, s[20:23], s40 offen lds\n\t"
      "buffer_load_dwordx4 v209, s[20:23], s40 offen offset:1024 lds\n\t"
      "buffer_load_dwordx4 v209, s[20:23], s40 offen offset:2048 lds\n\t"
      "s_cmp_eq_u32 s99, 0\n\t"
      "s_cselect_b64 s[96:97], s[20:21], s[28:29]\n\t"
      "s_load_dword s95, s[96:97], s98\n\t"
      "s_branch .Lvb1_%=\n\t"
      ".Lvb0_%=:\n\t"
      "s_cmp_eq_u32 s41, s43\n\t"
      "s_cbranch_scc1 .Lev0_%=\n\t"
      "s_cmp_eq_u32 s99, 0\n\t"
      "s_cselect_b64 s[96:97], s[20:21], s[28:29]\n\t"
      "s_load_dword s95, s[96:97], s98\n\t"
      "s_cmp_eq_u32 s94, 0\n\t"
      "s_cbranch_scc1 .Lev1_%=\n\t"
      "s_sleep 8\n\t"
      "s_branch .Lev1_%=\n\t"
      ".Lev0_%=:\n\t"
      "s_andn2_b32 s79, s52, 15\n\t"
      "v_mov_b32_e32 v241, s53\n\t"
      "s_waitcnt lgkmcnt(2)\n\t"
      "s_nop 7\n\t"
      "s_getreg_b32 s78, hwreg(HW_REG_IB_STS, 8, 4)\n\t"
      "s_cmp_eq_u32 s78, 1\n\t"
      "s_cselect_b32 s51, 2, 0\n\t"
      "s_xor_b32 s77, s77, s51\n\t"
      "s_cmp_lg_u32 s78, 0\n\t"
      "s_cselect_b32 s77, s77, 1\n\t"
      "s_or_b32 s79, s79, s77\n\t"
      "v_mov_b32_e32 v240, s79\n\t"
      "buffer_store_dwordx2 v[240:241], v244, s[24:27], 0 offen sc1\n\t"
      "s_cmp_eq_u32 s99, 0\n\t"
      "s_cselect_b64 s[96:97], s[20:21], s[28:29]\n\t"
      "s_load_dword s95, s[96:97], s98\n\t"
      ".Lev1_%=:\n\t"
      "s_add_u32 m0, s41, 0x3000\n\t"
      "s_nop 0\n\t"
      "buffer_load_dwordx4 v209, s[20:23], s40 offen lds\n\t"
      "buffer_load_dwordx4 v209, s[20:23], s40 offen offset:1024 lds\n\t"
      "buffer_load_dwordx4 v209, s[20:23], s40 offen offset:2048 lds\n\t"
      "s_add_u32 m0, s41, 0x6000\n\t"
      "s_add_u32 s51, s40, 0x600000\n\t"
      "buffer_load_dwordx4 v208, s[20:23], s51 offen lds\n\t"
      "buffer_load_dwordx4 v208, s[20:23], s51 offen offset:1024 lds\n\t"
      "buffer_load_dwordx4 v208, s[20:23], s51 offen offset:2048 lds\n\t"
      "s_add_u32 m0, s41, 0x9000\n\t"
      "s_add_u32 s51, s40, 0x600000\n\t"
      "buffer_load_dwordx4 v209, s[20:23], s51 offen lds\n\t"
      "buffer_load_dwordx4 v209, s[20:23], s51 offen offset:1024 lds\n\t"
      "buffer_load_dwordx4 v209, s[20:23], s51 offen offset:2048 lds\n\t"
      ".Lvb1_%=:\n\t"
      : "={s95}"(tres), "={s[60:61]}"(pres), "={s69}"(pre1), "={v[226:229]}"(br0), "={v[230:233]}"(br1), "={v[234:237]}"(br2),
        "+v"(lane), "+{s77}"(pfire), "+{s78}"(psel), "+{v6}"(cpo)
      : "{s[16:31]}"(d16), "{s[36:39]}"(db), "{s40}"(t0), "{s41}"(p0), "{s43}"(lds0), "{s94}"(ty),
        "{v208}"(ga), "{v209}"(gb), "{v215}"(boff), "{v216}"(pfv),
        "{s42}"(pfo), "{s98}"(toff), "{s99}"(tsel), "{s76}"(pop), "{s[52:53]}"(rs), "{v244}"(vpub)
      : "memory", "scc", "s51", "s92", "s93", "s96", "s97", "s79", "v240", "v241");
  asm volatile("" : "+s"(wv), "+s"(pk));
  __builtin_amdgcn_sched_barrier(0);
  const unsigned vcell = lane < 2u ? 2048u + pk * 8u + 4u * (role ^ lane) : 0x40000000u;
  const unsigned ob = (unsigned)(((lane < 12u ? 0x41ccd62ca418820ull >> (5u * lane) : 0x22a4c1eeull >> (5u * ((lane - 12u) & 7u)))) & 31u) * 6291456u + pk * 49152u;
  const unsigned pg = (ob + 24576u) >> 21;
  const unsigned ta = lane < 18u ? ob + 24576u * sst : 113246208u, t1 = ob + 24576u * (sst ^ 1u);
  const unsigned tq = (lane >= 9u ? 83u : 80u), tp = lane >= 14u ? 0u : tq;
  const unsigned mk = lane >= 4u + ty && lane < 18u ? 1u << (pg < 52u ? (pg >> 3) * 4u : 28u) : 0u;
  const unsigned plm = (unsigned)__builtin_amdgcn_ballot_w64(((pg ^ role ^ sst) & 1u) != 0u), plb = wv < 2u ? plm : ~plm;
  const unsigned vgv = 0u + (wv & 1u) * 512u + lane * 8u;
  const unsigned vxr = lds0 + 159744u + lane * 16u, vxw = vxr + wv * 4u;
  const unsigned li = lane & 15, lg = lane >> 4, rl = li & 3, hi = li >> 2;
  const unsigned rb = 768u * li + 16u * (lg ^ ((4u - hi) & 3)) - 3072u * wv;
  unsigned cfa[4], csw[3];
#pragma unroll
  for (unsigned u = 0; u < 4; ++u) cfa[u] = rb + 64u * ((u - rl) & 3);
#pragma unroll
  for (unsigned j = 0; j < 3; ++j) {
    const unsigned e = 3u * wv + j;
    csw[j] = rb + 256u * (e >> 2) + 64u * (((e & 3) + (e >> 2) - rl) & 3);
  }
  const unsigned csr = 16u * lane;
  const unsigned tbase = p0 + (4u * lg + (li >> 2)) * 192u + 16u * (li & 3);
  unsigned woff[3];
#pragma unroll
  for (unsigned j = 0; j < 3; ++j) {
    const unsigned rr = (__umul24(lane, 171u) + 10944u * j) >> 11;
    woff[j] = __umul24(rr, 576u) + __umul24(rr & 12u, 768u) + (16u * lane + 192u * wv);
  }
  asm volatile(
      "s_cmp_eq_u32 s94, 0\n\t"
      "s_cbranch_scc0 .Lc2_%=\n\t"
      "s_add_u32 m0, s41, 0x1e000\n\t"
      "s_mov_b32 s51, 0x0\n\t"
      "buffer_load_dwordx4 v211, s[16:19], s51 offen lds\n\t"
      "buffer_load_dwordx4 v212, s[16:19], s51 offen offset:1024 lds\n\t"
      "buffer_load_dwordx4 v213, s[16:19], s51 offen offset:2048 lds\n\t"
      "s_add_u32 m0, s41, 0x21000\n\t"
      "s_mov_b32 s51, 0xc00\n\t"
      "buffer_load_dwordx4 v211, s[16:19], s51 offen lds\n\t"
      "buffer_load_dwordx4 v212, s[16:19], s51 offen offset:1024 lds\n\t"
      "buffer_load_dwordx4 v213, s[16:19], s51 offen offset:2048 lds\n\t"
      "s_add_u32 m0, s41, 0x24000\n\t"
      "s_mov_b32 s51, 0x6000\n\t"
      "buffer_load_dwordx4 v211, s[16:19], s51 offen lds\n\t"
      "buffer_load_dwordx4 v212, s[16:19], s51 offen offset:1024 lds\n\t"
      "buffer_load_dwordx4 v213, s[16:19], s51 offen offset:2048 lds\n\t"
      "s_add_u32 m0, s41, 0x12000\n\t"
      "s_mov_b32 s51, 0x6c00\n\t"
      "buffer_load_dwordx4 v211, s[16:19], s51 offen lds\n\t"
      "buffer_load_dwordx4 v212, s[16:19], s51 offen offset:1024 lds\n\t"
      "buffer_load_dwordx4 v213, s[16:19], s51 offen offset:2048 lds\n\t"
      ".Lc2_%=:\n\t"
      "s_mov_b32 s62, 0\n\t"
      "s_mov_b32 s70, 0\n\t"
      "s_add_u32 s50, s41, 0x12000\n\t"
      "s_add_u32 s51, s41, 0x12000\n\t"
      "s_sub_u32 s63, s52, 800\n\t"
      "s_andn2_b32 s63, s63, 15\n\t"
      "v_add_u32_e32 v196, s41, v200\n\t"
      "v_add_u32_e32 v197, s41, v201\n\t"
      "v_add_u32_e32 v198, s41, v202\n\t"
      "v_add_u32_e32 v199, s41, v203\n\t"
      "v_add_u32_e32 v217, s51, v204\n\t"
      "v_add_u32_e32 v218, s51, v205\n\t"
      "v_add_u32_e32 v219, s51, v206\n\t"
      "v_add_u32_e32 v220, s51, v207\n\t"
      "v_add_u32_e32 v238, s51, v207\n\t"
      "s_and_b32 s49, s71, 31\n\t"
      "s_lshr_b32 s71, s71, 5\n\t"
      "s_cmp_eq_u32 s94, 0\n\t"
      "s_cbranch_scc0 .Lpb_%=\n\t"
      "s_add_u32 m0, s41, 0x15000\n\t"
      "s_mov_b32 s51, 0xc000\n\t"
      "buffer_load_dwordx4 v211, s[16:19], s51 offen lds\n\t"
      "buffer_load_dwordx4 v212, s[16:19], s51 offen offset:1024 lds\n\t"
      "buffer_load_dwordx4 v213, s[16:19], s51 offen offset:2048 lds\n\t"
      "s_waitcnt vmcnt(21)\n\t"
      "v_lshlrev_b32_e32 v8, 16, v226\n\t"
      "v_and_b32_e32 v9, 0xffff0000, v226\n\t"
      "v_lshlrev_b32_e32 v10, 16, v227\n\t"
      "v_and_b32_e32 v11, 0xffff0000, v227\n\t"
      "v_lshlrev_b32_e32 v12, 16, v228\n\t"
      "v_and_b32_e32 v13, 0xffff0000, v228\n\t"
      "v_lshlrev_b32_e32 v14, 16, v229\n\t"
      "v_and_b32_e32 v15, 0xffff0000, v229\n\t"
      "v_lshlrev_b32_e32 v16, 16, v230\n\t"
      "v_and_b32_e32 v17, 0xffff0000, v230\n\t"
      "v_lshlrev_b32_e32 v18, 16, v231\n\t"
      "v_and_b32_e32 v19, 0xffff0000, v231\n\t"
      "v_lshlrev_b32_e32 v20, 16, v232\n\t"
      "v_and_b32_e32 v21, 0xffff0000, v232\n\t"
      "v_lshlrev_b32_e32 v22, 16, v233\n\t"
      "v_and_b32_e32 v23, 0xffff0000, v233\n\t"
      "v_lshlrev_b32_e32 v24, 16, v234\n\t"
      "v_and_b32_e32 v25, 0xffff0000, v234\n\t"
      "v_lshlrev_b32_e32 v26, 16, v235\n\t"
      "v_and_b32_e32 v27, 0xffff0000, v235\n\t"
      "v_lshlrev_b32_e32 v28, 16, v236\n\t"
      "v_and_b32_e32 v29, 0xffff0000, v236\n\t"
      "v_lshlrev_b32_e32 v30, 16, v237\n\t"
      "v_and_b32_e32 v31, 0xffff0000, v237\n\t"
      "s_add_u32 m0, s41, 0x18000\n\t"
      "s_mov_b32 s51, 0xcc00\n\t"
      "buffer_load_dwordx4 v211, s[16:19], s51 offen lds\n\t"
      "buffer_load_dwordx4 v212, s[16:19], s51 offen offset:1024 lds\n\t"
      "buffer_load_dwordx4 v213, s[16:19], s51 offen offset:2048 lds\n\t"
      "s_add_u32 m0, s41, 0x1b000\n\t"
      "s_mov_b32 s51, 0x12000\n\t"
      "buffer_load_dwordx4 v211, s[16:19], s51 offen lds\n\t"
      "buffer_load_dwordx4 v212, s[16:19], s51 offen offset:1024 lds\n\t"
      "buffer_load_dwordx4 v213, s[16:19], s51 offen offset:2048 lds\n\t"
      "s_mov_b32 s44, 0\n\t"
      "s_mov_b32 s85, 3\n\t"
      "s_mov_b32 s45, s41\n\t"
      "s_add_u32 s46, s41, 0x6000\n\t"
      "s_add_u32 s47, s41, 0xc000\n\t"
      "s_mov_b64 vcc, 0\n\t"
      "s_waitcnt vmcnt(18)\n\t"
      "s_barrier\n\t"
      "ds_read_b64_tr_b16 a[0:1], v214 offset:49152\n\t"
      "ds_read_b64_tr_b16 a[4:5], v214 offset:49160\n\t"
      "ds_read_b64_tr_b16 a[8:9], v214 offset:49216\n\t"
      "ds_read_b64_tr_b16 a[12:13], v214 offset:49224\n\t"
      "ds_read_b64_tr_b16 a[16:17], v214 offset:49280\n\t"
      "ds_read_b64_tr_b16 a[20:21], v214 offset:49288\n\t"
      "ds_read_b128 v[140:143], v196 offset:0\n\t"
      "s_waitcnt vmcnt(15)\n\t"
      "ds_read_b64_tr_b16 a[2:3], v221 offset:0\n\t"
      "ds_read_b64_tr_b16 a[6:7], v221 offset:8\n\t"
      "ds_read_b64_tr_b16 a[10:11], v221 offset:64\n\t"
      "ds_read_b64_tr_b16 a[14:15], v221 offset:72\n\t"
      "ds_read_b64_tr_b16 a[18:19], v221 offset:128\n\t"
      "ds_read_b64_tr_b16 a[22:23], v221 offset:136\n\t"
      "ds_read_b128 v[152:155], v196 offset:12288\n\t"
      "s_waitcnt lgkmcnt(8)\n\t"
      "s_add_u32 m0, s41, 0x1e000\n\t"
      "s_mov_b32 s51, 0x12c00\n\t"
      "buffer_load_dwordx4 v211, s[16:19], s51 offen lds\n\t"
      "buffer_load_dwordx4 v212, s[16:19], s51 offen offset:1024 lds\n\t"
      "buffer_load_dwordx4 v213, s[16:19], s51 offen offset:2048 lds\n\t"
      "s_waitcnt vmcnt(15)\n\t"
      "ds_read_b64_tr_b16 a[24:25], v221 offset:12288\n\t"
      "ds_read_b64_tr_b16 a[28:29], v221 offset:12296\n\t"
      "ds_read_b64_tr_b16 a[32:33], v221 offset:12352\n\t"
      "ds_read_b64_tr_b16 a[36:37], v221 offset:12360\n\t"
      "ds_read_b64_tr_b16 a[40:41], v221 offset:12416\n\t"
      "ds_read_b64_tr_b16 a[44:45], v221 offset:12424\n\t"
      "ds_read_b128 v[144:147], v197 offset:0\n\t"
      "s_waitcnt lgkmcnt(8)\n\t"
      "v_mfma_f32_16x16x32_bf16 v[80:83], a[0:3], v[140:143], v[8:11]\n\t"
      "s_add_u32 m0, s41, 0x21000\n\t"
      "s_mov_b32 s51, 0x18000\n\t"
      "buffer_load_dwordx4 v211, s[16:19], s51 offen lds\n\t"
      "v_mfma_f32_16x16x32_bf16 v[84:87], a[4:7], v[140:143], v[12:15]\n\t"
      "buffer_load_dwordx4 v212, s[16:19], s51 offen offset:1024 lds\n\t"
      "v_mfma_f32_16x16x32_bf16 v[88:91], a[8:11], v[140:143], v[16:19]\n\t"
      "buffer_load_dwordx4 v213, s[16:19], s51 offen offset:2048 lds\n\t"
      "s_waitcnt vmcnt(15)\n\t"
      "ds_read_b64_tr_b16 a[26:27], v214 offset:0\n\t"
      "v_mfma_f32_16x16x32_bf16 v[92:95], a[12:15], v[140:143], v[20:23]\n\t"
      "ds_read_b64_tr_b16 a[30:31], v214 offset:8\n\t"
      "ds_read_b64_tr_b16 a[34:35], v214 offset:64\n\t"
      "v_mfma_f32_16x16x32_bf16 v[96:99], a[16:19], v[140:143], v[24:27]\n\t"
      "ds_read_b64_tr_b16 a[38:39], v214 offset:72\n\t"
      "ds_read_b64_tr_b16 a[42:43], v214 offset:128\n\t"
      "v_mfma_f32_16x16x32_bf16 v[100:103], a[20:23], v[140:143], v[28:31]\n\t"
      "ds_read_b64_tr_b16 a[46:47], v214 offset:136\n\t"
      "ds_read_b128 v[156:159], v197 offset:12288\n\t"
      "s_waitcnt lgkmcnt(8)\n\t"
      "v_mfma_f32_16x16x32_bf16 v[104:107], a[0:3], v[152:155], v[8:11]\n\t"
      "s_add_u32 m0, s41, 0x24000\n\t"
      "s_mov_b32 s51, 0x18c00\n\t"
      "buffer_load_dwordx4 v211, s[16:19], s51 offen lds\n\t"
      "v_mfma_f32_16x16x32_bf16 v[108:111], a[4:7], v[152:155], v[12:15]\n\t"
      "buffer_load_dwordx4 v212, s[16:19], s51 offen offset:1024 lds\n\t"
      "v_mfma_f32_16x16x32_bf16 v[112:115], a[8:11], v[152:155], v[16:19]\n\t"
      "buffer_load_dwordx4 v213, s[16:19], s51 offen offset:2048 lds\n\t"
      "s_waitcnt vmcnt(15)\n\t"
      "ds_read_b64_tr_b16 a[48:49], v214 offset:12288\n\t"
      "v_mfma_f32_16x16x32_bf16 v[116:119], a[12:15], v[152:155], v[20:23]\n\t"
      "ds_read_b64_tr_b16 a[52:53], v214 offset:12296\n\t"
      "ds_read_b64_tr_b16 a[56:57], v214 offset:12352\n\t"
      "v_mfma_f32_16x16x32_bf16 v[120:123], a[16:19], v[152:155], v[24:27]\n\t"
      "ds_read_b64_tr_b16 a[60:61], v214 offset:12360\n\t"
      "ds_read_b64_tr_b16 a[64:65], v214 offset:12416\n\t"
      "v_mfma_f32_16x16x32_bf16 v[132:135], a[20:23], v[152:155], v[28:31]\n\t"
      "ds_read_b64_tr_b16 a[68:69], v214 offset:12424\n\t"
      "ds_read_b128 v[148:151], v198 offset:0\n\t"
      "s_waitcnt lgkmcnt(8)\n\t"
      "v_mfma_f32_16x16x32_bf16 v[80:83], a[24:27], v[144:147], v[80:83]\n\t"
      "s_add_u32 m0, s41, 0x12000\n\t"
      "s_mov_b32 s51, 0x1e000\n\t"
      "buffer_load_dwordx4 v211, s[16:19], s51 offen lds\n\t"
      "v_mfma_f32_16x16x32_bf16 v[84:87], a[28:31], v[144:147], v[84:87]\n\t"
      "buffer_load_dwordx4 v212, s[16:19], s51 offen offset:1024 lds\n\t"
      "v_mfma_f32_16x16x32_bf16 v[88:91], a[32:35], v[144:147], v[88:91]\n\t"
      "buffer_load_dwordx4 v213, s[16:19], s51 offen offset:2048 lds\n\t"
      "s_waitcnt vmcnt(15)\n\t"
      "ds_read_b64_tr_b16 a[50:51], v214 offset:24576\n\t"
      "v_mfma_f32_16x16x32_bf16 v[92:95], a[36:39], v[144:147], v[92:95]\n\t"
      "ds_read_b64_tr_b16 a[54:55], v214 offset:24584\n\t"
      "ds_read_b64_tr_b16 a[58:59], v214 offset:24640\n\t"
      "v_mfma_f32_16x16x32_bf16 v[96:99], a[40:43], v[144:147], v[96:99]\n\t"
      "ds_read_b64_tr_b16 a[62:63], v214 offset:24648\n\t"
      "ds_read_b64_tr_b16 a[66:67], v214 offset:24704\n\t"
      "v_mfma_f32_16x16x32_bf16 v[100:103], a[44:47], v[144:147], v[100:103]\n\t"
      "ds_read_b64_tr_b16 a[70:71], v214 offset:24712\n\t"
      "ds_read_b128 v[160:163], v198 offset:12288\n\t"
      "s_waitcnt lgkmcnt(8)\n\t"
      "v_mfma_f32_16x16x32_bf16 v[104:107], a[24:27], v[156:159], v[104:107]\n\t"
      "s_add_u32 m0, s41, 0x15000\n\t"
      "s_mov_b32 s51, 0x1ec00\n\t"
      "buffer_load_dwordx4 v211, s[16:19], s51 offen lds\n\t"
      "v_mfma_f32_16x16x32_bf16 v[108:111], a[28:31], v[156:159], v[108:111]\n\t"
      "buffer_load_dwordx4 v212, s[16:19], s51 offen offset:1024 lds\n\t"
      "v_mfma_f32_16x16x32_bf16 v[112:115], a[32:35], v[156:159], v[112:115]\n\t"
      "buffer_load_dwordx4 v213, s[16:19], s51 offen offset:2048 lds\n\t"
      "s_waitcnt vmcnt(15)\n\t"
      "ds_read_b64_tr_b16 a[72:73], v214 offset:36864\n\t"
      "v_mfma_f32_16x16x32_bf16 v[116:119], a[36:39], v[156:159], v[116:119]\n\t"
      "ds_read_b64_tr_b16 a[76:77], v214 offset:36872\n\t"
      "ds_read_b64_tr_b16 a[80:81], v214 offset:36928\n\t"
      "v_mfma_f32_16x16x32_bf16 v[120:123], a[40:43], v[156:159], v[120:123]\n\t"
      "ds_read_b64_tr_b16 a[84:85], v214 offset:36936\n\t"
      "ds_read_b64_tr_b16 a[88:89], v214 offset:36992\n\t"
      "v_mfma_f32_16x16x32_bf16 v[132:135], a[44:47], v[156:159], v[132:135]\n\t"
      "ds_read_b64_tr_b16 a[92:93], v214 offset:37000\n\t"
      "ds_read_b128 v[140:143], v199 offset:0\n\t"
      "s_waitcnt lgkmcnt(8)\n\t"
      "v_mfma_f32_16x16x32_bf16 v[80:83], a[48:51], v[148:151], v[80:83]\n\t"
      "s_add_u32 m0, s41, 0x18000\n\t"
      "s_mov_b32 s51, 0x24000\n\t"
      "buffer_load_dwordx4 v211, s[16:19], s51 offen lds\n\t"
      "v_mfma_f32_16x16x32_bf16 v[84:87], a[52:55], v[148:151], v[84:87]\n\t"
      "buffer_load_dwordx4 v212, s[16:19], s51 offen offset:1024 lds\n\t"
      "v_mfma_f32_16x16x32_bf16 v[88:91], a[56:59], v[148:151], v[88:91]\n\t"
      "buffer_load_dwordx4 v213, s[16:19], s51 offen offset:2048 lds\n\t"
      "s_waitcnt vmcnt(15)\n\t"
      "ds_read_b64_tr_b16 a[74:75], v214 offset:49152\n\t"
      "v_mfma_f32_16x16x32_bf16 v[92:95], a[60:63], v[148:151], v[92:95]\n\t"
      "ds_read_b64_tr_b16 a[78:79], v214 offset:49160\n\t"
      "ds_read_b64_tr_b16 a[82:83], v214 offset:49216\n\t"
      "v_mfma_f32_16x16x32_bf16 v[96:99], a[64:67], v[148:151], v[96:99]\n\t"
      "ds_read_b64_tr_b16 a[86:87], v214 offset:49224\n\t"
      "ds_read_b64_tr_b16 a[90:91], v214 offset:49280\n\t"
      "v_mfma_f32_16x16x32_bf16 v[100:103], a[68:71], v[148:151], v[100:103]\n\t"
      "ds_read_b64_tr_b16 a[94:95], v214 offset:49288\n\t"
      "ds_read_b128 v[152:155], v199 offset:12288\n\t"
      "s_waitcnt lgkmcnt(8)\n\t"
      "v_mfma_f32_16x16x32_bf16 v[104:107], a[48:51], v[160:163], v[104:107]\n\t"
      "s_add_u32 m0, s41, 0x1b000\n\t"
      "s_mov_b32 s51, 0x24c00\n\t"
      "buffer_load_dwordx4 v211, s[16:19], s51 offen lds\n\t"
      "v_mfma_f32_16x16x32_bf16 v[108:111], a[52:55], v[160:163], v[108:111]\n\t"
      "buffer_load_dwordx4 v212, s[16:19], s51 offen offset:1024 lds\n\t"
      "v_mfma_f32_16x16x32_bf16 v[112:115], a[56:59], v[160:163], v[112:115]\n\t"
      "buffer_load_dwordx4 v213, s[16:19], s51 offen offset:2048 lds\n\t"
      "s_waitcnt vmcnt(15)\n\t"
      "ds_read_b64_tr_b16 a[96:97], v221 offset:0\n\t"
      "v_mfma_f32_16x16x32_bf16 v[116:119], a[60:63], v[160:163], v[116:119]\n\t"
      "ds_read_b64_tr_b16 a[100:101], v221 offset:8\n\t"
      "ds_read_b64_tr_b16 a[104:105], v221 offset:64\n\t"
      "v_mfma_f32_16x16x32_bf16 v[120:123], a[64:67], v[160:163], v[120:123]\n\t"
      "ds_read_b64_tr_b16 a[108:109], v221 offset:72\n\t"
      "ds_read_b64_tr_b16 a[112:113], v221 offset:128\n\t"
      "v_mfma_f32_16x16x32_bf16 v[132:135], a[68:71], v[160:163], v[132:135]\n\t"
      "ds_read_b64_tr_b16 a[116:117], v221 offset:136\n\t"
      "ds_read_b128 v[144:147], v197 offset:256\n\t"
      "s_waitcnt lgkmcnt(8)\n\t"
      "v_mfma_f32_16x16x32_bf16 v[80:83], a[72:75], v[140:143], v[80:83]\n\t"
      "s_add_u32 m0, s41, 0x1e000\n\t"
      "s_mov_b32 s51, 0x2a000\n\t"
      "buffer_load_dwordx4 v211, s[16:19], s51 offen lds\n\t"
      "v_mfma_f32_16x16x32_bf16 v[84:87], a[76:79], v[140:143], v[84:87]\n\t"
      "buffer_load_dwordx4 v212, s[16:19], s51 offen offset:1024 lds\n\t"
      "v_mfma_f32_16x16x32_bf16 v[88:91], a[80:83], v[140:143], v[88:91]\n\t"
      "buffer_load_dwordx4 v213, s[16:19], s51 offen offset:2048 lds\n\t"
      "s_waitcnt vmcnt(15)\n\t"
      "ds_read_b64_tr_b16 a[98:99], v221 offset:12288\n\t"
      "v_mfma_f32_16x16x32_bf16 v[92:95], a[84:87], v[140:143], v[92:95]\n\t"
      "ds_read_b64_tr_b16 a[102:103], v221 offset:12296\n\t"
      "ds_read_b64_tr_b16 a[106:107], v221 offset:12352\n\t"
      "v_mfma_f32_16x16x32_bf16 v[96:99], a[88:91], v[140:143], v[96:99]\n\t"
      "ds_read_b64_tr_b16 a[110:111], v221 offset:12360\n\t"
      "ds_read_b64_tr_b16 a[114:115], v221 offset:12416\n\t"
      "v_mfma_f32_16x16x32_bf16 v[100:103], a[92:95], v[140:143], v[100:103]\n\t"
      "ds_read_b64_tr_b16 a[118:119], v221 offset:12424\n\t"
      "ds_read_b128 v[156:159], v197 offset:12544\n\t"
      "s_waitcnt lgkmcnt(8)\n\t"
      "v_mfma_f32_16x16x32_bf16 v[104:107], a[72:75], v[152:155], v[104:107]\n\t"
      "s_add_u32 m0, s41, 0x21000\n\t"
      "s_mov_b32 s51, 0x2ac00\n\t"
      "buffer_load_dwordx4 v211, s[16:19], s51 offen lds\n\t"
      "v_mfma_f32_16x16x32_bf16 v[108:111], a[76:79], v[152:155], v[108:111]\n\t"
      "buffer_load_dwordx4 v212, s[16:19], s51 offen offset:1024 lds\n\t"
      "v_mfma_f32_16x16x32_bf16 v[112:115], a[80:83], v[152:155], v[112:115]\n\t"
      "buffer_load_dwordx4 v213, s[16:19], s51 offen offset:2048 lds\n\t"
      "s_waitcnt vmcnt(15)\n\t"
      "ds_read_b64_tr_b16 a[120:121], v214 offset:0\n\t"
      "v_mfma_f32_16x16x32_bf16 v[116:119], a[84:87], v[152:155], v[116:119]\n\t"
      "ds_read_b64_tr_b16 a[124:125], v214 offset:8\n\t"
      "ds_read_b64_tr_b16 a[128:129], v214 offset:64\n\t"
      "v_mfma_f32_16x16x32_bf16 v[120:123], a[88:91], v[152:155], v[120:123]\n\t"
      "ds_read_b64_tr_b16 a[132:133], v214 offset:72\n\t"
      "ds_read_b64_tr_b16 a[136:137], v214 offset:128\n\t"
      "v_mfma_f32_16x16x32_bf16 v[132:135], a[92:95], v[152:155], v[132:135]\n\t"
      "ds_read_b64_tr_b16 a[140:141], v214 offset:136\n\t"
      "ds_read_b128 v[148:151], v198 offset:256\n\t"
      "s_waitcnt lgkmcnt(8)\n\t"
      "v_mfma_f32_16x16x32_bf16 v[80:83], a[96:99], v[144:147], v[80:83]\n\t"
      "s_add_u32 m0, s41, 0x24000\n\t"
      "s_mov_b32 s51, 0x30000\n\t"
      "buffer_load_dwordx4 v211, s[16:19], s51 offen lds\n\t"
      "v_mfma_f32_16x16x32_bf16 v[84:87], a[100:103], v[144:147], v[84:87]\n\t"
      "buffer_load_dwordx4 v212, s[16:19], s51 offen offset:1024 lds\n\t"
      "v_mfma_f32_16x16x32_bf16 v[88:91], a[104:107], v[144:147], v[88:91]\n\t"
      "buffer_load_dwordx4 v213, s[16:19], s51 offen offset:2048 lds\n\t"
      "s_waitcnt vmcnt(15)\n\t"
      "ds_read_b64_tr_b16 a[122:123], v214 offset:12288\n\t"
      "v_mfma_f32_16x16x32_bf16 v[92:95], a[108:111], v[144:147], v[92:95]\n\t"
      "ds_read_b64_tr_b16 a[126:127], v214 offset:12296\n\t"
      "ds_read_b64_tr_b16 a[130:131], v214 offset:12352\n\t"
      "v_mfma_f32_16x16x32_bf16 v[96:99], a[112:115], v[144:147], v[96:99]\n\t"
      "ds_read_b64_tr_b16 a[134:135], v214 offset:12360\n\t"
      "ds_read_b64_tr_b16 a[138:139], v214 offset:12416\n\t"
      "v_mfma_f32_16x16x32_bf16 v[100:103], a[116:119], v[144:147], v[100:103]\n\t"
      "ds_read_b64_tr_b16 a[142:143], v214 offset:12424\n\t"
      "ds_read_b128 v[160:163], v198 offset:12544\n\t"
      "s_waitcnt lgkmcnt(8)\n\t"
      "v_mfma_f32_16x16x32_bf16 v[104:107], a[96:99], v[156:159], v[104:107]\n\t"
      "s_add_u32 m0, s41, 0x12000\n\t"
      "s_mov_b32 s51, 0x30c00\n\t"
      "buffer_load_dwordx4 v211, s[16:19], s51 offen lds\n\t"
      "v_mfma_f32_16x16x32_bf16 v[108:111], a[100:103], v[156:159], v[108:111]\n\t"
      "buffer_load_dwordx4 v212, s[16:19], s51 offen offset:1024 lds\n\t"
      "v_mfma_f32_16x16x32_bf16 v[112:115], a[104:107], v[156:159], v[112:115]\n\t"
      "buffer_load_dwordx4 v213, s[16:19], s51 offen offset:2048 lds\n\t"
      "s_waitcnt vmcnt(15)\n\t"
      "ds_read_b64_tr_b16 a[144:145], v214 offset:24576\n\t"
      "v_mfma_f32_16x16x32_bf16 v[116:119], a[108:111], v[156:159], v[116:119]\n\t"
      "ds_read_b64_tr_b16 a[148:149], v214 offset:24584\n\t"
      "ds_read_b64_tr_b16 a[152:153], v214 offset:24640\n\t"
      "v_mfma_f32_16x16x32_bf16 v[120:123], a[112:115], v[156:159], v[120:123]\n\t"
      "ds_read_b64_tr_b16 a[156:157], v214 offset:24648\n\t"
      "ds_read_b64_tr_b16 a[160:161], v214 offset:24704\n\t"
      "v_mfma_f32_16x16x32_bf16 v[132:135], a[116:119], v[156:159], v[132:135]\n\t"
      "ds_read_b64_tr_b16 a[164:165], v214 offset:24712\n\t"
      "ds_read_b128 v[140:143], v199 offset:256\n\t"
      "s_waitcnt lgkmcnt(8)\n\t"
      "v_mfma_f32_16x16x32_bf16 v[80:83], a[120:123], v[148:151], v[80:83]\n\t"
      "s_add_u32 m0, s41, 0x15000\n\t"
      "s_mov_b32 s51, 0x36000\n\t"
      "buffer_load_dwordx4 v211, s[16:19], s51 offen lds\n\t"
      "v_mfma_f32_16x16x32_bf16 v[84:87], a[124:127], v[148:151], v[84:87]\n\t"
      "buffer_load_dwordx4 v212, s[16:19], s51 offen offset:1024 lds\n\t"
      "v_mfma_f32_16x16x32_bf16 v[88:91], a[128:131], v[148:151], v[88:91]\n\t"
      "buffer_load_dwordx4 v213, s[16:19], s51 offen offset:2048 lds\n\t"
      "s_waitcnt vmcnt(15)\n\t"
      "ds_read_b64_tr_b16 a[146:147], v214 offset:36864\n\t"
      "v_mfma_f32_16x16x32_bf16 v[92:95], a[132:135], v[148:151], v[92:95]\n\t"
      "ds_read_b64_tr_b16 a[150:151], v214 offset:36872\n\t"
      "ds_read_b64_tr_b16 a[154:155], v214 offset:36928\n\t"
      "v_mfma_f32_16x16x32_bf16 v[96:99], a[136:139], v[148:151], v[96:99]\n\t"
      "ds_read_b64_tr_b16 a[158:159], v214 offset:36936\n\t"
      "ds_read_b64_tr_b16 a[162:163], v214 offset:36992\n\t"
      "v_mfma_f32_16x16x32_bf16 v[100:103], a[140:143], v[148:151], v[100:103]\n\t"
      "ds_read_b64_tr_b16 a[166:167], v214 offset:37000\n\t"
      "ds_read_b128 v[152:155], v199 offset:12544\n\t"
      "s_waitcnt lgkmcnt(8)\n\t"
      "v_mfma_f32_16x16x32_bf16 v[104:107], a[120:123], v[160:163], v[104:107]\n\t"
      "s_add_u32 m0, s41, 0x18000\n\t"
      "s_mov_b32 s51, 0x36c00\n\t"
      "buffer_load_dwordx4 v211, s[16:19], s51 offen lds\n\t"
      "v_mfma_f32_16x16x32_bf16 v[108:111], a[124:127], v[160:163], v[108:111]\n\t"
      "buffer_load_dwordx4 v212, s[16:19], s51 offen offset:1024 lds\n\t"
      "v_mfma_f32_16x16x32_bf16 v[112:115], a[128:131], v[160:163], v[112:115]\n\t"
      "buffer_load_dwordx4 v213, s[16:19], s51 offen offset:2048 lds\n\t"
      "s_waitcnt vmcnt(15)\n\t"
      "ds_read_b64_tr_b16 a[168:169], v214 offset:49152\n\t"
      "v_mfma_f32_16x16x32_bf16 v[116:119], a[132:135], v[160:163], v[116:119]\n\t"
      "ds_read_b64_tr_b16 a[172:173], v214 offset:49160\n\t"
      "ds_read_b64_tr_b16 a[176:177], v214 offset:49216\n\t"
      "v_mfma_f32_16x16x32_bf16 v[120:123], a[136:139], v[160:163], v[120:123]\n\t"
      "ds_read_b64_tr_b16 a[180:181], v214 offset:49224\n\t"
      "ds_read_b64_tr_b16 a[184:185], v214 offset:49280\n\t"
      "v_mfma_f32_16x16x32_bf16 v[132:135], a[140:143], v[160:163], v[132:135]\n\t"
      "ds_read_b64_tr_b16 a[188:189], v214 offset:49288\n\t"
      "ds_read_b128 v[144:147], v196 offset:256\n\t"
      "s_waitcnt lgkmcnt(8)\n\t"
      "v_mfma_f32_16x16x32_bf16 v[80:83], a[144:147], v[140:143], v[80:83]\n\t"
      "s_add_u32 m0, s41, 0x1b000\n\t"
      "s_mov_b32 s51, 0x3c000\n\t"
      "buffer_load_dwordx4 v211, s[16:19], s51 offen lds\n\t"
      "v_mfma_f32_16x16x32_bf16 v[84:87], a[148:151], v[140:143], v[84:87]\n\t"
      "buffer_load_dwordx4 v212, s[16:19], s51 offen offset:1024 lds\n\t"
      "v_mfma_f32_16x16x32_bf16 v[88:91], a[152:155], v[140:143], v[88:91]\n\t"
      "buffer_load_dwordx4 v213, s[16:19], s51 offen offset:2048 lds\n\t"
      "s_waitcnt vmcnt(15)\n\t"
      "ds_read_b64_tr_b16 a[170:171], v221 offset:0\n\t"
      "v_mfma_f32_16x16x32_bf16 v[92:95], a[156:159], v[140:143], v[92:95]\n\t"
      "ds_read_b64_tr_b16 a[174:175], v221 offset:8\n\t"
      "ds_read_b64_tr_b16 a[178:179], v221 offset:64\n\t"
      "v_mfma_f32_16x16x32_bf16 v[96:99], a[160:163], v[140:143], v[96:99]\n\t"
      "ds_read_b64_tr_b16 a[182:183], v221 offset:72\n\t"
      "ds_read_b64_tr_b16 a[186:187], v221 offset:128\n\t"
      "v_mfma_f32_16x16x32_bf16 v[100:103], a[164:167], v[140:143], v[100:103]\n\t"
      "ds_read_b64_tr_b16 a[190:191], v221 offset:136\n\t"
      "ds_read_b128 v[156:159], v196 offset:12544\n\t"
      "s_waitcnt lgkmcnt(8)\n\t"
      "v_mfma_f32_16x16x32_bf16 v[104:107], a[144:147], v[152:155], v[104:107]\n\t"
      "s_add_u32 m0, s41, 0x1e000\n\t"
      "s_mov_b32 s51, 0x3cc00\n\t"
      "buffer_load_dwordx4 v211, s[16:19], s51 offen lds\n\t"
      "v_mfma_f32_16x16x32_bf16 v[108:111], a[148:151], v[152:155], v[108:111]\n\t"
      "buffer_load_dwordx4 v212, s[16:19], s51 offen offset:1024 lds\n\t"
      "v_mfma_f32_16x16x32_bf16 v[112:115], a[152:155], v[152:155], v[112:115]\n\t"
      "buffer_load_dwordx4 v213, s[16:19], s51 offen offset:2048 lds\n\t"
      "s_waitcnt vmcnt(15)\n\t"
      "ds_read_b64_tr_b16 a[192:193], v221 offset:12288\n\t"
      "v_mfma_f32_16x16x32_bf16 v[116:119], a[156:159], v[152:155], v[116:119]\n\t"
      "ds_read_b64_tr_b16 a[196:197], v221 offset:12296\n\t"
      "ds_read_b64_tr_b16 a[200:201], v221 offset:12352\n\t"
      "v_mfma_f32_16x16x32_bf16 v[120:123], a[160:163], v[152:155], v[120:123]\n\t"
      "ds_read_b64_tr_b16 a[204:205], v221 offset:12360\n\t"
      "ds_read_b64_tr_b16 a[208:209], v221 offset:12416\n\t"
      "v_mfma_f32_16x16x32_bf16 v[132:135], a[164:167], v[152:155], v[132:135]\n\t"
      "ds_read_b64_tr_b16 a[212:213], v221 offset:12424\n\t"
      "ds_read_b128 v[148:151], v198 offset:512\n\t"
      "s_waitcnt lgkmcnt(8)\n\t"
      "v_mfma_f32_16x16x32_bf16 v[80:83], a[168:171], v[144:147], v[80:83]\n\t"
      "s_add_u32 m0, s41, 0x21000\n\t"
      "s_mov_b32 s51, 0x42000\n\t"
      "buffer_load_dwordx4 v211, s[16:19], s51 offen lds\n\t"
      "v_mfma_f32_16x16x32_bf16 v[84:87], a[172:175], v[144:147], v[84:87]\n\t"
      "buffer_load_dwordx4 v212, s[16:19], s51 offen offset:1024 lds\n\t"
      "v_mfma_f32_16x16x32_bf16 v[88:91], a[176:179], v[144:147], v[88:91]\n\t"
      "buffer_load_dwordx4 v213, s[16:19], s51 offen offset:2048 lds\n\t"
      "s_waitcnt vmcnt(15)\n\t"
      "ds_read_b64_tr_b16 a[194:195], v214 offset:0\n\t"
      "v_mfma_f32_16x16x32_bf16 v[92:95], a[180:183], v[144:147], v[92:95]\n\t"
      "ds_read_b64_tr_b16 a[198:199], v214 offset:8\n\t"
      "ds_read_b64_tr_b16 a[202:203], v214 offset:64\n\t"
      "v_mfma_f32_16x16x32_bf16 v[96:99], a[184:187], v[144:147], v[96:99]\n\t"
      "ds_read_b64_tr_b16 a[206:207], v214 offset:72\n\t"
      "ds_read_b64_tr_b16 a[210:211], v214 offset:128\n\t"
      "v_mfma_f32_16x16x32_bf16 v[100:103], a[188:191], v[144:147], v[100:103]\n\t"
      "ds_read_b64_tr_b16 a[214:215], v214 offset:136\n\t"
      "ds_read_b128 v[160:163], v198 offset:12800\n\t"
      "s_waitcnt lgkmcnt(8)\n\t"
      "v_mfma_f32_16x16x32_bf16 v[104:107], a[168:171], v[156:159], v[104:107]\n\t"
      "s_add_u32 m0, s41, 0x24000\n\t"
      "s_mov_b32 s51, 0x42c00\n\t"
      "buffer_load_dwordx4 v211, s[16:19], s51 offen lds\n\t"
      "v_mfma_f32_16x16x32_bf16 v[108:111], a[172:175], v[156:159], v[108:111]\n\t"
      "buffer_load_dwordx4 v212, s[16:19], s51 offen offset:1024 lds\n\t"
      "v_mfma_f32_16x16x32_bf16 v[112:115], a[176:179], v[156:159], v[112:115]\n\t"
      "buffer_load_dwordx4 v213, s[16:19], s51 offen offset:2048 lds\n\t"
      "s_waitcnt vmcnt(15)\n\t"
      "ds_read_b64_tr_b16 a[216:217], v214 offset:12288\n\t"
      "v_mfma_f32_16x16x32_bf16 v[116:119], a[180:183], v[156:159], v[116:119]\n\t"
      "ds_read_b64_tr_b16 a[220:221], v214 offset:12296\n\t"
      "ds_read_b64_tr_b16 a[224:225], v214 offset:12352\n\t"
      "v_mfma_f32_16x16x32_bf16 v[120:123], a[184:187], v[156:159], v[120:123]\n\t"
      "ds_read_b64_tr_b16 a[228:229], v214 offset:12360\n\t"
      "ds_read_b64_tr_b16 a[232:233], v214 offset:12416\n\t"
      "v_mfma_f32_16x16x32_bf16 v[132:135], a[188:191], v[156:159], v[132:135]\n\t"
      "ds_read_b64_tr_b16 a[236:237], v214 offset:12424\n\t"
      "ds_read_b128 v[140:143], v199 offset:512\n\t"
      "s_waitcnt lgkmcnt(8)\n\t"
      "v_mfma_f32_16x16x32_bf16 v[80:83], a[192:195], v[148:151], v[80:83]\n\t"
      "s_add_u32 m0, s41, 0x6000\n\t"
      "s_add_u32 s51, s40, 0x600000\n\t"
      "buffer_load_dwordx4 v208, s[20:23], s51 offen lds\n\t"
      "v_mfma_f32_16x16x32_bf16 v[84:87], a[196:199], v[148:151], v[84:87]\n\t"
      "buffer_load_dwordx4 v208, s[20:23], s51 offen offset:1024 lds\n\t"
      "v_mfma_f32_16x16x32_bf16 v[88:91], a[200:203], v[148:151], v[88:91]\n\t"
      "buffer_load_dwordx4 v208, s[20:23], s51 offen offset:2048 lds\n\t"
      "s_waitcnt vmcnt(15)\n\t"
      "ds_read_b64_tr_b16 a[218:219], v214 offset:24576\n\t"
      "v_mfma_f32_16x16x32_bf16 v[92:95], a[204:207], v[148:151], v[92:95]\n\t"
      "ds_read_b64_tr_b16 a[222:223], v214 offset:24584\n\t"
      "ds_read_b64_tr_b16 a[226:227], v214 offset:24640\n\t"
      "v_mfma_f32_16x16x32_bf16 v[96:99], a[208:211], v[148:151], v[96:99]\n\t"
      "ds_read_b64_tr_b16 a[230:231], v214 offset:24648\n\t"
      "ds_read_b64_tr_b16 a[234:235], v214 offset:24704\n\t"
      "v_mfma_f32_16x16x32_bf16 v[100:103], a[212:215], v[148:151], v[100:103]\n\t"
      "ds_read_b64_tr_b16 a[238:239], v214 offset:24712\n\t"
      "ds_read_b128 v[152:155], v199 offset:12800\n\t"
      "s_waitcnt lgkmcnt(14)\n\t"
      "v_mfma_f32_16x16x32_bf16 v[104:107], a[192:195], v[160:163], v[104:107]\n\t"
      "s_add_u32 m0, s41, 0x9000\n\t"
      "s_add_u32 s51, s40, 0x600000\n\t"
      "buffer_load_dwordx4 v209, s[20:23], s51 offen lds\n\t"
      "v_mfma_f32_16x16x32_bf16 v[108:111], a[196:199], v[160:163], v[108:111]\n\t"
      "buffer_load_dwordx4 v209, s[20:23], s51 offen offset:1024 lds\n\t"
      "v_mfma_f32_16x16x32_bf16 v[112:115], a[200:203], v[160:163], v[112:115]\n\t"
      "buffer_load_dwordx4 v209, s[20:23], s51 offen offset:2048 lds\n\t"
      "s_waitcnt vmcnt(15)\n\t"
      "ds_read_b64_tr_b16 a[240:241], v214 offset:36864\n\t"
      "v_mfma_f32_16x16x32_bf16 v[116:119], a[204:207], v[160:163], v[116:119]\n\t"
      "ds_read_b64_tr_b16 a[244:245], v214 offset:36872\n\t"
      "ds_read_b64_tr_b16 a[248:249], v214 offset:36928\n\t"
      "v_mfma_f32_16x16x32_bf16 v[120:123], a[208:211], v[160:163], v[120:123]\n\t"
      "ds_read_b64_tr_b16 a[252:253], v214 offset:36936\n\t"
      "ds_read_b64_tr_b16 v[164:165], v214 offset:36992\n\t"
      "v_mfma_f32_16x16x32_bf16 v[132:135], a[212:215], v[160:163], v[132:135]\n\t"
      "ds_read_b64_tr_b16 v[168:169], v214 offset:37000\n\t"
      "ds_read_b128 v[144:147], v196 offset:512\n\t"
      "s_waitcnt lgkmcnt(8)\n\t"
      "v_mfma_f32_16x16x32_bf16 v[80:83], a[216:219], v[140:143], v[80:83]\n\t"
      "s_add_u32 m0, s41, 0xc000\n\t"
      "s_add_u32 s51, s40, 0xc00000\n\t"
      "buffer_load_dwordx4 v208, s[20:23], s51 offen lds\n\t"
      "v_mfma_f32_16x16x32_bf16 v[84:87], a[220:223], v[140:143], v[84:87]\n\t"
      "buffer_load_dwordx4 v208, s[20:23], s51 offen offset:1024 lds\n\t"
      "v_mfma_f32_16x16x32_bf16 v[88:91], a[224:227], v[140:143], v[88:91]\n\t"
      "buffer_load_dwordx4 v208, s[20:23], s51 offen offset:2048 lds\n\t"
      "s_waitcnt vmcnt(15)\n\t"
      "ds_read_b64_tr_b16 a[242:243], v214 offset:49152\n\t"
      "v_mfma_f32_16x16x32_bf16 v[92:95], a[228:231], v[140:143], v[92:95]\n\t"
      "ds_read_b64_tr_b16 a[246:247], v214 offset:49160\n\t"
      "ds_read_b64_tr_b16 a[250:251], v214 offset:49216\n\t"
      "v_mfma_f32_16x16x32_bf16 v[96:99], a[232:235], v[140:143], v[96:99]\n\t"
      "ds_read_b64_tr_b16 a[254:255], v214 offset:49224\n\t"
      "ds_read_b64_tr_b16 v[166:167], v214 offset:49280\n\t"
      "v_mfma_f32_16x16x32_bf16 v[100:103], a[236:239], v[140:143], v[100:103]\n\t"
      "ds_read_b64_tr_b16 v[170:171], v214 offset:49288\n\t"
      "ds_read_b128 v[156:159], v196 offset:12800\n\t"
      "s_waitcnt lgkmcnt(14)\n\t"
      "v_mfma_f32_16x16x32_bf16 v[104:107], a[216:219], v[152:155], v[104:107]\n\t"
      "s_add_u32 m0, s41, 0xf000\n\t"
      "s_add_u32 s51, s40, 0xc00000\n\t"
      "buffer_load_dwordx4 v209, s[20:23], s51 offen lds\n\t"
      "v_mfma_f32_16x16x32_bf16 v[108:111], a[220:223], v[152:155], v[108:111]\n\t"
      "buffer_load_dwordx4 v209, s[20:23], s51 offen offset:1024 lds\n\t"
      "v_mfma_f32_16x16x32_bf16 v[112:115], a[224:227], v[152:155], v[112:115]\n\t"
      "buffer_load_dwordx4 v209, s[20:23], s51 offen offset:2048 lds\n\t"
      "s_waitcnt vmcnt(15)\n\t"
      "ds_read_b64_tr_b16 v[172:173], v221 offset:0\n\t"
      "v_mfma_f32_16x16x32_bf16 v[116:119], a[228:231], v[152:155], v[116:119]\n\t"
      "ds_read_b64_tr_b16 v[176:177], v221 offset:8\n\t"
      "ds_read_b64_tr_b16 v[180:181], v221 offset:64\n\t"
      "ds_read_b64_tr_b16 v[184:185], v221 offset:72\n\t"
      "v_mfma_f32_16x16x32_bf16 v[120:123], a[232:235], v[152:155], v[120:123]\n\t"
      "ds_read_b64_tr_b16 v[188:189], v221 offset:128\n\t"
      "ds_read_b64_tr_b16 v[192:193], v221 offset:136\n\t"
      "v_mfma_f32_16x16x32_bf16 v[132:135], a[236:239], v[152:155], v[132:135]\n\t"
      "ds_read_b128 v[148:151], v197 offset:512\n\t"
      "ds_read_b128 v[160:163], v197 offset:12800\n\t"
      "s_waitcnt lgkmcnt(9)\n\t"
      "v_mfma_f32_16x16x32_bf16 v[80:83], a[240:243], v[144:147], v[80:83]\n\t"
      "buffer_load_dwordx2 v[240:241], v245, s[24:27], 0 offen sc1\n\t"
      "v_mfma_f32_16x16x32_bf16 v[84:87], a[244:247], v[144:147], v[84:87]\n\t"
      "s_cmp_eq_u32 s41, s43\n\t"
      "s_cbranch_scc0 .Lc3_%=\n\t"
      "buffer_load_dword v254, v253, s[24:27], 0 offen sc1\n\t"
      ".Lc3_%=:\n\t"
      "v_mfma_f32_16x16x32_bf16 v[88:91], a[248:251], v[144:147], v[88:91]\n\t"
      "s_waitcnt vmcnt(13)\n\t"
      "ds_read_b64_tr_b16 v[174:175], v221 offset:12288\n\t"
      "v_mfma_f32_16x16x32_bf16 v[92:95], a[252:255], v[144:147], v[92:95]\n\t"
      "ds_read_b64_tr_b16 v[178:179], v221 offset:12296\n\t"
      "ds_read_b64_tr_b16 v[182:183], v221 offset:12352\n\t"
      "v_mfma_f32_16x16x32_bf16 v[96:99], v[164:167], v[144:147], v[96:99]\n\t"
      "ds_read_b64_tr_b16 v[186:187], v221 offset:12360\n\t"
      "ds_read_b64_tr_b16 v[190:191], v221 offset:12416\n\t"
      "v_mfma_f32_16x16x32_bf16 v[100:103], v[168:171], v[144:147], v[100:103]\n\t"
      "ds_read_b64_tr_b16 v[194:195], v221 offset:12424\n\t"
      "s_memrealtime s[80:81]\n\t"
      "v_add_u32_e32 v196, s46, v200\n\t"
      "v_add_u32_e32 v197, s46, v201\n\t"
      "v_add_u32_e32 v198, s46, v202\n\t"
      "v_add_u32_e32 v199, s46, v203\n\t"
      "s_waitcnt vmcnt(10)\n\t"
      "s_waitcnt lgkmcnt(6)\n\t"
      "s_barrier\n\t"
      "v_mfma_f32_16x16x32_bf16 v[104:107], a[240:243], v[156:159], v[104:107]\n\t"
      "ds_read_b128 v[32:35], v196 offset:0\n\t"
      "v_mfma_f32_16x16x32_bf16 v[108:111], a[244:247], v[156:159], v[108:111]\n\t"
      "ds_read_b128 v[36:39], v197 offset:0\n\t"
      "v_mfma_f32_16x16x32_bf16 v[112:115], a[248:251], v[156:159], v[112:115]\n\t"
      "ds_read_b128 v[40:43], v198 offset:0\n\t"
      "v_mfma_f32_16x16x32_bf16 v[116:119], a[252:255], v[156:159], v[116:119]\n\t"
      "ds_read_b128 v[44:47], v199 offset:0\n\t"
      "v_mfma_f32_16x16x32_bf16 v[120:123], v[164:167], v[156:159], v[120:123]\n\t"
      "ds_read_b128 v[48:51], v197 offset:256\n\t"
      "v_mfma_f32_16x16x32_bf16 v[132:135], v[168:171], v[156:159], v[132:135]\n\t"
      "s_waitcnt lgkmcnt(5)\n\t"
      "s_mov_b32 s87, 75\n\t"
      "s_mov_b32 s86, 80\n\t"
      "s_mov_b32 s84, 100\n\t"
      "v_mfma_f32_16x16x32_bf16 v[80:83], v[172:175], v[148:151], v[80:83]\n\t"
      "ds_read_b128 v[52:55], v198 offset:256\n\t"
      "v_mfma_f32_16x16x32_bf16 v[84:87], v[176:179], v[148:151], v[84:87]\n\t"
      "ds_read_b128 v[56:59], v199 offset:256\n\t"
      "v_mfma_f32_16x16x32_bf16 v[88:91], v[180:183], v[148:151], v[88:91]\n\t"
      "ds_read_b128 v[60:63], v196 offset:256\n\t"
      "v_mfma_f32_16x16x32_bf16 v[92:95], v[184:187], v[148:151], v[92:95]\n\t"
      "ds_read_b128 v[64:67], v198 offset:512\n\t"
      "v_mfma_f32_16x16x32_bf16 v[96:99], v[188:191], v[148:151], v[96:99]\n\t"
      "ds_read_b128 v[68:71], v199 offset:512\n\t"
      "v_mfma_f32_16x16x32_bf16 v[100:103], v[192:195], v[148:151], v[100:103]\n\t"
      "ds_read_b128 v[72:75], v196 offset:512\n\t"
      "v_mfma_f32_16x16x32_bf16 v[104:107], v[172:175], v[160:163], v[104:107]\n\t"
      "ds_read_b128 v[76:79], v197 offset:512\n\t"
      "v_mfma_f32_16x16x32_bf16 v[108:111], v[176:179], v[160:163], v[108:111]\n\t"
      "v_mfma_f32_16x16x32_bf16 v[112:115], v[180:183], v[160:163], v[112:115]\n\t"
      "v_mfma_f32_16x16x32_bf16 v[116:119], v[184:187], v[160:163], v[116:119]\n\t"
      "v_mfma_f32_16x16x32_bf16 v[120:123], v[188:191], v[160:163], v[120:123]\n\t"
      "v_mfma_f32_16x16x32_bf16 v[132:135], v[192:195], v[160:163], v[132:135]\n\t"
      ".Lp2_%=:\n\t"
      "s_cbranch_vccnz .Lwsb_%=\n\t"
      "s_waitcnt lgkmcnt(8)\n\t"
      "v_mfma_f32_16x16x32_bf16 v[128:131], a[0:3], v[32:35], v[8:11]\n\t"
      "v_cvt_pk_bf16_f32 v80, v80, v81\n\t"
      "v_cvt_pk_bf16_f32 v81, v82, v83\n\t"
      "v_mfma_f32_16x16x32_bf16 v[128:131], a[24:27], v[36:39], v[128:131]\n\t"
      "v_cvt_pk_bf16_f32 v136, v120, v121\n\t"
      "v_cvt_pk_bf16_f32 v137, v122, v123\n\t"
      "v_mfma_f32_16x16x32_bf16 v[128:131], a[48:51], v[40:43], v[128:131]\n\t"
      "v_cvt_pk_bf16_f32 v138, v132, v133\n\t"
      "v_mfma_f32_16x16x32_bf16 v[128:131], a[72:75], v[44:47], v[128:131]\n\t"
      "v_cvt_pk_bf16_f32 v139, v134, v135\n\t"
      "s_waitcnt lgkmcnt(4)\n\t"
      "v_mfma_f32_16x16x32_bf16 v[128:131], a[96:99], v[48:51], v[128:131]\n\t"
      "ds_write_b128 v219, v[136:139] offset:12288\n\t"
      "v_mfma_f32_16x16x32_bf16 v[128:131], a[120:123], v[52:55], v[128:131]\n\t"
      "v_cvt_pk_bf16_f32 v82, v84, v85\n\t"
      "v_cvt_pk_bf16_f32 v83, v86, v87\n\t"
      "v_mfma_f32_16x16x32_bf16 v[128:131], a[144:147], v[56:59], v[128:131]\n\t"
      "v_cvt_pk_bf16_f32 v88, v88, v89\n\t"
      "v_cvt_pk_bf16_f32 v89, v90, v91\n\t"
      "v_mfma_f32_16x16x32_bf16 v[128:131], a[168:171], v[60:63], v[128:131]\n\t"
      "ds_write_b128 v217, v[80:83] offset:0\n\t"
      "v_cvt_pk_bf16_f32 v90, v92, v93\n\t"
      "s_waitcnt lgkmcnt(2)\n\t"
      "v_mfma_f32_16x16x32_bf16 v[128:131], a[192:195], v[64:67], v[128:131]\n\t"
      "v_cvt_pk_bf16_f32 v91, v94, v95\n\t"
      "v_cvt_pk_bf16_f32 v96, v96, v97\n\t"
      "v_mfma_f32_16x16x32_bf16 v[128:131], a[216:219], v[68:71], v[128:131]\n\t"
      "ds_write_b128 v218, v[88:91] offset:0\n\t"
      "v_cvt_pk_bf16_f32 v97, v98, v99\n\t"
      "v_mfma_f32_16x16x32_bf16 v[128:131], a[240:243], v[72:75], v[128:131]\n\t"
      "v_cvt_pk_bf16_f32 v98, v100, v101\n\t"
      "v_cvt_pk_bf16_f32 v99, v102, v103\n\t"
      "v_mfma_f32_16x16x32_bf16 v[128:131], v[172:175], v[76:79], v[128:131]\n\t"
      "v_cvt_pk_bf16_f32 v104, v104, v105\n\t"
      "v_cvt_pk_bf16_f32 v105, v106, v107\n\t"
      "v_mfma_f32_16x16x32_bf16 v[132:135], a[4:7], v[32:35], v[12:15]\n\t"
      "ds_write_b128 v219, v[96:99] offset:0\n\t"
      "v_cvt_pk_bf16_f32 v106, v108, v109\n\t"
      "v_mfma_f32_16x16x32_bf16 v[132:135], a[28:31], v[36:39], v[132:135]\n\t"
      "v_cvt_pk_bf16_f32 v107, v110, v111\n\t"
      "v_cvt_pk_bf16_f32 v112, v112, v113\n\t"
      "v_mfma_f32_16x16x32_bf16 v[132:135], a[52:55], v[40:43], v[132:135]\n\t"
      "v_cvt_pk_bf16_f32 v136, v128, v129\n\t"
      "v_mfma_f32_16x16x32_bf16 v[132:135], a[76:79], v[44:47], v[132:135]\n\t"
      "v_cvt_pk_bf16_f32 v137, v130, v131\n\t"
      "v_mfma_f32_16x16x32_bf16 v[132:135], a[100:103], v[48:51], v[132:135]\n\t"
      "ds_write_b128 v217, v[104:107] offset:12288\n\t"
      "v_cvt_pk_bf16_f32 v113, v114, v115\n\t"
      "s_add_u32 s48, s47, 0x6000\n\t"
      "v_mfma_f32_16x16x32_bf16 v[132:135], a[124:127], v[52:55], v[132:135]\n\t"
      "v_cvt_pk_bf16_f32 v114, v116, v117\n\t"
      "v_cvt_pk_bf16_f32 v115, v118, v119\n\t"
      "s_cmp_eq_u32 s48, s50\n\t"
      "s_cselect_b32 s48, s41, s48\n\t"
      "v_mfma_f32_16x16x32_bf16 v[132:135], a[148:151], v[56:59], v[132:135]\n\t"
      "ds_write_b128 v218, v[112:115] offset:12288\n\t"
      "v_mfma_f32_16x16x32_bf16 v[132:135], a[172:175], v[60:63], v[132:135]\n\t"
      "v_add_u32_e32 v217, s46, v204\n\t"
      "v_mfma_f32_16x16x32_bf16 v[132:135], a[196:199], v[64:67], v[132:135]\n\t"
      "v_add_u32_e32 v218, s46, v205\n\t"
      "v_mfma_f32_16x16x32_bf16 v[132:135], a[220:223], v[68:71], v[132:135]\n\t"
      "v_readlane_b32 s73, v248, s85\n\t"
      "v_add_u32_e32 v219, s46, v206\n\t"
      "v_mfma_f32_16x16x32_bf16 v[132:135], a[244:247], v[72:75], v[132:135]\n\t"
      "s_mov_b32 m0, s45\n\t"
      "v_mfma_f32_16x16x32_bf16 v[132:135], v[176:179], v[76:79], v[132:135]\n\t"
      "s_waitcnt lgkmcnt(0)\n\t"
      "s_barrier\n\t"
      "v_mfma_f32_16x16x32_bf16 v[128:131], a[8:11], v[32:35], v[16:19]\n\t"
      "s_mov_b32 s82, s80\n\t"
      "v_mfma_f32_16x16x32_bf16 v[128:131], a[32:35], v[36:39], v[128:131]\n\t"
      "s_memrealtime s[80:81]\n\t"
      "v_mfma_f32_16x16x32_bf16 v[128:131], a[56:59], v[40:43], v[128:131]\n\t"
      "v_cvt_pk_bf16_f32 v138, v132, v133\n\t"
      "v_mfma_f32_16x16x32_bf16 v[128:131], a[80:83], v[44:47], v[128:131]\n\t"
      "v_cvt_pk_bf16_f32 v139, v134, v135\n\t"
      "v_mfma_f32_16x16x32_bf16 v[128:131], a[104:107], v[48:51], v[128:131]\n\t"
      "ds_write_b128 v217, v[136:139] offset:0\n\t"
      "v_mfma_f32_16x16x32_bf16 v[128:131], a[128:131], v[52:55], v[128:131]\n\t"
      "buffer_load_dwordx4 v208, s[20:23], s73 offen lds\n\t"
      "v_mfma_f32_16x16x32_bf16 v[128:131], a[152:155], v[56:59], v[128:131]\n\t"
      "v_mfma_f32_16x16x32_bf16 v[128:131], a[176:179], v[60:63], v[128:131]\n\t"
      "v_mfma_f32_16x16x32_bf16 v[128:131], a[200:203], v[64:67], v[128:131]\n\t"
      "v_mfma_f32_16x16x32_bf16 v[128:131], a[224:227], v[68:71], v[128:131]\n\t"
      "v_mfma_f32_16x16x32_bf16 v[128:131], a[248:251], v[72:75], v[128:131]\n\t"
      "v_mfma_f32_16x16x32_bf16 v[128:131], v[180:183], v[76:79], v[128:131]\n\t"
      "v_mfma_f32_16x16x32_bf16 v[132:135], a[12:15], v[32:35], v[20:23]\n\t"
      "v_mfma_f32_16x16x32_bf16 v[132:135], a[36:39], v[36:39], v[132:135]\n\t"
      "buffer_load_dwordx4 v208, s[20:23], s73 offen offset:1024 lds\n\t"
      "v_mfma_f32_16x16x32_bf16 v[132:135], a[60:63], v[40:43], v[132:135]\n\t"
      "v_cvt_pk_bf16_f32 v136, v128, v129\n\t"
      "v_mfma_f32_16x16x32_bf16 v[132:135], a[84:87], v[44:47], v[132:135]\n\t"
      "v_cvt_pk_bf16_f32 v137, v130, v131\n\t"
      "v_mfma_f32_16x16x32_bf16 v[132:135], a[108:111], v[48:51], v[132:135]\n\t"
      "v_mfma_f32_16x16x32_bf16 v[132:135], a[132:135], v[52:55], v[132:135]\n\t"
      "v_mfma_f32_16x16x32_bf16 v[132:135], a[156:159], v[56:59], v[132:135]\n\t"
      "v_mfma_f32_16x16x32_bf16 v[132:135], a[180:183], v[60:63], v[132:135]\n\t"
      "v_mfma_f32_16x16x32_bf16 v[132:135], a[204:207], v[64:67], v[132:135]\n\t"
      "v_mfma_f32_16x16x32_bf16 v[132:135], a[228:231], v[68:71], v[132:135]\n\t"
      "buffer_load_dwordx4 v208, s[20:23], s73 offen offset:2048 lds\n\t"
      "v_mfma_f32_16x16x32_bf16 v[132:135], a[252:255], v[72:75], v[132:135]\n\t"
      "v_readlane_b32 s73, v249, s85\n\t"
      "s_add_u32 m0, s45, 0x3000\n\t"
      "v_mfma_f32_16x16x32_bf16 v[132:135], v[184:187], v[76:79], v[132:135]\n\t"
      "s_waitcnt vmcnt(10)\n\t"
      "s_barrier\n\t"
      "v_mfma_f32_16x16x32_bf16 v[128:131], a[16:19], v[32:35], v[24:27]\n\t"
      "ds_read_b128 v[80:83], v196 offset:12288\n\t"
      "v_mfma_f32_16x16x32_bf16 v[128:131], a[40:43], v[36:39], v[128:131]\n\t"
      "v_mfma_f32_16x16x32_bf16 v[128:131], a[64:67], v[40:43], v[128:131]\n\t"
      "ds_read_b128 v[84:87], v197 offset:12288\n\t"
      "v_cvt_pk_bf16_f32 v138, v132, v133\n\t"
      "v_mfma_f32_16x16x32_bf16 v[128:131], a[88:91], v[44:47], v[128:131]\n\t"
      "ds_read_b128 v[88:91], v198 offset:12288\n\t"
      "v_cvt_pk_bf16_f32 v139, v134, v135\n\t"
      "v_mfma_f32_16x16x32_bf16 v[128:131], a[112:115], v[48:51], v[128:131]\n\t"
      "ds_write_b128 v218, v[136:139] offset:0\n\t"
      "v_mfma_f32_16x16x32_bf16 v[128:131], a[136:139], v[52:55], v[128:131]\n\t"
      "buffer_load_dwordx4 v209, s[20:23], s73 offen lds\n\t"
      "s_waitcnt vmcnt(10)\n\t"
      "s_cmp_eq_u32 s94, 0\n\t"
      "s_cbranch_scc0 .Lwgvw_0_%=\n\t"
      "s_waitcnt vmcnt(4)\n\t"
      ".Lwgvw_0_%=:\n\t"
      "v_subrev_u32_e32 v242, s63, v240\n\t"
      "v_xor_b32_e32 v241, s53, v241\n\t"
      "v_mfma_f32_16x16x32_bf16 v[128:131], a[160:163], v[56:59], v[128:131]\n\t"
      "ds_read_b128 v[92:95], v199 offset:12288\n\t"
      "v_lshrrev_b32_e32 v240, 13, v242\n\t"
      "v_or_b32_e32 v240, v240, v241\n\t"
      "v_mfma_f32_16x16x32_bf16 v[128:131], a[184:187], v[60:63], v[128:131]\n\t"
      "ds_read_b128 v[96:99], v197 offset:12544\n\t"
      "v_mfma_f32_16x16x32_bf16 v[128:131], a[208:211], v[64:67], v[128:131]\n\t"
      "v_cmp_ne_u32_e32 vcc, 0, v240\n\t"
      "v_and_b32_e32 v242, 15, v242\n\t"
      "v_mfma_f32_16x16x32_bf16 v[128:131], a[232:235], v[68:71], v[128:131]\n\t"
      "v_mfma_f32_16x16x32_bf16 v[128:131], v[164:167], v[72:75], v[128:131]\n\t"
      "ds_read_b128 v[100:103], v198 offset:12544\n\t"
      "s_cbranch_vccnz .Lgs_%=\n\t"
      ".Lgr_%=:\n\t"
      "ds_write_b32 v246, v242\n\t"
      "v_mfma_f32_16x16x32_bf16 v[128:131], v[188:191], v[76:79], v[128:131]\n\t"
      "ds_read_b128 v[104:107], v199 offset:12544\n\t"
      "v_mfma_f32_16x16x32_bf16 v[132:135], a[20:23], v[32:35], v[28:31]\n\t"
      "ds_read_b128 v[108:111], v196 offset:12544\n\t"
      "v_mfma_f32_16x16x32_bf16 v[132:135], a[44:47], v[36:39], v[132:135]\n\t"
      "buffer_load_dwordx4 v209, s[20:23], s73 offen offset:1024 lds\n\t"
      "v_mfma_f32_16x16x32_bf16 v[132:135], a[68:71], v[40:43], v[132:135]\n\t"
      "ds_read_b128 v[112:115], v198 offset:12800\n\t"
      "v_cvt_pk_bf16_f32 v136, v128, v129\n\t"
      "v_add_u32_e32 v220, s46, v207\n\t"
      "v_mfma_f32_16x16x32_bf16 v[132:135], a[92:95], v[44:47], v[132:135]\n\t"
      "ds_read_b128 v[116:119], v199 offset:12800\n\t"
      "v_cvt_pk_bf16_f32 v137, v130, v131\n\t"
      "s_sub_u32 s84, s84, 4\n\t"
      "s_max_i32 s84, s84, s86\n\t"
      "v_mfma_f32_16x16x32_bf16 v[132:135], a[116:119], v[48:51], v[132:135]\n\t"
      "s_sub_u32 s83, s82, s80\n\t"
      "v_mfma_f32_16x16x32_bf16 v[132:135], a[140:143], v[52:55], v[132:135]\n\t"
      "s_max_i32 s83, s83, 0\n\t"
      "s_add_u32 s82, s80, s83\n\t"
      "v_mfma_f32_16x16x32_bf16 v[132:135], a[164:167], v[56:59], v[132:135]\n\t"
      "ds_read_b128 v[120:123], v196 offset:12800\n\t"
      "v_mfma_f32_16x16x32_bf16 v[132:135], a[188:191], v[60:63], v[132:135]\n\t"
      "ds_read_b128 v[124:127], v197 offset:12800\n\t"
      "v_mfma_f32_16x16x32_bf16 v[132:135], a[212:215], v[64:67], v[132:135]\n\t"
      "v_mfma_f32_16x16x32_bf16 v[132:135], a[236:239], v[68:71], v[132:135]\n\t"
      "s_add_u32 s82, s82, s84\n\t"
      "v_mfma_f32_16x16x32_bf16 v[132:135], v[168:171], v[72:75], v[132:135]\n\t"
      "s_add_u32 s44, s44, 1\n\t"
      "s_add_u32 s85, s85, 1\n\t"
      "v_mfma_f32_16x16x32_bf16 v[132:135], v[192:195], v[76:79], v[132:135]\n\t"
      "s_waitcnt lgkmcnt(9)\n\t"
      "v_mfma_f32_16x16x32_bf16 v[128:131], a[0:3], v[80:83], v[8:11]\n\t"
      "v_mfma_f32_16x16x32_bf16 v[128:131], a[24:27], v[84:87], v[128:131]\n\t"
      "v_readlane_b32 s72, v248, s44\n\t"
      "v_mfma_f32_16x16x32_bf16 v[128:131], a[48:51], v[88:91], v[128:131]\n\t"
      "v_cvt_pk_bf16_f32 v138, v132, v133\n\t"
      "v_mfma_f32_16x16x32_bf16 v[128:131], a[72:75], v[92:95], v[128:131]\n\t"
      "v_cvt_pk_bf16_f32 v139, v134, v135\n\t"
      "s_waitcnt lgkmcnt(4)\n\t"
      "v_mfma_f32_16x16x32_bf16 v[128:131], a[96:99], v[96:99], v[128:131]\n\t"
      "ds_write_b128 v219, v[136:139] offset:0\n\t"
      "v_mfma_f32_16x16x32_bf16 v[128:131], a[120:123], v[100:103], v[128:131]\n\t"
      "v_mfma_f32_16x16x32_bf16 v[128:131], a[144:147], v[104:107], v[128:131]\n\t"
      "buffer_load_dwordx4 v209, s[20:23], s73 offen offset:2048 lds\n\t"
      "v_mfma_f32_16x16x32_bf16 v[128:131], a[168:171], v[108:111], v[128:131]\n\t"
      "v_add_u32_e32 v196, s47, v200\n\t"
      "s_waitcnt lgkmcnt(1)\n\t"
      "v_mfma_f32_16x16x32_bf16 v[128:131], a[192:195], v[112:115], v[128:131]\n\t"
      "v_add_u32_e32 v197, s47, v201\n\t"
      "v_mfma_f32_16x16x32_bf16 v[128:131], a[216:219], v[116:119], v[128:131]\n\t"
      "v_add_u32_e32 v198, s47, v202\n\t"
      "v_mfma_f32_16x16x32_bf16 v[128:131], a[240:243], v[120:123], v[128:131]\n\t"
      "v_add_u32_e32 v199, s47, v203\n\t"
      "v_mfma_f32_16x16x32_bf16 v[128:131], v[172:175], v[124:127], v[128:131]\n\t"
      "s_cmp_gt_i32 s83, 1\n\t"
      "s_cbranch_scc1 .LslpP3_%=\n\t"
      ".LretP3_%=:\n\t"
      "s_cmp_eq_u32 s94, 1\n\t"
      "s_cbranch_scc0 .LwP3barv_1_%=\n\t"
      "s_waitcnt vmcnt(9)\n\t"
      ".LwP3barv_1_%=:\n\t"
      "s_waitcnt lgkmcnt(0)\n\t"
      "s_barrier\n\t"
      "v_mfma_f32_16x16x32_bf16 v[132:135], a[4:7], v[80:83], v[12:15]\n\t"
      "ds_read_b128 v[140:143], v220 offset:0\n\t"
      "v_mfma_f32_16x16x32_bf16 v[132:135], a[28:31], v[84:87], v[132:135]\n\t"
      "ds_read_b128 v[240:243], v247 offset:0\n\t"
      "v_mfma_f32_16x16x32_bf16 v[132:135], a[52:55], v[88:91], v[132:135]\n\t"
      "v_cvt_pk_bf16_f32 v136, v128, v129\n\t"
      "v_mfma_f32_16x16x32_bf16 v[132:135], a[76:79], v[92:95], v[132:135]\n\t"
      "ds_read_b128 v[32:35], v196 offset:0\n\t"
      "v_cvt_pk_bf16_f32 v137, v130, v131\n\t"
      "v_mfma_f32_16x16x32_bf16 v[132:135], a[100:103], v[96:99], v[132:135]\n\t"
      "v_readlane_b32 s68, v248, s94\n\t"
      "v_mfma_f32_16x16x32_bf16 v[132:135], a[124:127], v[100:103], v[132:135]\n\t"
      "ds_read_b128 v[152:155], v238 offset:0\n\t"
      "v_mfma_f32_16x16x32_bf16 v[132:135], a[148:151], v[104:107], v[132:135]\n\t"
      "ds_read_b128 v[36:39], v197 offset:0\n\t"
      "v_mfma_f32_16x16x32_bf16 v[132:135], a[172:175], v[108:111], v[132:135]\n\t"
      "v_mfma_f32_16x16x32_bf16 v[132:135], a[196:199], v[112:115], v[132:135]\n\t"
      "s_waitcnt lgkmcnt(3)\n\t"
      "v_add3_u32 v240, v240, v241, v242\n\t"
      "v_mfma_f32_16x16x32_bf16 v[132:135], a[220:223], v[116:119], v[132:135]\n\t"
      "ds_read_b128 v[40:43], v198 offset:0\n\t"
      "v_mfma_f32_16x16x32_bf16 v[132:135], a[244:247], v[120:123], v[132:135]\n\t"
      "v_add_u32_e32 v240, v240, v243\n\t"
      "v_mfma_f32_16x16x32_bf16 v[132:135], v[176:179], v[124:127], v[132:135]\n\t"
      "buffer_store_dwordx4 v[140:143], v208, s[28:31], s72 offen sc1\n\t"
      "v_mfma_f32_16x16x32_bf16 v[128:131], a[8:11], v[80:83], v[16:19]\n\t"
      "ds_read_b128 v[44:47], v199 offset:0\n\t"
      "v_mfma_f32_16x16x32_bf16 v[128:131], a[32:35], v[84:87], v[128:131]\n\t"
      "v_add_u32_dpp v241, v240, v240 quad_perm:[1,0,3,2] row_mask:0xf bank_mask:0xf\n\t"
      "s_memrealtime s[80:81]\n\t"
      "v_mfma_f32_16x16x32_bf16 v[128:131], a[56:59], v[88:91], v[128:131]\n\t"
      "v_cvt_pk_bf16_f32 v138, v132, v133\n\t"
      "v_mfma_f32_16x16x32_bf16 v[128:131], a[80:83], v[92:95], v[128:131]\n\t"
      "v_cvt_pk_bf16_f32 v139, v134, v135\n\t"
      "v_mfma_f32_16x16x32_bf16 v[128:131], a[104:107], v[96:99], v[128:131]\n\t"
      "ds_write_b128 v217, v[136:139] offset:12288\n\t"
      "v_add_u32_dpp v240, v241, v241 quad_perm:[2,3,0,1] row_mask:0xf bank_mask:0xf\n\t"
      "v_mfma_f32_16x16x32_bf16 v[128:131], a[128:131], v[100:103], v[128:131]\n\t"
      "s_mov_b32 m0, s46\n\t"
      "v_mfma_f32_16x16x32_bf16 v[128:131], a[152:155], v[104:107], v[128:131]\n\t"
      "s_waitcnt lgkmcnt(4)\n\t"
      "buffer_store_dwordx4 v[152:155], v208, s[28:31], s68 offen sc1\n\t"
      "v_cmp_le_u32_e64 s[76:77], 18, v240\n\t"
      "v_cmp_ge_u32_e64 s[78:79], 14, v240\n\t"
      "v_mfma_f32_16x16x32_bf16 v[128:131], a[176:179], v[108:111], v[128:131]\n\t"
      "ds_read_b128 v[48:51], v197 offset:256\n\t"
      "v_mfma_f32_16x16x32_bf16 v[128:131], a[200:203], v[112:115], v[128:131]\n\t"
      "v_cmp_lt_u32_e32 vcc, 63, v240\n\t"
      "s_and_b32 s65, s76, s77\n\t"
      "v_mfma_f32_16x16x32_bf16 v[128:131], a[224:227], v[116:119], v[128:131]\n\t"
      "ds_read_b128 v[52:55], v198 offset:256\n\t"
      "v_mfma_f32_16x16x32_bf16 v[128:131], a[248:251], v[120:123], v[128:131]\n\t"
      "ds_read_b128 v[156:159], v238 offset:1024\n\t"
      "s_and_b32 s78, s78, s79\n\t"
      "s_and_b32 s67, s65, 0x11111111\n\t"
      "v_mfma_f32_16x16x32_bf16 v[128:131], v[180:183], v[124:127], v[128:131]\n\t"
      "v_mfma_f32_16x16x32_bf16 v[132:135], a[12:15], v[80:83], v[20:23]\n\t"
      "ds_read_b128 v[56:59], v199 offset:256\n\t"
      "s_or_b32 s65, s65, s78\n\t"
      "s_cmp_lg_u64 vcc, 0\n\t"
      "s_cselect_b32 s65, 0, s65\n\t"
      "s_cselect_b32 s67, 0, s67\n\t"
      "v_mfma_f32_16x16x32_bf16 v[132:135], a[36:39], v[84:87], v[132:135]\n\t"
      "v_and_b32_e32 v240, s67, v252\n\t"
      "v_and_b32_e32 v241, s65, v252\n\t"
      "v_mfma_f32_16x16x32_bf16 v[132:135], a[60:63], v[88:91], v[132:135]\n\t"
      "v_cvt_pk_bf16_f32 v136, v128, v129\n\t"
      "v_mfma_f32_16x16x32_bf16 v[132:135], a[84:87], v[92:95], v[132:135]\n\t"
      "v_cvt_pk_bf16_f32 v137, v130, v131\n\t"
      "ds_read_b128 v[144:147], v220 offset:1024\n\t"
      "v_mfma_f32_16x16x32_bf16 v[132:135], a[108:111], v[96:99], v[132:135]\n\t"
      "v_cmp_ne_u32_e64 s[76:77], 0, v240\n\t"
      "v_cmp_ne_u32_e64 s[78:79], 0, v241\n\t"
      "v_mfma_f32_16x16x32_bf16 v[132:135], a[132:135], v[100:103], v[132:135]\n\t"
      "s_waitcnt lgkmcnt(2)\n\t"
      "buffer_store_dwordx4 v[156:159], v208, s[28:31], s68 offen offset:1024 sc1\n\t"
      "v_subrev_u32_e32 v243, 5, v250\n\t"
      "s_and_b32 s64, s65, 0x22222222\n\t"
      "v_mfma_f32_16x16x32_bf16 v[132:135], a[156:159], v[104:107], v[132:135]\n\t"
      "ds_read_b128 v[60:63], v196 offset:256\n\t"
      "v_subrev_u32_e32 v242, 3, v250\n\t"
      "v_mfma_f32_16x16x32_bf16 v[132:135], a[180:183], v[108:111], v[132:135]\n\t"
      "s_xor_b32 s75, s75, s76\n\t"
      "s_xor_b32 s74, s74, s76\n\t"
      "v_mfma_f32_16x16x32_bf16 v[132:135], a[204:207], v[112:115], v[132:135]\n\t"
      "s_and_b32 s76, s75, s78\n\t"
      "v_cndmask_b32_e64 v249, v248, v251, s[76:77]\n\t"
      "v_mfma_f32_16x16x32_bf16 v[132:135], a[228:231], v[116:119], v[132:135]\n\t"
      "ds_read_b128 v[64:67], v198 offset:512\n\t"
      "s_bcnt1_i32_b32 s51, s78\n\t"
      "s_cmp_lt_u32 s51, 4\n\t"
      "s_cselect_b32 s51, 2, 0\n\t"
      "v_mfma_f32_16x16x32_bf16 v[132:135], a[252:255], v[120:123], v[132:135]\n\t"
      "s_and_b32 s76, s74, s78\n\t"
      "v_cndmask_b32_e64 v248, v248, v251, s[76:77]\n\t"
      "v_mfma_f32_16x16x32_bf16 v[132:135], v[184:187], v[124:127], v[132:135]\n\t"
      "s_waitcnt lgkmcnt(2)\n\t"
      "buffer_store_dwordx4 v[144:147], v208, s[28:31], s72 offen offset:1024 sc1\n\t"
      "v_mfma_f32_16x16x32_bf16 v[128:131], a[16:19], v[80:83], v[24:27]\n\t"
      "ds_read_b128 v[68:71], v199 offset:512\n\t"
      "v_add_u32_e32 v250, s51, v250\n\t"
      "v_mfma_f32_16x16x32_bf16 v[128:131], a[40:43], v[84:87], v[128:131]\n\t"
      "s_lshr_b32 s74, s78, 3\n\t"
      "s_or_b32 s76, s74, s78\n\t"
      "v_mfma_f32_16x16x32_bf16 v[128:131], a[64:67], v[88:91], v[128:131]\n\t"
      "v_cvt_pk_bf16_f32 v138, v132, v133\n\t"
      "v_readlane_b32 s73, v248, s85\n\t"
      "v_mfma_f32_16x16x32_bf16 v[128:131], a[88:91], v[92:95], v[128:131]\n\t"
      "v_cvt_pk_bf16_f32 v139, v134, v135\n\t"
      "ds_read_b128 v[160:163], v238 offset:2048\n\t"
      "v_mfma_f32_16x16x32_bf16 v[128:131], a[112:115], v[96:99], v[128:131]\n\t"
      "ds_write_b128 v218, v[136:139] offset:12288\n\t"
      "v_mfma_f32_16x16x32_bf16 v[128:131], a[136:139], v[100:103], v[128:131]\n\t"
      "v_cndmask_b32_e64 v250, v250, v242, s[76:77]\n\t"
      "s_and_b32 s78, s74, s78\n\t"
      "v_mfma_f32_16x16x32_bf16 v[128:131], a[160:163], v[104:107], v[128:131]\n\t"
      "s_and_b32 s78, s78, 0x3fff\n\t"
      "v_cndmask_b32_e64 v250, v250, v243, s[78:79]\n\t"
      "v_mfma_f32_16x16x32_bf16 v[128:131], a[184:187], v[108:111], v[128:131]\n\t"
      "ds_read_b128 v[148:151], v220 offset:2048\n\t"
      "v_mfma_f32_16x16x32_bf16 v[128:131], a[208:211], v[112:115], v[128:131]\n\t"
      "buffer_load_dwordx4 v208, s[20:23], s73 offen lds\n\t"
      "v_mfma_f32_16x16x32_bf16 v[128:131], a[232:235], v[116:119], v[128:131]\n\t"
      "ds_read_b128 v[72:75], v196 offset:512\n\t"
      "s_or_b32 s64, s64, s67\n\t"
      "v_mfma_f32_16x16x32_bf16 v[128:131], v[164:167], v[120:123], v[128:131]\n\t"
      "v_readlane_b32 s86, v250, s44\n\t"
      "v_mfma_f32_16x16x32_bf16 v[128:131], v[188:191], v[124:127], v[128:131]\n\t"
      "v_mfma_f32_16x16x32_bf16 v[132:135], a[20:23], v[80:83], v[28:31]\n\t"
      "ds_read_b128 v[76:79], v197 offset:512\n\t"
      "v_mfma_f32_16x16x32_bf16 v[132:135], a[44:47], v[84:87], v[132:135]\n\t"
      "buffer_load_dwordx4 v208, s[20:23], s73 offen offset:1024 lds\n\t"
      "v_mfma_f32_16x16x32_bf16 v[132:135], a[68:71], v[88:91], v[132:135]\n\t"
      "v_cvt_pk_bf16_f32 v136, v128, v129\n\t"
      "v_mfma_f32_16x16x32_bf16 v[132:135], a[92:95], v[92:95], v[132:135]\n\t"
      "v_cvt_pk_bf16_f32 v137, v130, v131\n\t"
      "s_sub_u32 s84, s84, 4\n\t"
      "s_max_i32 s84, s84, s86\n\t"
      "v_mfma_f32_16x16x32_bf16 v[132:135], a[116:119], v[96:99], v[132:135]\n\t"
      "s_sub_u32 s83, s82, s80\n\t"
      "v_mfma_f32_16x16x32_bf16 v[132:135], a[140:143], v[100:103], v[132:135]\n\t"
      "s_waitcnt lgkmcnt(4)\n\t"
      "buffer_store_dwordx4 v[160:163], v208, s[28:31], s68 offen offset:2048 sc1\n\t"
      "s_max_i32 s83, s83, 0\n\t"
      "s_add_u32 s82, s80, s83\n\t"
      "v_mfma_f32_16x16x32_bf16 v[132:135], a[164:167], v[104:107], v[132:135]\n\t"
      "s_mov_b32 s45, s46\n\t"
      "v_mfma_f32_16x16x32_bf16 v[132:135], a[188:191], v[108:111], v[132:135]\n\t"
      "s_mov_b32 s46, s47\n\t"
      "v_mfma_f32_16x16x32_bf16 v[132:135], a[212:215], v[112:115], v[132:135]\n\t"
      "s_mov_b32 s47, s48\n\t"
      "v_mfma_f32_16x16x32_bf16 v[132:135], a[236:239], v[116:119], v[132:135]\n\t"
      "s_add_u32 s82, s82, s84\n\t"
      "v_mfma_f32_16x16x32_bf16 v[132:135], v[168:171], v[120:123], v[132:135]\n\t"
      "s_cmp_lg_u32 s44, s49\n\t"
      "v_mfma_f32_16x16x32_bf16 v[132:135], v[192:195], v[124:127], v[132:135]\n\t"
      "s_waitcnt lgkmcnt(2)\n\t"
      "buffer_store_dwordx4 v[148:151], v208, s[28:31], s72 offen offset:2048 sc1\n\t"
      "v_mfma_f32_16x16x32_bf16 v[128:131], a[0:3], v[32:35], v[8:11]\n\t"
      "v_mfma_f32_16x16x32_bf16 v[128:131], a[24:27], v[36:39], v[128:131]\n\t"
      "v_readlane_b32 s72, v249, s44\n\t"
      "v_mfma_f32_16x16x32_bf16 v[128:131], a[48:51], v[40:43], v[128:131]\n\t"
      "v_cvt_pk_bf16_f32 v138, v132, v133\n\t"
      "v_mfma_f32_16x16x32_bf16 v[128:131], a[72:75], v[44:47], v[128:131]\n\t"
      "v_cvt_pk_bf16_f32 v139, v134, v135\n\t"
      "v_mfma_f32_16x16x32_bf16 v[128:131], a[96:99], v[48:51], v[128:131]\n\t"
      "ds_write_b128 v219, v[136:139] offset:12288\n\t"
      "v_mfma_f32_16x16x32_bf16 v[128:131], a[120:123], v[52:55], v[128:131]\n\t"
      "v_mfma_f32_16x16x32_bf16 v[128:131], a[144:147], v[56:59], v[128:131]\n\t"
      "buffer_load_dwordx4 v208, s[20:23], s73 offen offset:2048 lds\n\t"
      "v_mfma_f32_16x16x32_bf16 v[128:131], a[168:171], v[60:63], v[128:131]\n\t"
      "s_waitcnt lgkmcnt(1)\n\t"
      "v_mfma_f32_16x16x32_bf16 v[128:131], a[192:195], v[64:67], v[128:131]\n\t"
      "v_add_u32_e32 v217, s46, v204\n\t"
      "v_mfma_f32_16x16x32_bf16 v[128:131], a[216:219], v[68:71], v[128:131]\n\t"
      "v_add_u32_e32 v218, s46, v205\n\t"
      "v_mfma_f32_16x16x32_bf16 v[128:131], a[240:243], v[72:75], v[128:131]\n\t"
      "v_add_u32_e32 v219, s46, v206\n\t"
      "v_mfma_f32_16x16x32_bf16 v[128:131], v[172:175], v[76:79], v[128:131]\n\t"
      "s_cmp_gt_i32 s83, 1\n\t"
      "s_cbranch_scc1 .LslpP4_%=\n\t"
      ".LretP4_%=:\n\t"
      "s_waitcnt vmcnt(15) lgkmcnt(0)\n\t"
      "s_barrier\n\t"
      "v_mfma_f32_16x16x32_bf16 v[132:135], a[4:7], v[32:35], v[12:15]\n\t"
      "ds_read_b128 v[140:143], v220 offset:12288\n\t"
      "v_mfma_f32_16x16x32_bf16 v[132:135], a[28:31], v[36:39], v[132:135]\n\t"
      "v_mfma_f32_16x16x32_bf16 v[132:135], a[52:55], v[40:43], v[132:135]\n\t"
      "v_cvt_pk_bf16_f32 v136, v128, v129\n\t"
      "v_mfma_f32_16x16x32_bf16 v[132:135], a[76:79], v[44:47], v[132:135]\n\t"
      "ds_read_b128 v[80:83], v196 offset:12288\n\t"
      "v_cvt_pk_bf16_f32 v137, v130, v131\n\t"
      "v_mfma_f32_16x16x32_bf16 v[132:135], a[100:103], v[48:51], v[132:135]\n\t"
      "v_readlane_b32 s68, v248, s94\n\t"
      "s_add_u32 s48, s47, 0x6000\n\t"
      "v_mfma_f32_16x16x32_bf16 v[132:135], a[124:127], v[52:55], v[132:135]\n\t"
      "ds_read_b128 v[152:155], v238 offset:12288\n\t"
      "s_cmp_eq_u32 s48, s50\n\t"
      "s_cselect_b32 s48, s41, s48\n\t"
      "v_mfma_f32_16x16x32_bf16 v[132:135], a[148:151], v[56:59], v[132:135]\n\t"
      "ds_read_b128 v[84:87], v197 offset:12288\n\t"
      "v_mfma_f32_16x16x32_bf16 v[132:135], a[172:175], v[60:63], v[132:135]\n\t"
      "v_readlane_b32 s73, v249, s85\n\t"
      "v_mfma_f32_16x16x32_bf16 v[132:135], a[196:199], v[64:67], v[132:135]\n\t"
      "v_mfma_f32_16x16x32_bf16 v[132:135], a[220:223], v[68:71], v[132:135]\n\t"
      "ds_read_b128 v[88:91], v198 offset:12288\n\t"
      "v_mfma_f32_16x16x32_bf16 v[132:135], a[244:247], v[72:75], v[132:135]\n\t"
      "v_mfma_f32_16x16x32_bf16 v[132:135], v[176:179], v[76:79], v[132:135]\n\t"
      "s_waitcnt lgkmcnt(4)\n\t"
      "buffer_store_dwordx4 v[140:143], v209, s[28:31], s72 offen sc1\n\t"
      "v_mfma_f32_16x16x32_bf16 v[128:131], a[8:11], v[32:35], v[16:19]\n\t"
      "ds_read_b128 v[92:95], v199 offset:12288\n\t"
      "v_mfma_f32_16x16x32_bf16 v[128:131], a[32:35], v[36:39], v[128:131]\n\t"
      "s_memrealtime s[80:81]\n\t"
      "v_mfma_f32_16x16x32_bf16 v[128:131], a[56:59], v[40:43], v[128:131]\n\t"
      "v_cvt_pk_bf16_f32 v138, v132, v133\n\t"
      "v_mfma_f32_16x16x32_bf16 v[128:131], a[80:83], v[44:47], v[128:131]\n\t"
      "v_cvt_pk_bf16_f32 v139, v134, v135\n\t"
      "v_mfma_f32_16x16x32_bf16 v[128:131], a[104:107], v[48:51], v[128:131]\n\t"
      "ds_write_b128 v217, v[136:139] offset:0\n\t"
      "v_mfma_f32_16x16x32_bf16 v[128:131], a[128:131], v[52:55], v[128:131]\n\t"
      "s_add_u32 m0, s45, 0x3000\n\t"
      "v_mfma_f32_16x16x32_bf16 v[128:131], a[152:155], v[56:59], v[128:131]\n\t"
      "s_waitcnt lgkmcnt(4)\n\t"
      "buffer_store_dwordx4 v[152:155], v209, s[28:31], s68 offen sc1\n\t"
      "v_mfma_f32_16x16x32_bf16 v[128:131], a[176:179], v[60:63], v[128:131]\n\t"
      "ds_read_b128 v[96:99], v197 offset:12544\n\t"
      "v_mfma_f32_16x16x32_bf16 v[128:131], a[200:203], v[64:67], v[128:131]\n\t"
      "v_mfma_f32_16x16x32_bf16 v[128:131], a[224:227], v[68:71], v[128:131]\n\t"
      "ds_read_b128 v[100:103], v198 offset:12544\n\t"
      "v_mfma_f32_16x16x32_bf16 v[128:131], a[248:251], v[72:75], v[128:131]\n\t"
      "ds_read_b128 v[156:159], v238 offset:13312\n\t"
      "v_mfma_f32_16x16x32_bf16 v[128:131], v[180:183], v[76:79], v[128:131]\n\t"
      "buffer_load_dwordx4 v209, s[20:23], s73 offen lds\n\t"
      "v_mfma_f32_16x16x32_bf16 v[132:135], a[12:15], v[32:35], v[20:23]\n\t"
      "ds_read_b128 v[104:107], v199 offset:12544\n\t"
      "v_mfma_f32_16x16x32_bf16 v[132:135], a[36:39], v[36:39], v[132:135]\n\t"
      "v_mfma_f32_16x16x32_bf16 v[132:135], a[60:63], v[40:43], v[132:135]\n\t"
      "v_cvt_pk_bf16_f32 v136, v128, v129\n\t"
      "v_mfma_f32_16x16x32_bf16 v[132:135], a[84:87], v[44:47], v[132:135]\n\t"
      "v_cvt_pk_bf16_f32 v137, v130, v131\n\t"
      "ds_read_b128 v[144:147], v220 offset:13312\n\t"
      "v_mfma_f32_16x16x32_bf16 v[132:135], a[108:111], v[48:51], v[132:135]\n\t"
      "v_mfma_f32_16x16x32_bf16 v[132:135], a[132:135], v[52:55], v[132:135]\n\t"
      "s_waitcnt lgkmcnt(2)\n\t"
      "buffer_store_dwordx4 v[156:159], v209, s[28:31], s68 offen offset:1024 sc1\n\t"
      "v_mfma_f32_16x16x32_bf16 v[132:135], a[156:159], v[56:59], v[132:135]\n\t"
      "ds_read_b128 v[108:111], v196 offset:12544\n\t"
      "v_mfma_f32_16x16x32_bf16 v[132:135], a[180:183], v[60:63], v[132:135]\n\t"
      "v_mfma_f32_16x16x32_bf16 v[132:135], a[204:207], v[64:67], v[132:135]\n\t"
      "v_mfma_f32_16x16x32_bf16 v[132:135], a[228:231], v[68:71], v[132:135]\n\t"
      "ds_read_b128 v[112:115], v198 offset:12800\n\t"
      "v_mfma_f32_16x16x32_bf16 v[132:135], a[252:255], v[72:75], v[132:135]\n\t"
      "v_mfma_f32_16x16x32_bf16 v[132:135], v[184:187], v[76:79], v[132:135]\n\t"
      "s_waitcnt lgkmcnt(2)\n\t"
      "buffer_store_dwordx4 v[144:147], v209, s[28:31], s72 offen offset:1024 sc1\n\t"
      "v_mfma_f32_16x16x32_bf16 v[128:131], a[16:19], v[32:35], v[24:27]\n\t"
      "ds_read_b128 v[116:119], v199 offset:12800\n\t"
      "v_mfma_f32_16x16x32_bf16 v[128:131], a[40:43], v[36:39], v[128:131]\n\t"
      "v_mfma_f32_16x16x32_bf16 v[128:131], a[64:67], v[40:43], v[128:131]\n\t"
      "v_cvt_pk_bf16_f32 v138, v132, v133\n\t"
      "v_mfma_f32_16x16x32_bf16 v[128:131], a[88:91], v[44:47], v[128:131]\n\t"
      "v_cvt_pk_bf16_f32 v139, v134, v135\n\t"
      "ds_read_b128 v[160:163], v238 offset:14336\n\t"
      "v_mfma_f32_16x16x32_bf16 v[128:131], a[112:115], v[48:51], v[128:131]\n\t"
      "ds_write_b128 v218, v[136:139] offset:0\n\t"
      "v_mfma_f32_16x16x32_bf16 v[128:131], a[136:139], v[52:55], v[128:131]\n\t"
      "v_mfma_f32_16x16x32_bf16 v[128:131], a[160:163], v[56:59], v[128:131]\n\t"
      "v_mfma_f32_16x16x32_bf16 v[128:131], a[184:187], v[60:63], v[128:131]\n\t"
      "ds_read_b128 v[148:151], v220 offset:14336\n\t"
      "v_mfma_f32_16x16x32_bf16 v[128:131], a[208:211], v[64:67], v[128:131]\n\t"
      "v_mfma_f32_16x16x32_bf16 v[128:131], a[232:235], v[68:71], v[128:131]\n\t"
      "ds_read_b128 v[120:123], v196 offset:12800\n\t"
      "v_mfma_f32_16x16x32_bf16 v[128:131], v[164:167], v[72:75], v[128:131]\n\t"
      "v_mfma_f32_16x16x32_bf16 v[128:131], v[188:191], v[76:79], v[128:131]\n\t"
      "buffer_load_dwordx4 v209, s[20:23], s73 offen offset:1024 lds\n\t"
      "v_mfma_f32_16x16x32_bf16 v[132:135], a[20:23], v[32:35], v[28:31]\n\t"
      "ds_read_b128 v[124:127], v197 offset:12800\n\t"
      "v_mfma_f32_16x16x32_bf16 v[132:135], a[44:47], v[36:39], v[132:135]\n\t"
      "v_mfma_f32_16x16x32_bf16 v[132:135], a[68:71], v[40:43], v[132:135]\n\t"
      "v_cvt_pk_bf16_f32 v136, v128, v129\n\t"
      "v_add_u32_e32 v220, s46, v207\n\t"
      "v_mfma_f32_16x16x32_bf16 v[132:135], a[92:95], v[44:47], v[132:135]\n\t"
      "v_cvt_pk_bf16_f32 v137, v130, v131\n\t"
      "s_sub_u32 s84, s84, 4\n\t"
      "s_max_i32 s84, s84, s86\n\t"
      "v_mfma_f32_16x16x32_bf16 v[132:135], a[116:119], v[48:51], v[132:135]\n\t"
      "s_sub_u32 s83, s82, s80\n\t"
      "v_mfma_f32_16x16x32_bf16 v[132:135], a[140:143], v[52:55], v[132:135]\n\t"
      "s_waitcnt lgkmcnt(4)\n\t"
      "buffer_store_dwordx4 v[160:163], v209, s[28:31], s68 offen offset:2048 sc1\n\t"
      "s_max_i32 s83, s83, 0\n\t"
      "s_add_u32 s82, s80, s83\n\t"
      "v_mfma_f32_16x16x32_bf16 v[132:135], a[164:167], v[56:59], v[132:135]\n\t"
      "v_mfma_f32_16x16x32_bf16 v[132:135], a[188:191], v[60:63], v[132:135]\n\t"
      "v_mfma_f32_16x16x32_bf16 v[132:135], a[212:215], v[64:67], v[132:135]\n\t"
      "s_add_u32 s44, s44, 1\n\t"
      "v_mfma_f32_16x16x32_bf16 v[132:135], a[236:239], v[68:71], v[132:135]\n\t"
      "s_add_u32 s82, s82, s84\n\t"
      "v_mfma_f32_16x16x32_bf16 v[132:135], v[168:171], v[72:75], v[132:135]\n\t"
      "s_add_u32 s85, s85, 1\n\t"
      "v_mfma_f32_16x16x32_bf16 v[132:135], v[192:195], v[76:79], v[132:135]\n\t"
      "s_waitcnt lgkmcnt(2)\n\t"
      "buffer_store_dwordx4 v[148:151], v209, s[28:31], s72 offen offset:2048 sc1\n\t"
      "v_mfma_f32_16x16x32_bf16 v[128:131], a[0:3], v[80:83], v[8:11]\n\t"
      "v_mfma_f32_16x16x32_bf16 v[128:131], a[24:27], v[84:87], v[128:131]\n\t"
      "v_readlane_b32 s72, v248, s44\n\t"
      "v_mfma_f32_16x16x32_bf16 v[128:131], a[48:51], v[88:91], v[128:131]\n\t"
      "v_cvt_pk_bf16_f32 v138, v132, v133\n\t"
      "v_mfma_f32_16x16x32_bf16 v[128:131], a[72:75], v[92:95], v[128:131]\n\t"
      "v_cvt_pk_bf16_f32 v139, v134, v135\n\t"
      "v_mfma_f32_16x16x32_bf16 v[128:131], a[96:99], v[96:99], v[128:131]\n\t"
      "ds_write_b128 v219, v[136:139] offset:0\n\t"
      "v_mfma_f32_16x16x32_bf16 v[128:131], a[120:123], v[100:103], v[128:131]\n\t"
      "v_mfma_f32_16x16x32_bf16 v[128:131], a[144:147], v[104:107], v[128:131]\n\t"
      "buffer_load_dwordx4 v209, s[20:23], s73 offen offset:2048 lds\n\t"
      "v_mfma_f32_16x16x32_bf16 v[128:131], a[168:171], v[108:111], v[128:131]\n\t"
      "v_add_u32_e32 v196, s47, v200\n\t"
      "s_waitcnt lgkmcnt(1)\n\t"
      "v_mfma_f32_16x16x32_bf16 v[128:131], a[192:195], v[112:115], v[128:131]\n\t"
      "v_add_u32_e32 v197, s47, v201\n\t"
      "v_mfma_f32_16x16x32_bf16 v[128:131], a[216:219], v[116:119], v[128:131]\n\t"
      "v_add_u32_e32 v198, s47, v202\n\t"
      "v_mfma_f32_16x16x32_bf16 v[128:131], a[240:243], v[120:123], v[128:131]\n\t"
      "v_add_u32_e32 v199, s47, v203\n\t"
      "v_mfma_f32_16x16x32_bf16 v[128:131], v[172:175], v[124:127], v[128:131]\n\t"
      "s_cmp_gt_i32 s83, 1\n\t"
      "s_cbranch_scc1 .LslpP5_%=\n\t"
      ".LretP5_%=:\n\t"
      "s_waitcnt vmcnt(21) lgkmcnt(0)\n\t"
      "s_barrier\n\t"
      "v_mfma_f32_16x16x32_bf16 v[132:135], a[4:7], v[80:83], v[12:15]\n\t"
      "ds_read_b128 v[140:143], v220 offset:0\n\t"
      "v_mfma_f32_16x16x32_bf16 v[132:135], a[28:31], v[84:87], v[132:135]\n\t"
      "v_mfma_f32_16x16x32_bf16 v[132:135], a[52:55], v[88:91], v[132:135]\n\t"
      "v_cvt_pk_bf16_f32 v136, v128, v129\n\t"
      "v_mfma_f32_16x16x32_bf16 v[132:135], a[76:79], v[92:95], v[132:135]\n\t"
      "ds_read_b128 v[32:35], v196 offset:0\n\t"
      "v_cvt_pk_bf16_f32 v137, v130, v131\n\t"
      "v_mfma_f32_16x16x32_bf16 v[132:135], a[100:103], v[96:99], v[132:135]\n\t"
      "v_mfma_f32_16x16x32_bf16 v[132:135], a[124:127], v[100:103], v[132:135]\n\t"
      "v_mfma_f32_16x16x32_bf16 v[132:135], a[148:151], v[104:107], v[132:135]\n\t"
      "ds_read_b128 v[36:39], v197 offset:0\n\t"
      "v_mfma_f32_16x16x32_bf16 v[132:135], a[172:175], v[108:111], v[132:135]\n\t"
      "v_readlane_b32 s73, v248, s85\n\t"
      "v_mfma_f32_16x16x32_bf16 v[132:135], a[196:199], v[112:115], v[132:135]\n\t"
      "v_mfma_f32_16x16x32_bf16 v[132:135], a[220:223], v[116:119], v[132:135]\n\t"
      "ds_read_b128 v[40:43], v198 offset:0\n\t"
      "v_mfma_f32_16x16x32_bf16 v[132:135], a[244:247], v[120:123], v[132:135]\n\t"
      "v_mfma_f32_16x16x32_bf16 v[132:135], v[176:179], v[124:127], v[132:135]\n\t"
      "s_waitcnt lgkmcnt(3)\n\t"
      "buffer_store_dwordx4 v[140:143], v208, s[28:31], s72 offen sc1\n\t"
      "v_mfma_f32_16x16x32_bf16 v[128:131], a[8:11], v[80:83], v[16:19]\n\t"
      "ds_read_b128 v[44:47], v199 offset:0\n\t"
      "v_mfma_f32_16x16x32_bf16 v[128:131], a[32:35], v[84:87], v[128:131]\n\t"
      "s_memrealtime s[80:81]\n\t"
      "v_mfma_f32_16x16x32_bf16 v[128:131], a[56:59], v[88:91], v[128:131]\n\t"
      "v_cvt_pk_bf16_f32 v138, v132, v133\n\t"
      "v_mfma_f32_16x16x32_bf16 v[128:131], a[80:83], v[92:95], v[128:131]\n\t"
      "v_cvt_pk_bf16_f32 v139, v134, v135\n\t"
      "v_mfma_f32_16x16x32_bf16 v[128:131], a[104:107], v[96:99], v[128:131]\n\t"
      "ds_write_b128 v217, v[136:139] offset:12288\n\t"
      "v_mfma_f32_16x16x32_bf16 v[128:131], a[128:131], v[100:103], v[128:131]\n\t"
      "s_mov_b32 m0, s46\n\t"
      "v_mfma_f32_16x16x32_bf16 v[128:131], a[152:155], v[104:107], v[128:131]\n\t"
      "v_mfma_f32_16x16x32_bf16 v[128:131], a[176:179], v[108:111], v[128:131]\n\t"
      "ds_read_b128 v[48:51], v197 offset:256\n\t"
      "v_mfma_f32_16x16x32_bf16 v[128:131], a[200:203], v[112:115], v[128:131]\n\t"
      "v_mfma_f32_16x16x32_bf16 v[128:131], a[224:227], v[116:119], v[128:131]\n\t"
      "ds_read_b128 v[52:55], v198 offset:256\n\t"
      "v_mfma_f32_16x16x32_bf16 v[128:131], a[248:251], v[120:123], v[128:131]\n\t"
      "v_mfma_f32_16x16x32_bf16 v[128:131], v[180:183], v[124:127], v[128:131]\n\t"
      "buffer_load_dwordx4 v208, s[20:23], s73 offen lds\n\t"
      "v_mfma_f32_16x16x32_bf16 v[132:135], a[12:15], v[80:83], v[20:23]\n\t"
      "ds_read_b128 v[56:59], v199 offset:256\n\t"
      "v_mfma_f32_16x16x32_bf16 v[132:135], a[36:39], v[84:87], v[132:135]\n\t"
      "v_mfma_f32_16x16x32_bf16 v[132:135], a[60:63], v[88:91], v[132:135]\n\t"
      "v_cvt_pk_bf16_f32 v136, v128, v129\n\t"
      "v_mfma_f32_16x16x32_bf16 v[132:135], a[84:87], v[92:95], v[132:135]\n\t"
      "v_cvt_pk_bf16_f32 v137, v130, v131\n\t"
      "ds_read_b128 v[144:147], v220 offset:1024\n\t"
      "v_mfma_f32_16x16x32_bf16 v[132:135], a[108:111], v[96:99], v[132:135]\n\t"
      "v_mfma_f32_16x16x32_bf16 v[132:135], a[132:135], v[100:103], v[132:135]\n\t"
      "v_mfma_f32_16x16x32_bf16 v[132:135], a[156:159], v[104:107], v[132:135]\n\t"
      "ds_read_b128 v[60:63], v196 offset:256\n\t"
      "v_mfma_f32_16x16x32_bf16 v[132:135], a[180:183], v[108:111], v[132:135]\n\t"
      "v_mfma_f32_16x16x32_bf16 v[132:135], a[204:207], v[112:115], v[132:135]\n\t"
      "v_mfma_f32_16x16x32_bf16 v[132:135], a[228:231], v[116:119], v[132:135]\n\t"
      "ds_read_b128 v[64:67], v198 offset:512\n\t"
      "v_mfma_f32_16x16x32_bf16 v[132:135], a[252:255], v[120:123], v[132:135]\n\t"
      "v_mfma_f32_16x16x32_bf16 v[132:135], v[184:187], v[124:127], v[132:135]\n\t"
      "s_waitcnt lgkmcnt(2)\n\t"
      "buffer_store_dwordx4 v[144:147], v208, s[28:31], s72 offen offset:1024 sc1\n\t"
      "v_mfma_f32_16x16x32_bf16 v[128:131], a[16:19], v[80:83], v[24:27]\n\t"
      "ds_read_b128 v[68:71], v199 offset:512\n\t"
      "v_mfma_f32_16x16x32_bf16 v[128:131], a[40:43], v[84:87], v[128:131]\n\t"
      "v_readlane_b32 s86, v250, s44\n\t"
      "v_mfma_f32_16x16x32_bf16 v[128:131], a[64:67], v[88:91], v[128:131]\n\t"
      "v_cvt_pk_bf16_f32 v138, v132, v133\n\t"
      "v_mfma_f32_16x16x32_bf16 v[128:131], a[88:91], v[92:95], v[128:131]\n\t"
      "v_cvt_pk_bf16_f32 v139, v134, v135\n\t"
      "v_mfma_f32_16x16x32_bf16 v[128:131], a[112:115], v[96:99], v[128:131]\n\t"
      "ds_write_b128 v218, v[136:139] offset:12288\n\t"
      "v_mfma_f32_16x16x32_bf16 v[128:131], a[136:139], v[100:103], v[128:131]\n\t"
      "v_mfma_f32_16x16x32_bf16 v[128:131], a[160:163], v[104:107], v[128:131]\n\t"
      "v_mfma_f32_16x16x32_bf16 v[128:131], a[184:187], v[108:111], v[128:131]\n\t"
      "ds_read_b128 v[148:151], v220 offset:2048\n\t"
      "v_mfma_f32_16x16x32_bf16 v[128:131], a[208:211], v[112:115], v[128:131]\n\t"
      "v_mfma_f32_16x16x32_bf16 v[128:131], a[232:235], v[116:119], v[128:131]\n\t"
      "ds_read_b128 v[72:75], v196 offset:512\n\t"
      "v_mfma_f32_16x16x32_bf16 v[128:131], v[164:167], v[120:123], v[128:131]\n\t"
      "v_mfma_f32_16x16x32_bf16 v[128:131], v[188:191], v[124:127], v[128:131]\n\t"
      "buffer_load_dwordx4 v208, s[20:23], s73 offen offset:1024 lds\n\t"
      "v_mfma_f32_16x16x32_bf16 v[132:135], a[20:23], v[80:83], v[28:31]\n\t"
      "ds_read_b128 v[76:79], v197 offset:512\n\t"
      "v_mfma_f32_16x16x32_bf16 v[132:135], a[44:47], v[84:87], v[132:135]\n\t"
      "v_mfma_f32_16x16x32_bf16 v[132:135], a[68:71], v[88:91], v[132:135]\n\t"
      "v_cvt_pk_bf16_f32 v136, v128, v129\n\t"
      "v_mfma_f32_16x16x32_bf16 v[132:135], a[92:95], v[92:95], v[132:135]\n\t"
      "v_cvt_pk_bf16_f32 v137, v130, v131\n\t"
      "s_sub_u32 s84, s84, 4\n\t"
      "s_max_i32 s84, s84, s86\n\t"
      "v_mfma_f32_16x16x32_bf16 v[132:135], a[116:119], v[96:99], v[132:135]\n\t"
      "s_sub_u32 s83, s82, s80\n\t"
      "v_mfma_f32_16x16x32_bf16 v[132:135], a[140:143], v[100:103], v[132:135]\n\t"
      "s_max_i32 s83, s83, 0\n\t"
      "s_add_u32 s82, s80, s83\n\t"
      "v_mfma_f32_16x16x32_bf16 v[132:135], a[164:167], v[104:107], v[132:135]\n\t"
      "s_mov_b32 s45, s46\n\t"
      "v_mfma_f32_16x16x32_bf16 v[132:135], a[188:191], v[108:111], v[132:135]\n\t"
      "s_mov_b32 s46, s47\n\t"
      "v_mfma_f32_16x16x32_bf16 v[132:135], a[212:215], v[112:115], v[132:135]\n\t"
      "s_mov_b32 s47, s48\n\t"
      "v_mfma_f32_16x16x32_bf16 v[132:135], a[236:239], v[116:119], v[132:135]\n\t"
      "s_add_u32 s82, s82, s84\n\t"
      "v_mfma_f32_16x16x32_bf16 v[132:135], v[168:171], v[120:123], v[132:135]\n\t"
      "s_cmp_lg_u32 s44, s49\n\t"
      "v_mfma_f32_16x16x32_bf16 v[132:135], v[192:195], v[124:127], v[132:135]\n\t"
      "s_waitcnt lgkmcnt(2)\n\t"
      "buffer_store_dwordx4 v[148:151], v208, s[28:31], s72 offen offset:2048 sc1\n\t"
      ".p2align 6\n\t"
      ".Lloop_%=:\n\t"
      "s_waitcnt lgkmcnt(12)\n\t"
      "v_mfma_f32_16x16x32_bf16 v[128:131], a[0:3], v[32:35], v[8:11]\n\t"
      "v_mfma_f32_16x16x32_bf16 v[128:131], a[24:27], v[36:39], v[128:131]\n\t"
      "v_readlane_b32 s72, v249, s44\n\t"
      "v_mfma_f32_16x16x32_bf16 v[128:131], a[48:51], v[40:43], v[128:131]\n\t"
      "v_cvt_pk_bf16_f32 v138, v132, v133\n\t"
      "v_mfma_f32_16x16x32_bf16 v[128:131], a[72:75], v[44:47], v[128:131]\n\t"
      "v_cvt_pk_bf16_f32 v139, v134, v135\n\t"
      "s_waitcnt lgkmcnt(6)\n\t"
      "v_mfma_f32_16x16x32_bf16 v[128:131], a[96:99], v[48:51], v[128:131]\n\t"
      "ds_write_b128 v219, v[136:139] offset:12288\n\t"
      "v_mfma_f32_16x16x32_bf16 v[128:131], a[120:123], v[52:55], v[128:131]\n\t"
      "v_mfma_f32_16x16x32_bf16 v[128:131], a[144:147], v[56:59], v[128:131]\n\t"
      "buffer_load_dwordx4 v208, s[20:23], s73 offen offset:2048 lds\n\t"
      "v_mfma_f32_16x16x32_bf16 v[128:131], a[168:171], v[60:63], v[128:131]\n\t"
      "s_waitcnt lgkmcnt(1)\n\t"
      "v_mfma_f32_16x16x32_bf16 v[128:131], a[192:195], v[64:67], v[128:131]\n\t"
      "v_add_u32_e32 v217, s46, v204\n\t"
      "v_mfma_f32_16x16x32_bf16 v[128:131], a[216:219], v[68:71], v[128:131]\n\t"
      "v_add_u32_e32 v218, s46, v205\n\t"
      "v_mfma_f32_16x16x32_bf16 v[128:131], a[240:243], v[72:75], v[128:131]\n\t"
      "v_add_u32_e32 v219, s46, v206\n\t"
      "v_mfma_f32_16x16x32_bf16 v[128:131], v[172:175], v[76:79], v[128:131]\n\t"
      "s_cmp_gt_i32 s83, 1\n\t"
      "s_cbranch_scc1 .LslpA_%=\n\t"
      ".LretA_%=:\n\t"
      "s_waitcnt vmcnt(18) lgkmcnt(0)\n\t"
      "s_barrier\n\t"
      "v_mfma_f32_16x16x32_bf16 v[132:135], a[4:7], v[32:35], v[12:15]\n\t"
      "ds_read_b128 v[140:143], v220 offset:12288\n\t"
      "v_mfma_f32_16x16x32_bf16 v[132:135], a[28:31], v[36:39], v[132:135]\n\t"
      "v_mfma_f32_16x16x32_bf16 v[132:135], a[52:55], v[40:43], v[132:135]\n\t"
      "v_cvt_pk_bf16_f32 v136, v128, v129\n\t"
      "v_mfma_f32_16x16x32_bf16 v[132:135], a[76:79], v[44:47], v[132:135]\n\t"
      "ds_read_b128 v[80:83], v196 offset:12288\n\t"
      "v_cvt_pk_bf16_f32 v137, v130, v131\n\t"
      "v_mfma_f32_16x16x32_bf16 v[132:135], a[100:103], v[48:51], v[132:135]\n\t"
      "s_add_u32 s48, s47, 0x6000\n\t"
      "v_mfma_f32_16x16x32_bf16 v[132:135], a[124:127], v[52:55], v[132:135]\n\t"
      "s_cmp_eq_u32 s48, s50\n\t"
      "s_cselect_b32 s48, s41, s48\n\t"
      "v_mfma_f32_16x16x32_bf16 v[132:135], a[148:151], v[56:59], v[132:135]\n\t"
      "ds_read_b128 v[84:87], v197 offset:12288\n\t"
      "v_mfma_f32_16x16x32_bf16 v[132:135], a[172:175], v[60:63], v[132:135]\n\t"
      "v_readlane_b32 s73, v249, s85\n\t"
      "v_mfma_f32_16x16x32_bf16 v[132:135], a[196:199], v[64:67], v[132:135]\n\t"
      "v_mfma_f32_16x16x32_bf16 v[132:135], a[220:223], v[68:71], v[132:135]\n\t"
      "ds_read_b128 v[88:91], v198 offset:12288\n\t"
      "v_mfma_f32_16x16x32_bf16 v[132:135], a[244:247], v[72:75], v[132:135]\n\t"
      "v_mfma_f32_16x16x32_bf16 v[132:135], v[176:179], v[76:79], v[132:135]\n\t"
      "s_waitcnt lgkmcnt(3)\n\t"
      "buffer_store_dwordx4 v[140:143], v209, s[28:31], s72 offen sc1\n\t"
      "v_mfma_f32_16x16x32_bf16 v[128:131], a[8:11], v[32:35], v[16:19]\n\t"
      "ds_read_b128 v[92:95], v199 offset:12288\n\t"
      "v_mfma_f32_16x16x32_bf16 v[128:131], a[32:35], v[36:39], v[128:131]\n\t"
      "s_memrealtime s[80:81]\n\t"
      "v_mfma_f32_16x16x32_bf16 v[128:131], a[56:59], v[40:43], v[128:131]\n\t"
      "v_cvt_pk_bf16_f32 v138, v132, v133\n\t"
      "v_mfma_f32_16x16x32_bf16 v[128:131], a[80:83], v[44:47], v[128:131]\n\t"
      "v_cvt_pk_bf16_f32 v139, v134, v135\n\t"
      "v_mfma_f32_16x16x32_bf16 v[128:131], a[104:107], v[48:51], v[128:131]\n\t"
      "ds_write_b128 v217, v[136:139] offset:0\n\t"
      "v_mfma_f32_16x16x32_bf16 v[128:131], a[128:131], v[52:55], v[128:131]\n\t"
      "s_add_u32 m0, s45, 0x3000\n\t"
      "v_mfma_f32_16x16x32_bf16 v[128:131], a[152:155], v[56:59], v[128:131]\n\t"
      "v_mfma_f32_16x16x32_bf16 v[128:131], a[176:179], v[60:63], v[128:131]\n\t"
      "ds_read_b128 v[96:99], v197 offset:12544\n\t"
      "v_mfma_f32_16x16x32_bf16 v[128:131], a[200:203], v[64:67], v[128:131]\n\t"
      "v_mfma_f32_16x16x32_bf16 v[128:131], a[224:227], v[68:71], v[128:131]\n\t"
      "ds_read_b128 v[100:103], v198 offset:12544\n\t"
      "v_mfma_f32_16x16x32_bf16 v[128:131], a[248:251], v[72:75], v[128:131]\n\t"
      "v_mfma_f32_16x16x32_bf16 v[128:131], v[180:183], v[76:79], v[128:131]\n\t"
      "buffer_load_dwordx4 v209, s[20:23], s73 offen lds\n\t"
      "v_mfma_f32_16x16x32_bf16 v[132:135], a[12:15], v[32:35], v[20:23]\n\t"
      "ds_read_b128 v[104:107], v199 offset:12544\n\t"
      "v_mfma_f32_16x16x32_bf16 v[132:135], a[36:39], v[36:39], v[132:135]\n\t"
      "v_mfma_f32_16x16x32_bf16 v[132:135], a[60:63], v[40:43], v[132:135]\n\t"
      "v_cvt_pk_bf16_f32 v136, v128, v129\n\t"
      "v_mfma_f32_16x16x32_bf16 v[132:135], a[84:87], v[44:47], v[132:135]\n\t"
      "v_cvt_pk_bf16_f32 v137, v130, v131\n\t"
      "ds_read_b128 v[144:147], v220 offset:13312\n\t"
      "v_mfma_f32_16x16x32_bf16 v[132:135], a[108:111], v[48:51], v[132:135]\n\t"
      "v_mfma_f32_16x16x32_bf16 v[132:135], a[132:135], v[52:55], v[132:135]\n\t"
      "v_mfma_f32_16x16x32_bf16 v[132:135], a[156:159], v[56:59], v[132:135]\n\t"
      "ds_read_b128 v[108:111], v196 offset:12544\n\t"
      "v_mfma_f32_16x16x32_bf16 v[132:135], a[180:183], v[60:63], v[132:135]\n\t"
      "v_mfma_f32_16x16x32_bf16 v[132:135], a[204:207], v[64:67], v[132:135]\n\t"
      "v_mfma_f32_16x16x32_bf16 v[132:135], a[228:231], v[68:71], v[132:135]\n\t"
      "ds_read_b128 v[112:115], v198 offset:12800\n\t"
      "v_mfma_f32_16x16x32_bf16 v[132:135], a[252:255], v[72:75], v[132:135]\n\t"
      "v_mfma_f32_16x16x32_bf16 v[132:135], v[184:187], v[76:79], v[132:135]\n\t"
      "s_waitcnt lgkmcnt(2)\n\t"
      "buffer_store_dwordx4 v[144:147], v209, s[28:31], s72 offen offset:1024 sc1\n\t"
      "v_mfma_f32_16x16x32_bf16 v[128:131], a[16:19], v[32:35], v[24:27]\n\t"
      "ds_read_b128 v[116:119], v199 offset:12800\n\t"
      "v_mfma_f32_16x16x32_bf16 v[128:131], a[40:43], v[36:39], v[128:131]\n\t"
      "v_mfma_f32_16x16x32_bf16 v[128:131], a[64:67], v[40:43], v[128:131]\n\t"
      "v_cvt_pk_bf16_f32 v138, v132, v133\n\t"
      "v_mfma_f32_16x16x32_bf16 v[128:131], a[88:91], v[44:47], v[128:131]\n\t"
      "v_cvt_pk_bf16_f32 v139, v134, v135\n\t"
      "v_mfma_f32_16x16x32_bf16 v[128:131], a[112:115], v[48:51], v[128:131]\n\t"
      "ds_write_b128 v218, v[136:139] offset:0\n\t"
      "v_mfma_f32_16x16x32_bf16 v[128:131], a[136:139], v[52:55], v[128:131]\n\t"
      "v_mfma_f32_16x16x32_bf16 v[128:131], a[160:163], v[56:59], v[128:131]\n\t"
      "v_mfma_f32_16x16x32_bf16 v[128:131], a[184:187], v[60:63], v[128:131]\n\t"
      "ds_read_b128 v[148:151], v220 offset:14336\n\t"
      "v_mfma_f32_16x16x32_bf16 v[128:131], a[208:211], v[64:67], v[128:131]\n\t"
      "v_mfma_f32_16x16x32_bf16 v[128:131], a[232:235], v[68:71], v[128:131]\n\t"
      "ds_read_b128 v[120:123], v196 offset:12800\n\t"
      "v_mfma_f32_16x16x32_bf16 v[128:131], v[164:167], v[72:75], v[128:131]\n\t"
      "v_mfma_f32_16x16x32_bf16 v[128:131], v[188:191], v[76:79], v[128:131]\n\t"
      "buffer_load_dwordx4 v209, s[20:23], s73 offen offset:1024 lds\n\t"
      "v_mfma_f32_16x16x32_bf16 v[132:135], a[20:23], v[32:35], v[28:31]\n\t"
      "ds_read_b128 v[124:127], v197 offset:12800\n\t"
      "v_mfma_f32_16x16x32_bf16 v[132:135], a[44:47], v[36:39], v[132:135]\n\t"
      "v_mfma_f32_16x16x32_bf16 v[132:135], a[68:71], v[40:43], v[132:135]\n\t"
      "v_cvt_pk_bf16_f32 v136, v128, v129\n\t"
      "v_add_u32_e32 v220, s46, v207\n\t"
      "v_mfma_f32_16x16x32_bf16 v[132:135], a[92:95], v[44:47], v[132:135]\n\t"
      "v_cvt_pk_bf16_f32 v137, v130, v131\n\t"
      "s_sub_u32 s84, s84, 4\n\t"
      "s_max_i32 s84, s84, s86\n\t"
      "v_mfma_f32_16x16x32_bf16 v[132:135], a[116:119], v[48:51], v[132:135]\n\t"
      "s_sub_u32 s83, s82, s80\n\t"
      "v_mfma_f32_16x16x32_bf16 v[132:135], a[140:143], v[52:55], v[132:135]\n\t"
      "s_max_i32 s83, s83, 0\n\t"
      "s_add_u32 s82, s80, s83\n\t"
      "v_mfma_f32_16x16x32_bf16 v[132:135], a[164:167], v[56:59], v[132:135]\n\t"
      "v_mfma_f32_16x16x32_bf16 v[132:135], a[188:191], v[60:63], v[132:135]\n\t"
      "v_mfma_f32_16x16x32_bf16 v[132:135], a[212:215], v[64:67], v[132:135]\n\t"
      "s_add_u32 s44, s44, 1\n\t"
      "v_mfma_f32_16x16x32_bf16 v[132:135], a[236:239], v[68:71], v[132:135]\n\t"
      "s_add_u32 s82, s82, s84\n\t"
      "v_mfma_f32_16x16x32_bf16 v[132:135], v[168:171], v[72:75], v[132:135]\n\t"
      "s_add_u32 s85, s85, 1\n\t"
      "v_mfma_f32_16x16x32_bf16 v[132:135], v[192:195], v[76:79], v[132:135]\n\t"
      "s_waitcnt lgkmcnt(2)\n\t"
      "buffer_store_dwordx4 v[148:151], v209, s[28:31], s72 offen offset:2048 sc1\n\t"
      "s_waitcnt lgkmcnt(12)\n\t"
      "v_mfma_f32_16x16x32_bf16 v[128:131], a[0:3], v[80:83], v[8:11]\n\t"
      "v_mfma_f32_16x16x32_bf16 v[128:131], a[24:27], v[84:87], v[128:131]\n\t"
      "v_readlane_b32 s72, v248, s44\n\t"
      "v_mfma_f32_16x16x32_bf16 v[128:131], a[48:51], v[88:91], v[128:131]\n\t"
      "v_cvt_pk_bf16_f32 v138, v132, v133\n\t"
      "v_mfma_f32_16x16x32_bf16 v[128:131], a[72:75], v[92:95], v[128:131]\n\t"
      "v_cvt_pk_bf16_f32 v139, v134, v135\n\t"
      "s_waitcnt lgkmcnt(6)\n\t"
      "v_mfma_f32_16x16x32_bf16 v[128:131], a[96:99], v[96:99], v[128:131]\n\t"
      "ds_write_b128 v219, v[136:139] offset:0\n\t"
      "v_mfma_f32_16x16x32_bf16 v[128:131], a[120:123], v[100:103], v[128:131]\n\t"
      "v_mfma_f32_16x16x32_bf16 v[128:131], a[144:147], v[104:107], v[128:131]\n\t"
      "buffer_load_dwordx4 v209, s[20:23], s73 offen offset:2048 lds\n\t"
      "v_mfma_f32_16x16x32_bf16 v[128:131], a[168:171], v[108:111], v[128:131]\n\t"
      "v_add_u32_e32 v196, s47, v200\n\t"
      "s_waitcnt lgkmcnt(1)\n\t"
      "v_mfma_f32_16x16x32_bf16 v[128:131], a[192:195], v[112:115], v[128:131]\n\t"
      "v_add_u32_e32 v197, s47, v201\n\t"
      "v_mfma_f32_16x16x32_bf16 v[128:131], a[216:219], v[116:119], v[128:131]\n\t"
      "v_add_u32_e32 v198, s47, v202\n\t"
      "v_mfma_f32_16x16x32_bf16 v[128:131], a[240:243], v[120:123], v[128:131]\n\t"
      "v_add_u32_e32 v199, s47, v203\n\t"
      "v_mfma_f32_16x16x32_bf16 v[128:131], v[172:175], v[124:127], v[128:131]\n\t"
      "s_cmp_gt_i32 s83, 1\n\t"
      "s_cbranch_scc1 .LslpB_%=\n\t"
      ".LretB_%=:\n\t"
      "s_waitcnt vmcnt(18) lgkmcnt(0)\n\t"
      "s_barrier\n\t"
      "v_mfma_f32_16x16x32_bf16 v[132:135], a[4:7], v[80:83], v[12:15]\n\t"
      "ds_read_b128 v[140:143], v220 offset:0\n\t"
      "v_mfma_f32_16x16x32_bf16 v[132:135], a[28:31], v[84:87], v[132:135]\n\t"
      "v_mfma_f32_16x16x32_bf16 v[132:135], a[52:55], v[88:91], v[132:135]\n\t"
      "v_cvt_pk_bf16_f32 v136, v128, v129\n\t"
      "v_mfma_f32_16x16x32_bf16 v[132:135], a[76:79], v[92:95], v[132:135]\n\t"
      "ds_read_b128 v[32:35], v196 offset:0\n\t"
      "v_cvt_pk_bf16_f32 v137, v130, v131\n\t"
      "v_mfma_f32_16x16x32_bf16 v[132:135], a[100:103], v[96:99], v[132:135]\n\t"
      "v_mfma_f32_16x16x32_bf16 v[132:135], a[124:127], v[100:103], v[132:135]\n\t"
      "v_mfma_f32_16x16x32_bf16 v[132:135], a[148:151], v[104:107], v[132:135]\n\t"
      "ds_read_b128 v[36:39], v197 offset:0\n\t"
      "v_mfma_f32_16x16x32_bf16 v[132:135], a[172:175], v[108:111], v[132:135]\n\t"
      "v_readlane_b32 s73, v248, s85\n\t"
      "v_mfma_f32_16x16x32_bf16 v[132:135], a[196:199], v[112:115], v[132:135]\n\t"
      "v_mfma_f32_16x16x32_bf16 v[132:135], a[220:223], v[116:119], v[132:135]\n\t"
      "ds_read_b128 v[40:43], v198 offset:0\n\t"
      "v_mfma_f32_16x16x32_bf16 v[132:135], a[244:247], v[120:123], v[132:135]\n\t"
      "v_mfma_f32_16x16x32_bf16 v[132:135], v[176:179], v[124:127], v[132:135]\n\t"
      "s_waitcnt lgkmcnt(3)\n\t"
      "buffer_store_dwordx4 v[140:143], v208, s[28:31], s72 offen sc1\n\t"
      "v_mfma_f32_16x16x32_bf16 v[128:131], a[8:11], v[80:83], v[16:19]\n\t"
      "ds_read_b128 v[44:47], v199 offset:0\n\t"
      "v_mfma_f32_16x16x32_bf16 v[128:131], a[32:35], v[84:87], v[128:131]\n\t"
      "s_memrealtime s[80:81]\n\t"
      "v_mfma_f32_16x16x32_bf16 v[128:131], a[56:59], v[88:91], v[128:131]\n\t"
      "v_cvt_pk_bf16_f32 v138, v132, v133\n\t"
      "v_mfma_f32_16x16x32_bf16 v[128:131], a[80:83], v[92:95], v[128:131]\n\t"
      "v_cvt_pk_bf16_f32 v139, v134, v135\n\t"
      "v_mfma_f32_16x16x32_bf16 v[128:131], a[104:107], v[96:99], v[128:131]\n\t"
      "ds_write_b128 v217, v[136:139] offset:12288\n\t"
      "v_mfma_f32_16x16x32_bf16 v[128:131], a[128:131], v[100:103], v[128:131]\n\t"
      "s_mov_b32 m0, s46\n\t"
      "v_mfma_f32_16x16x32_bf16 v[128:131], a[152:155], v[104:107], v[128:131]\n\t"
      "v_mfma_f32_16x16x32_bf16 v[128:131], a[176:179], v[108:111], v[128:131]\n\t"
      "ds_read_b128 v[48:51], v197 offset:256\n\t"
      "v_mfma_f32_16x16x32_bf16 v[128:131], a[200:203], v[112:115], v[128:131]\n\t"
      "v_mfma_f32_16x16x32_bf16 v[128:131], a[224:227], v[116:119], v[128:131]\n\t"
      "ds_read_b128 v[52:55], v198 offset:256\n\t"
      "v_mfma_f32_16x16x32_bf16 v[128:131], a[248:251], v[120:123], v[128:131]\n\t"
      "v_mfma_f32_16x16x32_bf16 v[128:131], v[180:183], v[124:127], v[128:131]\n\t"
      "buffer_load_dwordx4 v208, s[20:23], s73 offen lds\n\t"
      "v_mfma_f32_16x16x32_bf16 v[132:135], a[12:15], v[80:83], v[20:23]\n\t"
      "ds_read_b128 v[56:59], v199 offset:256\n\t"
      "v_mfma_f32_16x16x32_bf16 v[132:135], a[36:39], v[84:87], v[132:135]\n\t"
      "v_mfma_f32_16x16x32_bf16 v[132:135], a[60:63], v[88:91], v[132:135]\n\t"
      "v_cvt_pk_bf16_f32 v136, v128, v129\n\t"
      "v_mfma_f32_16x16x32_bf16 v[132:135], a[84:87], v[92:95], v[132:135]\n\t"
      "v_cvt_pk_bf16_f32 v137, v130, v131\n\t"
      "ds_read_b128 v[144:147], v220 offset:1024\n\t"
      "v_mfma_f32_16x16x32_bf16 v[132:135], a[108:111], v[96:99], v[132:135]\n\t"
      "v_mfma_f32_16x16x32_bf16 v[132:135], a[132:135], v[100:103], v[132:135]\n\t"
      "v_mfma_f32_16x16x32_bf16 v[132:135], a[156:159], v[104:107], v[132:135]\n\t"
      "ds_read_b128 v[60:63], v196 offset:256\n\t"
      "v_mfma_f32_16x16x32_bf16 v[132:135], a[180:183], v[108:111], v[132:135]\n\t"
      "v_mfma_f32_16x16x32_bf16 v[132:135], a[204:207], v[112:115], v[132:135]\n\t"
      "v_mfma_f32_16x16x32_bf16 v[132:135], a[228:231], v[116:119], v[132:135]\n\t"
      "ds_read_b128 v[64:67], v198 offset:512\n\t"
      "v_mfma_f32_16x16x32_bf16 v[132:135], a[252:255], v[120:123], v[132:135]\n\t"
      "v_mfma_f32_16x16x32_bf16 v[132:135], v[184:187], v[124:127], v[132:135]\n\t"
      "s_waitcnt lgkmcnt(2)\n\t"
      "buffer_store_dwordx4 v[144:147], v208, s[28:31], s72 offen offset:1024 sc1\n\t"
      "v_mfma_f32_16x16x32_bf16 v[128:131], a[16:19], v[80:83], v[24:27]\n\t"
      "ds_read_b128 v[68:71], v199 offset:512\n\t"
      "v_mfma_f32_16x16x32_bf16 v[128:131], a[40:43], v[84:87], v[128:131]\n\t"
      "v_readlane_b32 s86, v250, s44\n\t"
      "v_mfma_f32_16x16x32_bf16 v[128:131], a[64:67], v[88:91], v[128:131]\n\t"
      "v_cvt_pk_bf16_f32 v138, v132, v133\n\t"
      "v_mfma_f32_16x16x32_bf16 v[128:131], a[88:91], v[92:95], v[128:131]\n\t"
      "v_cvt_pk_bf16_f32 v139, v134, v135\n\t"
      "v_mfma_f32_16x16x32_bf16 v[128:131], a[112:115], v[96:99], v[128:131]\n\t"
      "ds_write_b128 v218, v[136:139] offset:12288\n\t"
      "v_mfma_f32_16x16x32_bf16 v[128:131], a[136:139], v[100:103], v[128:131]\n\t"
      "v_mfma_f32_16x16x32_bf16 v[128:131], a[160:163], v[104:107], v[128:131]\n\t"
      "v_mfma_f32_16x16x32_bf16 v[128:131], a[184:187], v[108:111], v[128:131]\n\t"
      "ds_read_b128 v[148:151], v220 offset:2048\n\t"
      "v_mfma_f32_16x16x32_bf16 v[128:131], a[208:211], v[112:115], v[128:131]\n\t"
      "v_mfma_f32_16x16x32_bf16 v[128:131], a[232:235], v[116:119], v[128:131]\n\t"
      "ds_read_b128 v[72:75], v196 offset:512\n\t"
      "v_mfma_f32_16x16x32_bf16 v[128:131], v[164:167], v[120:123], v[128:131]\n\t"
      "v_mfma_f32_16x16x32_bf16 v[128:131], v[188:191], v[124:127], v[128:131]\n\t"
      "buffer_load_dwordx4 v208, s[20:23], s73 offen offset:1024 lds\n\t"
      "v_mfma_f32_16x16x32_bf16 v[132:135], a[20:23], v[80:83], v[28:31]\n\t"
      "ds_read_b128 v[76:79], v197 offset:512\n\t"
      "v_mfma_f32_16x16x32_bf16 v[132:135], a[44:47], v[84:87], v[132:135]\n\t"
      "v_mfma_f32_16x16x32_bf16 v[132:135], a[68:71], v[88:91], v[132:135]\n\t"
      "v_cvt_pk_bf16_f32 v136, v128, v129\n\t"
      "v_mfma_f32_16x16x32_bf16 v[132:135], a[92:95], v[92:95], v[132:135]\n\t"
      "v_cvt_pk_bf16_f32 v137, v130, v131\n\t"
      "s_sub_u32 s84, s84, 4\n\t"
      "s_max_i32 s84, s84, s86\n\t"
      "v_mfma_f32_16x16x32_bf16 v[132:135], a[116:119], v[96:99], v[132:135]\n\t"
      "s_sub_u32 s83, s82, s80\n\t"
      "v_mfma_f32_16x16x32_bf16 v[132:135], a[140:143], v[100:103], v[132:135]\n\t"
      "s_max_i32 s83, s83, 0\n\t"
      "s_add_u32 s82, s80, s83\n\t"
      "v_mfma_f32_16x16x32_bf16 v[132:135], a[164:167], v[104:107], v[132:135]\n\t"
      "s_mov_b32 s45, s46\n\t"
      "v_mfma_f32_16x16x32_bf16 v[132:135], a[188:191], v[108:111], v[132:135]\n\t"
      "s_mov_b32 s46, s47\n\t"
      "v_mfma_f32_16x16x32_bf16 v[132:135], a[212:215], v[112:115], v[132:135]\n\t"
      "s_mov_b32 s47, s48\n\t"
      "v_mfma_f32_16x16x32_bf16 v[132:135], a[236:239], v[116:119], v[132:135]\n\t"
      "s_add_u32 s82, s82, s84\n\t"
      "v_mfma_f32_16x16x32_bf16 v[132:135], v[168:171], v[120:123], v[132:135]\n\t"
      "s_cmp_lg_u32 s44, s49\n\t"
      "v_mfma_f32_16x16x32_bf16 v[132:135], v[192:195], v[124:127], v[132:135]\n\t"
      "s_waitcnt lgkmcnt(2)\n\t"
      "buffer_store_dwordx4 v[148:151], v208, s[28:31], s72 offen offset:2048 sc1\n\t"
      "s_cbranch_scc1 .Lloop_%=\n\t"
      "s_cmp_lg_u32 s44, 17\n\t"
      "s_cbranch_scc1 .Lstop_%=\n\t"
      "v_readlane_b32 s72, v249, s44\n\t"
      "ds_read_b32 v241, v247\n\t"
      "s_nop 2\n\t"
      "v_cvt_pk_bf16_f32 v138, v132, v133\n\t"
      "v_cvt_pk_bf16_f32 v139, v134, v135\n\t"
      "ds_write_b128 v219, v[136:139] offset:12288\n\t"
      "s_waitcnt lgkmcnt(0)\n\t"
      "s_barrier\n\t"
      "ds_read_b128 v[32:35], v220 offset:12288\n\t"
      "ds_read_b128 v[36:39], v220 offset:13312\n\t"
      "ds_read_b128 v[40:43], v220 offset:14336\n\t"
      "v_cmp_ne_u32_e32 vcc, 0, v241\n\t"
      "s_cmp_lg_u32 s62, 0\n\t"
      "s_waitcnt lgkmcnt(2)\n\t"
      "buffer_store_dwordx4 v[32:35], v209, s[28:31], s72 offen sc1\n\t"
      "s_waitcnt lgkmcnt(1)\n\t"
      "buffer_store_dwordx4 v[36:39], v209, s[28:31], s72 offen offset:1024 sc1\n\t"
      "s_waitcnt lgkmcnt(0)\n\t"
      "buffer_store_dwordx4 v[40:43], v209, s[28:31], s72 offen offset:2048 sc1\n\t"
      "s_cbranch_scc1 .Lfin_%=\n\t"
      "s_cbranch_vccnz .Lsec_%=\n\t"
      ".Lfin_%=:\n\t"
      "s_endpgm\n\t"
      ".Lstop_%=:\n\t"
      "s_and_b32 s49, s71, 31\n\t"
      "s_lshr_b32 s71, s71, 5\n\t"
      "s_sub_u32 s78, s44, s89\n\t"
      "s_cmp_eq_u32 s78, 10\n\t"
      "s_cbranch_scc1 .Lstop2_%=\n\t"
      "s_cmp_eq_u32 s78, 5\n\t"
      "s_cbranch_scc1 .Lstop1_%=\n\t"
      "s_waitcnt vmcnt(12)\n\t"
      "v_readlane_b32 s76, v254, 1\n\t"
      "v_readlane_b32 s78, v255, 1\n\t"
      "s_andn2_b32 s76, 4, s76\n\t"
      "s_or_b32 s76, s76, s64\n\t"
      "s_xor_b32 s76, s76, s78\n\t"
      "v_mov_b32_e32 v240, s76\n\t"
      "ds_write_b32 v247, v240\n\t"
      "s_branch .Lloop_%=\n\t"
      ".Lstop1_%=:\n\t"
      "s_cmp_eq_u32 s94, 0\n\t"
      "s_cbranch_scc1 .Llt0_%=\n\t"
      "ds_read_b128 v[226:229], v238 offset:24576\n\t"
      "ds_read_b128 v[230:233], v238 offset:25600\n\t"
      "ds_read_b128 v[234:237], v238 offset:26624\n\t"
      "v_readlane_b32 s68, v248, 0\n\t"
      "s_nop 4\n\t"
      "s_waitcnt lgkmcnt(2)\n\t"
      "buffer_store_dwordx4 v[226:229], v208, s[28:31], s68 offen sc1\n\t"
      "s_waitcnt lgkmcnt(1)\n\t"
      "buffer_store_dwordx4 v[230:233], v208, s[28:31], s68 offen offset:1024 sc1\n\t"
      "s_waitcnt lgkmcnt(0)\n\t"
      "buffer_store_dwordx4 v[234:237], v208, s[28:31], s68 offen offset:2048 sc1\n\t"
      ".Llt0_%=:\n\t"
      "v_readlane_b32 s76, v254, 0\n\t"
      "s_cmp_lg_u32 s41, s43\n\t"
      "s_cbranch_scc1 .Lloop_%=\n\t"
      "s_andn2_b32 s76, 4, s76\n\t"
      "s_or_b32 s76, s76, s64\n\t"
      "v_mov_b32_e32 v240, s76\n\t"
      "s_mov_b64 exec, 1\n\t"
      "buffer_store_dword v240, v253, s[24:27], 0 offen sc1\n\t"
      "s_mov_b64 exec, -1\n\t"
      "s_branch .Lloop_%=\n\t"
      ".Lstop2_%=:\n\t"
      "s_cmp_eq_u32 s94, 0\n\t"
      "s_cbranch_scc1 .Llt1_%=\n\t"
      "ds_read_b128 v[226:229], v238 offset:36864\n\t"
      "ds_read_b128 v[230:233], v238 offset:37888\n\t"
      "ds_read_b128 v[234:237], v238 offset:38912\n\t"
      "v_readlane_b32 s68, v249, 0\n\t"
      "s_nop 4\n\t"
      "s_waitcnt lgkmcnt(2)\n\t"
      "buffer_store_dwordx4 v[226:229], v209, s[28:31], s68 offen sc1\n\t"
      "s_waitcnt lgkmcnt(1)\n\t"
      "buffer_store_dwordx4 v[230:233], v209, s[28:31], s68 offen offset:1024 sc1\n\t"
      "s_waitcnt lgkmcnt(0)\n\t"
      "buffer_store_dwordx4 v[234:237], v209, s[28:31], s68 offen offset:2048 sc1\n\t"
      ".Llt1_%=:\n\t"
      "s_cmp_lg_u32 s41, s43\n\t"
      "s_cbranch_scc1 .Lloop_%=\n\t"
      "buffer_load_dword v255, v253, s[24:27], 0 offen sc1\n\t"
      "s_branch .Lloop_%=\n\t"
      ".LslpP3_%=:\n\t"
      "s_sleep 1\n\t"
      "s_sub_u32 s83, s83, 3\n\t"
      "s_cmp_gt_i32 s83, 1\n\t"
      "s_cbranch_scc1 .LslpP3_%=\n\t"
      "s_branch .LretP3_%=\n\t"
      ".LslpP4_%=:\n\t"
      "s_sleep 1\n\t"
      "s_sub_u32 s83, s83, 3\n\t"
      "s_cmp_gt_i32 s83, 1\n\t"
      "s_cbranch_scc1 .LslpP4_%=\n\t"
      "s_branch .LretP4_%=\n\t"
      ".LslpP5_%=:\n\t"
      "s_sleep 1\n\t"
      "s_sub_u32 s83, s83, 3\n\t"
      "s_cmp_gt_i32 s83, 1\n\t"
      "s_cbranch_scc1 .LslpP5_%=\n\t"
      "s_branch .LretP5_%=\n\t"
      ".LslpA_%=:\n\t"
      "s_sleep 1\n\t"
      "s_sub_u32 s83, s83, 3\n\t"
      "s_cmp_gt_i32 s83, 1\n\t"
      "s_cbranch_scc1 .LslpA_%=\n\t"
      "s_branch .LretA_%=\n\t"
      ".LslpB_%=:\n\t"
      "s_sleep 1\n\t"
      "s_sub_u32 s83, s83, 3\n\t"
      "s_cmp_gt_i32 s83, 1\n\t"
      "s_cbranch_scc1 .LslpB_%=\n\t"
      "s_branch .LretB_%=\n\t"
      ".Lsec_%=:\n\t"
      "s_mov_b32 s62, 1\n\t"
      "v_lshl_add_u32 v240, v251, 1, s66\n\t"
      "v_sub_u32_e32 v248, v240, v248\n\t"
      "v_sub_u32_e32 v249, v240, v249\n\t"
      "v_cmp_eq_u32_e32 vcc, 0, v252\n\t"
      "v_mov_b32_e32 v240, 0x6c00000\n\t"
      "s_nop 1\n\t"
      "v_cndmask_b32_e32 v248, v248, v240, vcc\n\t"
      "v_cndmask_b32_e32 v249, v249, v240, vcc\n\t"
      "s_add_u32 s44, s94, 3\n\t"
      "s_add_u32 s85, s44, 1\n\t"
      "s_mov_b32 s83, 0\n\t"
      "s_waitcnt vmcnt(0) lgkmcnt(0)\n\t"
      "s_barrier\n\t"
      "v_readlane_b32 s73, v248, s85\n\t"
      "s_mov_b32 m0, s46\n\t"
      "s_nop 4\n\t"
      "buffer_load_dwordx4 v208, s[20:23], s73 offen lds\n\t"
      "buffer_load_dwordx4 v208, s[20:23], s73 offen offset:1024 lds\n\t"
      "buffer_load_dwordx4 v208, s[20:23], s73 offen offset:2048 lds\n\t"
      "v_readlane_b32 s73, v249, s85\n\t"
      "s_add_u32 m0, s46, 0x3000\n\t"
      "s_nop 4\n\t"
      "buffer_load_dwordx4 v209, s[20:23], s73 offen lds\n\t"
      "buffer_load_dwordx4 v209, s[20:23], s73 offen offset:1024 lds\n\t"
      "buffer_load_dwordx4 v209, s[20:23], s73 offen offset:2048 lds\n\t"
      "s_add_u32 s85, s85, 1\n\t"
      "s_nop 4\n\t"
      "v_readlane_b32 s73, v248, s85\n\t"
      "s_mov_b32 m0, s47\n\t"
      "s_nop 4\n\t"
      "buffer_load_dwordx4 v208, s[20:23], s73 offen lds\n\t"
      "buffer_load_dwordx4 v208, s[20:23], s73 offen offset:1024 lds\n\t"
      "buffer_load_dwordx4 v208, s[20:23], s73 offen offset:2048 lds\n\t"
      "v_readlane_b32 s73, v249, s85\n\t"
      "s_add_u32 m0, s47, 0x3000\n\t"
      "s_nop 4\n\t"
      "buffer_load_dwordx4 v209, s[20:23], s73 offen lds\n\t"
      "buffer_load_dwordx4 v209, s[20:23], s73 offen offset:1024 lds\n\t"
      "buffer_load_dwordx4 v209, s[20:23], s73 offen offset:2048 lds\n\t"
      "s_add_u32 s85, s85, 1\n\t"
      "s_nop 4\n\t"
      "v_readlane_b32 s73, v248, s85\n\t"
      "s_mov_b32 m0, s45\n\t"
      "s_nop 4\n\t"
      "buffer_load_dwordx4 v208, s[20:23], s73 offen lds\n\t"
      "buffer_load_dwordx4 v208, s[20:23], s73 offen offset:1024 lds\n\t"
      "buffer_load_dwordx4 v208, s[20:23], s73 offen offset:2048 lds\n\t"
      "s_waitcnt vmcnt(0)\n\t"
      "s_barrier\n\t"
      "ds_read_b128 v[32:35], v196 offset:0\n\t"
      "ds_read_b128 v[36:39], v197 offset:0\n\t"
      "ds_read_b128 v[40:43], v198 offset:0\n\t"
      "ds_read_b128 v[44:47], v199 offset:0\n\t"
      "ds_read_b128 v[48:51], v197 offset:256\n\t"
      "ds_read_b128 v[52:55], v198 offset:256\n\t"
      "ds_read_b128 v[56:59], v199 offset:256\n\t"
      "ds_read_b128 v[60:63], v196 offset:256\n\t"
      "ds_read_b128 v[64:67], v198 offset:512\n\t"
      "ds_read_b128 v[68:71], v199 offset:512\n\t"
      "ds_read_b128 v[72:75], v196 offset:512\n\t"
      "ds_read_b128 v[76:79], v197 offset:512\n\t"
      "s_waitcnt lgkmcnt(0)\n\t"
      "s_branch .Lloop_%=\n\t"
      ".Lgs_%=:\n\t"
      "s_sleep 8\n\t"
      "buffer_load_dwordx2 v[240:241], v245, s[24:27], 0 offen sc1\n\t"
      "s_memrealtime s[76:77]\n\t"
      "s_waitcnt vmcnt(0) lgkmcnt(0)\n\t"
      "v_subrev_u32_e32 v242, s63, v240\n\t"
      "v_xor_b32_e32 v241, s53, v241\n\t"
      "v_lshrrev_b32_e32 v240, 13, v242\n\t"
      "v_or_b32_e32 v240, v240, v241\n\t"
      "v_cmp_ne_u32_e32 vcc, 0, v240\n\t"
      "v_and_b32_e32 v242, 15, v242\n\t"
      "s_cbranch_vccz .Lgr_%=\n\t"
      "s_sub_u32 s76, s76, s52\n\t"
      "s_cmp_lt_u32 s76, 2000\n\t"
      "s_cbranch_scc1 .Lgs_%=\n\t"
      "v_mov_b32_e32 v242, 64\n\t"
      "s_branch .Lgr_%=\n\t"
      ".Lpb_%=:\n\t"
      "s_mov_b64 vcc, -1\n\t"
      "s_branch .Lp2_%=\n\t"
      ".Lwsb_%=:\n\t"
      "s_mov_b64 vcc, 0\n\t"
      "s_waitcnt vmcnt(12)\n\t"
      "v_lshlrev_b32_e32 v8, 16, v226\n\t"
      "v_and_b32_e32 v9, 0xffff0000, v226\n\t"
      "v_lshlrev_b32_e32 v10, 16, v227\n\t"
      "v_and_b32_e32 v11, 0xffff0000, v227\n\t"
      "v_lshlrev_b32_e32 v12, 16, v228\n\t"
      "v_and_b32_e32 v13, 0xffff0000, v228\n\t"
      "v_lshlrev_b32_e32 v14, 16, v229\n\t"
      "v_and_b32_e32 v15, 0xffff0000, v229\n\t"
      "v_lshlrev_b32_e32 v16, 16, v230\n\t"
      "v_and_b32_e32 v17, 0xffff0000, v230\n\t"
      "v_lshlrev_b32_e32 v18, 16, v231\n\t"
      "v_and_b32_e32 v19, 0xffff0000, v231\n\t"
      "v_lshlrev_b32_e32 v20, 16, v232\n\t"
      "v_and_b32_e32 v21, 0xffff0000, v232\n\t"
      "v_lshlrev_b32_e32 v22, 16, v233\n\t"
      "v_and_b32_e32 v23, 0xffff0000, v233\n\t"
      "v_lshlrev_b32_e32 v24, 16, v234\n\t"
      "v_and_b32_e32 v25, 0xffff0000, v234\n\t"
      "v_lshlrev_b32_e32 v26, 16, v235\n\t"
      "v_and_b32_e32 v27, 0xffff0000, v235\n\t"
      "v_lshlrev_b32_e32 v28, 16, v236\n\t"
      "v_and_b32_e32 v29, 0xffff0000, v236\n\t"
      "v_lshlrev_b32_e32 v30, 16, v237\n\t"
      "v_and_b32_e32 v31, 0xffff0000, v237\n\t"
      "s_add_u32 m0, s41, 0x1e000\n\t"
      "s_mov_b32 s51, 0x0\n\t"
      "buffer_load_dwordx4 v211, s[16:19], s51 offen lds\n\t"
      "buffer_load_dwordx4 v212, s[16:19], s51 offen offset:1024 lds\n\t"
      "buffer_load_dwordx4 v213, s[16:19], s51 offen offset:2048 lds\n\t"
      "s_add_u32 m0, s41, 0x21000\n\t"
      "s_mov_b32 s51, 0xc00\n\t"
      "buffer_load_dwordx4 v211, s[16:19], s51 offen lds\n\t"
      "buffer_load_dwordx4 v212, s[16:19], s51 offen offset:1024 lds\n\t"
      "buffer_load_dwordx4 v213, s[16:19], s51 offen offset:2048 lds\n\t"
      "s_add_u32 m0, s41, 0x24000\n\t"
      "s_mov_b32 s51, 0x6000\n\t"
      "buffer_load_dwordx4 v211, s[16:19], s51 offen lds\n\t"
      "buffer_load_dwordx4 v212, s[16:19], s51 offen offset:1024 lds\n\t"
      "buffer_load_dwordx4 v213, s[16:19], s51 offen offset:2048 lds\n\t"
      "s_add_u32 m0, s41, 0x12000\n\t"
      "s_mov_b32 s51, 0x6c00\n\t"
      "buffer_load_dwordx4 v211, s[16:19], s51 offen lds\n\t"
      "buffer_load_dwordx4 v212, s[16:19], s51 offen offset:1024 lds\n\t"
      "buffer_load_dwordx4 v213, s[16:19], s51 offen offset:2048 lds\n\t"
      "s_add_u32 m0, s41, 0x15000\n\t"
      "s_mov_b32 s51, 0xc000\n\t"
      "buffer_load_dwordx4 v211, s[16:19], s51 offen lds\n\t"
      "buffer_load_dwordx4 v212, s[16:19], s51 offen offset:1024 lds\n\t"
      "buffer_load_dwordx4 v213, s[16:19], s51 offen offset:2048 lds\n\t"
      "s_mov_b32 s44, 1\n\t"
      "s_mov_b32 s85, 4\n\t"
      "s_add_u32 s45, s41, 0x6000\n\t"
      "s_add_u32 s46, s41, 0xc000\n\t"
      "s_mov_b32 s47, s41\n\t"
      "s_waitcnt vmcnt(12)\n\t"
      "s_barrier\n\t"
      "ds_read_b64_tr_b16 a[0:1], v214 offset:49152\n\t"
      "ds_read_b64_tr_b16 a[4:5], v214 offset:49160\n\t"
      "ds_read_b64_tr_b16 a[8:9], v214 offset:49216\n\t"
      "ds_read_b64_tr_b16 a[12:13], v214 offset:49224\n\t"
      "ds_read_b64_tr_b16 a[16:17], v214 offset:49280\n\t"
      "ds_read_b64_tr_b16 a[20:21], v214 offset:49288\n\t"
      "ds_read_b128 v[156:159], v196 offset:0\n\t"
      "ds_read_b128 v[160:163], v196 offset:12288\n\t"
      "ds_read_b128 v[128:131], v196 offset:24576\n\t"
      "s_add_u32 m0, s41, 0x18000\n\t"
      "s_mov_b32 s51, 0xcc00\n\t"
      "buffer_load_dwordx4 v211, s[16:19], s51 offen lds\n\t"
      "buffer_load_dwordx4 v212, s[16:19], s51 offen offset:1024 lds\n\t"
      "buffer_load_dwordx4 v213, s[16:19], s51 offen offset:2048 lds\n\t"
      "s_waitcnt vmcnt(12)\n\t"
      "ds_read_b64_tr_b16 a[2:3], v221 offset:0\n\t"
      "ds_read_b64_tr_b16 a[6:7], v221 offset:8\n\t"
      "ds_read_b64_tr_b16 a[10:11], v221 offset:64\n\t"
      "ds_read_b64_tr_b16 a[14:15], v221 offset:72\n\t"
      "ds_read_b64_tr_b16 a[18:19], v221 offset:128\n\t"
      "ds_read_b64_tr_b16 a[22:23], v221 offset:136\n\t"
      "s_add_u32 m0, s41, 0x1b000\n\t"
      "s_mov_b32 s51, 0x12000\n\t"
      "buffer_load_dwordx4 v211, s[16:19], s51 offen lds\n\t"
      "buffer_load_dwordx4 v212, s[16:19], s51 offen offset:1024 lds\n\t"
      "buffer_load_dwordx4 v213, s[16:19], s51 offen offset:2048 lds\n\t"
      "s_waitcnt lgkmcnt(0)\n\t"
      "v_mfma_f32_16x16x32_bf16 v[32:35], a[0:3], v[156:159], v[8:11]\n\t"
      "v_mfma_f32_16x16x32_bf16 v[36:39], a[4:7], v[156:159], v[12:15]\n\t"
      "s_waitcnt vmcnt(12)\n\t"
      "ds_read_b64_tr_b16 a[24:25], v221 offset:12288\n\t"
      "v_mfma_f32_16x16x32_bf16 v[40:43], a[8:11], v[156:159], v[16:19]\n\t"
      "ds_read_b64_tr_b16 a[28:29], v221 offset:12296\n\t"
      "v_mfma_f32_16x16x32_bf16 v[44:47], a[12:15], v[156:159], v[20:23]\n\t"
      "ds_read_b64_tr_b16 a[32:33], v221 offset:12352\n\t"
      "v_mfma_f32_16x16x32_bf16 v[48:51], a[16:19], v[156:159], v[24:27]\n\t"
      "ds_read_b64_tr_b16 a[36:37], v221 offset:12360\n\t"
      "ds_read_b128 v[136:139], v196 offset:36864\n\t"
      "v_mfma_f32_16x16x32_bf16 v[52:55], a[20:23], v[156:159], v[28:31]\n\t"
      "ds_read_b64_tr_b16 a[40:41], v221 offset:12416\n\t"
      "v_mfma_f32_16x16x32_bf16 v[56:59], a[0:3], v[160:163], v[8:11]\n\t"
      "ds_read_b64_tr_b16 a[44:45], v221 offset:12424\n\t"
      "v_mfma_f32_16x16x32_bf16 v[60:63], a[4:7], v[160:163], v[12:15]\n\t"
      "v_mfma_f32_16x16x32_bf16 v[64:67], a[8:11], v[160:163], v[16:19]\n\t"
      "s_waitcnt vmcnt(9)\n\t"
      "ds_read_b64_tr_b16 a[26:27], v214 offset:0\n\t"
      "v_mfma_f32_16x16x32_bf16 v[68:71], a[12:15], v[160:163], v[20:23]\n\t"
      "ds_read_b64_tr_b16 a[30:31], v214 offset:8\n\t"
      "v_mfma_f32_16x16x32_bf16 v[72:75], a[16:19], v[160:163], v[24:27]\n\t"
      "ds_read_b64_tr_b16 a[34:35], v214 offset:64\n\t"
      "ds_read_b128 v[140:143], v197 offset:0\n\t"
      "v_mfma_f32_16x16x32_bf16 v[76:79], a[20:23], v[160:163], v[28:31]\n\t"
      "ds_read_b64_tr_b16 a[38:39], v214 offset:72\n\t"
      "v_mfma_f32_16x16x32_bf16 v[80:83], a[0:3], v[128:131], v[8:11]\n\t"
      "ds_read_b64_tr_b16 a[42:43], v214 offset:128\n\t"
      "s_add_u32 m0, s41, 0x1e000\n\t"
      "s_mov_b32 s51, 0x12c00\n\t"
      "buffer_load_dwordx4 v211, s[16:19], s51 offen lds\n\t"
      "v_mfma_f32_16x16x32_bf16 v[84:87], a[4:7], v[128:131], v[12:15]\n\t"
      "ds_read_b64_tr_b16 a[46:47], v214 offset:136\n\t"
      "v_mfma_f32_16x16x32_bf16 v[88:91], a[8:11], v[128:131], v[16:19]\n\t"
      "v_mfma_f32_16x16x32_bf16 v[92:95], a[12:15], v[128:131], v[20:23]\n\t"
      "buffer_load_dwordx4 v212, s[16:19], s51 offen offset:1024 lds\n\t"
      "v_mfma_f32_16x16x32_bf16 v[96:99], a[16:19], v[128:131], v[24:27]\n\t"
      "ds_read_b128 v[144:147], v197 offset:12288\n\t"
      "v_mfma_f32_16x16x32_bf16 v[100:103], a[20:23], v[128:131], v[28:31]\n\t"
      "s_waitcnt lgkmcnt(10)\n\t"
      "v_mfma_f32_16x16x32_bf16 v[104:107], a[0:3], v[136:139], v[8:11]\n\t"
      "buffer_load_dwordx4 v213, s[16:19], s51 offen offset:2048 lds\n\t"
      "v_mfma_f32_16x16x32_bf16 v[108:111], a[4:7], v[136:139], v[12:15]\n\t"
      "v_mfma_f32_16x16x32_bf16 v[112:115], a[8:11], v[136:139], v[16:19]\n\t"
      "s_waitcnt vmcnt(9)\n\t"
      "ds_read_b64_tr_b16 a[48:49], v214 offset:12288\n\t"
      "v_mfma_f32_16x16x32_bf16 v[116:119], a[12:15], v[136:139], v[20:23]\n\t"
      "ds_read_b64_tr_b16 a[52:53], v214 offset:12296\n\t"
      "s_add_u32 m0, s41, 0x21000\n\t"
      "s_mov_b32 s51, 0x18000\n\t"
      "buffer_load_dwordx4 v211, s[16:19], s51 offen lds\n\t"
      "v_mfma_f32_16x16x32_bf16 v[120:123], a[16:19], v[136:139], v[24:27]\n\t"
      "ds_read_b64_tr_b16 a[56:57], v214 offset:12352\n\t"
      "ds_read_b128 v[148:151], v197 offset:24576\n\t"
      "v_mfma_f32_16x16x32_bf16 v[132:135], a[20:23], v[136:139], v[28:31]\n\t"
      "ds_read_b64_tr_b16 a[60:61], v214 offset:12360\n\t"
      "s_waitcnt lgkmcnt(6)\n\t"
      "v_mfma_f32_16x16x32_bf16 v[32:35], a[24:27], v[140:143], v[32:35]\n\t"
      "ds_read_b64_tr_b16 a[64:65], v214 offset:12416\n\t"
      "buffer_load_dwordx4 v212, s[16:19], s51 offen offset:1024 lds\n\t"
      "v_mfma_f32_16x16x32_bf16 v[36:39], a[28:31], v[140:143], v[36:39]\n\t"
      "ds_read_b64_tr_b16 a[68:69], v214 offset:12424\n\t"
      "v_mfma_f32_16x16x32_bf16 v[40:43], a[32:35], v[140:143], v[40:43]\n\t"
      "v_mfma_f32_16x16x32_bf16 v[44:47], a[36:39], v[140:143], v[44:47]\n\t"
      "buffer_load_dwordx4 v213, s[16:19], s51 offen offset:2048 lds\n\t"
      "v_mfma_f32_16x16x32_bf16 v[48:51], a[40:43], v[140:143], v[48:51]\n\t"
      "ds_read_b128 v[152:155], v197 offset:36864\n\t"
      "v_mfma_f32_16x16x32_bf16 v[52:55], a[44:47], v[140:143], v[52:55]\n\t"
      "s_waitcnt lgkmcnt(8)\n\t"
      "v_mfma_f32_16x16x32_bf16 v[56:59], a[24:27], v[144:147], v[56:59]\n\t"
      "s_add_u32 m0, s41, 0x24000\n\t"
      "s_mov_b32 s51, 0x18c00\n\t"
      "buffer_load_dwordx4 v211, s[16:19], s51 offen lds\n\t"
      "v_mfma_f32_16x16x32_bf16 v[60:63], a[28:31], v[144:147], v[60:63]\n\t"
      "v_mfma_f32_16x16x32_bf16 v[64:67], a[32:35], v[144:147], v[64:67]\n\t"
      "s_waitcnt vmcnt(10)\n\t"
      "ds_read_b64_tr_b16 a[50:51], v214 offset:24576\n\t"
      "v_mfma_f32_16x16x32_bf16 v[68:71], a[36:39], v[144:147], v[68:71]\n\t"
      "ds_read_b64_tr_b16 a[54:55], v214 offset:24584\n\t"
      "buffer_load_dwordx4 v212, s[16:19], s51 offen offset:1024 lds\n\t"
      "v_mfma_f32_16x16x32_bf16 v[72:75], a[40:43], v[144:147], v[72:75]\n\t"
      "ds_read_b64_tr_b16 a[58:59], v214 offset:24640\n\t"
      "ds_read_b128 v[156:159], v198 offset:0\n\t"
      "v_mfma_f32_16x16x32_bf16 v[76:79], a[44:47], v[144:147], v[76:79]\n\t"
      "ds_read_b64_tr_b16 a[62:63], v214 offset:24648\n\t"
      "s_waitcnt lgkmcnt(9)\n\t"
      "v_mfma_f32_16x16x32_bf16 v[80:83], a[24:27], v[148:151], v[80:83]\n\t"
      "ds_read_b64_tr_b16 a[66:67], v214 offset:24704\n\t"
      "buffer_load_dwordx4 v213, s[16:19], s51 offen offset:2048 lds\n\t"
      "v_mfma_f32_16x16x32_bf16 v[84:87], a[28:31], v[148:151], v[84:87]\n\t"
      "ds_read_b64_tr_b16 a[70:71], v214 offset:24712\n\t"
      "v_mfma_f32_16x16x32_bf16 v[88:91], a[32:35], v[148:151], v[88:91]\n\t"
      "v_mfma_f32_16x16x32_bf16 v[92:95], a[36:39], v[148:151], v[92:95]\n\t"
      "s_add_u32 m0, s41, 0x12000\n\t"
      "s_mov_b32 s51, 0x1e000\n\t"
      "buffer_load_dwordx4 v211, s[16:19], s51 offen lds\n\t"
      "v_mfma_f32_16x16x32_bf16 v[96:99], a[40:43], v[148:151], v[96:99]\n\t"
      "ds_read_b128 v[160:163], v198 offset:12288\n\t"
      "v_mfma_f32_16x16x32_bf16 v[100:103], a[44:47], v[148:151], v[100:103]\n\t"
      "s_waitcnt lgkmcnt(8)\n\t"
      "v_mfma_f32_16x16x32_bf16 v[104:107], a[24:27], v[152:155], v[104:107]\n\t"
      "buffer_load_dwordx4 v212, s[16:19], s51 offen offset:1024 lds\n\t"
      "v_mfma_f32_16x16x32_bf16 v[108:111], a[28:31], v[152:155], v[108:111]\n\t"
      "v_mfma_f32_16x16x32_bf16 v[112:115], a[32:35], v[152:155], v[112:115]\n\t"
      "s_waitcnt vmcnt(11)\n\t"
      "ds_read_b64_tr_b16 a[72:73], v214 offset:36864\n\t"
      "v_mfma_f32_16x16x32_bf16 v[116:119], a[36:39], v[152:155], v[116:119]\n\t"
      "ds_read_b64_tr_b16 a[76:77], v214 offset:36872\n\t"
      "buffer_load_dwordx4 v213, s[16:19], s51 offen offset:2048 lds\n\t"
      "v_mfma_f32_16x16x32_bf16 v[120:123], a[40:43], v[152:155], v[120:123]\n\t"
      "ds_read_b64_tr_b16 a[80:81], v214 offset:36928\n\t"
      "ds_read_b128 v[128:131], v198 offset:24576\n\t"
      "v_mfma_f32_16x16x32_bf16 v[132:135], a[44:47], v[152:155], v[132:135]\n\t"
      "ds_read_b64_tr_b16 a[84:85], v214 offset:36936\n\t"
      "s_waitcnt lgkmcnt(6)\n\t"
      "v_mfma_f32_16x16x32_bf16 v[32:35], a[48:51], v[156:159], v[32:35]\n\t"
      "ds_read_b64_tr_b16 a[88:89], v214 offset:36992\n\t"
      "s_add_u32 m0, s41, 0x15000\n\t"
      "s_mov_b32 s51, 0x1ec00\n\t"
      "buffer_load_dwordx4 v211, s[16:19], s51 offen lds\n\t"
      "v_mfma_f32_16x16x32_bf16 v[36:39], a[52:55], v[156:159], v[36:39]\n\t"
      "ds_read_b64_tr_b16 a[92:93], v214 offset:37000\n\t"
      "v_mfma_f32_16x16x32_bf16 v[40:43], a[56:59], v[156:159], v[40:43]\n\t"
      "v_mfma_f32_16x16x32_bf16 v[44:47], a[60:63], v[156:159], v[44:47]\n\t"
      "buffer_load_dwordx4 v212, s[16:19], s51 offen offset:1024 lds\n\t"
      "v_mfma_f32_16x16x32_bf16 v[48:51], a[64:67], v[156:159], v[48:51]\n\t"
      "ds_read_b128 v[136:139], v198 offset:36864\n\t"
      "v_mfma_f32_16x16x32_bf16 v[52:55], a[68:71], v[156:159], v[52:55]\n\t"
      "s_waitcnt lgkmcnt(8)\n\t"
      "v_mfma_f32_16x16x32_bf16 v[56:59], a[48:51], v[160:163], v[56:59]\n\t"
      "buffer_load_dwordx4 v213, s[16:19], s51 offen offset:2048 lds\n\t"
      "v_mfma_f32_16x16x32_bf16 v[60:63], a[52:55], v[160:163], v[60:63]\n\t"
      "v_mfma_f32_16x16x32_bf16 v[64:67], a[56:59], v[160:163], v[64:67]\n\t"
      "s_waitcnt vmcnt(12)\n\t"
      "ds_read_b64_tr_b16 a[74:75], v214 offset:49152\n\t"
      "v_mfma_f32_16x16x32_bf16 v[68:71], a[60:63], v[160:163], v[68:71]\n\t"
      "ds_read_b64_tr_b16 a[78:79], v214 offset:49160\n\t"
      "s_add_u32 m0, s41, 0x18000\n\t"
      "s_mov_b32 s51, 0x24000\n\t"
      "buffer_load_dwordx4 v211, s[16:19], s51 offen lds\n\t"
      "v_mfma_f32_16x16x32_bf16 v[72:75], a[64:67], v[160:163], v[72:75]\n\t"
      "ds_read_b64_tr_b16 a[82:83], v214 offset:49216\n\t"
      "ds_read_b128 v[140:143], v199 offset:0\n\t"
      "v_mfma_f32_16x16x32_bf16 v[76:79], a[68:71], v[160:163], v[76:79]\n\t"
      "ds_read_b64_tr_b16 a[86:87], v214 offset:49224\n\t"
      "s_waitcnt lgkmcnt(9)\n\t"
      "v_mfma_f32_16x16x32_bf16 v[80:83], a[48:51], v[128:131], v[80:83]\n\t"
      "ds_read_b64_tr_b16 a[90:91], v214 offset:49280\n\t"
      "buffer_load_dwordx4 v212, s[16:19], s51 offen offset:1024 lds\n\t"
      "v_mfma_f32_16x16x32_bf16 v[84:87], a[52:55], v[128:131], v[84:87]\n\t"
      "ds_read_b64_tr_b16 a[94:95], v214 offset:49288\n\t"
      "v_mfma_f32_16x16x32_bf16 v[88:91], a[56:59], v[128:131], v[88:91]\n\t"
      "v_mfma_f32_16x16x32_bf16 v[92:95], a[60:63], v[128:131], v[92:95]\n\t"
      "buffer_load_dwordx4 v213, s[16:19], s51 offen offset:2048 lds\n\t"
      "v_mfma_f32_16x16x32_bf16 v[96:99], a[64:67], v[128:131], v[96:99]\n\t"
      "ds_read_b128 v[144:147], v199 offset:12288\n\t"
      "v_mfma_f32_16x16x32_bf16 v[100:103], a[68:71], v[128:131], v[100:103]\n\t"
      "s_waitcnt lgkmcnt(8)\n\t"
      "v_mfma_f32_16x16x32_bf16 v[104:107], a[48:51], v[136:139], v[104:107]\n\t"
      "s_add_u32 m0, s41, 0x1b000\n\t"
      "s_mov_b32 s51, 0x24c00\n\t"
      "buffer_load_dwordx4 v211, s[16:19], s51 offen lds\n\t"
      "v_mfma_f32_16x16x32_bf16 v[108:111], a[52:55], v[136:139], v[108:111]\n\t"
      "v_mfma_f32_16x16x32_bf16 v[112:115], a[56:59], v[136:139], v[112:115]\n\t"
      "s_waitcnt vmcnt(13)\n\t"
      "ds_read_b64_tr_b16 a[96:97], v221 offset:0\n\t"
      "v_mfma_f32_16x16x32_bf16 v[116:119], a[60:63], v[136:139], v[116:119]\n\t"
      "ds_read_b64_tr_b16 a[100:101], v221 offset:8\n\t"
      "buffer_load_dwordx4 v212, s[16:19], s51 offen offset:1024 lds\n\t"
      "v_mfma_f32_16x16x32_bf16 v[120:123], a[64:67], v[136:139], v[120:123]\n\t"
      "ds_read_b64_tr_b16 a[104:105], v221 offset:64\n\t"
      "ds_read_b128 v[148:151], v199 offset:24576\n\t"
      "v_mfma_f32_16x16x32_bf16 v[132:135], a[68:71], v[136:139], v[132:135]\n\t"
      "ds_read_b64_tr_b16 a[108:109], v221 offset:72\n\t"
      "s_waitcnt lgkmcnt(6)\n\t"
      "v_mfma_f32_16x16x32_bf16 v[32:35], a[72:75], v[140:143], v[32:35]\n\t"
      "ds_read_b64_tr_b16 a[112:113], v221 offset:128\n\t"
      "buffer_load_dwordx4 v213, s[16:19], s51 offen offset:2048 lds\n\t"
      "v_mfma_f32_16x16x32_bf16 v[36:39], a[76:79], v[140:143], v[36:39]\n\t"
      "ds_read_b64_tr_b16 a[116:117], v221 offset:136\n\t"
      "v_mfma_f32_16x16x32_bf16 v[40:43], a[80:83], v[140:143], v[40:43]\n\t"
      "v_mfma_f32_16x16x32_bf16 v[44:47], a[84:87], v[140:143], v[44:47]\n\t"
      "s_add_u32 m0, s41, 0x1e000\n\t"
      "s_mov_b32 s51, 0x2a000\n\t"
      "buffer_load_dwordx4 v211, s[16:19], s51 offen lds\n\t"
      "v_mfma_f32_16x16x32_bf16 v[48:51], a[88:91], v[140:143], v[48:51]\n\t"
      "ds_read_b128 v[152:155], v199 offset:36864\n\t"
      "v_mfma_f32_16x16x32_bf16 v[52:55], a[92:95], v[140:143], v[52:55]\n\t"
      "s_waitcnt lgkmcnt(8)\n\t"
      "v_mfma_f32_16x16x32_bf16 v[56:59], a[72:75], v[144:147], v[56:59]\n\t"
      "buffer_load_dwordx4 v212, s[16:19], s51 offen offset:1024 lds\n\t"
      "v_mfma_f32_16x16x32_bf16 v[60:63], a[76:79], v[144:147], v[60:63]\n\t"
      "v_mfma_f32_16x16x32_bf16 v[64:67], a[80:83], v[144:147], v[64:67]\n\t"
      "s_waitcnt vmcnt(14)\n\t"
      "ds_read_b64_tr_b16 a[98:99], v221 offset:12288\n\t"
      "v_mfma_f32_16x16x32_bf16 v[68:71], a[84:87], v[144:147], v[68:71]\n\t"
      "ds_read_b64_tr_b16 a[102:103], v221 offset:12296\n\t"
      "buffer_load_dwordx4 v213, s[16:19], s51 offen offset:2048 lds\n\t"
      "v_mfma_f32_16x16x32_bf16 v[72:75], a[88:91], v[144:147], v[72:75]\n\t"
      "ds_read_b64_tr_b16 a[106:107], v221 offset:12352\n\t"
      "ds_read_b128 v[156:159], v197 offset:256\n\t"
      "v_mfma_f32_16x16x32_bf16 v[76:79], a[92:95], v[144:147], v[76:79]\n\t"
      "ds_read_b64_tr_b16 a[110:111], v221 offset:12360\n\t"
      "s_waitcnt lgkmcnt(9)\n\t"
      "v_mfma_f32_16x16x32_bf16 v[80:83], a[72:75], v[148:151], v[80:83]\n\t"
      "ds_read_b64_tr_b16 a[114:115], v221 offset:12416\n\t"
      "s_waitcnt lgkmcnt(7)\n\t"
      "s_add_u32 m0, s41, 0x21000\n\t"
      "s_mov_b32 s51, 0x2ac00\n\t"
      "buffer_load_dwordx4 v211, s[16:19], s51 offen lds\n\t"
      "v_mfma_f32_16x16x32_bf16 v[84:87], a[76:79], v[148:151], v[84:87]\n\t"
      "ds_read_b64_tr_b16 a[118:119], v221 offset:12424\n\t"
      "v_mfma_f32_16x16x32_bf16 v[88:91], a[80:83], v[148:151], v[88:91]\n\t"
      "v_mfma_f32_16x16x32_bf16 v[92:95], a[84:87], v[148:151], v[92:95]\n\t"
      "buffer_load_dwordx4 v212, s[16:19], s51 offen offset:1024 lds\n\t"
      "v_mfma_f32_16x16x32_bf16 v[96:99], a[88:91], v[148:151], v[96:99]\n\t"
      "ds_read_b128 v[160:163], v197 offset:12544\n\t"
      "v_mfma_f32_16x16x32_bf16 v[100:103], a[92:95], v[148:151], v[100:103]\n\t"
      "s_waitcnt lgkmcnt(8)\n\t"
      "v_mfma_f32_16x16x32_bf16 v[104:107], a[72:75], v[152:155], v[104:107]\n\t"
      "buffer_load_dwordx4 v213, s[16:19], s51 offen offset:2048 lds\n\t"
      "v_mfma_f32_16x16x32_bf16 v[108:111], a[76:79], v[152:155], v[108:111]\n\t"
      "v_mfma_f32_16x16x32_bf16 v[112:115], a[80:83], v[152:155], v[112:115]\n\t"
      "s_waitcnt vmcnt(15)\n\t"
      "ds_read_b64_tr_b16 a[120:121], v214 offset:0\n\t"
      "v_mfma_f32_16x16x32_bf16 v[116:119], a[84:87], v[152:155], v[116:119]\n\t"
      "ds_read_b64_tr_b16 a[124:125], v214 offset:8\n\t"
      "s_waitcnt lgkmcnt(3)\n\t"
      "s_add_u32 m0, s41, 0x24000\n\t"
      "s_mov_b32 s51, 0x30000\n\t"
      "buffer_load_dwordx4 v211, s[16:19], s51 offen lds\n\t"
      "v_mfma_f32_16x16x32_bf16 v[120:123], a[88:91], v[152:155], v[120:123]\n\t"
      "ds_read_b64_tr_b16 a[128:129], v214 offset:64\n\t"
      "ds_read_b128 v[128:131], v197 offset:24832\n\t"
      "v_mfma_f32_16x16x32_bf16 v[132:135], a[92:95], v[152:155], v[132:135]\n\t"
      "ds_read_b64_tr_b16 a[132:133], v214 offset:72\n\t"
      "v_mfma_f32_16x16x32_bf16 v[32:35], a[96:99], v[156:159], v[32:35]\n\t"
      "ds_read_b64_tr_b16 a[136:137], v214 offset:128\n\t"
      "buffer_load_dwordx4 v212, s[16:19], s51 offen offset:1024 lds\n\t"
      "v_mfma_f32_16x16x32_bf16 v[36:39], a[100:103], v[156:159], v[36:39]\n\t"
      "ds_read_b64_tr_b16 a[140:141], v214 offset:136\n\t"
      "v_mfma_f32_16x16x32_bf16 v[40:43], a[104:107], v[156:159], v[40:43]\n\t"
      "v_mfma_f32_16x16x32_bf16 v[44:47], a[108:111], v[156:159], v[44:47]\n\t"
      "buffer_load_dwordx4 v213, s[16:19], s51 offen offset:2048 lds\n\t"
      "v_mfma_f32_16x16x32_bf16 v[48:51], a[112:115], v[156:159], v[48:51]\n\t"
      "ds_read_b128 v[136:139], v197 offset:37120\n\t"
      "v_mfma_f32_16x16x32_bf16 v[52:55], a[116:119], v[156:159], v[52:55]\n\t"
      "s_waitcnt lgkmcnt(8)\n\t"
      "v_mfma_f32_16x16x32_bf16 v[56:59], a[96:99], v[160:163], v[56:59]\n\t"
      "v_mfma_f32_16x16x32_bf16 v[60:63], a[100:103], v[160:163], v[60:63]\n\t"
      "v_mfma_f32_16x16x32_bf16 v[64:67], a[104:107], v[160:163], v[64:67]\n\t"
      "s_waitcnt vmcnt(15)\n\t"
      "ds_read_b64_tr_b16 a[122:123], v214 offset:12288\n\t"
      "s_waitcnt lgkmcnt(2)\n\t"
      "s_add_u32 m0, s41, 0x12000\n\t"
      "s_mov_b32 s51, 0x30c00\n\t"
      "buffer_load_dwordx4 v211, s[16:19], s51 offen lds\n\t"
      "v_mfma_f32_16x16x32_bf16 v[68:71], a[108:111], v[160:163], v[68:71]\n\t"
      "ds_read_b64_tr_b16 a[126:127], v214 offset:12296\n\t"
      "v_mfma_f32_16x16x32_bf16 v[72:75], a[112:115], v[160:163], v[72:75]\n\t"
      "ds_read_b64_tr_b16 a[130:131], v214 offset:12352\n\t"
      "ds_read_b128 v[140:143], v198 offset:256\n\t"
      "v_mfma_f32_16x16x32_bf16 v[76:79], a[116:119], v[160:163], v[76:79]\n\t"
      "ds_read_b64_tr_b16 a[134:135], v214 offset:12360\n\t"
      "buffer_load_dwordx4 v212, s[16:19], s51 offen offset:1024 lds\n\t"
      "v_mfma_f32_16x16x32_bf16 v[80:83], a[96:99], v[128:131], v[80:83]\n\t"
      "ds_read_b64_tr_b16 a[138:139], v214 offset:12416\n\t"
      "v_mfma_f32_16x16x32_bf16 v[84:87], a[100:103], v[128:131], v[84:87]\n\t"
      "ds_read_b64_tr_b16 a[142:143], v214 offset:12424\n\t"
      "v_mfma_f32_16x16x32_bf16 v[88:91], a[104:107], v[128:131], v[88:91]\n\t"
      "buffer_load_dwordx4 v213, s[16:19], s51 offen offset:2048 lds\n\t"
      "v_mfma_f32_16x16x32_bf16 v[92:95], a[108:111], v[128:131], v[92:95]\n\t"
      "v_mfma_f32_16x16x32_bf16 v[96:99], a[112:115], v[128:131], v[96:99]\n\t"
      "ds_read_b128 v[144:147], v198 offset:12544\n\t"
      "v_mfma_f32_16x16x32_bf16 v[100:103], a[116:119], v[128:131], v[100:103]\n\t"
      "s_waitcnt lgkmcnt(8)\n\t"
      "v_mfma_f32_16x16x32_bf16 v[104:107], a[96:99], v[136:139], v[104:107]\n\t"
      "v_mfma_f32_16x16x32_bf16 v[108:111], a[100:103], v[136:139], v[108:111]\n\t"
      "v_mfma_f32_16x16x32_bf16 v[112:115], a[104:107], v[136:139], v[112:115]\n\t"
      "s_waitcnt vmcnt(15)\n\t"
      "ds_read_b64_tr_b16 a[144:145], v214 offset:24576\n\t"
      "s_waitcnt lgkmcnt(2)\n\t"
      "s_add_u32 m0, s41, 0x15000\n\t"
      "s_mov_b32 s51, 0x36000\n\t"
      "buffer_load_dwordx4 v211, s[16:19], s51 offen lds\n\t"
      "v_mfma_f32_16x16x32_bf16 v[116:119], a[108:111], v[136:139], v[116:119]\n\t"
      "ds_read_b64_tr_b16 a[148:149], v214 offset:24584\n\t"
      "v_mfma_f32_16x16x32_bf16 v[120:123], a[112:115], v[136:139], v[120:123]\n\t"
      "ds_read_b64_tr_b16 a[152:153], v214 offset:24640\n\t"
      "ds_read_b128 v[148:151], v198 offset:24832\n\t"
      "v_mfma_f32_16x16x32_bf16 v[132:135], a[116:119], v[136:139], v[132:135]\n\t"
      "ds_read_b64_tr_b16 a[156:157], v214 offset:24648\n\t"
      "buffer_load_dwordx4 v212, s[16:19], s51 offen offset:1024 lds\n\t"
      "v_mfma_f32_16x16x32_bf16 v[32:35], a[120:123], v[140:143], v[32:35]\n\t"
      "ds_read_b64_tr_b16 a[160:161], v214 offset:24704\n\t"
      "v_mfma_f32_16x16x32_bf16 v[36:39], a[124:127], v[140:143], v[36:39]\n\t"
      "ds_read_b64_tr_b16 a[164:165], v214 offset:24712\n\t"
      "v_mfma_f32_16x16x32_bf16 v[40:43], a[128:131], v[140:143], v[40:43]\n\t"
      "buffer_load_dwordx4 v213, s[16:19], s51 offen offset:2048 lds\n\t"
      "v_mfma_f32_16x16x32_bf16 v[44:47], a[132:135], v[140:143], v[44:47]\n\t"
      "v_mfma_f32_16x16x32_bf16 v[48:51], a[136:139], v[140:143], v[48:51]\n\t"
      "ds_read_b128 v[152:155], v198 offset:37120\n\t"
      "v_mfma_f32_16x16x32_bf16 v[52:55], a[140:143], v[140:143], v[52:55]\n\t"
      "s_waitcnt lgkmcnt(8)\n\t"
      "v_mfma_f32_16x16x32_bf16 v[56:59], a[120:123], v[144:147], v[56:59]\n\t"
      "v_mfma_f32_16x16x32_bf16 v[60:63], a[124:127], v[144:147], v[60:63]\n\t"
      "v_mfma_f32_16x16x32_bf16 v[64:67], a[128:131], v[144:147], v[64:67]\n\t"
      "s_waitcnt vmcnt(15)\n\t"
      "ds_read_b64_tr_b16 a[146:147], v214 offset:36864\n\t"
      "s_waitcnt lgkmcnt(2)\n\t"
      "s_add_u32 m0, s41, 0x18000\n\t"
      "s_mov_b32 s51, 0x36c00\n\t"
      "buffer_load_dwordx4 v211, s[16:19], s51 offen lds\n\t"
      "v_mfma_f32_16x16x32_bf16 v[68:71], a[132:135], v[144:147], v[68:71]\n\t"
      "ds_read_b64_tr_b16 a[150:151], v214 offset:36872\n\t"
      "v_mfma_f32_16x16x32_bf16 v[72:75], a[136:139], v[144:147], v[72:75]\n\t"
      "ds_read_b64_tr_b16 a[154:155], v214 offset:36928\n\t"
      "ds_read_b128 v[156:159], v199 offset:256\n\t"
      "v_mfma_f32_16x16x32_bf16 v[76:79], a[140:143], v[144:147], v[76:79]\n\t"
      "ds_read_b64_tr_b16 a[158:159], v214 offset:36936\n\t"
      "buffer_load_dwordx4 v212, s[16:19], s51 offen offset:1024 lds\n\t"
      "v_mfma_f32_16x16x32_bf16 v[80:83], a[120:123], v[148:151], v[80:83]\n\t"
      "ds_read_b64_tr_b16 a[162:163], v214 offset:36992\n\t"
      "v_mfma_f32_16x16x32_bf16 v[84:87], a[124:127], v[148:151], v[84:87]\n\t"
      "ds_read_b64_tr_b16 a[166:167], v214 offset:37000\n\t"
      "v_mfma_f32_16x16x32_bf16 v[88:91], a[128:131], v[148:151], v[88:91]\n\t"
      "buffer_load_dwordx4 v213, s[16:19], s51 offen offset:2048 lds\n\t"
      "v_mfma_f32_16x16x32_bf16 v[92:95], a[132:135], v[148:151], v[92:95]\n\t"
      "v_mfma_f32_16x16x32_bf16 v[96:99], a[136:139], v[148:151], v[96:99]\n\t"
      "ds_read_b128 v[160:163], v199 offset:12544\n\t"
      "v_mfma_f32_16x16x32_bf16 v[100:103], a[140:143], v[148:151], v[100:103]\n\t"
      "s_waitcnt lgkmcnt(8)\n\t"
      "v_mfma_f32_16x16x32_bf16 v[104:107], a[120:123], v[152:155], v[104:107]\n\t"
      "v_mfma_f32_16x16x32_bf16 v[108:111], a[124:127], v[152:155], v[108:111]\n\t"
      "v_mfma_f32_16x16x32_bf16 v[112:115], a[128:131], v[152:155], v[112:115]\n\t"
      "s_waitcnt vmcnt(15)\n\t"
      "ds_read_b64_tr_b16 a[168:169], v214 offset:49152\n\t"
      "s_waitcnt lgkmcnt(2)\n\t"
      "s_add_u32 m0, s41, 0x1b000\n\t"
      "s_mov_b32 s51, 0x3c000\n\t"
      "buffer_load_dwordx4 v211, s[16:19], s51 offen lds\n\t"
      "v_mfma_f32_16x16x32_bf16 v[116:119], a[132:135], v[152:155], v[116:119]\n\t"
      "ds_read_b64_tr_b16 a[172:173], v214 offset:49160\n\t"
      "v_mfma_f32_16x16x32_bf16 v[120:123], a[136:139], v[152:155], v[120:123]\n\t"
      "ds_read_b64_tr_b16 a[176:177], v214 offset:49216\n\t"
      "ds_read_b128 v[128:131], v199 offset:24832\n\t"
      "v_mfma_f32_16x16x32_bf16 v[132:135], a[140:143], v[152:155], v[132:135]\n\t"
      "ds_read_b64_tr_b16 a[180:181], v214 offset:49224\n\t"
      "buffer_load_dwordx4 v212, s[16:19], s51 offen offset:1024 lds\n\t"
      "v_mfma_f32_16x16x32_bf16 v[32:35], a[144:147], v[156:159], v[32:35]\n\t"
      "ds_read_b64_tr_b16 a[184:185], v214 offset:49280\n\t"
      "v_mfma_f32_16x16x32_bf16 v[36:39], a[148:151], v[156:159], v[36:39]\n\t"
      "ds_read_b64_tr_b16 a[188:189], v214 offset:49288\n\t"
      "v_mfma_f32_16x16x32_bf16 v[40:43], a[152:155], v[156:159], v[40:43]\n\t"
      "buffer_load_dwordx4 v213, s[16:19], s51 offen offset:2048 lds\n\t"
      "v_mfma_f32_16x16x32_bf16 v[44:47], a[156:159], v[156:159], v[44:47]\n\t"
      "v_mfma_f32_16x16x32_bf16 v[48:51], a[160:163], v[156:159], v[48:51]\n\t"
      "ds_read_b128 v[136:139], v199 offset:37120\n\t"
      "v_mfma_f32_16x16x32_bf16 v[52:55], a[164:167], v[156:159], v[52:55]\n\t"
      "s_waitcnt lgkmcnt(8)\n\t"
      "v_mfma_f32_16x16x32_bf16 v[56:59], a[144:147], v[160:163], v[56:59]\n\t"
      "v_mfma_f32_16x16x32_bf16 v[60:63], a[148:151], v[160:163], v[60:63]\n\t"
      "v_mfma_f32_16x16x32_bf16 v[64:67], a[152:155], v[160:163], v[64:67]\n\t"
      "s_waitcnt vmcnt(15)\n\t"
      "ds_read_b64_tr_b16 a[170:171], v221 offset:0\n\t"
      "s_waitcnt lgkmcnt(2)\n\t"
      "s_add_u32 m0, s41, 0x1e000\n\t"
      "s_mov_b32 s51, 0x3cc00\n\t"
      "buffer_load_dwordx4 v211, s[16:19], s51 offen lds\n\t"
      "v_mfma_f32_16x16x32_bf16 v[68:71], a[156:159], v[160:163], v[68:71]\n\t"
      "ds_read_b64_tr_b16 a[174:175], v221 offset:8\n\t"
      "v_mfma_f32_16x16x32_bf16 v[72:75], a[160:163], v[160:163], v[72:75]\n\t"
      "ds_read_b64_tr_b16 a[178:179], v221 offset:64\n\t"
      "ds_read_b128 v[140:143], v196 offset:256\n\t"
      "v_mfma_f32_16x16x32_bf16 v[76:79], a[164:167], v[160:163], v[76:79]\n\t"
      "ds_read_b64_tr_b16 a[182:183], v221 offset:72\n\t"
      "buffer_load_dwordx4 v212, s[16:19], s51 offen offset:1024 lds\n\t"
      "v_mfma_f32_16x16x32_bf16 v[80:83], a[144:147], v[128:131], v[80:83]\n\t"
      "ds_read_b64_tr_b16 a[186:187], v221 offset:128\n\t"
      "v_mfma_f32_16x16x32_bf16 v[84:87], a[148:151], v[128:131], v[84:87]\n\t"
      "ds_read_b64_tr_b16 a[190:191], v221 offset:136\n\t"
      "v_mfma_f32_16x16x32_bf16 v[88:91], a[152:155], v[128:131], v[88:91]\n\t"
      "buffer_load_dwordx4 v213, s[16:19], s51 offen offset:2048 lds\n\t"
      "v_mfma_f32_16x16x32_bf16 v[92:95], a[156:159], v[128:131], v[92:95]\n\t"
      "v_mfma_f32_16x16x32_bf16 v[96:99], a[160:163], v[128:131], v[96:99]\n\t"
      "ds_read_b128 v[144:147], v196 offset:12544\n\t"
      "v_mfma_f32_16x16x32_bf16 v[100:103], a[164:167], v[128:131], v[100:103]\n\t"
      "s_waitcnt lgkmcnt(8)\n\t"
      "v_mfma_f32_16x16x32_bf16 v[104:107], a[144:147], v[136:139], v[104:107]\n\t"
      "v_mfma_f32_16x16x32_bf16 v[108:111], a[148:151], v[136:139], v[108:111]\n\t"
      "v_mfma_f32_16x16x32_bf16 v[112:115], a[152:155], v[136:139], v[112:115]\n\t"
      "s_waitcnt vmcnt(15)\n\t"
      "ds_read_b64_tr_b16 a[192:193], v221 offset:12288\n\t"
      "s_waitcnt lgkmcnt(2)\n\t"
      "s_add_u32 m0, s41, 0x21000\n\t"
      "s_mov_b32 s51, 0x42000\n\t"
      "buffer_load_dwordx4 v211, s[16:19], s51 offen lds\n\t"
      "v_mfma_f32_16x16x32_bf16 v[116:119], a[156:159], v[136:139], v[116:119]\n\t"
      "ds_read_b64_tr_b16 a[196:197], v221 offset:12296\n\t"
      "v_mfma_f32_16x16x32_bf16 v[120:123], a[160:163], v[136:139], v[120:123]\n\t"
      "ds_read_b64_tr_b16 a[200:201], v221 offset:12352\n\t"
      "ds_read_b128 v[148:151], v196 offset:24832\n\t"
      "v_mfma_f32_16x16x32_bf16 v[132:135], a[164:167], v[136:139], v[132:135]\n\t"
      "ds_read_b64_tr_b16 a[204:205], v221 offset:12360\n\t"
      "buffer_load_dwordx4 v212, s[16:19], s51 offen offset:1024 lds\n\t"
      "v_mfma_f32_16x16x32_bf16 v[32:35], a[168:171], v[140:143], v[32:35]\n\t"
      "ds_read_b64_tr_b16 a[208:209], v221 offset:12416\n\t"
      "v_mfma_f32_16x16x32_bf16 v[36:39], a[172:175], v[140:143], v[36:39]\n\t"
      "ds_read_b64_tr_b16 a[212:213], v221 offset:12424\n\t"
      "v_mfma_f32_16x16x32_bf16 v[40:43], a[176:179], v[140:143], v[40:43]\n\t"
      "buffer_load_dwordx4 v213, s[16:19], s51 offen offset:2048 lds\n\t"
      "v_mfma_f32_16x16x32_bf16 v[44:47], a[180:183], v[140:143], v[44:47]\n\t"
      "v_mfma_f32_16x16x32_bf16 v[48:51], a[184:187], v[140:143], v[48:51]\n\t"
      "ds_read_b128 v[152:155], v196 offset:37120\n\t"
      "v_mfma_f32_16x16x32_bf16 v[52:55], a[188:191], v[140:143], v[52:55]\n\t"
      "s_waitcnt lgkmcnt(8)\n\t"
      "v_mfma_f32_16x16x32_bf16 v[56:59], a[168:171], v[144:147], v[56:59]\n\t"
      "v_mfma_f32_16x16x32_bf16 v[60:63], a[172:175], v[144:147], v[60:63]\n\t"
      "v_mfma_f32_16x16x32_bf16 v[64:67], a[176:179], v[144:147], v[64:67]\n\t"
      "s_waitcnt vmcnt(15)\n\t"
      "ds_read_b64_tr_b16 a[194:195], v214 offset:0\n\t"
      "s_waitcnt lgkmcnt(2)\n\t"
      "s_add_u32 m0, s41, 0x24000\n\t"
      "s_mov_b32 s51, 0x42c00\n\t"
      "buffer_load_dwordx4 v211, s[16:19], s51 offen lds\n\t"
      "v_mfma_f32_16x16x32_bf16 v[68:71], a[180:183], v[144:147], v[68:71]\n\t"
      "ds_read_b64_tr_b16 a[198:199], v214 offset:8\n\t"
      "v_mfma_f32_16x16x32_bf16 v[72:75], a[184:187], v[144:147], v[72:75]\n\t"
      "ds_read_b64_tr_b16 a[202:203], v214 offset:64\n\t"
      "ds_read_b128 v[156:159], v198 offset:512\n\t"
      "v_mfma_f32_16x16x32_bf16 v[76:79], a[188:191], v[144:147], v[76:79]\n\t"
      "ds_read_b64_tr_b16 a[206:207], v214 offset:72\n\t"
      "buffer_load_dwordx4 v212, s[16:19], s51 offen offset:1024 lds\n\t"
      "v_mfma_f32_16x16x32_bf16 v[80:83], a[168:171], v[148:151], v[80:83]\n\t"
      "ds_read_b64_tr_b16 a[210:211], v214 offset:128\n\t"
      "v_mfma_f32_16x16x32_bf16 v[84:87], a[172:175], v[148:151], v[84:87]\n\t"
      "ds_read_b64_tr_b16 a[214:215], v214 offset:136\n\t"
      "v_mfma_f32_16x16x32_bf16 v[88:91], a[176:179], v[148:151], v[88:91]\n\t"
      "buffer_load_dwordx4 v213, s[16:19], s51 offen offset:2048 lds\n\t"
      "v_mfma_f32_16x16x32_bf16 v[92:95], a[180:183], v[148:151], v[92:95]\n\t"
      "v_mfma_f32_16x16x32_bf16 v[96:99], a[184:187], v[148:151], v[96:99]\n\t"
      "ds_read_b128 v[160:163], v198 offset:12800\n\t"
      "v_mfma_f32_16x16x32_bf16 v[100:103], a[188:191], v[148:151], v[100:103]\n\t"
      "s_waitcnt lgkmcnt(8)\n\t"
      "v_mfma_f32_16x16x32_bf16 v[104:107], a[168:171], v[152:155], v[104:107]\n\t"
      "s_add_u32 m0, s41, 0xc000\n\t"
      "s_add_u32 s51, s40, 0xc00000\n\t"
      "buffer_load_dwordx4 v208, s[20:23], s51 offen lds\n\t"
      "v_mfma_f32_16x16x32_bf16 v[108:111], a[172:175], v[152:155], v[108:111]\n\t"
      "v_mfma_f32_16x16x32_bf16 v[112:115], a[176:179], v[152:155], v[112:115]\n\t"
      "s_waitcnt vmcnt(16)\n\t"
      "ds_read_b64_tr_b16 a[216:217], v214 offset:12288\n\t"
      "buffer_load_dwordx4 v208, s[20:23], s51 offen offset:1024 lds\n\t"
      "v_mfma_f32_16x16x32_bf16 v[116:119], a[180:183], v[152:155], v[116:119]\n\t"
      "ds_read_b64_tr_b16 a[220:221], v214 offset:12296\n\t"
      "v_mfma_f32_16x16x32_bf16 v[120:123], a[184:187], v[152:155], v[120:123]\n\t"
      "ds_read_b64_tr_b16 a[224:225], v214 offset:12352\n\t"
      "ds_read_b128 v[128:131], v198 offset:25088\n\t"
      "buffer_load_dwordx4 v208, s[20:23], s51 offen offset:2048 lds\n\t"
      "v_mfma_f32_16x16x32_bf16 v[132:135], a[188:191], v[152:155], v[132:135]\n\t"
      "ds_read_b64_tr_b16 a[228:229], v214 offset:12360\n\t"
      "s_waitcnt lgkmcnt(6)\n\t"
      "v_mfma_f32_16x16x32_bf16 v[32:35], a[192:195], v[156:159], v[32:35]\n\t"
      "ds_read_b64_tr_b16 a[232:233], v214 offset:12416\n\t"
      "s_add_u32 m0, s41, 0xf000\n\t"
      "s_add_u32 s51, s40, 0xc00000\n\t"
      "buffer_load_dwordx4 v209, s[20:23], s51 offen lds\n\t"
      "v_mfma_f32_16x16x32_bf16 v[36:39], a[196:199], v[156:159], v[36:39]\n\t"
      "ds_read_b64_tr_b16 a[236:237], v214 offset:12424\n\t"
      "v_mfma_f32_16x16x32_bf16 v[40:43], a[200:203], v[156:159], v[40:43]\n\t"
      "buffer_load_dwordx4 v209, s[20:23], s51 offen offset:1024 lds\n\t"
      "v_mfma_f32_16x16x32_bf16 v[44:47], a[204:207], v[156:159], v[44:47]\n\t"
      "v_mfma_f32_16x16x32_bf16 v[48:51], a[208:211], v[156:159], v[48:51]\n\t"
      "ds_read_b128 v[136:139], v198 offset:37376\n\t"
      "buffer_load_dwordx4 v209, s[20:23], s51 offen offset:2048 lds\n\t"
      "v_mfma_f32_16x16x32_bf16 v[52:55], a[212:215], v[156:159], v[52:55]\n\t"
      "s_waitcnt lgkmcnt(8)\n\t"
      "v_mfma_f32_16x16x32_bf16 v[56:59], a[192:195], v[160:163], v[56:59]\n\t"
      "v_mfma_f32_16x16x32_bf16 v[60:63], a[196:199], v[160:163], v[60:63]\n\t"
      "v_mfma_f32_16x16x32_bf16 v[64:67], a[200:203], v[160:163], v[64:67]\n\t"
      "s_waitcnt vmcnt(18)\n\t"
      "ds_read_b64_tr_b16 a[218:219], v214 offset:24576\n\t"
      "v_mfma_f32_16x16x32_bf16 v[68:71], a[204:207], v[160:163], v[68:71]\n\t"
      "ds_read_b64_tr_b16 a[222:223], v214 offset:24584\n\t"
      "v_mfma_f32_16x16x32_bf16 v[72:75], a[208:211], v[160:163], v[72:75]\n\t"
      "ds_read_b64_tr_b16 a[226:227], v214 offset:24640\n\t"
      "ds_read_b128 v[140:143], v199 offset:512\n\t"
      "v_mfma_f32_16x16x32_bf16 v[76:79], a[212:215], v[160:163], v[76:79]\n\t"
      "ds_read_b64_tr_b16 a[230:231], v214 offset:24648\n\t"
      "s_waitcnt lgkmcnt(9)\n\t"
      "v_mfma_f32_16x16x32_bf16 v[80:83], a[192:195], v[128:131], v[80:83]\n\t"
      "ds_read_b64_tr_b16 a[234:235], v214 offset:24704\n\t"
      "v_mfma_f32_16x16x32_bf16 v[84:87], a[196:199], v[128:131], v[84:87]\n\t"
      "ds_read_b64_tr_b16 a[238:239], v214 offset:24712\n\t"
      "v_mfma_f32_16x16x32_bf16 v[88:91], a[200:203], v[128:131], v[88:91]\n\t"
      "v_mfma_f32_16x16x32_bf16 v[92:95], a[204:207], v[128:131], v[92:95]\n\t"
      "v_mfma_f32_16x16x32_bf16 v[96:99], a[208:211], v[128:131], v[96:99]\n\t"
      "ds_read_b128 v[144:147], v199 offset:12800\n\t"
      "v_mfma_f32_16x16x32_bf16 v[100:103], a[212:215], v[128:131], v[100:103]\n\t"
      "s_waitcnt lgkmcnt(8)\n\t"
      "v_mfma_f32_16x16x32_bf16 v[104:107], a[192:195], v[136:139], v[104:107]\n\t"
      "v_mfma_f32_16x16x32_bf16 v[108:111], a[196:199], v[136:139], v[108:111]\n\t"
      "v_mfma_f32_16x16x32_bf16 v[112:115], a[200:203], v[136:139], v[112:115]\n\t"
      "s_waitcnt vmcnt(15)\n\t"
      "ds_read_b64_tr_b16 a[240:241], v214 offset:36864\n\t"
      "v_mfma_f32_16x16x32_bf16 v[116:119], a[204:207], v[136:139], v[116:119]\n\t"
      "ds_read_b64_tr_b16 a[244:245], v214 offset:36872\n\t"
      "v_mfma_f32_16x16x32_bf16 v[120:123], a[208:211], v[136:139], v[120:123]\n\t"
      "ds_read_b64_tr_b16 a[248:249], v214 offset:36928\n\t"
      "ds_read_b128 v[148:151], v199 offset:25088\n\t"
      "v_mfma_f32_16x16x32_bf16 v[132:135], a[212:215], v[136:139], v[132:135]\n\t"
      "ds_read_b64_tr_b16 a[252:253], v214 offset:36936\n\t"
      "s_waitcnt lgkmcnt(6)\n\t"
      "v_mfma_f32_16x16x32_bf16 v[32:35], a[216:219], v[140:143], v[32:35]\n\t"
      "ds_read_b64_tr_b16 v[164:165], v214 offset:36992\n\t"
      "v_mfma_f32_16x16x32_bf16 v[36:39], a[220:223], v[140:143], v[36:39]\n\t"
      "ds_read_b64_tr_b16 v[168:169], v214 offset:37000\n\t"
      "v_mfma_f32_16x16x32_bf16 v[40:43], a[224:227], v[140:143], v[40:43]\n\t"
      "s_waitcnt vmcnt(12)\n\t"
      "ds_read_b64_tr_b16 a[242:243], v214 offset:49152\n\t"
      "v_mfma_f32_16x16x32_bf16 v[44:47], a[228:231], v[140:143], v[44:47]\n\t"
      "ds_read_b64_tr_b16 a[246:247], v214 offset:49160\n\t"
      "v_mfma_f32_16x16x32_bf16 v[48:51], a[232:235], v[140:143], v[48:51]\n\t"
      "ds_read_b64_tr_b16 a[250:251], v214 offset:49216\n\t"
      "ds_read_b128 v[152:155], v199 offset:37376\n\t"
      "v_mfma_f32_16x16x32_bf16 v[52:55], a[236:239], v[140:143], v[52:55]\n\t"
      "ds_read_b64_tr_b16 a[254:255], v214 offset:49224\n\t"
      "s_waitcnt lgkmcnt(12)\n\t"
      "v_mfma_f32_16x16x32_bf16 v[56:59], a[216:219], v[144:147], v[56:59]\n\t"
      "ds_read_b64_tr_b16 v[166:167], v214 offset:49280\n\t"
      "v_mfma_f32_16x16x32_bf16 v[60:63], a[220:223], v[144:147], v[60:63]\n\t"
      "ds_read_b64_tr_b16 v[170:171], v214 offset:49288\n\t"
      "v_mfma_f32_16x16x32_bf16 v[64:67], a[224:227], v[144:147], v[64:67]\n\t"
      "s_waitcnt vmcnt(9)\n\t"
      "ds_read_b64_tr_b16 v[172:173], v221 offset:0\n\t"
      "v_mfma_f32_16x16x32_bf16 v[68:71], a[228:231], v[144:147], v[68:71]\n\t"
      "ds_read_b64_tr_b16 v[176:177], v221 offset:8\n\t"
      "v_mfma_f32_16x16x32_bf16 v[72:75], a[232:235], v[144:147], v[72:75]\n\t"
      "ds_read_b64_tr_b16 v[180:181], v221 offset:64\n\t"
      "ds_read_b128 v[156:159], v196 offset:512\n\t"
      "v_mfma_f32_16x16x32_bf16 v[76:79], a[236:239], v[144:147], v[76:79]\n\t"
      "ds_read_b64_tr_b16 v[184:185], v221 offset:72\n\t"
      "s_waitcnt lgkmcnt(14)\n\t"
      "v_mfma_f32_16x16x32_bf16 v[80:83], a[216:219], v[148:151], v[80:83]\n\t"
      "ds_read_b64_tr_b16 v[188:189], v221 offset:128\n\t"
      "v_mfma_f32_16x16x32_bf16 v[84:87], a[220:223], v[148:151], v[84:87]\n\t"
      "ds_read_b64_tr_b16 v[192:193], v221 offset:136\n\t"
      "v_mfma_f32_16x16x32_bf16 v[88:91], a[224:227], v[148:151], v[88:91]\n\t"
      "v_mfma_f32_16x16x32_bf16 v[92:95], a[228:231], v[148:151], v[92:95]\n\t"
      "v_mfma_f32_16x16x32_bf16 v[96:99], a[232:235], v[148:151], v[96:99]\n\t"
      "ds_read_b128 v[160:163], v196 offset:12800\n\t"
      "v_mfma_f32_16x16x32_bf16 v[100:103], a[236:239], v[148:151], v[100:103]\n\t"
      "s_waitcnt lgkmcnt(11)\n\t"
      "v_mfma_f32_16x16x32_bf16 v[104:107], a[216:219], v[152:155], v[104:107]\n\t"
      "v_mfma_f32_16x16x32_bf16 v[108:111], a[220:223], v[152:155], v[108:111]\n\t"
      "v_mfma_f32_16x16x32_bf16 v[112:115], a[224:227], v[152:155], v[112:115]\n\t"
      "s_waitcnt vmcnt(6)\n\t"
      "ds_read_b64_tr_b16 v[174:175], v221 offset:12288\n\t"
      "buffer_load_dwordx2 v[240:241], v245, s[24:27], 0 offen sc1\n\t"
      "v_mfma_f32_16x16x32_bf16 v[116:119], a[228:231], v[152:155], v[116:119]\n\t"
      "ds_read_b64_tr_b16 v[178:179], v221 offset:12296\n\t"
      "s_cmp_eq_u32 s41, s43\n\t"
      "s_cbranch_scc0 .Lc4_%=\n\t"
      "buffer_load_dword v254, v253, s[24:27], 0 offen sc1\n\t"
      ".Lc4_%=:\n\t"
      "v_mfma_f32_16x16x32_bf16 v[120:123], a[232:235], v[152:155], v[120:123]\n\t"
      "ds_read_b64_tr_b16 v[182:183], v221 offset:12352\n\t"
      "ds_read_b128 v[140:143], v197 offset:512\n\t"
      "v_mfma_f32_16x16x32_bf16 v[132:135], a[236:239], v[152:155], v[132:135]\n\t"
      "ds_read_b64_tr_b16 v[186:187], v221 offset:12360\n\t"
      "s_waitcnt lgkmcnt(9)\n\t"
      "v_mfma_f32_16x16x32_bf16 v[32:35], a[240:243], v[156:159], v[32:35]\n\t"
      "ds_read_b64_tr_b16 v[190:191], v221 offset:12416\n\t"
      "v_mfma_f32_16x16x32_bf16 v[36:39], a[244:247], v[156:159], v[36:39]\n\t"
      "ds_read_b64_tr_b16 v[194:195], v221 offset:12424\n\t"
      "v_mfma_f32_16x16x32_bf16 v[40:43], a[248:251], v[156:159], v[40:43]\n\t"
      "v_mfma_f32_16x16x32_bf16 v[44:47], a[252:255], v[156:159], v[44:47]\n\t"
      "v_mfma_f32_16x16x32_bf16 v[48:51], v[164:167], v[156:159], v[48:51]\n\t"
      "ds_read_b128 v[144:147], v197 offset:12800\n\t"
      "v_mfma_f32_16x16x32_bf16 v[52:55], v[168:171], v[156:159], v[52:55]\n\t"
      "s_waitcnt lgkmcnt(8)\n\t"
      "v_mfma_f32_16x16x32_bf16 v[56:59], a[240:243], v[160:163], v[56:59]\n\t"
      "v_mfma_f32_16x16x32_bf16 v[60:63], a[244:247], v[160:163], v[60:63]\n\t"
      "v_mfma_f32_16x16x32_bf16 v[64:67], a[248:251], v[160:163], v[64:67]\n\t"
      "v_mfma_f32_16x16x32_bf16 v[68:71], a[252:255], v[160:163], v[68:71]\n\t"
      "v_mfma_f32_16x16x32_bf16 v[72:75], v[164:167], v[160:163], v[72:75]\n\t"
      "ds_read_b128 v[128:131], v196 offset:25088\n\t"
      "v_mfma_f32_16x16x32_bf16 v[76:79], v[168:171], v[160:163], v[76:79]\n\t"
      "s_waitcnt lgkmcnt(2)\n\t"
      "v_mfma_f32_16x16x32_bf16 v[32:35], v[172:175], v[140:143], v[32:35]\n\t"
      "v_mfma_f32_16x16x32_bf16 v[36:39], v[176:179], v[140:143], v[36:39]\n\t"
      "v_mfma_f32_16x16x32_bf16 v[40:43], v[180:183], v[140:143], v[40:43]\n\t"
      "v_mfma_f32_16x16x32_bf16 v[44:47], v[184:187], v[140:143], v[44:47]\n\t"
      "v_mfma_f32_16x16x32_bf16 v[48:51], v[188:191], v[140:143], v[48:51]\n\t"
      "ds_read_b128 v[136:139], v196 offset:37376\n\t"
      "v_mfma_f32_16x16x32_bf16 v[52:55], v[192:195], v[140:143], v[52:55]\n\t"
      "s_waitcnt lgkmcnt(2)\n\t"
      "v_mfma_f32_16x16x32_bf16 v[56:59], v[172:175], v[144:147], v[56:59]\n\t"
      "v_mfma_f32_16x16x32_bf16 v[60:63], v[176:179], v[144:147], v[60:63]\n\t"
      "s_waitcnt vmcnt(4) lgkmcnt(2)\n\t"
      "s_barrier\n\t"
      "v_mfma_f32_16x16x32_bf16 v[64:67], v[180:183], v[144:147], v[64:67]\n\t"
      "v_cvt_pk_bf16_f32 v32, v32, v33\n\t"
      "v_cvt_pk_bf16_f32 v33, v34, v35\n\t"
      "v_mfma_f32_16x16x32_bf16 v[68:71], v[184:187], v[144:147], v[68:71]\n\t"
      "v_cvt_pk_bf16_f32 v34, v36, v37\n\t"
      "v_cvt_pk_bf16_f32 v35, v38, v39\n\t"
      "v_mfma_f32_16x16x32_bf16 v[72:75], v[188:191], v[144:147], v[72:75]\n\t"
      "ds_read_b128 v[148:151], v197 offset:25088\n\t"
      "ds_write_b128 v217, v[32:35] offset:24576\n\t"
      "v_cvt_pk_bf16_f32 v40, v40, v41\n\t"
      "v_mfma_f32_16x16x32_bf16 v[76:79], v[192:195], v[144:147], v[76:79]\n\t"
      "v_cvt_pk_bf16_f32 v41, v42, v43\n\t"
      "v_cvt_pk_bf16_f32 v42, v44, v45\n\t"
      "s_mov_b32 m0, s41\n\t"
      "s_add_u32 s51, s40, 0x1200000\n\t"
      "buffer_load_dwordx4 v208, s[20:23], s51 offen lds\n\t"
      "s_waitcnt lgkmcnt(3)\n\t"
      "v_mfma_f32_16x16x32_bf16 v[80:83], a[240:243], v[128:131], v[80:83]\n\t"
      "v_cvt_pk_bf16_f32 v43, v46, v47\n\t"
      "ds_write_b128 v218, v[40:43] offset:24576\n\t"
      "ds_read_b128 v[32:35], v196 offset:49152\n\t"
      "v_mfma_f32_16x16x32_bf16 v[84:87], a[244:247], v[128:131], v[84:87]\n\t"
      "v_cvt_pk_bf16_f32 v48, v48, v49\n\t"
      "v_cvt_pk_bf16_f32 v49, v50, v51\n\t"
      "v_mfma_f32_16x16x32_bf16 v[88:91], a[248:251], v[128:131], v[88:91]\n\t"
      "v_cvt_pk_bf16_f32 v50, v52, v53\n\t"
      "v_cvt_pk_bf16_f32 v51, v54, v55\n\t"
      "buffer_load_dwordx4 v208, s[20:23], s51 offen offset:1024 lds\n\t"
      "v_mfma_f32_16x16x32_bf16 v[92:95], a[252:255], v[128:131], v[92:95]\n\t"
      "ds_write_b128 v219, v[48:51] offset:24576\n\t"
      "ds_read_b128 v[36:39], v197 offset:49152\n\t"
      "v_cvt_pk_bf16_f32 v56, v56, v57\n\t"
      "v_mfma_f32_16x16x32_bf16 v[96:99], v[164:167], v[128:131], v[96:99]\n\t"
      "ds_read_b128 v[152:155], v197 offset:37376\n\t"
      "v_cvt_pk_bf16_f32 v57, v58, v59\n\t"
      "v_cvt_pk_bf16_f32 v58, v60, v61\n\t"
      "v_mfma_f32_16x16x32_bf16 v[100:103], v[168:171], v[128:131], v[100:103]\n\t"
      "v_cvt_pk_bf16_f32 v59, v62, v63\n\t"
      "ds_write_b128 v217, v[56:59] offset:36864\n\t"
      "ds_read_b128 v[40:43], v198 offset:49152\n\t"
      "s_waitcnt lgkmcnt(9)\n\t"
      "v_mfma_f32_16x16x32_bf16 v[104:107], a[240:243], v[136:139], v[104:107]\n\t"
      "v_cvt_pk_bf16_f32 v64, v64, v65\n\t"
      "v_cvt_pk_bf16_f32 v65, v66, v67\n\t"
      "buffer_load_dwordx4 v208, s[20:23], s51 offen offset:2048 lds\n\t"
      "v_mfma_f32_16x16x32_bf16 v[108:111], a[244:247], v[136:139], v[108:111]\n\t"
      "v_cvt_pk_bf16_f32 v66, v68, v69\n\t"
      "v_cvt_pk_bf16_f32 v67, v70, v71\n\t"
      "v_mfma_f32_16x16x32_bf16 v[112:115], a[248:251], v[136:139], v[112:115]\n\t"
      "ds_write_b128 v218, v[64:67] offset:36864\n\t"
      "ds_read_b128 v[44:47], v199 offset:49152\n\t"
      "v_cvt_pk_bf16_f32 v72, v72, v73\n\t"
      "v_mfma_f32_16x16x32_bf16 v[116:119], a[252:255], v[136:139], v[116:119]\n\t"
      "v_cvt_pk_bf16_f32 v73, v74, v75\n\t"
      "v_cvt_pk_bf16_f32 v74, v76, v77\n\t"
      "v_mfma_f32_16x16x32_bf16 v[120:123], v[164:167], v[136:139], v[120:123]\n\t"
      "v_cvt_pk_bf16_f32 v75, v78, v79\n\t"
      "ds_write_b128 v219, v[72:75] offset:36864\n\t"
      "ds_read_b128 v[48:51], v197 offset:49408\n\t"
      "v_mfma_f32_16x16x32_bf16 v[132:135], v[168:171], v[136:139], v[132:135]\n\t"
      "s_add_u32 m0, s41, 0x3000\n\t"
      "s_add_u32 s51, s40, 0x1200000\n\t"
      "buffer_load_dwordx4 v209, s[20:23], s51 offen lds\n\t"
      "ds_read_b128 v[52:55], v198 offset:49408\n\t"
      "s_waitcnt lgkmcnt(13)\n\t"
      "v_mfma_f32_16x16x32_bf16 v[80:83], v[172:175], v[148:151], v[80:83]\n\t"
      "ds_read_b128 v[56:59], v199 offset:49408\n\t"
      "ds_read_b128 v[60:63], v196 offset:49408\n\t"
      "v_mfma_f32_16x16x32_bf16 v[84:87], v[176:179], v[148:151], v[84:87]\n\t"
      "ds_read_b128 v[64:67], v198 offset:49664\n\t"
      "s_memrealtime s[80:81]\n\t"
      "v_mfma_f32_16x16x32_bf16 v[88:91], v[180:183], v[148:151], v[88:91]\n\t"
      "buffer_load_dwordx4 v209, s[20:23], s51 offen offset:1024 lds\n\t"
      "ds_read_b128 v[68:71], v199 offset:49664\n\t"
      "v_mfma_f32_16x16x32_bf16 v[92:95], v[184:187], v[148:151], v[92:95]\n\t"
      "ds_read_b128 v[72:75], v196 offset:49664\n\t"
      "ds_read_b128 v[76:79], v197 offset:49664\n\t"
      "v_mfma_f32_16x16x32_bf16 v[96:99], v[188:191], v[148:151], v[96:99]\n\t"
      "v_mfma_f32_16x16x32_bf16 v[100:103], v[192:195], v[148:151], v[100:103]\n\t"
      "buffer_load_dwordx4 v209, s[20:23], s51 offen offset:2048 lds\n\t"
      "s_waitcnt lgkmcnt(13)\n\t"
      "v_mfma_f32_16x16x32_bf16 v[104:107], v[172:175], v[152:155], v[104:107]\n\t"
      "v_mfma_f32_16x16x32_bf16 v[108:111], v[176:179], v[152:155], v[108:111]\n\t"
      "v_mfma_f32_16x16x32_bf16 v[112:115], v[180:183], v[152:155], v[112:115]\n\t"
      "v_mfma_f32_16x16x32_bf16 v[116:119], v[184:187], v[152:155], v[116:119]\n\t"
      "v_mfma_f32_16x16x32_bf16 v[120:123], v[188:191], v[152:155], v[120:123]\n\t"
      "v_mfma_f32_16x16x32_bf16 v[132:135], v[192:195], v[152:155], v[132:135]\n\t"
      "v_add_u32_e32 v196, s46, v200\n\t"
      "v_add_u32_e32 v197, s46, v201\n\t"
      "v_add_u32_e32 v198, s46, v202\n\t"
      "v_add_u32_e32 v199, s46, v203\n\t"
      "s_mov_b32 s87, 75\n\t"
      "s_mov_b32 s86, 80\n\t"
      "s_mov_b32 s84, 96\n\t"
      "s_branch .Lp2_%=\n\t"
      ".Lend_%=:\n\t"
      ".Lconv_end:\n\t"
      :
      : "{s[16:31]}"(d16), "{s[36:39]}"(db), "{s[52:53]}"(rs), "{s40}"(t0), "{s41}"(p0), "{s43}"(lds0), "{s94}"(ty), "{s95}"(tres), "{s[60:61]}"(pres),
      "{s69}"(pre1), "{s74}"(plm), "{s75}"(plb), "{s66}"(sst ? 24576u : 0u - 24576u), "{s89}"((blk >> 4) % 5u),
      "{s71}"(wv == 0u ? 568645u + ((blk >> 4) % 5u) * 1057u : ty ? 17733u + ((blk >> 4) % 5u) * 33u : 17u), "{v208}"(ga), "{v209}"(gb),
      "{v211}"(woff[0]), "{v212}"(woff[1]), "{v213}"(woff[2]), "{v248}"(ta), "{v249}"(ta), "{v250}"(tp), "{v251}"(t1), "{v252}"(mk), "{v245}"(vgv),
      "{v246}"(vxw), "{v247}"(vxr), "{v253}"(vcell), "{v200}"(cfa[0]), "{v201}"(cfa[1]), "{v202}"(cfa[2]), "{v203}"(cfa[3]), "{v204}"(csw[0]),
      "{v205}"(csw[1]), "{v206}"(csw[2]), "{v207}"(csr), "{v214}"(tbase + 73728u), "{v221}"(tbase + 135168u), "{v[226:229]}"(br0), "{v[230:233]}"(br1),
      "{v[234:237]}"(br2), "{v6}"(cpo)
      : "memory", "scc", "vcc",
      "s42", "s44", "s45", "s46", "s47", "s48", "s49", "s50", "s51", "s54", "s55", "s56", "s57", "s58", "s59", "s62", "s63", "s64", "s65", "s67",
      "s68", "s70", "s72", "s73", "s76", "s77", "s78", "s79", "s80", "s81", "s82", "s83", "s84", "s85", "s86", "s87", "s88", "s90", "s91", "s92",
      "s93", "s96", "s97", "s98", "s99",
      "v1", "v2", "v3", "v4", "v5", "v7", "v8", "v9", "v10", "v11", "v12", "v13", "v14", "v15", "v16", "v17", "v18", "v19", "v20", "v21", "v22", "v23",
      "v24", "v25", "v26", "v27", "v28", "v29", "v30", "v31", "v32", "v33", "v34", "v35", "v36", "v37", "v38", "v39", "v40", "v41", "v42", "v43",
      "v44", "v45", "v46", "v47", "v48", "v49", "v50", "v51", "v52", "v53", "v54", "v55", "v56", "v57", "v58", "v59", "v60", "v61", "v62", "v63",
      "v64", "v65", "v66", "v67", "v68", "v69", "v70", "v71", "v72", "v73", "v74", "v75", "v76", "v77", "v78", "v79", "v80", "v81", "v82", "v83",
      "v84", "v85", "v86", "v87", "v88", "v89", "v90", "v91", "v92", "v93", "v94", "v95", "v96", "v97", "v98", "v99", "v100", "v101", "v102", "v103",
      "v104", "v105", "v106", "v107", "v108", "v109", "v110", "v111", "v112", "v113", "v114", "v115", "v116", "v117", "v118", "v119", "v120", "v121",
      "v122", "v123", "v124", "v125", "v126", "v127", "v128", "v129", "v130", "v131", "v132", "v133", "v134", "v135", "v136", "v137", "v138", "v139",
      "v140", "v141", "v142", "v143", "v144", "v145", "v146", "v147", "v148", "v149", "v150", "v151", "v152", "v153", "v154", "v155", "v156", "v157",
      "v158", "v159", "v160", "v161", "v162", "v163", "v164", "v165", "v166", "v167", "v168", "v169", "v170", "v171", "v172", "v173", "v174", "v175",
      "v176", "v177", "v178", "v179", "v180", "v181", "v182", "v183", "v184", "v185", "v186", "v187", "v188", "v189", "v190", "v191", "v192", "v193",
      "v194", "v195", "v196", "v197", "v198", "v199", "v210", "v215", "v216", "v217", "v218", "v219", "v220", "v222", "v223", "v224", "v225", "v238",
      "v239", "v240", "v241", "v242", "v243", "v244", "v254", "v255",
      "a0", "a1", "a2", "a3", "a4", "a5", "a6", "a7", "a8", "a9", "a10", "a11", "a12", "a13", "a14", "a15", "a16", "a17", "a18", "a19", "a20", "a21",
      "a22", "a23", "a24", "a25", "a26", "a27", "a28", "a29", "a30", "a31", "a32", "a33", "a34", "a35", "a36", "a37", "a38", "a39", "a40", "a41",
      "a42", "a43", "a44", "a45", "a46", "a47", "a48", "a49", "a50", "a51", "a52", "a53", "a54", "a55", "a56", "a57", "a58", "a59", "a60", "a61",
      "a62", "a63", "a64", "a65", "a66", "a67", "a68", "a69", "a70", "a71", "a72", "a73", "a74", "a75", "a76", "a77", "a78", "a79", "a80", "a81",
      "a82", "a83", "a84", "a85", "a86", "a87", "a88", "a89", "a90", "a91", "a92", "a93", "a94", "a95", "a96", "a97", "a98", "a99", "a100", "a101",
      "a102", "a103", "a104", "a105", "a106", "a107", "a108", "a109", "a110", "a111", "a112", "a113", "a114", "a115", "a116", "a117", "a118", "a119",
      "a120", "a121", "a122", "a123", "a124", "a125", "a126", "a127", "a128", "a129", "a130", "a131", "a132", "a133", "a134", "a135", "a136", "a137",
      "a138", "a139", "a140", "a141", "a142", "a143", "a144", "a145", "a146", "a147", "a148", "a149", "a150", "a151", "a152", "a153", "a154", "a155",
      "a156", "a157", "a158", "a159", "a160", "a161", "a162", "a163", "a164", "a165", "a166", "a167", "a168", "a169", "a170", "a171", "a172", "a173",
      "a174", "a175", "a176", "a177", "a178", "a179", "a180", "a181", "a182", "a183", "a184", "a185", "a186", "a187", "a188", "a189", "a190", "a191",
      "a192", "a193", "a194", "a195", "a196", "a197", "a198", "a199", "a200", "a201", "a202", "a203", "a204", "a205", "a206", "a207", "a208", "a209",
      "a210", "a211", "a212", "a213", "a214", "a215", "a216", "a217", "a218", "a219", "a220", "a221", "a222", "a223", "a224", "a225", "a226", "a227",
      "a228", "a229", "a230", "a231", "a232", "a233", "a234", "a235", "a236", "a237", "a238", "a239", "a240", "a241", "a242", "a243", "a244", "a245",
      "a246", "a247", "a248", "a249", "a250", "a251", "a252", "a253", "a254", "a255");
}

extern "C" void kernel_launch(void* const* d_in, const int* in_sizes, int n_in,
                              void* d_out, int out_size, void* d_ws, size_t ws_size,
                              hipStream_t stream) {
  conv1x1_kernel<<<256, 256, 0, stream>>>(d_in[1], 0x0002000000048000ull, d_in[0], 0x0002000006c00000ull, d_ws, 0x0002000000000c00ull, d_out, 0x0002000006c00000ull,
                                          d_in[2], 0x0002000000000300ull);
}
```

```cpp
#include <hip/hip_runtime.h>
#include <stdint.h>

#define LAS __attribute__((address_space(3)))
typedef __attribute__((ext_vector_type(16))) unsigned u32x16;
typedef __attribute__((ext_vector_type(4))) unsigned u32x4;
typedef unsigned long long u64;

__global__ __launch_bounds__(256, 1) void conv1x1_kernel(const void* w, u64 cw, const void* x, u64 cx, void* ws, u64 cs, void* out, const void* bias) {
  __shared__ __attribute__((aligned(1024))) unsigned char lds[160768];

  u32x16 d16;
  u32x4 db;
  u64 rs;
  asm volatile(
      "s_load_dwordx16 s[16:31], %3, 0x0\n\t"
      "s_memrealtime s[52:53]\n\t"
      "s_movk_i32 s38, 0x300\n\t"
      "s_mov_b32 s39, 0x20000\n\t"
      : "={s[16:31]}"(d16), "={s[36:39]}"(db), "={s[52:53]}"(rs)
      : "s"(__builtin_amdgcn_kernarg_segment_ptr())
      : "memory");
  unsigned cpo = ((blockIdx.x >> 3) * 256u + threadIdx.x) * 4u;
  asm volatile(
      "s_getpc_b64 s[92:93]\n\t"
      ".Lconv_cp:\n\t"
      "v_min_u32_e32 v6, .Lconv_end-.Lconv_cp-4, v6\n\t"
      "global_load_dword v6, v6, s[92:93]\n\t"
      : "+{v6}"(cpo)
      :
      : "memory", "s92", "s93");
  __builtin_amdgcn_sched_barrier(0);
  unsigned tid  = threadIdx.x;
  unsigned lane = tid & 63;
  unsigned wv   = __builtin_amdgcn_readfirstlane(tid >> 6);
  const unsigned lds0 = (unsigned)(size_t)(LAS unsigned char*)lds;
  unsigned blk  = blockIdx.x;
  const unsigned p0   = lds0 + 3072u * wv;
  const unsigned lq = lane >> 4, ph = (lane >> 2) & 3, pl = lane & 3;
  const unsigned gs = 256u * lq + 64u * ((ph - lq) & 3) + 16u * (pl ^ ((4u - wv) & 3));
  const unsigned ga = gs + 3072u * (wv < 3u ? wv : 5u);
  const unsigned gb = gs + 3072u * (wv < 2u ? wv + 6u : wv + 1u);
  const unsigned boff = 192u * wv + 16u * lq;
  const unsigned role = (blk >> 2) & 1u;
  unsigned pk = (blk & 3u) * 32u + (blk >> 3);
  const unsigned sst  = (role ^ (blk >> 4)) & 1u;
  const unsigned t0   = pk * 49152u + sst * 24576u;
  const unsigned pv = blk >> 7, ps = ((blk & 7u) | ((blk >> 1) & 56u)) ^ (4u * pv), pgr = (ps >> 2) & 7u, pi = (ps & 3u) * 2u + pv;
  const unsigned ppg = min(pgr * 8u, 52u) + (pi & (0x13777777u >> (4u * pgr)) & 7u);
  const unsigned pop = (ppg << 21) + ((ps & 3u) * 4u + pv) * 0x1c080u + (ps >> 5) * 0x38000u + 0x24080u;
  unsigned pfire = ((role ^ ppg) & 1u) * 2u;
  const unsigned vpub = lane == 0u ? 0u + (pv * 64u + ps) * 8u : 0x40000000u;
  const unsigned pfo = (blk >> 4) * 18432u;
  const unsigned pfv = tid * 16u;
  const unsigned ty = (blk >> 3) & 1u;
  const unsigned tidx = (blk >> 3) * 4u + wv, tsel = tidx >> 6;
  const unsigned toff = (tidx & 63u) < 54u ? (tidx & 63u) << 21 : 113246204u;
  unsigned tres, pre1;
  u64 pres;
  u32x4 br0, br1, br2;
  asm volatile(
      "s_add_u32 m0, s43, 0x27000\n\t"
      "s_waitcnt lgkmcnt(0)\n\t"
      "s_mov_b64 s[36:37], s[30:31]\n\t"
      "s_mov_b64 s[30:31], s[22:23]\n\t"
      "s_cmp_eq_u32 s94, 1\n\t"
      "s_cbranch_scc0 .Lc1_%=\n\t"
      "s_cmp_eq_u32 s41, s43\n\t"
      "s_cbranch_scc0 .Lc0_%=\n\t"
      "s_load_dword s69, s[20:21], s76 offset:0x0\n\t"
      "s_load_dwordx2 s[60:61], s[20:21], s76 offset:0x3300\n\t"
      ".Lc0_%=:\n\t"
      "buffer_load_dwordx4 v216, s[16:19], s42 offen lds\n\t"
      "s_add_u32 s51, s42, 0x1000\n\t"
      "buffer_load_dwordx4 v216, s[16:19], s51 offen lds\n\t"
      "s_add_u32 s51, s42, 0x2000\n\t"
      "buffer_load_dwordx4 v216, s[16:19], s51 offen lds\n\t"
      "s_add_u32 s51, s42, 0x3000\n\t"
      "buffer_load_dwordx4 v216, s[16:19], s51 offen lds\n\t"
      "s_add_u32 s51, s42, 0x4000\n\t"
      "buffer_load_dwordx4 v216, s[16:19], s51 offen lds\n\t"
      ".Lc1_%=:\n\t"
      "buffer_load_dwordx4 v[226:229], v215, s[36:39], 0 offen offset:0\n\t"
      "buffer_load_dwordx4 v[230:233], v215, s[36:39], 0 offen offset:64\n\t"
      "buffer_load_dwordx4 v[234:237], v215, s[36:39], 0 offen offset:128\n\t"
      "s_mov_b32 m0, s41\n\t"
      "s_nop 0\n\t"
      "buffer_load_dwordx4 v208, s[20:23], s40 offen lds\n\t"
      "buffer_load_dwordx4 v208, s[20:23], s40 offen offset:1024 lds\n\t"
      "buffer_load_dwordx4 v208, s[20:23], s40 offen offset:2048 lds\n\t"
      "s_cmp_eq_u32 s94, 0\n\t"
      "s_cbranch_scc0 .Lvb0_%=\n\t"
      "s_add_u32 m0, s41, 0x3000\n\t"
      "s_nop 0\n\t"
      "buffer_load_dwordx4 v209, s[20:23], s40 offen lds\n\t"
      "buffer_load_dwordx4 v209, s[20:23], s40 offen offset:1024 lds\n\t"
      "buffer_load_dwordx4 v209, s[20:23], s40 offen offset:2048 lds\n\t"
      "s_cmp_eq_u32 s99, 0\n\t"
      "s_cselect_b64 s[96:97], s[20:21], s[28:29]\n\t"
      "s_load_dword s95, s[96:97], s98\n\t"
      "s_branch .Lvb1_%=\n\t"
      ".Lvb0_%=:\n\t"
      "s_cmp_eq_u32 s41, s43\n\t"
      "s_cbranch_scc1 .Lev0_%=\n\t"
      "s_cmp_eq_u32 s99, 0\n\t"
      "s_cselect_b64 s[96:97], s[20:21], s[28:29]\n\t"
      "s_load_dword s95, s[96:97], s98\n\t"
      "s_cmp_eq_u32 s94, 0\n\t"
      "s_cbranch_scc1 .Lev1_%=\n\t"
      "s_sleep 8\n\t"
      "s_branch .Lev1_%=\n\t"
      ".Lev0_%=:\n\t"
      "s_andn2_b32 s79, s52, 15\n\t"
      "v_mov_b32_e32 v241, s53\n\t"
      "s_waitcnt lgkmcnt(2)\n\t"
      "s_nop 7\n\t"
      "s_getreg_b32 s78, hwreg(HW_REG_IB_STS, 8, 4)\n\t"
      "s_cmp_eq_u32 s78, 1\n\t"
      "s_cselect_b32 s51, 2, 0\n\t"
      "s_xor_b32 s77, s77, s51\n\t"
      "s_cmp_lg_u32 s78, 0\n\t"
      "s_cselect_b32 s77, s77, 1\n\t"
      "s_or_b32 s79, s79, s77\n\t"
      "v_mov_b32_e32 v240, s79\n\t"
      "buffer_store_dwordx2 v[240:241], v244, s[24:27], 0 offen sc1\n\t"
      "s_cmp_eq_u32 s99, 0\n\t"
      "s_cselect_b64 s[96:97], s[20:21], s[28:29]\n\t"
      "s_load_dword s95, s[96:97], s98\n\t"
      ".Lev1_%=:\n\t"
      "s_add_u32 m0, s41, 0x3000\n\t"
      "s_nop 0\n\t"
      "buffer_load_dwordx4 v209, s[20:23], s40 offen lds\n\t"
      "buffer_load_dwordx4 v209, s[20:23], s40 offen offset:1024 lds\n\t"
      "buffer_load_dwordx4 v209, s[20:23], s40 offen offset:2048 lds\n\t"
      "s_add_u32 m0, s41, 0x6000\n\t"
      "s_add_u32 s51, s40, 0x600000\n\t"
      "buffer_load_dwordx4 v208, s[20:23], s51 offen lds\n\t"
      "buffer_load_dwordx4 v208, s[20:23], s51 offen offset:1024 lds\n\t"
      "buffer_load_dwordx4 v208, s[20:23], s51 offen offset:2048 lds\n\t"
      "s_add_u32 m0, s41, 0x9000\n\t"
      "s_add_u32 s51, s40, 0x600000\n\t"
      "buffer_load_dwordx4 v209, s[20:23], s51 offen lds\n\t"
      "buffer_load_dwordx4 v209, s[20:23], s51 offen offset:1024 lds\n\t"
      "buffer_load_dwordx4 v209, s[20:23], s51 offen offset:2048 lds\n\t"
      ".Lvb1_%=:\n\t"
      : "={s95}"(tres), "={s[60:61]}"(pres), "={s69}"(pre1), "={v[226:229]}"(br0), "={v[230:233]}"(br1), "={v[234:237]}"(br2),
        "+{s[16:31]}"(d16), "+{s[36:39]}"(db), "+v"(lane), "+{s77}"(pfire), "+{v6}"(cpo)
      : "{s40}"(t0), "{s41}"(p0), "{s43}"(lds0), "{s94}"(ty),
        "{v208}"(ga), "{v209}"(gb), "{v215}"(boff), "{v216}"(pfv),
        "{s42}"(pfo), "{s98}"(toff), "{s99}"(tsel), "{s76}"(pop), "{s[52:53]}"(rs), "{v244}"(vpub)
      : "memory", "scc", "s51", "s92", "s93", "s96", "s97", "s78", "s79", "v240", "v241");
  asm volatile("" : "+s"(wv), "+s"(pk));
  __builtin_amdgcn_sched_barrier(0);
  const unsigned vcell = lane < 2u ? 2048u + pk * 8u + 4u * (role ^ lane) : 0x40000000u;
  const unsigned ob = (unsigned)(((lane < 12u ? 0x434ec32ca418820ull >> (5u * lane) : 0x23053d2eull >> (5u * ((lane - 12u) & 7u)))) & 31u) * 6291456u + pk * 49152u;
  const unsigned pg = (ob + 24576u) >> 21;
  const unsigned ta = lane < 18u ? ob + 24576u * sst : 113246208u, t1 = ob + 24576u * (sst ^ 1u);
  const unsigned tq = (lane >= 9u ? 79u : 76u) + (lane < 4u ? 3u : 0u), tp = lane >= 14u ? 0u : tq;
  const unsigned mk = lane >= 4u + ty && lane < 18u ? 1u << (pg < 52u ? (pg >> 3) * 4u : 28u) : 0u;
  const unsigned dyn = (0x3fff0u << ty) & 0x3ffffu;
  const unsigned plm = (unsigned)__builtin_amdgcn_ballot_w64(((pg ^ role ^ sst) & 1u) != 0u) & dyn, plb = plm ^ (wv < 2u ? 0u : dyn);
  const unsigned vgv = 0u + (wv & 1u) * 512u + (lane ^ ((wv & 2u) << 4)) * 8u;
  const unsigned vxr = lds0 + 159744u + lane * 16u, vxw = vxr + wv * 4u;
  const unsigned li = lane & 15, lg = lane >> 4, rl = li & 3, hi = li >> 2;
  const unsigned rb = 768u * li + 16u * (lg ^ ((4u - hi) & 3)) - 3072u * wv;
  unsigned cfa[4], csw[3];
#pragma unroll
  for (unsigned u = 0; u < 4; ++u) cfa[u] = rb + 64u * ((u - rl) & 3);
#pragma unroll
  for (unsigned j = 0; j < 3; ++j) {
    const unsigned e = 3u * wv + j;
    csw[j] = rb + 256u * (e >> 2) + 64u * (((e & 3) + (e >> 2) - rl) & 3);
  }
  const unsigned csr = 16u * lane;
  const unsigned tbase = p0 + (4u * lg + (li >> 2)) * 192u + 16u * (li & 3);
  unsigned woff[3];
#pragma unroll
  for (unsigned j = 0; j < 3; ++j) {
    const unsigned rr = (__umul24(lane, 171u) + 10944u * j) >> 11;
    woff[j] = __umul24(rr, 576u) + __umul24(rr & 12u, 768u) + (16u * lane + 192u * wv);
  }
  asm volatile(
      "s_cmp_eq_u32 s94, 0\n\t"
      "s_cbranch_scc0 .Lc2_%=\n\t"
      "s_add_u32 m0, s41, 0x1e000\n\t"
      "s_mov_b32 s51, 0x0\n\t"
      "buffer_load_dwordx4 v211, s[16:19], s51 offen lds\n\t"
      "buffer_load_dwordx4 v212, s[16:19], s51 offen offset:1024 lds\n\t"
      "buffer_load_dwordx4 v213, s[16:19], s51 offen offset:2048 lds\n\t"
      "s_add_u32 m0, s41, 0x21000\n\t"
      "s_mov_b32 s51, 0xc00\n\t"
      "buffer_load_dwordx4 v211, s[16:19], s51 offen lds\n\t"
      "buffer_load_dwordx4 v212, s[16:19], s51 offen offset:1024 lds\n\t"
      "buffer_load_dwordx4 v213, s[16:19], s51 offen offset:2048 lds\n\t"
      "s_add_u32 m0, s41, 0x24000\n\t"
      "s_mov_b32 s51, 0x6000\n\t"
      "buffer_load_dwordx4 v211, s[16:19], s51 offen lds\n\t"
      "buffer_load_dwordx4 v212, s[16:19], s51 offen offset:1024 lds\n\t"
      "buffer_load_dwordx4 v213, s[16:19], s51 offen offset:2048 lds\n\t"
      "s_add_u32 m0, s41, 0x12000\n\t"
      "s_mov_b32 s51, 0x6c00\n\t"
      "buffer_load_dwordx4 v211, s[16:19], s51 offen lds\n\t"
      "buffer_load_dwordx4 v212, s[16:19], s51 offen offset:1024 lds\n\t"
      "buffer_load_dwordx4 v213, s[16:19], s51 offen offset:2048 lds\n\t"
      ".Lc2_%=:\n\t"
      "s_mov_b32 s62, 0\n\t"
      "s_add_u32 s50, s41, 0x12000\n\t"
      "s_add_u32 s51, s41, 0x12000\n\t"
      "s_sub_u32 s63, s52, 800\n\t"
      "s_andn2_b32 s63, s63, 15\n\t"
      "v_add_u32_e32 v196, s41, v200\n\t"
      "v_add_u32_e32 v197, s41, v201\n\t"
      "v_add_u32_e32 v198, s41, v202\n\t"
      "v_add_u32_e32 v199, s41, v203\n\t"
      "v_add_u32_e32 v217, s51, v204\n\t"
      "v_add_u32_e32 v218, s51, v205\n\t"
      "v_add_u32_e32 v219, s51, v206\n\t"
      "v_add_u32_e32 v220, s51, v207\n\t"
      "v_add_u32_e32 v238, s51, v207\n\t"
      "s_and_b32 s49, s71, 31\n\t"
      "s_lshr_b32 s71, s71, 5\n\t"
      "s_cmp_eq_u32 s94, 0\n\t"
      "s_cbranch_scc0 .Lpb_%=\n\t"
      "s_add_u32 m0, s41, 0x15000\n\t"
      "s_mov_b32 s51, 0xc000\n\t"
      "buffer_load_dwordx4 v211, s[16:19], s51 offen lds\n\t"
      "buffer_load_dwordx4 v212, s[16:19], s51 offen offset:1024 lds\n\t"
      "buffer_load_dwordx4 v213, s[16:19], s51 offen offset:2048 lds\n\t"
      "s_waitcnt vmcnt(21)\n\t"
      "v_lshlrev_b32_e32 v8, 16, v226\n\t"
      "v_and_b32_e32 v9, 0xffff0000, v226\n\t"
      "v_lshlrev_b32_e32 v10, 16, v227\n\t"
      "v_and_b32_e32 v11, 0xffff0000, v227\n\t"
      "v_lshlrev_b32_e32 v12, 16, v228\n\t"
      "v_and_b32_e32 v13, 0xffff0000, v228\n\t"
      "v_lshlrev_b32_e32 v14, 16, v229\n\t"
      "v_and_b32_e32 v15, 0xffff0000, v229\n\t"
      "v_lshlrev_b32_e32 v16, 16, v230\n\t"
      "v_and_b32_e32 v17, 0xffff0000, v230\n\t"
      "v_lshlrev_b32_e32 v18, 16, v231\n\t"
      "v_and_b32_e32 v19, 0xffff0000, v231\n\t"
      "v_lshlrev_b32_e32 v20, 16, v232\n\t"
      "v_and_b32_e32 v21, 0xffff0000, v232\n\t"
      "v_lshlrev_b32_e32 v22, 16, v233\n\t"
      "v_and_b32_e32 v23, 0xffff0000, v233\n\t"
      "v_lshlrev_b32_e32 v24, 16, v234\n\t"
      "v_and_b32_e32 v25, 0xffff0000, v234\n\t"
      "v_lshlrev_b32_e32 v26, 16, v235\n\t"
      "v_and_b32_e32 v27, 0xffff0000, v235\n\t"
      "v_lshlrev_b32_e32 v28, 16, v236\n\t"
      "v_and_b32_e32 v29, 0xffff0000, v236\n\t"
      "v_lshlrev_b32_e32 v30, 16, v237\n\t"
      "v_and_b32_e32 v31, 0xffff0000, v237\n\t"
      "s_add_u32 m0, s41, 0x18000\n\t"
      "s_mov_b32 s51, 0xcc00\n\t"
      "buffer_load_dwordx4 v211, s[16:19], s51 offen lds\n\t"
      "buffer_load_dwordx4 v212, s[16:19], s51 offen offset:1024 lds\n\t"
      "buffer_load_dwordx4 v213, s[16:19], s51 offen offset:2048 lds\n\t"
      "s_add_u32 m0, s41, 0x1b000\n\t"
      "s_mov_b32 s51, 0x12000\n\t"
      "buffer_load_dwordx4 v211, s[16:19], s51 offen lds\n\t"
      "buffer_load_dwordx4 v212, s[16:19], s51 offen offset:1024 lds\n\t"
      "buffer_load_dwordx4 v213, s[16:19], s51 offen offset:2048 lds\n\t"
      "s_mov_b32 s44, 0\n\t"
      "s_mov_b32 s85, 3\n\t"
      "s_mov_b32 s45, s41\n\t"
      "s_add_u32 s46, s41, 0x6000\n\t"
      "s_add_u32 s47, s41, 0xc000\n\t"
      "s_mov_b64 vcc, 0\n\t"
      "s_waitcnt vmcnt(18)\n\t"
      "s_barrier\n\t"
      "ds_read_b64_tr_b16 a[0:1], v214 offset:49152\n\t"
      "ds_read_b64_tr_b16 a[4:5], v214 offset:49160\n\t"
      "ds_read_b64_tr_b16 a[8:9], v214 offset:49216\n\t"
      "ds_read_b64_tr_b16 a[12:13], v214 offset:49224\n\t"
      "ds_read_b64_tr_b16 a[16:17], v214 offset:49280\n\t"
      "ds_read_b64_tr_b16 a[20:21], v214 offset:49288\n\t"
      "ds_read_b128 v[140:143], v196 offset:0\n\t"
      "s_waitcnt vmcnt(15)\n\t"
      "ds_read_b64_tr_b16 a[2:3], v221 offset:0\n\t"
      "ds_read_b64_tr_b16 a[6:7], v221 offset:8\n\t"
      "ds_read_b64_tr_b16 a[10:11], v221 offset:64\n\t"
      "ds_read_b64_tr_b16 a[14:15], v221 offset:72\n\t"
      "ds_read_b64_tr_b16 a[18:19], v221 offset:128\n\t"
      "ds_read_b64_tr_b16 a[22:23], v221 offset:136\n\t"
      "ds_read_b128 v[152:155], v196 offset:12288\n\t"
      "s_waitcnt lgkmcnt(8)\n\t"
      "s_add_u32 m0, s41, 0x1e000\n\t"
      "s_mov_b32 s51, 0x12c00\n\t"
      "buffer_load_dwordx4 v211, s[16:19], s51 offen lds\n\t"
      "buffer_load_dwordx4 v212, s[16:19], s51 offen offset:1024 lds\n\t"
      "buffer_load_dwordx4 v213, s[16:19], s51 offen offset:2048 lds\n\t"
      "s_waitcnt vmcnt(15)\n\t"
      "ds_read_b64_tr_b16 a[24:25], v221 offset:12288\n\t"
      "ds_read_b64_tr_b16 a[28:29], v221 offset:12296\n\t"
      "ds_read_b64_tr_b16 a[32:33], v221 offset:12352\n\t"
      "ds_read_b64_tr_b16 a[36:37], v221 offset:12360\n\t"
      "ds_read_b64_tr_b16 a[40:41], v221 offset:12416\n\t"
      "ds_read_b64_tr_b16 a[44:45], v221 offset:12424\n\t"
      "ds_read_b128 v[144:147], v197 offset:0\n\t"
      "s_waitcnt lgkmcnt(8)\n\t"
      "v_mfma_f32_16x16x32_bf16 v[80:83], a[0:3], v[140:143], v[8:11]\n\t"
      "s_add_u32 m0, s41, 0x21000\n\t"
      "s_mov_b32 s51, 0x18000\n\t"
      "buffer_load_dwordx4 v211, s[16:19], s51 offen lds\n\t"
      "v_mfma_f32_16x16x32_bf16 v[84:87], a[4:7], v[140:143], v[12:15]\n\t"
      "buffer_load_dwordx4 v212, s[16:19], s51 offen offset:1024 lds\n\t"
      "v_mfma_f32_16x16x32_bf16 v[88:91], a[8:11], v[140:143], v[16:19]\n\t"
      "buffer_load_dwordx4 v213, s[16:19], s51 offen offset:2048 lds\n\t"
      "s_waitcnt vmcnt(15)\n\t"
      "ds_read_b64_tr_b16 a[26:27], v214 offset:0\n\t"
      "v_mfma_f32_16x16x32_bf16 v[92:95], a[12:15], v[140:143], v[20:23]\n\t"
      "ds_read_b64_tr_b16 a[30:31], v214 offset:8\n\t"
      "ds_read_b64_tr_b16 a[34:35], v214 offset:64\n\t"
      "v_mfma_f32_16x16x32_bf16 v[96:99], a[16:19], v[140:143], v[24:27]\n\t"
      "ds_read_b64_tr_b16 a[38:39], v214 offset:72\n\t"
      "ds_read_b64_tr_b16 a[42:43], v214 offset:128\n\t"
      "v_mfma_f32_16x16x32_bf16 v[100:103], a[20:23], v[140:143], v[28:31]\n\t"
      "ds_read_b64_tr_b16 a[46:47], v214 offset:136\n\t"
      "ds_read_b128 v[156:159], v197 offset:12288\n\t"
      "s_waitcnt lgkmcnt(8)\n\t"
      "v_mfma_f32_16x16x32_bf16 v[104:107], a[0:3], v[152:155], v[8:11]\n\t"
      "s_add_u32 m0, s41, 0x24000\n\t"
      "s_mov_b32 s51, 0x18c00\n\t"
      "buffer_load_dwordx4 v211, s[16:19], s51 offen lds\n\t"
      "v_mfma_f32_16x16x32_bf16 v[108:111], a[4:7], v[152:155], v[12:15]\n\t"
      "buffer_load_dwordx4 v212, s[16:19], s51 offen offset:1024 lds\n\t"
      "v_mfma_f32_16x16x32_bf16 v[112:115], a[8:11], v[152:155], v[16:19]\n\t"
      "buffer_load_dwordx4 v213, s[16:19], s51 offen offset:2048 lds\n\t"
      "s_waitcnt vmcnt(15)\n\t"
      "ds_read_b64_tr_b16 a[48:49], v214 offset:12288\n\t"
      "v_mfma_f32_16x16x32_bf16 v[116:119], a[12:15], v[152:155], v[20:23]\n\t"
      "ds_read_b64_tr_b16 a[52:53], v214 offset:12296\n\t"
      "ds_read_b64_tr_b16 a[56:57], v214 offset:12352\n\t"
      "v_mfma_f32_16x16x32_bf16 v[120:123], a[16:19], v[152:155], v[24:27]\n\t"
      "ds_read_b64_tr_b16 a[60:61], v214 offset:12360\n\t"
      "ds_read_b64_tr_b16 a[64:65], v214 offset:12416\n\t"
      "v_mfma_f32_16x16x32_bf16 v[132:135], a[20:23], v[152:155], v[28:31]\n\t"
      "ds_read_b64_tr_b16 a[68:69], v214 offset:12424\n\t"
      "ds_read_b128 v[148:151], v198 offset:0\n\t"
      "s_waitcnt lgkmcnt(8)\n\t"
      "v_mfma_f32_16x16x32_bf16 v[80:83], a[24:27], v[144:147], v[80:83]\n\t"
      "s_add_u32 m0, s41, 0x12000\n\t"
      "s_mov_b32 s51, 0x1e000\n\t"
      "buffer_load_dwordx4 v211, s[16:19], s51 offen lds\n\t"
      "v_mfma_f32_16x16x32_bf16 v[84:87], a[28:31], v[144:147], v[84:87]\n\t"
      "buffer_load_dwordx4 v212, s[16:19], s51 offen offset:1024 lds\n\t"
      "v_mfma_f32_16x16x32_bf16 v[88:91], a[32:35], v[144:147], v[88:91]\n\t"
      "buffer_load_dwordx4 v213, s[16:19], s51 offen offset:2048 lds\n\t"
      "s_waitcnt vmcnt(15)\n\t"
      "ds_read_b64_tr_b16 a[50:51], v214 offset:24576\n\t"
      "v_mfma_f32_16x16x32_bf16 v[92:95], a[36:39], v[144:147], v[92:95]\n\t"
      "ds_read_b64_tr_b16 a[54:55], v214 offset:24584\n\t"
      "ds_read_b64_tr_b16 a[58:59], v214 offset:24640\n\t"
      "v_mfma_f32_16x16x32_bf16 v[96:99], a[40:43], v[144:147], v[96:99]\n\t"
      "ds_read_b64_tr_b16 a[62:63], v214 offset:24648\n\t"
      "ds_read_b64_tr_b16 a[66:67], v214 offset:24704\n\t"
      "v_mfma_f32_16x16x32_bf16 v[100:103], a[44:47], v[144:147], v[100:103]\n\t"
      "ds_read_b64_tr_b16 a[70:71], v214 offset:24712\n\t"
      "ds_read_b128 v[160:163], v198 offset:12288\n\t"
      "s_waitcnt lgkmcnt(8)\n\t"
      "v_mfma_f32_16x16x32_bf16 v[104:107], a[24:27], v[156:159], v[104:107]\n\t"
      "s_add_u32 m0, s41, 0x15000\n\t"
      "s_mov_b32 s51, 0x1ec00\n\t"
      "buffer_load_dwordx4 v211, s[16:19], s51 offen lds\n\t"
      "v_mfma_f32_16x16x32_bf16 v[108:111], a[28:31], v[156:159], v[108:111]\n\t"
      "buffer_load_dwordx4 v212, s[16:19], s51 offen offset:1024 lds\n\t"
      "v_mfma_f32_16x16x32_bf16 v[112:115], a[32:35], v[156:159], v[112:115]\n\t"
      "buffer_load_dwordx4 v213, s[16:19], s51 offen offset:2048 lds\n\t"
      "s_waitcnt vmcnt(15)\n\t"
      "ds_read_b64_tr_b16 a[72:73], v214 offset:36864\n\t"
      "v_mfma_f32_16x16x32_bf16 v[116:119], a[36:39], v[156:159], v[116:119]\n\t"
      "ds_read_b64_tr_b16 a[76:77], v214 offset:36872\n\t"
      "ds_read_b64_tr_b16 a[80:81], v214 offset:36928\n\t"
      "v_mfma_f32_16x16x32_bf16 v[120:123], a[40:43], v[156:159], v[120:123]\n\t"
      "ds_read_b64_tr_b16 a[84:85], v214 offset:36936\n\t"
      "ds_read_b64_tr_b16 a[88:89], v214 offset:36992\n\t"
      "v_mfma_f32_16x16x32_bf16 v[132:135], a[44:47], v[156:159], v[132:135]\n\t"
      "ds_read_b64_tr_b16 a[92:93], v214 offset:37000\n\t"
      "ds_read_b128 v[140:143], v199 offset:0\n\t"
      "s_waitcnt lgkmcnt(8)\n\t"
      "v_mfma_f32_16x16x32_bf16 v[80:83], a[48:51], v[148:151], v[80:83]\n\t"
      "s_add_u32 m0, s41, 0x18000\n\t"
      "s_mov_b32 s51, 0x24000\n\t"
      "buffer_load_dwordx4 v211, s[16:19], s51 offen lds\n\t"
      "v_mfma_f32_16x16x32_bf16 v[84:87], a[52:55], v[148:151], v[84:87]\n\t"
      "buffer_load_dwordx4 v212, s[16:19], s51 offen offset:1024 lds\n\t"
      "v_mfma_f32_16x16x32_bf16 v[88:91], a[56:59], v[148:151], v[88:91]\n\t"
      "buffer_load_dwordx4 v213, s[16:19], s51 offen offset:2048 lds\n\t"
      "s_waitcnt vmcnt(15)\n\t"
      "ds_read_b64_tr_b16 a[74:75], v214 offset:49152\n\t"
      "v_mfma_f32_16x16x32_bf16 v[92:95], a[60:63], v[148:151], v[92:95]\n\t"
      "ds_read_b64_tr_b16 a[78:79], v214 offset:49160\n\t"
      "ds_read_b64_tr_b16 a[82:83], v214 offset:49216\n\t"
      "v_mfma_f32_16x16x32_bf16 v[96:99], a[64:67], v[148:151], v[96:99]\n\t"
      "ds_read_b64_tr_b16 a[86:87], v214 offset:49224\n\t"
      "ds_read_b64_tr_b16 a[90:91], v214 offset:49280\n\t"
      "v_mfma_f32_16x16x32_bf16 v[100:103], a[68:71], v[148:151], v[100:103]\n\t"
      "ds_read_b64_tr_b16 a[94:95], v214 offset:49288\n\t"
      "ds_read_b128 v[152:155], v199 offset:12288\n\t"
      "s_waitcnt lgkmcnt(8)\n\t"
      "v_mfma_f32_16x16x32_bf16 v[104:107], a[48:51], v[160:163], v[104:107]\n\t"
      "s_add_u32 m0, s41, 0x1b000\n\t"
      "s_mov_b32 s51, 0x24c00\n\t"
      "buffer_load_dwordx4 v211, s[16:19], s51 offen lds\n\t"
      "v_mfma_f32_16x16x32_bf16 v[108:111], a[52:55], v[160:163], v[108:111]\n\t"
      "buffer_load_dwordx4 v212, s[16:19], s51 offen offset:1024 lds\n\t"
      "v_mfma_f32_16x16x32_bf16 v[112:115], a[56:59], v[160:163], v[112:115]\n\t"
      "buffer_load_dwordx4 v213, s[16:19], s51 offen offset:2048 lds\n\t"
      "s_waitcnt vmcnt(15)\n\t"
      "ds_read_b64_tr_b16 a[96:97], v221 offset:0\n\t"
      "v_mfma_f32_16x16x32_bf16 v[116:119], a[60:63], v[160:163], v[116:119]\n\t"
      "ds_read_b64_tr_b16 a[100:101], v221 offset:8\n\t"
      "ds_read_b64_tr_b16 a[104:105], v221 offset:64\n\t"
      "v_mfma_f32_16x16x32_bf16 v[120:123], a[64:67], v[160:163], v[120:123]\n\t"
      "ds_read_b64_tr_b16 a[108:109], v221 offset:72\n\t"
      "ds_read_b64_tr_b16 a[112:113], v221 offset:128\n\t"
      "v_mfma_f32_16x16x32_bf16 v[132:135], a[68:71], v[160:163], v[132:135]\n\t"
      "ds_read_b64_tr_b16 a[116:117], v221 offset:136\n\t"
      "ds_read_b128 v[144:147], v197 offset:256\n\t"
      "s_waitcnt lgkmcnt(8)\n\t"
      "v_mfma_f32_16x16x32_bf16 v[80:83], a[72:75], v[140:143], v[80:83]\n\t"
      "s_add_u32 m0, s41, 0x1e000\n\t"
      "s_mov_b32 s51, 0x2a000\n\t"
      "buffer_load_dwordx4 v211, s[16:19], s51 offen lds\n\t"
      "v_mfma_f32_16x16x32_bf16 v[84:87], a[76:79], v[140:143], v[84:87]\n\t"
      "buffer_load_dwordx4 v212, s[16:19], s51 offen offset:1024 lds\n\t"
      "v_mfma_f32_16x16x32_bf16 v[88:91], a[80:83], v[140:143], v[88:91]\n\t"
      "buffer_load_dwordx4 v213, s[16:19], s51 offen offset:2048 lds\n\t"
      "s_waitcnt vmcnt(15)\n\t"
      "ds_read_b64_tr_b16 a[98:99], v221 offset:12288\n\t"
      "v_mfma_f32_16x16x32_bf16 v[92:95], a[84:87], v[140:143], v[92:95]\n\t"
      "ds_read_b64_tr_b16 a[102:103], v221 offset:12296\n\t"
      "ds_read_b64_tr_b16 a[106:107], v221 offset:12352\n\t"
      "v_mfma_f32_16x16x32_bf16 v[96:99], a[88:91], v[140:143], v[96:99]\n\t"
      "ds_read_b64_tr_b16 a[110:111], v221 offset:12360\n\t"
      "ds_read_b64_tr_b16 a[114:115], v221 offset:12416\n\t"
      "v_mfma_f32_16x16x32_bf16 v[100:103], a[92:95], v[140:143], v[100:103]\n\t"
      "ds_read_b64_tr_b16 a[118:119], v221 offset:12424\n\t"
      "ds_read_b128 v[156:159], v197 offset:12544\n\t"
      "s_waitcnt lgkmcnt(8)\n\t"
      "v_mfma_f32_16x16x32_bf16 v[104:107], a[72:75], v[152:155], v[104:107]\n\t"
      "s_add_u32 m0, s41, 0x21000\n\t"
      "s_mov_b32 s51, 0x2ac00\n\t"
      "buffer_load_dwordx4 v211, s[16:19], s51 offen lds\n\t"
      "v_mfma_f32_16x16x32_bf16 v[108:111], a[76:79], v[152:155], v[108:111]\n\t"
      "buffer_load_dwordx4 v212, s[16:19], s51 offen offset:1024 lds\n\t"
      "v_mfma_f32_16x16x32_bf16 v[112:115], a[80:83], v[152:155], v[112:115]\n\t"
      "buffer_load_dwordx4 v213, s[16:19], s51 offen offset:2048 lds\n\t"
      "s_waitcnt vmcnt(15)\n\t"
      "ds_read_b64_tr_b16 a[120:121], v214 offset:0\n\t"
      "v_mfma_f32_16x16x32_bf16 v[116:119], a[84:87], v[152:155], v[116:119]\n\t"
      "ds_read_b64_tr_b16 a[124:125], v214 offset:8\n\t"
      "ds_read_b64_tr_b16 a[128:129], v214 offset:64\n\t"
      "v_mfma_f32_16x16x32_bf16 v[120:123], a[88:91], v[152:155], v[120:123]\n\t"
      "ds_read_b64_tr_b16 a[132:133], v214 offset:72\n\t"
      "ds_read_b64_tr_b16 a[136:137], v214 offset:128\n\t"
      "v_mfma_f32_16x16x32_bf16 v[132:135], a[92:95], v[152:155], v[132:135]\n\t"
      "ds_read_b64_tr_b16 a[140:141], v214 offset:136\n\t"
      "ds_read_b128 v[148:151], v198 offset:256\n\t"
      "s_waitcnt lgkmcnt(8)\n\t"
      "v_mfma_f32_16x16x32_bf16 v[80:83], a[96:99], v[144:147], v[80:83]\n\t"
      "s_add_u32 m0, s41, 0x24000\n\t"
      "s_mov_b32 s51, 0x30000\n\t"
      "buffer_load_dwordx4 v211, s[16:19], s51 offen lds\n\t"
      "v_mfma_f32_16x16x32_bf16 v[84:87], a[100:103], v[144:147], v[84:87]\n\t"
      "buffer_load_dwordx4 v212, s[16:19], s51 offen offset:1024 lds\n\t"
      "v_mfma_f32_16x16x32_bf16 v[88:91], a[104:107], v[144:147], v[88:91]\n\t"
      "buffer_load_dwordx4 v213, s[16:19], s51 offen offset:2048 lds\n\t"
      "s_waitcnt vmcnt(15)\n\t"
      "ds_read_b64_tr_b16 a[122:123], v214 offset:12288\n\t"
      "v_mfma_f32_16x16x32_bf16 v[92:95], a[108:111], v[144:147], v[92:95]\n\t"
      "ds_read_b64_tr_b16 a[126:127], v214 offset:12296\n\t"
      "ds_read_b64_tr_b16 a[130:131], v214 offset:12352\n\t"
      "v_mfma_f32_16x16x32_bf16 v[96:99], a[112:115], v[144:147], v[96:99]\n\t"
      "ds_read_b64_tr_b16 a[134:135], v214 offset:12360\n\t"
      "ds_read_b64_tr_b16 a[138:139], v214 offset:12416\n\t"
      "v_mfma_f32_16x16x32_bf16 v[100:103], a[116:119], v[144:147], v[100:103]\n\t"
      "ds_read_b64_tr_b16 a[142:143], v214 offset:12424\n\t"
      "ds_read_b128 v[160:163], v198 offset:12544\n\t"
      "s_waitcnt lgkmcnt(8)\n\t"
      "v_mfma_f32_16x16x32_bf16 v[104:107], a[96:99], v[156:159], v[104:107]\n\t"
      "s_add_u32 m0, s41, 0x12000\n\t"
      "s_mov_b32 s51, 0x30c00\n\t"
      "buffer_load_dwordx4 v211, s[16:19], s51 offen lds\n\t"
      "v_mfma_f32_16x16x32_bf16 v[108:111], a[100:103], v[156:159], v[108:111]\n\t"
      "buffer_load_dwordx4 v212, s[16:19], s51 offen offset:1024 lds\n\t"
      "v_mfma_f32_16x16x32_bf16 v[112:115], a[104:107], v[156:159], v[112:115]\n\t"
      "buffer_load_dwordx4 v213, s[16:19], s51 offen offset:2048 lds\n\t"
      "s_waitcnt vmcnt(15)\n\t"
      "ds_read_b64_tr_b16 a[144:145], v214 offset:24576\n\t"
      "v_mfma_f32_16x16x32_bf16 v[116:119], a[108:111], v[156:159], v[116:119]\n\t"
      "ds_read_b64_tr_b16 a[148:149], v214 offset:24584\n\t"
      "ds_read_b64_tr_b16 a[152:153], v214 offset:24640\n\t"
      "v_mfma_f32_16x16x32_bf16 v[120:123], a[112:115], v[156:159], v[120:123]\n\t"
      "ds_read_b64_tr_b16 a[156:157], v214 offset:24648\n\t"
      "ds_read_b64_tr_b16 a[160:161], v214 offset:24704\n\t"
      "v_mfma_f32_16x16x32_bf16 v[132:135], a[116:119], v[156:159], v[132:135]\n\t"
      "ds_read_b64_tr_b16 a[164:165], v214 offset:24712\n\t"
      "ds_read_b128 v[140:143], v199 offset:256\n\t"
      "s_waitcnt lgkmcnt(8)\n\t"
      "v_mfma_f32_16x16x32_bf16 v[80:83], a[120:123], v[148:151], v[80:83]\n\t"
      "s_add_u32 m0, s41, 0x15000\n\t"
      "s_mov_b32 s51, 0x36000\n\t"
      "buffer_load_dwordx4 v211, s[16:19], s51 offen lds\n\t"
      "v_mfma_f32_16x16x32_bf16 v[84:87], a[124:127], v[148:151], v[84:87]\n\t"
      "buffer_load_dwordx4 v212, s[16:19], s51 offen offset:1024 lds\n\t"
      "v_mfma_f32_16x16x32_bf16 v[88:91], a[128:131], v[148:151], v[88:91]\n\t"
      "buffer_load_dwordx4 v213, s[16:19], s51 offen offset:2048 lds\n\t"
      "s_waitcnt vmcnt(15)\n\t"
      "ds_read_b64_tr_b16 a[146:147], v214 offset:36864\n\t"
      "v_mfma_f32_16x16x32_bf16 v[92:95], a[132:135], v[148:151], v[92:95]\n\t"
      "ds_read_b64_tr_b16 a[150:151], v214 offset:36872\n\t"
      "ds_read_b64_tr_b16 a[154:155], v214 offset:36928\n\t"
      "v_mfma_f32_16x16x32_bf16 v[96:99], a[136:139], v[148:151], v[96:99]\n\t"
      "ds_read_b64_tr_b16 a[158:159], v214 offset:36936\n\t"
      "ds_read_b64_tr_b16 a[162:163], v214 offset:36992\n\t"
      "v_mfma_f32_16x16x32_bf16 v[100:103], a[140:143], v[148:151], v[100:103]\n\t"
      "ds_read_b64_tr_b16 a[166:167], v214 offset:37000\n\t"
      "ds_read_b128 v[152:155], v199 offset:12544\n\t"
      "s_waitcnt lgkmcnt(8)\n\t"
      "v_mfma_f32_16x16x32_bf16 v[104:107], a[120:123], v[160:163], v[104:107]\n\t"
      "s_add_u32 m0, s41, 0x18000\n\t"
      "s_mov_b32 s51, 0x36c00\n\t"
      "buffer_load_dwordx4 v211, s[16:19], s51 offen lds\n\t"
      "v_mfma_f32_16x16x32_bf16 v[108:111], a[124:127], v[160:163], v[108:111]\n\t"
      "buffer_load_dwordx4 v212, s[16:19], s51 offen offset:1024 lds\n\t"
      "v_mfma_f32_16x16x32_bf16 v[112:115], a[128:131], v[160:163], v[112:115]\n\t"
      "buffer_load_dwordx4 v213, s[16:19], s51 offen offset:2048 lds\n\t"
      "s_waitcnt vmcnt(15)\n\t"
      "ds_read_b64_tr_b16 a[168:169], v214 offset:49152\n\t"
      "v_mfma_f32_16x16x32_bf16 v[116:119], a[132:135], v[160:163], v[116:119]\n\t"
      "ds_read_b64_tr_b16 a[172:173], v214 offset:49160\n\t"
      "ds_read_b64_tr_b16 a[176:177], v214 offset:49216\n\t"
      "v_mfma_f32_16x16x32_bf16 v[120:123], a[136:139], v[160:163], v[120:123]\n\t"
      "ds_read_b64_tr_b16 a[180:181], v214 offset:49224\n\t"
      "ds_read_b64_tr_b16 a[184:185], v214 offset:49280\n\t"
      "v_mfma_f32_16x16x32_bf16 v[132:135], a[140:143], v[160:163], v[132:135]\n\t"
      "ds_read_b64_tr_b16 a[188:189], v214 offset:49288\n\t"
      "ds_read_b128 v[144:147], v196 offset:256\n\t"
      "s_waitcnt lgkmcnt(8)\n\t"
      "v_mfma_f32_16x16x32_bf16 v[80:83], a[144:147], v[140:143], v[80:83]\n\t"
      "s_add_u32 m0, s41, 0x1b000\n\t"
      "s_mov_b32 s51, 0x3c000\n\t"
      "buffer_load_dwordx4 v211, s[16:19], s51 offen lds\n\t"
      "v_mfma_f32_16x16x32_bf16 v[84:87], a[148:151], v[140:143], v[84:87]\n\t"
      "buffer_load_dwordx4 v212, s[16:19], s51 offen offset:1024 lds\n\t"
      "v_mfma_f32_16x16x32_bf16 v[88:91], a[152:155], v[140:143], v[88:91]\n\t"
      "buffer_load_dwordx4 v213, s[16:19], s51 offen offset:2048 lds\n\t"
      "s_waitcnt vmcnt(15)\n\t"
      "ds_read_b64_tr_b16 a[170:171], v221 offset:0\n\t"
      "v_mfma_f32_16x16x32_bf16 v[92:95], a[156:159], v[140:143], v[92:95]\n\t"
      "ds_read_b64_tr_b16 a[174:175], v221 offset:8\n\t"
      "ds_read_b64_tr_b16 a[178:179], v221 offset:64\n\t"
      "v_mfma_f32_16x16x32_bf16 v[96:99], a[160:163], v[140:143], v[96:99]\n\t"
      "ds_read_b64_tr_b16 a[182:183], v221 offset:72\n\t"
      "ds_read_b64_tr_b16 a[186:187], v221 offset:128\n\t"
      "v_mfma_f32_16x16x32_bf16 v[100:103], a[164:167], v[140:143], v[100:103]\n\t"
      "ds_read_b64_tr_b16 a[190:191], v221 offset:136\n\t"
      "ds_read_b128 v[156:159], v196 offset:12544\n\t"
      "s_waitcnt lgkmcnt(8)\n\t"
      "v_mfma_f32_16x16x32_bf16 v[104:107], a[144:147], v[152:155], v[104:107]\n\t"
      "s_add_u32 m0, s41, 0x1e000\n\t"
      "s_mov_b32 s51, 0x3cc00\n\t"
      "buffer_load_dwordx4 v211, s[16:19], s51 offen lds\n\t"
      "v_mfma_f32_16x16x32_bf16 v[108:111], a[148:151], v[152:155], v[108:111]\n\t"
      "buffer_load_dwordx4 v212, s[16:19], s51 offen offset:1024 lds\n\t"
      "v_mfma_f32_16x16x32_bf16 v[112:115], a[152:155], v[152:155], v[112:115]\n\t"
      "buffer_load_dwordx4 v213, s[16:19], s51 offen offset:2048 lds\n\t"
      "s_waitcnt vmcnt(15)\n\t"
      "ds_read_b64_tr_b16 a[192:193], v221 offset:12288\n\t"
      "v_mfma_f32_16x16x32_bf16 v[116:119], a[156:159], v[152:155], v[116:119]\n\t"
      "ds_read_b64_tr_b16 a[196:197], v221 offset:12296\n\t"
      "ds_read_b64_tr_b16 a[200:201], v221 offset:12352\n\t"
      "v_mfma_f32_16x16x32_bf16 v[120:123], a[160:163], v[152:155], v[120:123]\n\t"
      "ds_read_b64_tr_b16 a[204:205], v221 offset:12360\n\t"
      "ds_read_b64_tr_b16 a[208:209], v221 offset:12416\n\t"
      "v_mfma_f32_16x16x32_bf16 v[132:135], a[164:167], v[152:155], v[132:135]\n\t"
      "ds_read_b64_tr_b16 a[212:213], v221 offset:12424\n\t"
      "ds_read_b128 v[148:151], v198 offset:512\n\t"
      "s_waitcnt lgkmcnt(8)\n\t"
      "v_mfma_f32_16x16x32_bf16 v[80:83], a[168:171], v[144:147], v[80:83]\n\t"
      "s_add_u32 m0, s41, 0x21000\n\t"
      "s_mov_b32 s51, 0x42000\n\t"
      "buffer_load_dwordx4 v211, s[16:19], s51 offen lds\n\t"
      "v_mfma_f32_16x16x32_bf16 v[84:87], a[172:175], v[144:147], v[84:87]\n\t"
      "buffer_load_dwordx4 v212, s[16:19], s51 offen offset:1024 lds\n\t"
      "v_mfma_f32_16x16x32_bf16 v[88:91], a[176:179], v[144:147], v[88:91]\n\t"
      "buffer_load_dwordx4 v213, s[16:19], s51 offen offset:2048 lds\n\t"
      "s_waitcnt vmcnt(15)\n\t"
      "ds_read_b64_tr_b16 a[194:195], v214 offset:0\n\t"
      "v_mfma_f32_16x16x32_bf16 v[92:95], a[180:183], v[144:147], v[92:95]\n\t"
      "ds_read_b64_tr_b16 a[198:199], v214 offset:8\n\t"
      "ds_read_b64_tr_b16 a[202:203], v214 offset:64\n\t"
      "v_mfma_f32_16x16x32_bf16 v[96:99], a[184:187], v[144:147], v[96:99]\n\t"
      "ds_read_b64_tr_b16 a[206:207], v214 offset:72\n\t"
      "ds_read_b64_tr_b16 a[210:211], v214 offset:128\n\t"
      "v_mfma_f32_16x16x32_bf16 v[100:103], a[188:191], v[144:147], v[100:103]\n\t"
      "ds_read_b64_tr_b16 a[214:215], v214 offset:136\n\t"
      "ds_read_b128 v[160:163], v198 offset:12800\n\t"
      "s_waitcnt lgkmcnt(8)\n\t"
      "v_mfma_f32_16x16x32_bf16 v[104:107], a[168:171], v[156:159], v[104:107]\n\t"
      "s_add_u32 m0, s41, 0x24000\n\t"
      "s_mov_b32 s51, 0x42c00\n\t"
      "buffer_load_dwordx4 v211, s[16:19], s51 offen lds\n\t"
      "v_mfma_f32_16x16x32_bf16 v[108:111], a[172:175], v[156:159], v[108:111]\n\t"
      "buffer_load_dwordx4 v212, s[16:19], s51 offen offset:1024 lds\n\t"
      "v_mfma_f32_16x16x32_bf16 v[112:115], a[176:179], v[156:159], v[112:115]\n\t"
      "buffer_load_dwordx4 v213, s[16:19], s51 offen offset:2048 lds\n\t"
      "s_waitcnt vmcnt(15)\n\t"
      "ds_read_b64_tr_b16 a[216:217], v214 offset:12288\n\t"
      "v_mfma_f32_16x16x32_bf16 v[116:119], a[180:183], v[156:159], v[116:119]\n\t"
      "ds_read_b64_tr_b16 a[220:221], v214 offset:12296\n\t"
      "ds_read_b64_tr_b16 a[224:225], v214 offset:12352\n\t"
      "v_mfma_f32_16x16x32_bf16 v[120:123], a[184:187], v[156:159], v[120:123]\n\t"
      "ds_read_b64_tr_b16 a[228:229], v214 offset:12360\n\t"
      "ds_read_b64_tr_b16 a[232:233], v214 offset:12416\n\t"
      "v_mfma_f32_16x16x32_bf16 v[132:135], a[188:191], v[156:159], v[132:135]\n\t"
      "ds_read_b64_tr_b16 a[236:237], v214 offset:12424\n\t"
      "ds_read_b128 v[140:143], v199 offset:512\n\t"
      "s_waitcnt lgkmcnt(8)\n\t"
      "v_mfma_f32_16x16x32_bf16 v[80:83], a[192:195], v[148:151], v[80:83]\n\t"
      "s_add_u32 m0, s41, 0x6000\n\t"
      "s_add_u32 s51, s40, 0x600000\n\t"
      "buffer_load_dwordx4 v208, s[20:23], s51 offen lds\n\t"
      "v_mfma_f32_16x16x32_bf16 v[84:87], a[196:199], v[148:151], v[84:87]\n\t"
      "buffer_load_dwordx4 v208, s[20:23], s51 offen offset:1024 lds\n\t"
      "v_mfma_f32_16x16x32_bf16 v[88:91], a[200:203], v[148:151], v[88:91]\n\t"
      "buffer_load_dwordx4 v208, s[20:23], s51 offen offset:2048 lds\n\t"
      "s_waitcnt vmcnt(15)\n\t"
      "ds_read_b64_tr_b16 a[218:219], v214 offset:24576\n\t"
      "v_mfma_f32_16x16x32_bf16 v[92:95], a[204:207], v[148:151], v[92:95]\n\t"
      "ds_read_b64_tr_b16 a[222:223], v214 offset:24584\n\t"
      "ds_read_b64_tr_b16 a[226:227], v214 offset:24640\n\t"
      "v_mfma_f32_16x16x32_bf16 v[96:99], a[208:211], v[148:151], v[96:99]\n\t"
      "ds_read_b64_tr_b16 a[230:231], v214 offset:24648\n\t"
      "ds_read_b64_tr_b16 a[234:235], v214 offset:24704\n\t"
      "v_mfma_f32_16x16x32_bf16 v[100:103], a[212:215], v[148:151], v[100:103]\n\t"
      "ds_read_b64_tr_b16 a[238:239], v214 offset:24712\n\t"
      "ds_read_b128 v[152:155], v199 offset:12800\n\t"
      "s_waitcnt lgkmcnt(14)\n\t"
      "v_mfma_f32_16x16x32_bf16 v[104:107], a[192:195], v[160:163], v[104:107]\n\t"
      "s_add_u32 m0, s41, 0x9000\n\t"
      "s_add_u32 s51, s40, 0x600000\n\t"
      "buffer_load_dwordx4 v209, s[20:23], s51 offen lds\n\t"
      "v_mfma_f32_16x16x32_bf16 v[108:111], a[196:199], v[160:163], v[108:111]\n\t"
      "buffer_load_dwordx4 v209, s[20:23], s51 offen offset:1024 lds\n\t"
      "v_mfma_f32_16x16x32_bf16 v[112:115], a[200:203], v[160:163], v[112:115]\n\t"
      "buffer_load_dwordx4 v209, s[20:23], s51 offen offset:2048 lds\n\t"
      "s_waitcnt vmcnt(15)\n\t"
      "ds_read_b64_tr_b16 a[240:241], v214 offset:36864\n\t"
      "v_mfma_f32_16x16x32_bf16 v[116:119], a[204:207], v[160:163], v[116:119]\n\t"
      "ds_read_b64_tr_b16 a[244:245], v214 offset:36872\n\t"
      "ds_read_b64_tr_b16 a[248:249], v214 offset:36928\n\t"
      "v_mfma_f32_16x16x32_bf16 v[120:123], a[208:211], v[160:163], v[120:123]\n\t"
      "ds_read_b64_tr_b16 a[252:253], v214 offset:36936\n\t"
      "ds_read_b64_tr_b16 v[164:165], v214 offset:36992\n\t"
      "v_mfma_f32_16x16x32_bf16 v[132:135], a[212:215], v[160:163], v[132:135]\n\t"
      "ds_read_b64_tr_b16 v[168:169], v214 offset:37000\n\t"
      "ds_read_b128 v[144:147], v196 offset:512\n\t"
      "s_waitcnt lgkmcnt(8)\n\t"
      "v_mfma_f32_16x16x32_bf16 v[80:83], a[216:219], v[140:143], v[80:83]\n\t"
      "s_add_u32 m0, s41, 0xc000\n\t"
      "s_add_u32 s51, s40, 0xc00000\n\t"
      "buffer_load_dwordx4 v208, s[20:23], s51 offen lds\n\t"
      "v_mfma_f32_16x16x32_bf16 v[84:87], a[220:223], v[140:143], v[84:87]\n\t"
      "buffer_load_dwordx4 v208, s[20:23], s51 offen offset:1024 lds\n\t"
      "v_mfma_f32_16x16x32_bf16 v[88:91], a[224:227], v[140:143], v[88:91]\n\t"
      "buffer_load_dwordx4 v208, s[20:23], s51 offen offset:2048 lds\n\t"
      "s_waitcnt vmcnt(15)\n\t"
      "ds_read_b64_tr_b16 a[242:243], v214 offset:49152\n\t"
      "v_mfma_f32_16x16x32_bf16 v[92:95], a[228:231], v[140:143], v[92:95]\n\t"
      "ds_read_b64_tr_b16 a[246:247], v214 offset:49160\n\t"
      "ds_read_b64_tr_b16 a[250:251], v214 offset:49216\n\t"
      "v_mfma_f32_16x16x32_bf16 v[96:99], a[232:235], v[140:143], v[96:99]\n\t"
      "ds_read_b64_tr_b16 a[254:255], v214 offset:49224\n\t"
      "ds_read_b64_tr_b16 v[166:167], v214 offset:49280\n\t"
      "v_mfma_f32_16x16x32_bf16 v[100:103], a[236:239], v[140:143], v[100:103]\n\t"
      "ds_read_b64_tr_b16 v[170:171], v214 offset:49288\n\t"
      "ds_read_b128 v[156:159], v196 offset:12800\n\t"
      "s_waitcnt lgkmcnt(14)\n\t"
      "v_mfma_f32_16x16x32_bf16 v[104:107], a[216:219], v[152:155], v[104:107]\n\t"
      "s_add_u32 m0, s41, 0xf000\n\t"
      "s_add_u32 s51, s40, 0xc00000\n\t"
      "buffer_load_dwordx4 v209, s[20:23], s51 offen lds\n\t"
      "v_mfma_f32_16x16x32_bf16 v[108:111], a[220:223], v[152:155], v[108:111]\n\t"
      "buffer_load_dwordx4 v209, s[20:23], s51 offen offset:1024 lds\n\t"
      "v_mfma_f32_16x16x32_bf16 v[112:115], a[224:227], v[152:155], v[112:115]\n\t"
      "buffer_load_dwordx4 v209, s[20:23], s51 offen offset:2048 lds\n\t"
      "s_waitcnt vmcnt(15)\n\t"
      "ds_read_b64_tr_b16 v[172:173], v221 offset:0\n\t"
      "v_mfma_f32_16x16x32_bf16 v[116:119], a[228:231], v[152:155], v[116:119]\n\t"
      "ds_read_b64_tr_b16 v[176:177], v221 offset:8\n\t"
      "ds_read_b64_tr_b16 v[180:181], v221 offset:64\n\t"
      "ds_read_b64_tr_b16 v[184:185], v221 offset:72\n\t"
      "v_mfma_f32_16x16x32_bf16 v[120:123], a[232:235], v[152:155], v[120:123]\n\t"
      "ds_read_b64_tr_b16 v[188:189], v221 offset:128\n\t"
      "ds_read_b64_tr_b16 v[192:193], v221 offset:136\n\t"
      "v_mfma_f32_16x16x32_bf16 v[132:135], a[236:239], v[152:155], v[132:135]\n\t"
      "ds_read_b128 v[148:151], v197 offset:512\n\t"
      "ds_read_b128 v[160:163], v197 offset:12800\n\t"
      "s_waitcnt lgkmcnt(9)\n\t"
      "v_mfma_f32_16x16x32_bf16 v[80:83], a[240:243], v[144:147], v[80:83]\n\t"
      "buffer_load_dwordx2 v[240:241], v245, s[24:27], 0 offen sc1\n\t"
      "v_mfma_f32_16x16x32_bf16 v[84:87], a[244:247], v[144:147], v[84:87]\n\t"
      "s_cmp_eq_u32 s41, s43\n\t"
      "s_cbranch_scc0 .Lc3_%=\n\t"
      "buffer_load_dword v254, v253, s[24:27], 0 offen sc1\n\t"
      ".Lc3_%=:\n\t"
      "v_mfma_f32_16x16x32_bf16 v[88:91], a[248:251], v[144:147], v[88:91]\n\t"
      "s_waitcnt vmcnt(13)\n\t"
      "ds_read_b64_tr_b16 v[174:175], v221 offset:12288\n\t"
      "v_mfma_f32_16x16x32_bf16 v[92:95], a[252:255], v[144:147], v[92:95]\n\t"
      "ds_read_b64_tr_b16 v[178:179], v221 offset:12296\n\t"
      "ds_read_b64_tr_b16 v[182:183], v221 offset:12352\n\t"
      "v_mfma_f32_16x16x32_bf16 v[96:99], v[164:167], v[144:147], v[96:99]\n\t"
      "ds_read_b64_tr_b16 v[186:187], v221 offset:12360\n\t"
      "ds_read_b64_tr_b16 v[190:191], v221 offset:12416\n\t"
      "v_mfma_f32_16x16x32_bf16 v[100:103], v[168:171], v[144:147], v[100:103]\n\t"
      "ds_read_b64_tr_b16 v[194:195], v221 offset:12424\n\t"
      "s_memrealtime s[80:81]\n\t"
      "v_add_u32_e32 v196, s46, v200\n\t"
      "v_add_u32_e32 v197, s46, v201\n\t"
      "v_add_u32_e32 v198, s46, v202\n\t"
      "v_add_u32_e32 v199, s46, v203\n\t"
      "s_waitcnt vmcnt(10)\n\t"
      "s_waitcnt lgkmcnt(6)\n\t"
      "s_barrier\n\t"
      "v_mfma_f32_16x16x32_bf16 v[104:107], a[240:243], v[156:159], v[104:107]\n\t"
      "ds_read_b128 v[32:35], v196 offset:0\n\t"
      "v_mfma_f32_16x16x32_bf16 v[108:111], a[244:247], v[156:159], v[108:111]\n\t"
      "ds_read_b128 v[36:39], v197 offset:0\n\t"
      "v_mfma_f32_16x16x32_bf16 v[112:115], a[248:251], v[156:159], v[112:115]\n\t"
      "ds_read_b128 v[40:43], v198 offset:0\n\t"
      "v_mfma_f32_16x16x32_bf16 v[116:119], a[252:255], v[156:159], v[116:119]\n\t"
      "ds_read_b128 v[44:47], v199 offset:0\n\t"
      "v_mfma_f32_16x16x32_bf16 v[120:123], v[164:167], v[156:159], v[120:123]\n\t"
      "ds_read_b128 v[48:51], v197 offset:256\n\t"
      "v_mfma_f32_16x16x32_bf16 v[132:135], v[168:171], v[156:159], v[132:135]\n\t"
      "s_waitcnt lgkmcnt(5)\n\t"
      "s_mov_b32 s86, 76\n\t"
      "s_mov_b32 s84, 100\n\t"
      "v_mfma_f32_16x16x32_bf16 v[80:83], v[172:175], v[148:151], v[80:83]\n\t"
      "ds_read_b128 v[52:55], v198 offset:256\n\t"
      "v_mfma_f32_16x16x32_bf16 v[84:87], v[176:179], v[148:151], v[84:87]\n\t"
      "ds_read_b128 v[56:59], v199 offset:256\n\t"
      "v_mfma_f32_16x16x32_bf16 v[88:91], v[180:183], v[148:151], v[88:91]\n\t"
      "ds_read_b128 v[60:63], v196 offset:256\n\t"
      "v_mfma_f32_16x16x32_bf16 v[92:95], v[184:187], v[148:151], v[92:95]\n\t"
      "ds_read_b128 v[64:67], v198 offset:512\n\t"
      "v_mfma_f32_16x16x32_bf16 v[96:99], v[188:191], v[148:151], v[96:99]\n\t"
      "ds_read_b128 v[68:71], v199 offset:512\n\t"
      "v_mfma_f32_16x16x32_bf16 v[100:103], v[192:195], v[148:151], v[100:103]\n\t"
      "ds_read_b128 v[72:75], v196 offset:512\n\t"
      "v_mfma_f32_16x16x32_bf16 v[104:107], v[172:175], v[160:163], v[104:107]\n\t"
      "ds_read_b128 v[76:79], v197 offset:512\n\t"
      "v_mfma_f32_16x16x32_bf16 v[108:111], v[176:179], v[160:163], v[108:111]\n\t"
      "v_mfma_f32_16x16x32_bf16 v[112:115], v[180:183], v[160:163], v[112:115]\n\t"
      "v_mfma_f32_16x16x32_bf16 v[116:119], v[184:187], v[160:163], v[116:119]\n\t"
      "v_mfma_f32_16x16x32_bf16 v[120:123], v[188:191], v[160:163], v[120:123]\n\t"
      "v_mfma_f32_16x16x32_bf16 v[132:135], v[192:195], v[160:163], v[132:135]\n\t"
      ".Lp2_%=:\n\t"
      "s_cbranch_vccnz .Lwsb_%=\n\t"
      "s_waitcnt lgkmcnt(8)\n\t"
      "v_mfma_f32_16x16x32_bf16 v[128:131], a[0:3], v[32:35], v[8:11]\n\t"
      "v_cvt_pk_bf16_f32 v80, v80, v81\n\t"
      "v_cvt_pk_bf16_f32 v81, v82, v83\n\t"
      "v_mfma_f32_16x16x32_bf16 v[128:131], a[24:27], v[36:39], v[128:131]\n\t"
      "v_cvt_pk_bf16_f32 v136, v120, v121\n\t"
      "v_cvt_pk_bf16_f32 v137, v122, v123\n\t"
      "v_mfma_f32_16x16x32_bf16 v[128:131], a[48:51], v[40:43], v[128:131]\n\t"
      "v_cvt_pk_bf16_f32 v138, v132, v133\n\t"
      "v_mfma_f32_16x16x32_bf16 v[128:131], a[72:75], v[44:47], v[128:131]\n\t"
      "v_cvt_pk_bf16_f32 v139, v134, v135\n\t"
      "s_waitcnt lgkmcnt(4)\n\t"
      "v_mfma_f32_16x16x32_bf16 v[128:131], a[96:99], v[48:51], v[128:131]\n\t"
      "ds_write_b128 v219, v[136:139] offset:12288\n\t"
      "v_mfma_f32_16x16x32_bf16 v[128:131], a[120:123], v[52:55], v[128:131]\n\t"
      "v_cvt_pk_bf16_f32 v82, v84, v85\n\t"
      "v_cvt_pk_bf16_f32 v83, v86, v87\n\t"
      "v_mfma_f32_16x16x32_bf16 v[128:131], a[144:147], v[56:59], v[128:131]\n\t"
      "v_cvt_pk_bf16_f32 v88, v88, v89\n\t"
      "v_cvt_pk_bf16_f32 v89, v90, v91\n\t"
      "v_mfma_f32_16x16x32_bf16 v[128:131], a[168:171], v[60:63], v[128:131]\n\t"
      "ds_write_b128 v217, v[80:83] offset:0\n\t"
      "v_cvt_pk_bf16_f32 v90, v92, v93\n\t"
      "s_waitcnt lgkmcnt(2)\n\t"
      "v_mfma_f32_16x16x32_bf16 v[128:131], a[192:195], v[64:67], v[128:131]\n\t"
      "v_cvt_pk_bf16_f32 v91, v94, v95\n\t"
      "v_cvt_pk_bf16_f32 v96, v96, v97\n\t"
      "v_mfma_f32_16x16x32_bf16 v[128:131], a[216:219], v[68:71], v[128:131]\n\t"
      "ds_write_b128 v218, v[88:91] offset:0\n\t"
      "v_cvt_pk_bf16_f32 v97, v98, v99\n\t"
      "v_mfma_f32_16x16x32_bf16 v[128:131], a[240:243], v[72:75], v[128:131]\n\t"
      "v_cvt_pk_bf16_f32 v98, v100, v101\n\t"
      "v_cvt_pk_bf16_f32 v99, v102, v103\n\t"
      "v_mfma_f32_16x16x32_bf16 v[128:131], v[172:175], v[76:79], v[128:131]\n\t"
      "v_cvt_pk_bf16_f32 v104, v104, v105\n\t"
      "v_cvt_pk_bf16_f32 v105, v106, v107\n\t"
      "v_mfma_f32_16x16x32_bf16 v[132:135], a[4:7], v[32:35], v[12:15]\n\t"
      "ds_write_b128 v219, v[96:99] offset:0\n\t"
      "v_cvt_pk_bf16_f32 v106, v108, v109\n\t"
      "v_mfma_f32_16x16x32_bf16 v[132:135], a[28:31], v[36:39], v[132:135]\n\t"
      "v_cvt_pk_bf16_f32 v107, v110, v111\n\t"
      "v_cvt_pk_bf16_f32 v112, v112, v113\n\t"
      "v_mfma_f32_16x16x32_bf16 v[132:135], a[52:55], v[40:43], v[132:135]\n\t"
      "v_cvt_pk_bf16_f32 v136, v128, v129\n\t"
      "v_mfma_f32_16x16x32_bf16 v[132:135], a[76:79], v[44:47], v[132:135]\n\t"
      "v_cvt_pk_bf16_f32 v137, v130, v131\n\t"
      "v_mfma_f32_16x16x32_bf16 v[132:135], a[100:103], v[48:51], v[132:135]\n\t"
      "ds_write_b128 v217, v[104:107] offset:12288\n\t"
      "v_cvt_pk_bf16_f32 v113, v114, v115\n\t"
      "s_add_u32 s48, s47, 0x6000\n\t"
      "v_mfma_f32_16x16x32_bf16 v[132:135], a[124:127], v[52:55], v[132:135]\n\t"
      "v_cvt_pk_bf16_f32 v114, v116, v117\n\t"
      "v_cvt_pk_bf16_f32 v115, v118, v119\n\t"
      "s_cmp_eq_u32 s48, s50\n\t"
      "s_cselect_b32 s48, s41, s48\n\t"
      "v_mfma_f32_16x16x32_bf16 v[132:135], a[148:151], v[56:59], v[132:135]\n\t"
      "ds_write_b128 v218, v[112:115] offset:12288\n\t"
      "v_mfma_f32_16x16x32_bf16 v[132:135], a[172:175], v[60:63], v[132:135]\n\t"
      "v_add_u32_e32 v217, s46, v204\n\t"
      "v_mfma_f32_16x16x32_bf16 v[132:135], a[196:199], v[64:67], v[132:135]\n\t"
      "v_add_u32_e32 v218, s46, v205\n\t"
      "v_mfma_f32_16x16x32_bf16 v[132:135], a[220:223], v[68:71], v[132:135]\n\t"
      "v_readlane_b32 s73, v248, s85\n\t"
      "v_add_u32_e32 v219, s46, v206\n\t"
      "v_mfma_f32_16x16x32_bf16 v[132:135], a[244:247], v[72:75], v[132:135]\n\t"
      "s_mov_b32 m0, s45\n\t"
      "v_mfma_f32_16x16x32_bf16 v[132:135], v[176:179], v[76:79], v[132:135]\n\t"
      "s_waitcnt lgkmcnt(0)\n\t"
      "s_barrier\n\t"
      "v_mfma_f32_16x16x32_bf16 v[128:131], a[8:11], v[32:35], v[16:19]\n\t"
      "s_mov_b32 s82, s80\n\t"
      "v_mfma_f32_16x16x32_bf16 v[128:131], a[32:35], v[36:39], v[128:131]\n\t"
      "s_memrealtime s[80:81]\n\t"
      "v_mfma_f32_16x16x32_bf16 v[128:131], a[56:59], v[40:43], v[128:131]\n\t"
      "v_cvt_pk_bf16_f32 v138, v132, v133\n\t"
      "v_mfma_f32_16x16x32_bf16 v[128:131], a[80:83], v[44:47], v[128:131]\n\t"
      "v_cvt_pk_bf16_f32 v139, v134, v135\n\t"
      "v_mfma_f32_16x16x32_bf16 v[128:131], a[104:107], v[48:51], v[128:131]\n\t"
      "ds_write_b128 v217, v[136:139] offset:0\n\t"
      "v_mfma_f32_16x16x32_bf16 v[128:131], a[128:131], v[52:55], v[128:131]\n\t"
      "buffer_load_dwordx4 v208, s[20:23], s73 offen lds\n\t"
      "v_mfma_f32_16x16x32_bf16 v[128:131], a[152:155], v[56:59], v[128:131]\n\t"
      "v_mfma_f32_16x16x32_bf16 v[128:131], a[176:179], v[60:63], v[128:131]\n\t"
      "v_mfma_f32_16x16x32_bf16 v[128:131], a[200:203], v[64:67], v[128:131]\n\t"
      "v_mfma_f32_16x16x32_bf16 v[128:131], a[224:227], v[68:71], v[128:131]\n\t"
      "v_mfma_f32_16x16x32_bf16 v[128:131], a[248:251], v[72:75], v[128:131]\n\t"
      "v_mfma_f32_16x16x32_bf16 v[128:131], v[180:183], v[76:79], v[128:131]\n\t"
      "v_mfma_f32_16x16x32_bf16 v[132:135], a[12:15], v[32:35], v[20:23]\n\t"
      "v_mfma_f32_16x16x32_bf16 v[132:135], a[36:39], v[36:39], v[132:135]\n\t"
      "buffer_load_dwordx4 v208, s[20:23], s73 offen offset:1024 lds\n\t"
      "v_mfma_f32_16x16x32_bf16 v[132:135], a[60:63], v[40:43], v[132:135]\n\t"
      "v_cvt_pk_bf16_f32 v136, v128, v129\n\t"
      "v_mfma_f32_16x16x32_bf16 v[132:135], a[84:87], v[44:47], v[132:135]\n\t"
      "v_cvt_pk_bf16_f32 v137, v130, v131\n\t"
      "v_mfma_f32_16x16x32_bf16 v[132:135], a[108:111], v[48:51], v[132:135]\n\t"
      "v_mfma_f32_16x16x32_bf16 v[132:135], a[132:135], v[52:55], v[132:135]\n\t"
      "v_mfma_f32_16x16x32_bf16 v[132:135], a[156:159], v[56:59], v[132:135]\n\t"
      "v_mfma_f32_16x16x32_bf16 v[132:135], a[180:183], v[60:63], v[132:135]\n\t"
      "v_mfma_f32_16x16x32_bf16 v[132:135], a[204:207], v[64:67], v[132:135]\n\t"
      "v_mfma_f32_16x16x32_bf16 v[132:135], a[228:231], v[68:71], v[132:135]\n\t"
      "buffer_load_dwordx4 v208, s[20:23], s73 offen offset:2048 lds\n\t"
      "v_mfma_f32_16x16x32_bf16 v[132:135], a[252:255], v[72:75], v[132:135]\n\t"
      "v_readlane_b32 s73, v249, s85\n\t"
      "s_add_u32 m0, s45, 0x3000\n\t"
      "v_mfma_f32_16x16x32_bf16 v[132:135], v[184:187], v[76:79], v[132:135]\n\t"
      "s_waitcnt vmcnt(10)\n\t"
      "s_barrier\n\t"
      "v_mfma_f32_16x16x32_bf16 v[128:131], a[16:19], v[32:35], v[24:27]\n\t"
      "ds_read_b128 v[80:83], v196 offset:12288\n\t"
      "v_mfma_f32_16x16x32_bf16 v[128:131], a[40:43], v[36:39], v[128:131]\n\t"
      "v_mfma_f32_16x16x32_bf16 v[128:131], a[64:67], v[40:43], v[128:131]\n\t"
      "ds_read_b128 v[84:87], v197 offset:12288\n\t"
      "v_cvt_pk_bf16_f32 v138, v132, v133\n\t"
      "v_mfma_f32_16x16x32_bf16 v[128:131], a[88:91], v[44:47], v[128:131]\n\t"
      "ds_read_b128 v[88:91], v198 offset:12288\n\t"
      "v_cvt_pk_bf16_f32 v139, v134, v135\n\t"
      "v_mfma_f32_16x16x32_bf16 v[128:131], a[112:115], v[48:51], v[128:131]\n\t"
      "ds_write_b128 v218, v[136:139] offset:0\n\t"
      "v_mfma_f32_16x16x32_bf16 v[128:131], a[136:139], v[52:55], v[128:131]\n\t"
      "buffer_load_dwordx4 v209, s[20:23], s73 offen lds\n\t"
      "s_waitcnt vmcnt(10)\n\t"
      "s_cmp_eq_u32 s94, 0\n\t"
      "s_cbranch_scc0 .Lwgvw_0_%=\n\t"
      "s_waitcnt vmcnt(4)\n\t"
      ".Lwgvw_0_%=:\n\t"
      "v_subrev_u32_e32 v242, s63, v240\n\t"
      "v_xor_b32_e32 v241, s53, v241\n\t"
      "v_mfma_f32_16x16x32_bf16 v[128:131], a[160:163], v[56:59], v[128:131]\n\t"
      "ds_read_b128 v[92:95], v199 offset:12288\n\t"
      "v_lshrrev_b32_e32 v240, 13, v242\n\t"
      "v_or_b32_e32 v240, v240, v241\n\t"
      "v_mfma_f32_16x16x32_bf16 v[128:131], a[184:187], v[60:63], v[128:131]\n\t"
      "ds_read_b128 v[96:99], v197 offset:12544\n\t"
      "v_mfma_f32_16x16x32_bf16 v[128:131], a[208:211], v[64:67], v[128:131]\n\t"
      "v_cmp_ne_u32_e32 vcc, 0, v240\n\t"
      "v_and_b32_e32 v242, 15, v242\n\t"
      "v_mfma_f32_16x16x32_bf16 v[128:131], a[232:235], v[68:71], v[128:131]\n\t"
      "v_mfma_f32_16x16x32_bf16 v[128:131], v[164:167], v[72:75], v[128:131]\n\t"
      "ds_read_b128 v[100:103], v198 offset:12544\n\t"
      "s_cbranch_vccnz .Lgs_%=\n\t"
      ".Lgr_%=:\n\t"
      "ds_write_b32 v246, v242\n\t"
      "v_mfma_f32_16x16x32_bf16 v[128:131], v[188:191], v[76:79], v[128:131]\n\t"
      "ds_read_b128 v[104:107], v199 offset:12544\n\t"
      "v_mfma_f32_16x16x32_bf16 v[132:135], a[20:23], v[32:35], v[28:31]\n\t"
      "ds_read_b128 v[108:111], v196 offset:12544\n\t"
      "v_mfma_f32_16x16x32_bf16 v[132:135], a[44:47], v[36:39], v[132:135]\n\t"
      "buffer_load_dwordx4 v209, s[20:23], s73 offen offset:1024 lds\n\t"
      "v_mfma_f32_16x16x32_bf16 v[132:135], a[68:71], v[40:43], v[132:135]\n\t"
      "ds_read_b128 v[112:115], v198 offset:12800\n\t"
      "v_cvt_pk_bf16_f32 v136, v128, v129\n\t"
      "v_add_u32_e32 v220, s46, v207\n\t"
      "v_mfma_f32_16x16x32_bf16 v[132:135], a[92:95], v[44:47], v[132:135]\n\t"
      "ds_read_b128 v[116:119], v199 offset:12800\n\t"
      "v_cvt_pk_bf16_f32 v137, v130, v131\n\t"
      "s_sub_u32 s84, s84, 4\n\t"
      "s_max_i32 s84, s84, s86\n\t"
      "v_mfma_f32_16x16x32_bf16 v[132:135], a[116:119], v[48:51], v[132:135]\n\t"
      "s_sub_u32 s83, s82, s80\n\t"
      "v_mfma_f32_16x16x32_bf16 v[132:135], a[140:143], v[52:55], v[132:135]\n\t"
      "s_max_i32 s83, s83, 0\n\t"
      "s_add_u32 s82, s80, s83\n\t"
      "v_mfma_f32_16x16x32_bf16 v[132:135], a[164:167], v[56:59], v[132:135]\n\t"
      "ds_read_b128 v[120:123], v196 offset:12800\n\t"
      "v_mfma_f32_16x16x32_bf16 v[132:135], a[188:191], v[60:63], v[132:135]\n\t"
      "ds_read_b128 v[124:127], v197 offset:12800\n\t"
      "v_mfma_f32_16x16x32_bf16 v[132:135], a[212:215], v[64:67], v[132:135]\n\t"
      "v_mfma_f32_16x16x32_bf16 v[132:135], a[236:239], v[68:71], v[132:135]\n\t"
      "s_add_u32 s82, s82, s84\n\t"
      "v_mfma_f32_16x16x32_bf16 v[132:135], v[168:171], v[72:75], v[132:135]\n\t"
      "s_add_u32 s44, s44, 1\n\t"
      "s_add_u32 s85, s85, 1\n\t"
      "v_mfma_f32_16x16x32_bf16 v[132:135], v[192:195], v[76:79], v[132:135]\n\t"
      "s_waitcnt lgkmcnt(9)\n\t"
      "v_mfma_f32_16x16x32_bf16 v[128:131], a[0:3], v[80:83], v[8:11]\n\t"
      "v_mfma_f32_16x16x32_bf16 v[128:131], a[24:27], v[84:87], v[128:131]\n\t"
      "v_readlane_b32 s72, v248, s44\n\t"
      "v_mfma_f32_16x16x32_bf16 v[128:131], a[48:51], v[88:91], v[128:131]\n\t"
      "v_cvt_pk_bf16_f32 v138, v132, v133\n\t"
      "v_mfma_f32_16x16x32_bf16 v[128:131], a[72:75], v[92:95], v[128:131]\n\t"
      "v_cvt_pk_bf16_f32 v139, v134, v135\n\t"
      "s_waitcnt lgkmcnt(4)\n\t"
      "v_mfma_f32_16x16x32_bf16 v[128:131], a[96:99], v[96:99], v[128:131]\n\t"
      "ds_write_b128 v219, v[136:139] offset:0\n\t"
      "v_mfma_f32_16x16x32_bf16 v[128:131], a[120:123], v[100:103], v[128:131]\n\t"
      "v_mfma_f32_16x16x32_bf16 v[128:131], a[144:147], v[104:107], v[128:131]\n\t"
      "buffer_load_dwordx4 v209, s[20:23], s73 offen offset:2048 lds\n\t"
      "v_mfma_f32_16x16x32_bf16 v[128:131], a[168:171], v[108:111], v[128:131]\n\t"
      "v_add_u32_e32 v196, s47, v200\n\t"
      "s_waitcnt lgkmcnt(1)\n\t"
      "v_mfma_f32_16x16x32_bf16 v[128:131], a[192:195], v[112:115], v[128:131]\n\t"
      "v_add_u32_e32 v197, s47, v201\n\t"
      "v_mfma_f32_16x16x32_bf16 v[128:131], a[216:219], v[116:119], v[128:131]\n\t"
      "v_add_u32_e32 v198, s47, v202\n\t"
      "v_mfma_f32_16x16x32_bf16 v[128:131], a[240:243], v[120:123], v[128:131]\n\t"
      "v_add_u32_e32 v199, s47, v203\n\t"
      "v_mfma_f32_16x16x32_bf16 v[128:131], v[172:175], v[124:127], v[128:131]\n\t"
      "s_cmp_gt_i32 s83, 1\n\t"
      "s_cbranch_scc1 .LslpP3_%=\n\t"
      ".LretP3_%=:\n\t"
      "s_cmp_eq_u32 s94, 1\n\t"
      "s_cbranch_scc0 .LwP3barv_1_%=\n\t"
      "s_waitcnt vmcnt(9)\n\t"
      ".LwP3barv_1_%=:\n\t"
      "s_waitcnt lgkmcnt(0)\n\t"
      "s_barrier\n\t"
      "v_mfma_f32_16x16x32_bf16 v[132:135], a[4:7], v[80:83], v[12:15]\n\t"
      "ds_read_b128 v[140:143], v220 offset:0\n\t"
      "v_mfma_f32_16x16x32_bf16 v[132:135], a[28:31], v[84:87], v[132:135]\n\t"
      "ds_read_b128 v[240:243], v247 offset:0\n\t"
      "v_mfma_f32_16x16x32_bf16 v[132:135], a[52:55], v[88:91], v[132:135]\n\t"
      "v_cvt_pk_bf16_f32 v136, v128, v129\n\t"
      "v_mfma_f32_16x16x32_bf16 v[132:135], a[76:79], v[92:95], v[132:135]\n\t"
      "ds_read_b128 v[32:35], v196 offset:0\n\t"
      "v_cvt_pk_bf16_f32 v137, v130, v131\n\t"
      "v_mfma_f32_16x16x32_bf16 v[132:135], a[100:103], v[96:99], v[132:135]\n\t"
      "v_readlane_b32 s68, v248, s94\n\t"
      "v_mfma_f32_16x16x32_bf16 v[132:135], a[124:127], v[100:103], v[132:135]\n\t"
      "ds_read_b128 v[152:155], v238 offset:0\n\t"
      "v_mfma_f32_16x16x32_bf16 v[132:135], a[148:151], v[104:107], v[132:135]\n\t"
      "ds_read_b128 v[36:39], v197 offset:0\n\t"
      "v_mfma_f32_16x16x32_bf16 v[132:135], a[172:175], v[108:111], v[132:135]\n\t"
      "v_mfma_f32_16x16x32_bf16 v[132:135], a[196:199], v[112:115], v[132:135]\n\t"
      "s_waitcnt lgkmcnt(3)\n\t"
      "v_add3_u32 v240, v240, v241, v242\n\t"
      "v_mfma_f32_16x16x32_bf16 v[132:135], a[220:223], v[116:119], v[132:135]\n\t"
      "ds_read_b128 v[40:43], v198 offset:0\n\t"
      "v_mfma_f32_16x16x32_bf16 v[132:135], a[244:247], v[120:123], v[132:135]\n\t"
      "v_add_u32_e32 v240, v240, v243\n\t"
      "v_mfma_f32_16x16x32_bf16 v[132:135], v[176:179], v[124:127], v[132:135]\n\t"
      "buffer_store_dwordx4 v[140:143], v208, s[28:31], s72 offen sc1\n\t"
      "v_mfma_f32_16x16x32_bf16 v[128:131], a[8:11], v[80:83], v[16:19]\n\t"
      "ds_read_b128 v[44:47], v199 offset:0\n\t"
      "v_mfma_f32_16x16x32_bf16 v[128:131], a[32:35], v[84:87], v[128:131]\n\t"
      "v_add_u32_dpp v241, v240, v240 quad_perm:[1,0,3,2] row_mask:0xf bank_mask:0xf\n\t"
      "s_memrealtime s[80:81]\n\t"
      "v_mfma_f32_16x16x32_bf16 v[128:131], a[56:59], v[88:91], v[128:131]\n\t"
      "v_cvt_pk_bf16_f32 v138, v132, v133\n\t"
      "v_mfma_f32_16x16x32_bf16 v[128:131], a[80:83], v[92:95], v[128:131]\n\t"
      "v_cvt_pk_bf16_f32 v139, v134, v135\n\t"
      "v_mfma_f32_16x16x32_bf16 v[128:131], a[104:107], v[96:99], v[128:131]\n\t"
      "ds_write_b128 v217, v[136:139] offset:12288\n\t"
      "v_add_u32_dpp v240, v241, v241 quad_perm:[2,3,0,1] row_mask:0xf bank_mask:0xf\n\t"
      "v_mfma_f32_16x16x32_bf16 v[128:131], a[128:131], v[100:103], v[128:131]\n\t"
      "s_mov_b32 m0, s46\n\t"
      "v_mfma_f32_16x16x32_bf16 v[128:131], a[152:155], v[104:107], v[128:131]\n\t"
      "s_waitcnt lgkmcnt(4)\n\t"
      "buffer_store_dwordx4 v[152:155], v208, s[28:31], s68 offen sc1\n\t"
      "v_cmp_lt_u32_e64 s[76:77], 16, v240\n\t"
      "v_cmp_lt_u32_e32 vcc, 63, v240\n\t"
      "v_mfma_f32_16x16x32_bf16 v[128:131], a[176:179], v[108:111], v[128:131]\n\t"
      "ds_read_b128 v[48:51], v197 offset:256\n\t"
      "v_mfma_f32_16x16x32_bf16 v[128:131], a[200:203], v[112:115], v[128:131]\n\t"
      "v_mfma_f32_16x16x32_bf16 v[128:131], a[224:227], v[116:119], v[128:131]\n\t"
      "ds_read_b128 v[52:55], v198 offset:256\n\t"
      "v_mfma_f32_16x16x32_bf16 v[128:131], a[248:251], v[120:123], v[128:131]\n\t"
      "ds_read_b128 v[156:159], v238 offset:1024\n\t"
      "s_and_b32 s64, s76, 0x11111111\n\t"
      "s_cmp_lg_u64 vcc, 0\n\t"
      "s_cselect_b32 s64, 0, s64\n\t"
      "v_mfma_f32_16x16x32_bf16 v[128:131], v[180:183], v[124:127], v[128:131]\n\t"
      "v_mfma_f32_16x16x32_bf16 v[132:135], a[12:15], v[80:83], v[20:23]\n\t"
      "ds_read_b128 v[56:59], v199 offset:256\n\t"
      "v_mfma_f32_16x16x32_bf16 v[132:135], a[36:39], v[84:87], v[132:135]\n\t"
      "v_and_b32_e32 v240, s64, v252\n\t"
      "v_mfma_f32_16x16x32_bf16 v[132:135], a[60:63], v[88:91], v[132:135]\n\t"
      "v_cvt_pk_bf16_f32 v136, v128, v129\n\t"
      "v_mfma_f32_16x16x32_bf16 v[132:135], a[84:87], v[92:95], v[132:135]\n\t"
      "v_cvt_pk_bf16_f32 v137, v130, v131\n\t"
      "ds_read_b128 v[144:147], v220 offset:1024\n\t"
      "v_mfma_f32_16x16x32_bf16 v[132:135], a[108:111], v[96:99], v[132:135]\n\t"
      "v_cmp_ne_u32_e64 s[76:77], 0, v240\n\t"
      "v_mfma_f32_16x16x32_bf16 v[132:135], a[132:135], v[100:103], v[132:135]\n\t"
      "s_waitcnt lgkmcnt(2)\n\t"
      "buffer_store_dwordx4 v[156:159], v208, s[28:31], s68 offen offset:1024 sc1\n\t"
      "v_mfma_f32_16x16x32_bf16 v[132:135], a[156:159], v[104:107], v[132:135]\n\t"
      "ds_read_b128 v[60:63], v196 offset:256\n\t"
      "v_mfma_f32_16x16x32_bf16 v[132:135], a[180:183], v[108:111], v[132:135]\n\t"
      "s_xor_b32 s74, s74, s76\n\t"
      "s_xor_b32 s76, s75, s76\n\t"
      "v_mfma_f32_16x16x32_bf16 v[132:135], a[204:207], v[112:115], v[132:135]\n\t"
      "v_cndmask_b32_e64 v249, v248, v251, s[76:77]\n\t"
      "v_mfma_f32_16x16x32_bf16 v[132:135], a[228:231], v[116:119], v[132:135]\n\t"
      "ds_read_b128 v[64:67], v198 offset:512\n\t"
      "v_mfma_f32_16x16x32_bf16 v[132:135], a[252:255], v[120:123], v[132:135]\n\t"
      "s_mov_b32 s76, s74\n\t"
      "v_cndmask_b32_e64 v248, v248, v251, s[76:77]\n\t"
      "v_mfma_f32_16x16x32_bf16 v[132:135], v[184:187], v[124:127], v[132:135]\n\t"
      "s_waitcnt lgkmcnt(2)\n\t"
      "buffer_store_dwordx4 v[144:147], v208, s[28:31], s72 offen offset:1024 sc1\n\t"
      "v_mfma_f32_16x16x32_bf16 v[128:131], a[16:19], v[80:83], v[24:27]\n\t"
      "ds_read_b128 v[68:71], v199 offset:512\n\t"
      "v_mfma_f32_16x16x32_bf16 v[128:131], a[40:43], v[84:87], v[128:131]\n\t"
      "v_mfma_f32_16x16x32_bf16 v[128:131], a[64:67], v[88:91], v[128:131]\n\t"
      "v_cvt_pk_bf16_f32 v138, v132, v133\n\t"
      "v_readlane_b32 s73, v248, s85\n\t"
      "v_mfma_f32_16x16x32_bf16 v[128:131], a[88:91], v[92:95], v[128:131]\n\t"
      "v_cvt_pk_bf16_f32 v139, v134, v135\n\t"
      "ds_read_b128 v[160:163], v238 offset:2048\n\t"
      "v_mfma_f32_16x16x32_bf16 v[128:131], a[112:115], v[96:99], v[128:131]\n\t"
      "ds_write_b128 v218, v[136:139] offset:12288\n\t"
      "v_mfma_f32_16x16x32_bf16 v[128:131], a[136:139], v[100:103], v[128:131]\n\t"
      "v_mfma_f32_16x16x32_bf16 v[128:131], a[160:163], v[104:107], v[128:131]\n\t"
      "v_mfma_f32_16x16x32_bf16 v[128:131], a[184:187], v[108:111], v[128:131]\n\t"
      "ds_read_b128 v[148:151], v220 offset:2048\n\t"
      "v_mfma_f32_16x16x32_bf16 v[128:131], a[208:211], v[112:115], v[128:131]\n\t"
      "buffer_load_dwordx4 v208, s[20:23], s73 offen lds\n\t"
      "v_mfma_f32_16x16x32_bf16 v[128:131], a[232:235], v[116:119], v[128:131]\n\t"
      "ds_read_b128 v[72:75], v196 offset:512\n\t"
      "v_mfma_f32_16x16x32_bf16 v[128:131], v[164:167], v[120:123], v[128:131]\n\t"
      "v_readlane_b32 s86, v250, s44\n\t"
      "v_mfma_f32_16x16x32_bf16 v[128:131], v[188:191], v[124:127], v[128:131]\n\t"
      "v_mfma_f32_16x16x32_bf16 v[132:135], a[20:23], v[80:83], v[28:31]\n\t"
      "ds_read_b128 v[76:79], v197 offset:512\n\t"
      "v_mfma_f32_16x16x32_bf16 v[132:135], a[44:47], v[84:87], v[132:135]\n\t"
      "buffer_load_dwordx4 v208, s[20:23], s73 offen offset:1024 lds\n\t"
      "v_mfma_f32_16x16x32_bf16 v[132:135], a[68:71], v[88:91], v[132:135]\n\t"
      "v_cvt_pk_bf16_f32 v136, v128, v129\n\t"
      "v_mfma_f32_16x16x32_bf16 v[132:135], a[92:95], v[92:95], v[132:135]\n\t"
      "v_cvt_pk_bf16_f32 v137, v130, v131\n\t"
      "s_sub_u32 s84, s84, 4\n\t"
      "s_max_i32 s84, s84, s86\n\t"
      "v_mfma_f32_16x16x32_bf16 v[132:135], a[116:119], v[96:99], v[132:135]\n\t"
      "s_sub_u32 s83, s82, s80\n\t"
      "v_mfma_f32_16x16x32_bf16 v[132:135], a[140:143], v[100:103], v[132:135]\n\t"
      "s_waitcnt lgkmcnt(4)\n\t"
      "buffer_store_dwordx4 v[160:163], v208, s[28:31], s68 offen offset:2048 sc1\n\t"
      "s_max_i32 s83, s83, 0\n\t"
      "s_add_u32 s82, s80, s83\n\t"
      "v_mfma_f32_16x16x32_bf16 v[132:135], a[164:167], v[104:107], v[132:135]\n\t"
      "s_mov_b32 s45, s46\n\t"
      "v_mfma_f32_16x16x32_bf16 v[132:135], a[188:191], v[108:111], v[132:135]\n\t"
      "s_mov_b32 s46, s47\n\t"
      "v_mfma_f32_16x16x32_bf16 v[132:135], a[212:215], v[112:115], v[132:135]\n\t"
      "s_mov_b32 s47, s48\n\t"
      "v_mfma_f32_16x16x32_bf16 v[132:135], a[236:239], v[116:119], v[132:135]\n\t"
      "s_add_u32 s82, s82, s84\n\t"
      "v_mfma_f32_16x16x32_bf16 v[132:135], v[168:171], v[120:123], v[132:135]\n\t"
      "s_cmp_lg_u32 s44, s49\n\t"
      "v_mfma_f32_16x16x32_bf16 v[132:135], v[192:195], v[124:127], v[132:135]\n\t"
      "s_waitcnt lgkmcnt(2)\n\t"
      "buffer_store_dwordx4 v[148:151], v208, s[28:31], s72 offen offset:2048 sc1\n\t"
      "v_mfma_f32_16x16x32_bf16 v[128:131], a[0:3], v[32:35], v[8:11]\n\t"
      "v_mfma_f32_16x16x32_bf16 v[128:131], a[24:27], v[36:39], v[128:131]\n\t"
      "v_readlane_b32 s72, v249, s44\n\t"
      "v_mfma_f32_16x16x32_bf16 v[128:131], a[48:51], v[40:43], v[128:131]\n\t"
      "v_cvt_pk_bf16_f32 v138, v132, v133\n\t"
      "v_mfma_f32_16x16x32_bf16 v[128:131], a[72:75], v[44:47], v[128:131]\n\t"
      "v_cvt_pk_bf16_f32 v139, v134, v135\n\t"
      "v_mfma_f32_16x16x32_bf16 v[128:131], a[96:99], v[48:51], v[128:131]\n\t"
      "ds_write_b128 v219, v[136:139] offset:12288\n\t"
      "v_mfma_f32_16x16x32_bf16 v[128:131], a[120:123], v[52:55], v[128:131]\n\t"
      "v_mfma_f32_16x16x32_bf16 v[128:131], a[144:147], v[56:59], v[128:131]\n\t"
      "buffer_load_dwordx4 v208, s[20:23], s73 offen offset:2048 lds\n\t"
      "v_mfma_f32_16x16x32_bf16 v[128:131], a[168:171], v[60:63], v[128:131]\n\t"
      "s_waitcnt lgkmcnt(1)\n\t"
      "v_mfma_f32_16x16x32_bf16 v[128:131], a[192:195], v[64:67], v[128:131]\n\t"
      "v_add_u32_e32 v217, s46, v204\n\t"
      "v_mfma_f32_16x16x32_bf16 v[128:131], a[216:219], v[68:71], v[128:131]\n\t"
      "v_add_u32_e32 v218, s46, v205\n\t"
      "v_mfma_f32_16x16x32_bf16 v[128:131], a[240:243], v[72:75], v[128:131]\n\t"
      "v_add_u32_e32 v219, s46, v206\n\t"
      "v_mfma_f32_16x16x32_bf16 v[128:131], v[172:175], v[76:79], v[128:131]\n\t"
      "s_cmp_gt_i32 s83, 1\n\t"
      "s_cbranch_scc1 .LslpP4_%=\n\t"
      ".LretP4_%=:\n\t"
      "s_waitcnt vmcnt(15) lgkmcnt(0)\n\t"
      "s_barrier\n\t"
      "v_mfma_f32_16x16x32_bf16 v[132:135], a[4:7], v[32:35], v[12:15]\n\t"
      "ds_read_b128 v[140:143], v220 offset:12288\n\t"
      "v_mfma_f32_16x16x32_bf16 v[132:135], a[28:31], v[36:39], v[132:135]\n\t"
      "v_mfma_f32_16x16x32_bf16 v[132:135], a[52:55], v[40:43], v[132:135]\n\t"
      "v_cvt_pk_bf16_f32 v136, v128, v129\n\t"
      "v_mfma_f32_16x16x32_bf16 v[132:135], a[76:79], v[44:47], v[132:135]\n\t"
      "ds_read_b128 v[80:83], v196 offset:12288\n\t"
      "v_cvt_pk_bf16_f32 v137, v130, v131\n\t"
      "v_mfma_f32_16x16x32_bf16 v[132:135], a[100:103], v[48:51], v[132:135]\n\t"
      "v_readlane_b32 s68, v248, s94\n\t"
      "s_add_u32 s48, s47, 0x6000\n\t"
      "v_mfma_f32_16x16x32_bf16 v[132:135], a[124:127], v[52:55], v[132:135]\n\t"
      "ds_read_b128 v[152:155], v238 offset:12288\n\t"
      "s_cmp_eq_u32 s48, s50\n\t"
      "s_cselect_b32 s48, s41, s48\n\t"
      "v_mfma_f32_16x16x32_bf16 v[132:135], a[148:151], v[56:59], v[132:135]\n\t"
      "ds_read_b128 v[84:87], v197 offset:12288\n\t"
      "v_mfma_f32_16x16x32_bf16 v[132:135], a[172:175], v[60:63], v[132:135]\n\t"
      "v_readlane_b32 s73, v249, s85\n\t"
      "v_mfma_f32_16x16x32_bf16 v[132:135], a[196:199], v[64:67], v[132:135]\n\t"
      "v_mfma_f32_16x16x32_bf16 v[132:135], a[220:223], v[68:71], v[132:135]\n\t"
      "ds_read_b128 v[88:91], v198 offset:12288\n\t"
      "v_mfma_f32_16x16x32_bf16 v[132:135], a[244:247], v[72:75], v[132:135]\n\t"
      "v_mfma_f32_16x16x32_bf16 v[132:135], v[176:179], v[76:79], v[132:135]\n\t"
      "s_waitcnt lgkmcnt(4)\n\t"
      "buffer_store_dwordx4 v[140:143], v209, s[28:31], s72 offen sc1\n\t"
      "v_mfma_f32_16x16x32_bf16 v[128:131], a[8:11], v[32:35], v[16:19]\n\t"
      "ds_read_b128 v[92:95], v199 offset:12288\n\t"
      "v_mfma_f32_16x16x32_bf16 v[128:131], a[32:35], v[36:39], v[128:131]\n\t"
      "s_memrealtime s[80:81]\n\t"
      "v_mfma_f32_16x16x32_bf16 v[128:131], a[56:59], v[40:43], v[128:131]\n\t"
      "v_cvt_pk_bf16_f32 v138, v132, v133\n\t"
      "v_mfma_f32_16x16x32_bf16 v[128:131], a[80:83], v[44:47], v[128:131]\n\t"
      "v_cvt_pk_bf16_f32 v139, v134, v135\n\t"
      "v_mfma_f32_16x16x32_bf16 v[128:131], a[104:107], v[48:51], v[128:131]\n\t"
      "ds_write_b128 v217, v[136:139] offset:0\n\t"
      "v_mfma_f32_16x16x32_bf16 v[128:131], a[128:131], v[52:55], v[128:131]\n\t"
      "s_add_u32 m0, s45, 0x3000\n\t"
      "v_mfma_f32_16x16x32_bf16 v[128:131], a[152:155], v[56:59], v[128:131]\n\t"
      "s_waitcnt lgkmcnt(4)\n\t"
      "buffer_store_dwordx4 v[152:155], v209, s[28:31], s68 offen sc1\n\t"
      "v_mfma_f32_16x16x32_bf16 v[128:131], a[176:179], v[60:63], v[128:131]\n\t"
      "ds_read_b128 v[96:99], v197 offset:12544\n\t"
      "v_mfma_f32_16x16x32_bf16 v[128:131], a[200:203], v[64:67], v[128:131]\n\t"
      "v_mfma_f32_16x16x32_bf16 v[128:131], a[224:227], v[68:71], v[128:131]\n\t"
      "ds_read_b128 v[100:103], v198 offset:12544\n\t"
      "v_mfma_f32_16x16x32_bf16 v[128:131], a[248:251], v[72:75], v[128:131]\n\t"
      "ds_read_b128 v[156:159], v238 offset:13312\n\t"
      "v_mfma_f32_16x16x32_bf16 v[128:131], v[180:183], v[76:79], v[128:131]\n\t"
      "buffer_load_dwordx4 v209, s[20:23], s73 offen lds\n\t"
      "v_mfma_f32_16x16x32_bf16 v[132:135], a[12:15], v[32:35], v[20:23]\n\t"
      "ds_read_b128 v[104:107], v199 offset:12544\n\t"
      "v_mfma_f32_16x16x32_bf16 v[132:135], a[36:39], v[36:39], v[132:135]\n\t"
      "v_mfma_f32_16x16x32_bf16 v[132:135], a[60:63], v[40:43], v[132:135]\n\t"
      "v_cvt_pk_bf16_f32 v136, v128, v129\n\t"
      "v_mfma_f32_16x16x32_bf16 v[132:135], a[84:87], v[44:47], v[132:135]\n\t"
      "v_cvt_pk_bf16_f32 v137, v130, v131\n\t"
      "ds_read_b128 v[144:147], v220 offset:13312\n\t"
      "v_mfma_f32_16x16x32_bf16 v[132:135], a[108:111], v[48:51], v[132:135]\n\t"
      "v_mfma_f32_16x16x32_bf16 v[132:135], a[132:135], v[52:55], v[132:135]\n\t"
      "s_waitcnt lgkmcnt(2)\n\t"
      "buffer_store_dwordx4 v[156:159], v209, s[28:31], s68 offen offset:1024 sc1\n\t"
      "v_mfma_f32_16x16x32_bf16 v[132:135], a[156:159], v[56:59], v[132:135]\n\t"
      "ds_read_b128 v[108:111], v196 offset:12544\n\t"
      "v_mfma_f32_16x16x32_bf16 v[132:135], a[180:183], v[60:63], v[132:135]\n\t"
      "v_mfma_f32_16x16x32_bf16 v[132:135], a[204:207], v[64:67], v[132:135]\n\t"
      "v_mfma_f32_16x16x32_bf16 v[132:135], a[228:231], v[68:71], v[132:135]\n\t"
      "ds_read_b128 v[112:115], v198 offset:12800\n\t"
      "v_mfma_f32_16x16x32_bf16 v[132:135], a[252:255], v[72:75], v[132:135]\n\t"
      "v_mfma_f32_16x16x32_bf16 v[132:135], v[184:187], v[76:79], v[132:135]\n\t"
      "s_waitcnt lgkmcnt(2)\n\t"
      "buffer_store_dwordx4 v[144:147], v209, s[28:31], s72 offen offset:1024 sc1\n\t"
      "v_mfma_f32_16x16x32_bf16 v[128:131], a[16:19], v[32:35], v[24:27]\n\t"
      "ds_read_b128 v[116:119], v199 offset:12800\n\t"
      "v_mfma_f32_16x16x32_bf16 v[128:131], a[40:43], v[36:39], v[128:131]\n\t"
      "v_mfma_f32_16x16x32_bf16 v[128:131], a[64:67], v[40:43], v[128:131]\n\t"
      "v_cvt_pk_bf16_f32 v138, v132, v133\n\t"
      "v_mfma_f32_16x16x32_bf16 v[128:131], a[88:91], v[44:47], v[128:131]\n\t"
      "v_cvt_pk_bf16_f32 v139, v134, v135\n\t"
      "ds_read_b128 v[160:163], v238 offset:14336\n\t"
      "v_mfma_f32_16x16x32_bf16 v[128:131], a[112:115], v[48:51], v[128:131]\n\t"
      "ds_write_b128 v218, v[136:139] offset:0\n\t"
      "v_mfma_f32_16x16x32_bf16 v[128:131], a[136:139], v[52:55], v[128:131]\n\t"
      "v_mfma_f32_16x16x32_bf16 v[128:131], a[160:163], v[56:59], v[128:131]\n\t"
      "v_mfma_f32_16x16x32_bf16 v[128:131], a[184:187], v[60:63], v[128:131]\n\t"
      "ds_read_b128 v[148:151], v220 offset:14336\n\t"
      "v_mfma_f32_16x16x32_bf16 v[128:131], a[208:211], v[64:67], v[128:131]\n\t"
      "v_mfma_f32_16x16x32_bf16 v[128:131], a[232:235], v[68:71], v[128:131]\n\t"
      "ds_read_b128 v[120:123], v196 offset:12800\n\t"
      "v_mfma_f32_16x16x32_bf16 v[128:131], v[164:167], v[72:75], v[128:131]\n\t"
      "v_mfma_f32_16x16x32_bf16 v[128:131], v[188:191], v[76:79], v[128:131]\n\t"
      "buffer_load_dwordx4 v209, s[20:23], s73 offen offset:1024 lds\n\t"
      "v_mfma_f32_16x16x32_bf16 v[132:135], a[20:23], v[32:35], v[28:31]\n\t"
      "ds_read_b128 v[124:127], v197 offset:12800\n\t"
      "v_mfma_f32_16x16x32_bf16 v[132:135], a[44:47], v[36:39], v[132:135]\n\t"
      "v_mfma_f32_16x16x32_bf16 v[132:135], a[68:71], v[40:43], v[132:135]\n\t"
      "v_cvt_pk_bf16_f32 v136, v128, v129\n\t"
      "v_add_u32_e32 v220, s46, v207\n\t"
      "v_mfma_f32_16x16x32_bf16 v[132:135], a[92:95], v[44:47], v[132:135]\n\t"
      "v_cvt_pk_bf16_f32 v137, v130, v131\n\t"
      "s_sub_u32 s84, s84, 4\n\t"
      "s_max_i32 s84, s84, s86\n\t"
      "v_mfma_f32_16x16x32_bf16 v[132:135], a[116:119], v[48:51], v[132:135]\n\t"
      "s_sub_u32 s83, s82, s80\n\t"
      "v_mfma_f32_16x16x32_bf16 v[132:135], a[140:143], v[52:55], v[132:135]\n\t"
      "s_waitcnt lgkmcnt(4)\n\t"
      "buffer_store_dwordx4 v[160:163], v209, s[28:31], s68 offen offset:2048 sc1\n\t"
      "s_max_i32 s83, s83, 0\n\t"
      "s_add_u32 s82, s80, s83\n\t"
      "v_mfma_f32_16x16x32_bf16 v[132:135], a[164:167], v[56:59], v[132:135]\n\t"
      "v_mfma_f32_16x16x32_bf16 v[132:135], a[188:191], v[60:63], v[132:135]\n\t"
      "v_mfma_f32_16x16x32_bf16 v[132:135], a[212:215], v[64:67], v[132:135]\n\t"
      "s_add_u32 s44, s44, 1\n\t"
      "v_mfma_f32_16x16x32_bf16 v[132:135], a[236:239], v[68:71], v[132:135]\n\t"
      "s_add_u32 s82, s82, s84\n\t"
      "v_mfma_f32_16x16x32_bf16 v[132:135], v[168:171], v[72:75], v[132:135]\n\t"
      "s_add_u32 s85, s85, 1\n\t"
      "v_mfma_f32_16x16x32_bf16 v[132:135], v[192:195], v[76:79], v[132:135]\n\t"
      "s_waitcnt lgkmcnt(2)\n\t"
      "buffer_store_dwordx4 v[148:151], v209, s[28:31], s72 offen offset:2048 sc1\n\t"
      "v_mfma_f32_16x16x32_bf16 v[128:131], a[0:3], v[80:83], v[8:11]\n\t"
      "v_mfma_f32_16x16x32_bf16 v[128:131], a[24:27], v[84:87], v[128:131]\n\t"
      "v_readlane_b32 s72, v248, s44\n\t"
      "v_mfma_f32_16x16x32_bf16 v[128:131], a[48:51], v[88:91], v[128:131]\n\t"
      "v_cvt_pk_bf16_f32 v138, v132, v133\n\t"
      "v_mfma_f32_16x16x32_bf16 v[128:131], a[72:75], v[92:95], v[128:131]\n\t"
      "v_cvt_pk_bf16_f32 v139, v134, v135\n\t"
      "v_mfma_f32_16x16x32_bf16 v[128:131], a[96:99], v[96:99], v[128:131]\n\t"
      "ds_write_b128 v219, v[136:139] offset:0\n\t"
      "v_mfma_f32_16x16x32_bf16 v[128:131], a[120:123], v[100:103], v[128:131]\n\t"
      "v_mfma_f32_16x16x32_bf16 v[128:131], a[144:147], v[104:107], v[128:131]\n\t"
      "buffer_load_dwordx4 v209, s[20:23], s73 offen offset:2048 lds\n\t"
      "v_mfma_f32_16x16x32_bf16 v[128:131], a[168:171], v[108:111], v[128:131]\n\t"
      "v_add_u32_e32 v196, s47, v200\n\t"
      "s_waitcnt lgkmcnt(1)\n\t"
      "v_mfma_f32_16x16x32_bf16 v[128:131], a[192:195], v[112:115], v[128:131]\n\t"
      "v_add_u32_e32 v197, s47, v201\n\t"
      "v_mfma_f32_16x16x32_bf16 v[128:131], a[216:219], v[116:119], v[128:131]\n\t"
      "v_add_u32_e32 v198, s47, v202\n\t"
      "v_mfma_f32_16x16x32_bf16 v[128:131], a[240:243], v[120:123], v[128:131]\n\t"
      "v_add_u32_e32 v199, s47, v203\n\t"
      "v_mfma_f32_16x16x32_bf16 v[128:131], v[172:175], v[124:127], v[128:131]\n\t"
      "s_cmp_gt_i32 s83, 1\n\t"
      "s_cbranch_scc1 .LslpP5_%=\n\t"
      ".LretP5_%=:\n\t"
      "s_waitcnt vmcnt(21) lgkmcnt(0)\n\t"
      "s_barrier\n\t"
      "v_mfma_f32_16x16x32_bf16 v[132:135], a[4:7], v[80:83], v[12:15]\n\t"
      "ds_read_b128 v[140:143], v220 offset:0\n\t"
      "v_mfma_f32_16x16x32_bf16 v[132:135], a[28:31], v[84:87], v[132:135]\n\t"
      "v_mfma_f32_16x16x32_bf16 v[132:135], a[52:55], v[88:91], v[132:135]\n\t"
      "v_cvt_pk_bf16_f32 v136, v128, v129\n\t"
      "v_mfma_f32_16x16x32_bf16 v[132:135], a[76:79], v[92:95], v[132:135]\n\t"
      "ds_read_b128 v[32:35], v196 offset:0\n\t"
      "v_cvt_pk_bf16_f32 v137, v130, v131\n\t"
      "v_mfma_f32_16x16x32_bf16 v[132:135], a[100:103], v[96:99], v[132:135]\n\t"
      "v_mfma_f32_16x16x32_bf16 v[132:135], a[124:127], v[100:103], v[132:135]\n\t"
      "v_mfma_f32_16x16x32_bf16 v[132:135], a[148:151], v[104:107], v[132:135]\n\t"
      "ds_read_b128 v[36:39], v197 offset:0\n\t"
      "v_mfma_f32_16x16x32_bf16 v[132:135], a[172:175], v[108:111], v[132:135]\n\t"
      "v_readlane_b32 s73, v248, s85\n\t"
      "v_mfma_f32_16x16x32_bf16 v[132:135], a[196:199], v[112:115], v[132:135]\n\t"
      "v_mfma_f32_16x16x32_bf16 v[132:135], a[220:223], v[116:119], v[132:135]\n\t"
      "ds_read_b128 v[40:43], v198 offset:0\n\t"
      "v_mfma_f32_16x16x32_bf16 v[132:135], a[244:247], v[120:123], v[132:135]\n\t"
      "v_mfma_f32_16x16x32_bf16 v[132:135], v[176:179], v[124:127], v[132:135]\n\t"
      "s_waitcnt lgkmcnt(3)\n\t"
      "buffer_store_dwordx4 v[140:143], v208, s[28:31], s72 offen sc1\n\t"
      "v_mfma_f32_16x16x32_bf16 v[128:131], a[8:11], v[80:83], v[16:19]\n\t"
      "ds_read_b128 v[44:47], v199 offset:0\n\t"
      "v_mfma_f32_16x16x32_bf16 v[128:131], a[32:35], v[84:87], v[128:131]\n\t"
      "s_memrealtime s[80:81]\n\t"
      "v_mfma_f32_16x16x32_bf16 v[128:131], a[56:59], v[88:91], v[128:131]\n\t"
      "v_cvt_pk_bf16_f32 v138, v132, v133\n\t"
      "v_mfma_f32_16x16x32_bf16 v[128:131], a[80:83], v[92:95], v[128:131]\n\t"
      "v_cvt_pk_bf16_f32 v139, v134, v135\n\t"
      "v_mfma_f32_16x16x32_bf16 v[128:131], a[104:107], v[96:99], v[128:131]\n\t"
      "ds_write_b128 v217, v[136:139] offset:12288\n\t"
      "v_mfma_f32_16x16x32_bf16 v[128:131], a[128:131], v[100:103], v[128:131]\n\t"
      "s_mov_b32 m0, s46\n\t"
      "v_mfma_f32_16x16x32_bf16 v[128:131], a[152:155], v[104:107], v[128:131]\n\t"
      "v_mfma_f32_16x16x32_bf16 v[128:131], a[176:179], v[108:111], v[128:131]\n\t"
      "ds_read_b128 v[48:51], v197 offset:256\n\t"
      "v_mfma_f32_16x16x32_bf16 v[128:131], a[200:203], v[112:115], v[128:131]\n\t"
      "v_mfma_f32_16x16x32_bf16 v[128:131], a[224:227], v[116:119], v[128:131]\n\t"
      "ds_read_b128 v[52:55], v198 offset:256\n\t"
      "v_mfma_f32_16x16x32_bf16 v[128:131], a[248:251], v[120:123], v[128:131]\n\t"
      "v_mfma_f32_16x16x32_bf16 v[128:131], v[180:183], v[124:127], v[128:131]\n\t"
      "buffer_load_dwordx4 v208, s[20:23], s73 offen lds\n\t"
      "v_mfma_f32_16x16x32_bf16 v[132:135], a[12:15], v[80:83], v[20:23]\n\t"
      "ds_read_b128 v[56:59], v199 offset:256\n\t"
      "v_mfma_f32_16x16x32_bf16 v[132:135], a[36:39], v[84:87], v[132:135]\n\t"
      "v_mfma_f32_16x16x32_bf16 v[132:135], a[60:63], v[88:91], v[132:135]\n\t"
      "v_cvt_pk_bf16_f32 v136, v128, v129\n\t"
      "v_mfma_f32_16x16x32_bf16 v[132:135], a[84:87], v[92:95], v[132:135]\n\t"
      "v_cvt_pk_bf16_f32 v137, v130, v131\n\t"
      "ds_read_b128 v[144:147], v220 offset:1024\n\t"
      "v_mfma_f32_16x16x32_bf16 v[132:135], a[108:111], v[96:99], v[132:135]\n\t"
      "v_mfma_f32_16x16x32_bf16 v[132:135], a[132:135], v[100:103], v[132:135]\n\t"
      "v_mfma_f32_16x16x32_bf16 v[132:135], a[156:159], v[104:107], v[132:135]\n\t"
      "ds_read_b128 v[60:63], v196 offset:256\n\t"
      "v_mfma_f32_16x16x32_bf16 v[132:135], a[180:183], v[108:111], v[132:135]\n\t"
      "v_mfma_f32_16x16x32_bf16 v[132:135], a[204:207], v[112:115], v[132:135]\n\t"
      "v_mfma_f32_16x16x32_bf16 v[132:135], a[228:231], v[116:119], v[132:135]\n\t"
      "ds_read_b128 v[64:67], v198 offset:512\n\t"
      "v_mfma_f32_16x16x32_bf16 v[132:135], a[252:255], v[120:123], v[132:135]\n\t"
      "v_mfma_f32_16x16x32_bf16 v[132:135], v[184:187], v[124:127], v[132:135]\n\t"
      "s_waitcnt lgkmcnt(2)\n\t"
      "buffer_store_dwordx4 v[144:147], v208, s[28:31], s72 offen offset:1024 sc1\n\t"
      "v_mfma_f32_16x16x32_bf16 v[128:131], a[16:19], v[80:83], v[24:27]\n\t"
      "ds_read_b128 v[68:71], v199 offset:512\n\t"
      "v_mfma_f32_16x16x32_bf16 v[128:131], a[40:43], v[84:87], v[128:131]\n\t"
      "v_readlane_b32 s86, v250, s44\n\t"
      "v_mfma_f32_16x16x32_bf16 v[128:131], a[64:67], v[88:91], v[128:131]\n\t"
      "v_cvt_pk_bf16_f32 v138, v132, v133\n\t"
      "v_mfma_f32_16x16x32_bf16 v[128:131], a[88:91], v[92:95], v[128:131]\n\t"
      "v_cvt_pk_bf16_f32 v139, v134, v135\n\t"
      "v_mfma_f32_16x16x32_bf16 v[128:131], a[112:115], v[96:99], v[128:131]\n\t"
      "ds_write_b128 v218, v[136:139] offset:12288\n\t"
      "v_mfma_f32_16x16x32_bf16 v[128:131], a[136:139], v[100:103], v[128:131]\n\t"
      "v_mfma_f32_16x16x32_bf16 v[128:131], a[160:163], v[104:107], v[128:131]\n\t"
      "v_mfma_f32_16x16x32_bf16 v[128:131], a[184:187], v[108:111], v[128:131]\n\t"
      "ds_read_b128 v[148:151], v220 offset:2048\n\t"
      "v_mfma_f32_16x16x32_bf16 v[128:131], a[208:211], v[112:115], v[128:131]\n\t"
      "v_mfma_f32_16x16x32_bf16 v[128:131], a[232:235], v[116:119], v[128:131]\n\t"
      "ds_read_b128 v[72:75], v196 offset:512\n\t"
      "v_mfma_f32_16x16x32_bf16 v[128:131], v[164:167], v[120:123], v[128:131]\n\t"
      "v_mfma_f32_16x16x32_bf16 v[128:131], v[188:191], v[124:127], v[128:131]\n\t"
      "buffer_load_dwordx4 v208, s[20:23], s73 offen offset:1024 lds\n\t"
      "v_mfma_f32_16x16x32_bf16 v[132:135], a[20:23], v[80:83], v[28:31]\n\t"
      "ds_read_b128 v[76:79], v197 offset:512\n\t"
      "v_mfma_f32_16x16x32_bf16 v[132:135], a[44:47], v[84:87], v[132:135]\n\t"
      "v_mfma_f32_16x16x32_bf16 v[132:135], a[68:71], v[88:91], v[132:135]\n\t"
      "v_cvt_pk_bf16_f32 v136, v128, v129\n\t"
      "v_mfma_f32_16x16x32_bf16 v[132:135], a[92:95], v[92:95], v[132:135]\n\t"
      "v_cvt_pk_bf16_f32 v137, v130, v131\n\t"
      "s_sub_u32 s84, s84, 4\n\t"
      "s_max_i32 s84, s84, s86\n\t"
      "v_mfma_f32_16x16x32_bf16 v[132:135], a[116:119], v[96:99], v[132:135]\n\t"
      "s_sub_u32 s83, s82, s80\n\t"
      "v_mfma_f32_16x16x32_bf16 v[132:135], a[140:143], v[100:103], v[132:135]\n\t"
      "s_max_i32 s83, s83, 0\n\t"
      "s_add_u32 s82, s80, s83\n\t"
      "v_mfma_f32_16x16x32_bf16 v[132:135], a[164:167], v[104:107], v[132:135]\n\t"
      "s_mov_b32 s45, s46\n\t"
      "v_mfma_f32_16x16x32_bf16 v[132:135], a[188:191], v[108:111], v[132:135]\n\t"
      "s_mov_b32 s46, s47\n\t"
      "v_mfma_f32_16x16x32_bf16 v[132:135], a[212:215], v[112:115], v[132:135]\n\t"
      "s_mov_b32 s47, s48\n\t"
      "v_mfma_f32_16x16x32_bf16 v[132:135], a[236:239], v[116:119], v[132:135]\n\t"
      "s_add_u32 s82, s82, s84\n\t"
      "v_mfma_f32_16x16x32_bf16 v[132:135], v[168:171], v[120:123], v[132:135]\n\t"
      "s_cmp_lg_u32 s44, s49\n\t"
      "v_mfma_f32_16x16x32_bf16 v[132:135], v[192:195], v[124:127], v[132:135]\n\t"
      "s_waitcnt lgkmcnt(2)\n\t"
      "buffer_store_dwordx4 v[148:151], v208, s[28:31], s72 offen offset:2048 sc1\n\t"
      ".p2align 6\n\t"
      ".Lloop_%=:\n\t"
      "s_waitcnt lgkmcnt(12)\n\t"
      "v_mfma_f32_16x16x32_bf16 v[128:131], a[0:3], v[32:35], v[8:11]\n\t"
      "v_mfma_f32_16x16x32_bf16 v[128:131], a[24:27], v[36:39], v[128:131]\n\t"
      "v_readlane_b32 s72, v249, s44\n\t"
      "v_mfma_f32_16x16x32_bf16 v[128:131], a[48:51], v[40:43], v[128:131]\n\t"
      "v_cvt_pk_bf16_f32 v138, v132, v133\n\t"
      "v_mfma_f32_16x16x32_bf16 v[128:131], a[72:75], v[44:47], v[128:131]\n\t"
      "v_cvt_pk_bf16_f32 v139, v134, v135\n\t"
      "s_waitcnt lgkmcnt(6)\n\t"
      "v_mfma_f32_16x16x32_bf16 v[128:131], a[96:99], v[48:51], v[128:131]\n\t"
      "ds_write_b128 v219, v[136:139] offset:12288\n\t"
      "v_mfma_f32_16x16x32_bf16 v[128:131], a[120:123], v[52:55], v[128:131]\n\t"
      "v_mfma_f32_16x16x32_bf16 v[128:131], a[144:147], v[56:59], v[128:131]\n\t"
      "buffer_load_dwordx4 v208, s[20:23], s73 offen offset:2048 lds\n\t"
      "v_mfma_f32_16x16x32_bf16 v[128:131], a[168:171], v[60:63], v[128:131]\n\t"
      "s_waitcnt lgkmcnt(1)\n\t"
      "v_mfma_f32_16x16x32_bf16 v[128:131], a[192:195], v[64:67], v[128:131]\n\t"
      "v_add_u32_e32 v217, s46, v204\n\t"
      "v_mfma_f32_16x16x32_bf16 v[128:131], a[216:219], v[68:71], v[128:131]\n\t"
      "v_add_u32_e32 v218, s46, v205\n\t"
      "v_mfma_f32_16x16x32_bf16 v[128:131], a[240:243], v[72:75], v[128:131]\n\t"
      "v_add_u32_e32 v219, s46, v206\n\t"
      "v_mfma_f32_16x16x32_bf16 v[128:131], v[172:175], v[76:79], v[128:131]\n\t"
      "s_cmp_gt_i32 s83, 1\n\t"
      "s_cbranch_scc1 .LslpA_%=\n\t"
      ".LretA_%=:\n\t"
      "s_waitcnt vmcnt(18) lgkmcnt(0)\n\t"
      "s_barrier\n\t"
      "v_mfma_f32_16x16x32_bf16 v[132:135], a[4:7], v[32:35], v[12:15]\n\t"
      "ds_read_b128 v[140:143], v220 offset:12288\n\t"
      "v_mfma_f32_16x16x32_bf16 v[132:135], a[28:31], v[36:39], v[132:135]\n\t"
      "v_mfma_f32_16x16x32_bf16 v[132:135], a[52:55], v[40:43], v[132:135]\n\t"
      "v_cvt_pk_bf16_f32 v136, v128, v129\n\t"
      "v_mfma_f32_16x16x32_bf16 v[132:135], a[76:79], v[44:47], v[132:135]\n\t"
      "ds_read_b128 v[80:83], v196 offset:12288\n\t"
      "v_cvt_pk_bf16_f32 v137, v130, v131\n\t"
      "v_mfma_f32_16x16x32_bf16 v[132:135], a[100:103], v[48:51], v[132:135]\n\t"
      "s_add_u32 s48, s47, 0x6000\n\t"
      "v_mfma_f32_16x16x32_bf16 v[132:135], a[124:127], v[52:55], v[132:135]\n\t"
      "s_cmp_eq_u32 s48, s50\n\t"
      "s_cselect_b32 s48, s41, s48\n\t"
      "v_mfma_f32_16x16x32_bf16 v[132:135], a[148:151], v[56:59], v[132:135]\n\t"
      "ds_read_b128 v[84:87], v197 offset:12288\n\t"
      "v_mfma_f32_16x16x32_bf16 v[132:135], a[172:175], v[60:63], v[132:135]\n\t"
      "v_readlane_b32 s73, v249, s85\n\t"
      "v_mfma_f32_16x16x32_bf16 v[132:135], a[196:199], v[64:67], v[132:135]\n\t"
      "v_mfma_f32_16x16x32_bf16 v[132:135], a[220:223], v[68:71], v[132:135]\n\t"
      "ds_read_b128 v[88:91], v198 offset:12288\n\t"
      "v_mfma_f32_16x16x32_bf16 v[132:135], a[244:247], v[72:75], v[132:135]\n\t"
      "v_mfma_f32_16x16x32_bf16 v[132:135], v[176:179], v[76:79], v[132:135]\n\t"
      "s_waitcnt lgkmcnt(3)\n\t"
      "buffer_store_dwordx4 v[140:143], v209, s[28:31], s72 offen sc1\n\t"
      "v_mfma_f32_16x16x32_bf16 v[128:131], a[8:11], v[32:35], v[16:19]\n\t"
      "ds_read_b128 v[92:95], v199 offset:12288\n\t"
      "v_mfma_f32_16x16x32_bf16 v[128:131], a[32:35], v[36:39], v[128:131]\n\t"
      "s_memrealtime s[80:81]\n\t"
      "v_mfma_f32_16x16x32_bf16 v[128:131], a[56:59], v[40:43], v[128:131]\n\t"
      "v_cvt_pk_bf16_f32 v138, v132, v133\n\t"
      "v_mfma_f32_16x16x32_bf16 v[128:131], a[80:83], v[44:47], v[128:131]\n\t"
      "v_cvt_pk_bf16_f32 v139, v134, v135\n\t"
      "v_mfma_f32_16x16x32_bf16 v[128:131], a[104:107], v[48:51], v[128:131]\n\t"
      "ds_write_b128 v217, v[136:139] offset:0\n\t"
      "v_mfma_f32_16x16x32_bf16 v[128:131], a[128:131], v[52:55], v[128:131]\n\t"
      "s_add_u32 m0, s45, 0x3000\n\t"
      "v_mfma_f32_16x16x32_bf16 v[128:131], a[152:155], v[56:59], v[128:131]\n\t"
      "v_mfma_f32_16x16x32_bf16 v[128:131], a[176:179], v[60:63], v[128:131]\n\t"
      "ds_read_b128 v[96:99], v197 offset:12544\n\t"
      "v_mfma_f32_16x16x32_bf16 v[128:131], a[200:203], v[64:67], v[128:131]\n\t"
      "v_mfma_f32_16x16x32_bf16 v[128:131], a[224:227], v[68:71], v[128:131]\n\t"
      "ds_read_b128 v[100:103], v198 offset:12544\n\t"
      "v_mfma_f32_16x16x32_bf16 v[128:131], a[248:251], v[72:75], v[128:131]\n\t"
      "v_mfma_f32_16x16x32_bf16 v[128:131], v[180:183], v[76:79], v[128:131]\n\t"
      "buffer_load_dwordx4 v209, s[20:23], s73 offen lds\n\t"
      "v_mfma_f32_16x16x32_bf16 v[132:135], a[12:15], v[32:35], v[20:23]\n\t"
      "ds_read_b128 v[104:107], v199 offset:12544\n\t"
      "v_mfma_f32_16x16x32_bf16 v[132:135], a[36:39], v[36:39], v[132:135]\n\t"
      "v_mfma_f32_16x16x32_bf16 v[132:135], a[60:63], v[40:43], v[132:135]\n\t"
      "v_cvt_pk_bf16_f32 v136, v128, v129\n\t"
      "v_mfma_f32_16x16x32_bf16 v[132:135], a[84:87], v[44:47], v[132:135]\n\t"
      "v_cvt_pk_bf16_f32 v137, v130, v131\n\t"
      "ds_read_b128 v[144:147], v220 offset:13312\n\t"
      "v_mfma_f32_16x16x32_bf16 v[132:135], a[108:111], v[48:51], v[132:135]\n\t"
      "v_mfma_f32_16x16x32_bf16 v[132:135], a[132:135], v[52:55], v[132:135]\n\t"
      "v_mfma_f32_16x16x32_bf16 v[132:135], a[156:159], v[56:59], v[132:135]\n\t"
      "ds_read_b128 v[108:111], v196 offset:12544\n\t"
      "v_mfma_f32_16x16x32_bf16 v[132:135], a[180:183], v[60:63], v[132:135]\n\t"
      "v_mfma_f32_16x16x32_bf16 v[132:135], a[204:207], v[64:67], v[132:135]\n\t"
      "v_mfma_f32_16x16x32_bf16 v[132:135], a[228:231], v[68:71], v[132:135]\n\t"
      "ds_read_b128 v[112:115], v198 offset:12800\n\t"
      "v_mfma_f32_16x16x32_bf16 v[132:135], a[252:255], v[72:75], v[132:135]\n\t"
      "v_mfma_f32_16x16x32_bf16 v[132:135], v[184:187], v[76:79], v[132:135]\n\t"
      "s_waitcnt lgkmcnt(2)\n\t"
      "buffer_store_dwordx4 v[144:147], v209, s[28:31], s72 offen offset:1024 sc1\n\t"
      "v_mfma_f32_16x16x32_bf16 v[128:131], a[16:19], v[32:35], v[24:27]\n\t"
      "ds_read_b128 v[116:119], v199 offset:12800\n\t"
      "v_mfma_f32_16x16x32_bf16 v[128:131], a[40:43], v[36:39], v[128:131]\n\t"
      "v_mfma_f32_16x16x32_bf16 v[128:131], a[64:67], v[40:43], v[128:131]\n\t"
      "v_cvt_pk_bf16_f32 v138, v132, v133\n\t"
      "v_mfma_f32_16x16x32_bf16 v[128:131], a[88:91], v[44:47], v[128:131]\n\t"
      "v_cvt_pk_bf16_f32 v139, v134, v135\n\t"
      "v_mfma_f32_16x16x32_bf16 v[128:131], a[112:115], v[48:51], v[128:131]\n\t"
      "ds_write_b128 v218, v[136:139] offset:0\n\t"
      "v_mfma_f32_16x16x32_bf16 v[128:131], a[136:139], v[52:55], v[128:131]\n\t"
      "v_mfma_f32_16x16x32_bf16 v[128:131], a[160:163], v[56:59], v[128:131]\n\t"
      "v_mfma_f32_16x16x32_bf16 v[128:131], a[184:187], v[60:63], v[128:131]\n\t"
      "ds_read_b128 v[148:151], v220 offset:14336\n\t"
      "v_mfma_f32_16x16x32_bf16 v[128:131], a[208:211], v[64:67], v[128:131]\n\t"
      "v_mfma_f32_16x16x32_bf16 v[128:131], a[232:235], v[68:71], v[128:131]\n\t"
      "ds_read_b128 v[120:123], v196 offset:12800\n\t"
      "v_mfma_f32_16x16x32_bf16 v[128:131], v[164:167], v[72:75], v[128:131]\n\t"
      "v_mfma_f32_16x16x32_bf16 v[128:131], v[188:191], v[76:79], v[128:131]\n\t"
      "buffer_load_dwordx4 v209, s[20:23], s73 offen offset:1024 lds\n\t"
      "v_mfma_f32_16x16x32_bf16 v[132:135], a[20:23], v[32:35], v[28:31]\n\t"
      "ds_read_b128 v[124:127], v197 offset:12800\n\t"
      "v_mfma_f32_16x16x32_bf16 v[132:135], a[44:47], v[36:39], v[132:135]\n\t"
      "v_mfma_f32_16x16x32_bf16 v[132:135], a[68:71], v[40:43], v[132:135]\n\t"
      "v_cvt_pk_bf16_f32 v136, v128, v129\n\t"
      "v_add_u32_e32 v220, s46, v207\n\t"
      "v_mfma_f32_16x16x32_bf16 v[132:135], a[92:95], v[44:47], v[132:135]\n\t"
      "v_cvt_pk_bf16_f32 v137, v130, v131\n\t"
      "s_sub_u32 s84, s84, 4\n\t"
      "s_max_i32 s84, s84, s86\n\t"
      "v_mfma_f32_16x16x32_bf16 v[132:135], a[116:119], v[48:51], v[132:135]\n\t"
      "s_sub_u32 s83, s82, s80\n\t"
      "v_mfma_f32_16x16x32_bf16 v[132:135], a[140:143], v[52:55], v[132:135]\n\t"
      "s_max_i32 s83, s83, 0\n\t"
      "s_add_u32 s82, s80, s83\n\t"
      "v_mfma_f32_16x16x32_bf16 v[132:135], a[164:167], v[56:59], v[132:135]\n\t"
      "v_mfma_f32_16x16x32_bf16 v[132:135], a[188:191], v[60:63], v[132:135]\n\t"
      "v_mfma_f32_16x16x32_bf16 v[132:135], a[212:215], v[64:67], v[132:135]\n\t"
      "s_add_u32 s44, s44, 1\n\t"
      "v_mfma_f32_16x16x32_bf16 v[132:135], a[236:239], v[68:71], v[132:135]\n\t"
      "s_add_u32 s82, s82, s84\n\t"
      "v_mfma_f32_16x16x32_bf16 v[132:135], v[168:171], v[72:75], v[132:135]\n\t"
      "s_add_u32 s85, s85, 1\n\t"
      "v_mfma_f32_16x16x32_bf16 v[132:135], v[192:195], v[76:79], v[132:135]\n\t"
      "s_waitcnt lgkmcnt(2)\n\t"
      "buffer_store_dwordx4 v[148:151], v209, s[28:31], s72 offen offset:2048 sc1\n\t"
      "s_waitcnt lgkmcnt(12)\n\t"
      "v_mfma_f32_16x16x32_bf16 v[128:131], a[0:3], v[80:83], v[8:11]\n\t"
      "v_mfma_f32_16x16x32_bf16 v[128:131], a[24:27], v[84:87], v[128:131]\n\t"
      "v_readlane_b32 s72, v248, s44\n\t"
      "v_mfma_f32_16x16x32_bf16 v[128:131], a[48:51], v[88:91], v[128:131]\n\t"
      "v_cvt_pk_bf16_f32 v138, v132, v133\n\t"
      "v_mfma_f32_16x16x32_bf16 v[128:131], a[72:75], v[92:95], v[128:131]\n\t"
      "v_cvt_pk_bf16_f32 v139, v134, v135\n\t"
      "s_waitcnt lgkmcnt(6)\n\t"
      "v_mfma_f32_16x16x32_bf16 v[128:131], a[96:99], v[96:99], v[128:131]\n\t"
      "ds_write_b128 v219, v[136:139] offset:0\n\t"
      "v_mfma_f32_16x16x32_bf16 v[128:131], a[120:123], v[100:103], v[128:131]\n\t"
      "v_mfma_f32_16x16x32_bf16 v[128:131], a[144:147], v[104:107], v[128:131]\n\t"
      "buffer_load_dwordx4 v209, s[20:23], s73 offen offset:2048 lds\n\t"
      "v_mfma_f32_16x16x32_bf16 v[128:131], a[168:171], v[108:111], v[128:131]\n\t"
      "v_add_u32_e32 v196, s47, v200\n\t"
      "s_waitcnt lgkmcnt(1)\n\t"
      "v_mfma_f32_16x16x32_bf16 v[128:131], a[192:195], v[112:115], v[128:131]\n\t"
      "v_add_u32_e32 v197, s47, v201\n\t"
      "v_mfma_f32_16x16x32_bf16 v[128:131], a[216:219], v[116:119], v[128:131]\n\t"
      "v_add_u32_e32 v198, s47, v202\n\t"
      "v_mfma_f32_16x16x32_bf16 v[128:131], a[240:243], v[120:123], v[128:131]\n\t"
      "v_add_u32_e32 v199, s47, v203\n\t"
      "v_mfma_f32_16x16x32_bf16 v[128:131], v[172:175], v[124:127], v[128:131]\n\t"
      "s_cmp_gt_i32 s83, 1\n\t"
      "s_cbranch_scc1 .LslpB_%=\n\t"
      ".LretB_%=:\n\t"
      "s_waitcnt vmcnt(18) lgkmcnt(0)\n\t"
      "s_barrier\n\t"
      "v_mfma_f32_16x16x32_bf16 v[132:135], a[4:7], v[80:83], v[12:15]\n\t"
      "ds_read_b128 v[140:143], v220 offset:0\n\t"
      "v_mfma_f32_16x16x32_bf16 v[132:135], a[28:31], v[84:87], v[132:135]\n\t"
      "v_mfma_f32_16x16x32_bf16 v[132:135], a[52:55], v[88:91], v[132:135]\n\t"
      "v_cvt_pk_bf16_f32 v136, v128, v129\n\t"
      "v_mfma_f32_16x16x32_bf16 v[132:135], a[76:79], v[92:95], v[132:135]\n\t"
      "ds_read_b128 v[32:35], v196 offset:0\n\t"
      "v_cvt_pk_bf16_f32 v137, v130, v131\n\t"
      "v_mfma_f32_16x16x32_bf16 v[132:135], a[100:103], v[96:99], v[132:135]\n\t"
      "v_mfma_f32_16x16x32_bf16 v[132:135], a[124:127], v[100:103], v[132:135]\n\t"
      "v_mfma_f32_16x16x32_bf16 v[132:135], a[148:151], v[104:107], v[132:135]\n\t"
      "ds_read_b128 v[36:39], v197 offset:0\n\t"
      "v_mfma_f32_16x16x32_bf16 v[132:135], a[172:175], v[108:111], v[132:135]\n\t"
      "v_readlane_b32 s73, v248, s85\n\t"
      "v_mfma_f32_16x16x32_bf16 v[132:135], a[196:199], v[112:115], v[132:135]\n\t"
      "v_mfma_f32_16x16x32_bf16 v[132:135], a[220:223], v[116:119], v[132:135]\n\t"
      "ds_read_b128 v[40:43], v198 offset:0\n\t"
      "v_mfma_f32_16x16x32_bf16 v[132:135], a[244:247], v[120:123], v[132:135]\n\t"
      "v_mfma_f32_16x16x32_bf16 v[132:135], v[176:179], v[124:127], v[132:135]\n\t"
      "s_waitcnt lgkmcnt(3)\n\t"
      "buffer_store_dwordx4 v[140:143], v208, s[28:31], s72 offen sc1\n\t"
      "v_mfma_f32_16x16x32_bf16 v[128:131], a[8:11], v[80:83], v[16:19]\n\t"
      "ds_read_b128 v[44:47], v199 offset:0\n\t"
      "v_mfma_f32_16x16x32_bf16 v[128:131], a[32:35], v[84:87], v[128:131]\n\t"
      "s_memrealtime s[80:81]\n\t"
      "v_mfma_f32_16x16x32_bf16 v[128:131], a[56:59], v[88:91], v[128:131]\n\t"
      "v_cvt_pk_bf16_f32 v138, v132, v133\n\t"
      "v_mfma_f32_16x16x32_bf16 v[128:131], a[80:83], v[92:95], v[128:131]\n\t"
      "v_cvt_pk_bf16_f32 v139, v134, v135\n\t"
      "v_mfma_f32_16x16x32_bf16 v[128:131], a[104:107], v[96:99], v[128:131]\n\t"
      "ds_write_b128 v217, v[136:139] offset:12288\n\t"
      "v_mfma_f32_16x16x32_bf16 v[128:131], a[128:131], v[100:103], v[128:131]\n\t"
      "s_mov_b32 m0, s46\n\t"
      "v_mfma_f32_16x16x32_bf16 v[128:131], a[152:155], v[104:107], v[128:131]\n\t"
      "v_mfma_f32_16x16x32_bf16 v[128:131], a[176:179], v[108:111], v[128:131]\n\t"
      "ds_read_b128 v[48:51], v197 offset:256\n\t"
      "v_mfma_f32_16x16x32_bf16 v[128:131], a[200:203], v[112:115], v[128:131]\n\t"
      "v_mfma_f32_16x16x32_bf16 v[128:131], a[224:227], v[116:119], v[128:131]\n\t"
      "ds_read_b128 v[52:55], v198 offset:256\n\t"
      "v_mfma_f32_16x16x32_bf16 v[128:131], a[248:251], v[120:123], v[128:131]\n\t"
      "v_mfma_f32_16x16x32_bf16 v[128:131], v[180:183], v[124:127], v[128:131]\n\t"
      "buffer_load_dwordx4 v208, s[20:23], s73 offen lds\n\t"
      "v_mfma_f32_16x16x32_bf16 v[132:135], a[12:15], v[80:83], v[20:23]\n\t"
      "ds_read_b128 v[56:59], v199 offset:256\n\t"
      "v_mfma_f32_16x16x32_bf16 v[132:135], a[36:39], v[84:87], v[132:135]\n\t"
      "v_mfma_f32_16x16x32_bf16 v[132:135], a[60:63], v[88:91], v[132:135]\n\t"
      "v_cvt_pk_bf16_f32 v136, v128, v129\n\t"
      "v_mfma_f32_16x16x32_bf16 v[132:135], a[84:87], v[92:95], v[132:135]\n\t"
      "v_cvt_pk_bf16_f32 v137, v130, v131\n\t"
      "ds_read_b128 v[144:147], v220 offset:1024\n\t"
      "v_mfma_f32_16x16x32_bf16 v[132:135], a[108:111], v[96:99], v[132:135]\n\t"
      "v_mfma_f32_16x16x32_bf16 v[132:135], a[132:135], v[100:103], v[132:135]\n\t"
      "v_mfma_f32_16x16x32_bf16 v[132:135], a[156:159], v[104:107], v[132:135]\n\t"
      "ds_read_b128 v[60:63], v196 offset:256\n\t"
      "v_mfma_f32_16x16x32_bf16 v[132:135], a[180:183], v[108:111], v[132:135]\n\t"
      "v_mfma_f32_16x16x32_bf16 v[132:135], a[204:207], v[112:115], v[132:135]\n\t"
      "v_mfma_f32_16x16x32_bf16 v[132:135], a[228:231], v[116:119], v[132:135]\n\t"
      "ds_read_b128 v[64:67], v198 offset:512\n\t"
      "v_mfma_f32_16x16x32_bf16 v[132:135], a[252:255], v[120:123], v[132:135]\n\t"
      "v_mfma_f32_16x16x32_bf16 v[132:135], v[184:187], v[124:127], v[132:135]\n\t"
      "s_waitcnt lgkmcnt(2)\n\t"
      "buffer_store_dwordx4 v[144:147], v208, s[28:31], s72 offen offset:1024 sc1\n\t"
      "v_mfma_f32_16x16x32_bf16 v[128:131], a[16:19], v[80:83], v[24:27]\n\t"
      "ds_read_b128 v[68:71], v199 offset:512\n\t"
      "v_mfma_f32_16x16x32_bf16 v[128:131], a[40:43], v[84:87], v[128:131]\n\t"
      "v_readlane_b32 s86, v250, s44\n\t"
      "v_mfma_f32_16x16x32_bf16 v[128:131], a[64:67], v[88:91], v[128:131]\n\t"
      "v_cvt_pk_bf16_f32 v138, v132, v133\n\t"
      "v_mfma_f32_16x16x32_bf16 v[128:131], a[88:91], v[92:95], v[128:131]\n\t"
      "v_cvt_pk_bf16_f32 v139, v134, v135\n\t"
      "v_mfma_f32_16x16x32_bf16 v[128:131], a[112:115], v[96:99], v[128:131]\n\t"
      "ds_write_b128 v218, v[136:139] offset:12288\n\t"
      "v_mfma_f32_16x16x32_bf16 v[128:131], a[136:139], v[100:103], v[128:131]\n\t"
      "v_mfma_f32_16x16x32_bf16 v[128:131], a[160:163], v[104:107], v[128:131]\n\t"
      "v_mfma_f32_16x16x32_bf16 v[128:131], a[184:187], v[108:111], v[128:131]\n\t"
      "ds_read_b128 v[148:151], v220 offset:2048\n\t"
      "v_mfma_f32_16x16x32_bf16 v[128:131], a[208:211], v[112:115], v[128:131]\n\t"
      "v_mfma_f32_16x16x32_bf16 v[128:131], a[232:235], v[116:119], v[128:131]\n\t"
      "ds_read_b128 v[72:75], v196 offset:512\n\t"
      "v_mfma_f32_16x16x32_bf16 v[128:131], v[164:167], v[120:123], v[128:131]\n\t"
      "v_mfma_f32_16x16x32_bf16 v[128:131], v[188:191], v[124:127], v[128:131]\n\t"
      "buffer_load_dwordx4 v208, s[20:23], s73 offen offset:1024 lds\n\t"
      "v_mfma_f32_16x16x32_bf16 v[132:135], a[20:23], v[80:83], v[28:31]\n\t"
      "ds_read_b128 v[76:79], v197 offset:512\n\t"
      "v_mfma_f32_16x16x32_bf16 v[132:135], a[44:47], v[84:87], v[132:135]\n\t"
      "v_mfma_f32_16x16x32_bf16 v[132:135], a[68:71], v[88:91], v[132:135]\n\t"
      "v_cvt_pk_bf16_f32 v136, v128, v129\n\t"
      "v_mfma_f32_16x16x32_bf16 v[132:135], a[92:95], v[92:95], v[132:135]\n\t"
      "v_cvt_pk_bf16_f32 v137, v130, v131\n\t"
      "s_sub_u32 s84, s84, 4\n\t"
      "s_max_i32 s84, s84, s86\n\t"
      "v_mfma_f32_16x16x32_bf16 v[132:135], a[116:119], v[96:99], v[132:135]\n\t"
      "s_sub_u32 s83, s82, s80\n\t"
      "v_mfma_f32_16x16x32_bf16 v[132:135], a[140:143], v[100:103], v[132:135]\n\t"
      "s_max_i32 s83, s83, 0\n\t"
      "s_add_u32 s82, s80, s83\n\t"
      "v_mfma_f32_16x16x32_bf16 v[132:135], a[164:167], v[104:107], v[132:135]\n\t"
      "s_mov_b32 s45, s46\n\t"
      "v_mfma_f32_16x16x32_bf16 v[132:135], a[188:191], v[108:111], v[132:135]\n\t"
      "s_mov_b32 s46, s47\n\t"
      "v_mfma_f32_16x16x32_bf16 v[132:135], a[212:215], v[112:115], v[132:135]\n\t"
      "s_mov_b32 s47, s48\n\t"
      "v_mfma_f32_16x16x32_bf16 v[132:135], a[236:239], v[116:119], v[132:135]\n\t"
      "s_add_u32 s82, s82, s84\n\t"
      "v_mfma_f32_16x16x32_bf16 v[132:135], v[168:171], v[120:123], v[132:135]\n\t"
      "s_cmp_lg_u32 s44, s49\n\t"
      "v_mfma_f32_16x16x32_bf16 v[132:135], v[192:195], v[124:127], v[132:135]\n\t"
      "s_waitcnt lgkmcnt(2)\n\t"
      "buffer_store_dwordx4 v[148:151], v208, s[28:31], s72 offen offset:2048 sc1\n\t"
      "s_cbranch_scc1 .Lloop_%=\n\t"
      "s_cmp_lg_u32 s44, 17\n\t"
      "s_cbranch_scc1 .Lstop_%=\n\t"
      "v_readlane_b32 s72, v249, s44\n\t"
      "ds_read_b32 v241, v247\n\t"
      "s_nop 2\n\t"
      "v_cvt_pk_bf16_f32 v138, v132, v133\n\t"
      "v_cvt_pk_bf16_f32 v139, v134, v135\n\t"
      "ds_write_b128 v219, v[136:139] offset:12288\n\t"
      "s_waitcnt lgkmcnt(0)\n\t"
      "s_barrier\n\t"
      "ds_read_b128 v[32:35], v220 offset:12288\n\t"
      "ds_read_b128 v[36:39], v220 offset:13312\n\t"
      "ds_read_b128 v[40:43], v220 offset:14336\n\t"
      "v_cmp_ne_u32_e32 vcc, 0, v241\n\t"
      "s_cmp_lg_u32 s62, 0\n\t"
      "s_waitcnt lgkmcnt(2)\n\t"
      "buffer_store_dwordx4 v[32:35], v209, s[28:31], s72 offen sc1\n\t"
      "s_waitcnt lgkmcnt(1)\n\t"
      "buffer_store_dwordx4 v[36:39], v209, s[28:31], s72 offen offset:1024 sc1\n\t"
      "s_waitcnt lgkmcnt(0)\n\t"
      "buffer_store_dwordx4 v[40:43], v209, s[28:31], s72 offen offset:2048 sc1\n\t"
      "s_cbranch_scc1 .Lfin_%=\n\t"
      "s_cbranch_vccnz .Lsec_%=\n\t"
      ".Lfin_%=:\n\t"
      "s_endpgm\n\t"
      ".Lstop_%=:\n\t"
      "s_and_b32 s49, s71, 31\n\t"
      "s_lshr_b32 s71, s71, 5\n\t"
      "s_sub_u32 s78, s44, s89\n\t"
      "s_cmp_eq_u32 s78, 10\n\t"
      "s_cbranch_scc1 .Lstop2_%=\n\t"
      "s_cmp_eq_u32 s78, 5\n\t"
      "s_cbranch_scc1 .Lstop1_%=\n\t"
      "s_waitcnt vmcnt(12)\n\t"
      "v_readlane_b32 s76, v254, 1\n\t"
      "v_readlane_b32 s78, v255, 1\n\t"
      "s_andn2_b32 s76, 4, s76\n\t"
      "s_or_b32 s76, s76, s64\n\t"
      "s_xor_b32 s76, s76, s78\n\t"
      "v_mov_b32_e32 v240, s76\n\t"
      "ds_write_b32 v247, v240\n\t"
      "s_branch .Lloop_%=\n\t"
      ".Lstop1_%=:\n\t"
      "s_cmp_eq_u32 s94, 0\n\t"
      "s_cbranch_scc1 .Llt0_%=\n\t"
      "ds_read_b128 v[226:229], v238 offset:24576\n\t"
      "ds_read_b128 v[230:233], v238 offset:25600\n\t"
      "ds_read_b128 v[234:237], v238 offset:26624\n\t"
      "v_readlane_b32 s68, v248, 0\n\t"
      "s_nop 4\n\t"
      "s_waitcnt lgkmcnt(2)\n\t"
      "buffer_store_dwordx4 v[226:229], v208, s[28:31], s68 offen sc1\n\t"
      "s_waitcnt lgkmcnt(1)\n\t"
      "buffer_store_dwordx4 v[230:233], v208, s[28:31], s68 offen offset:1024 sc1\n\t"
      "s_waitcnt lgkmcnt(0)\n\t"
      "buffer_store_dwordx4 v[234:237], v208, s[28:31], s68 offen offset:2048 sc1\n\t"
      ".Llt0_%=:\n\t"
      "v_readlane_b32 s76, v254, 0\n\t"
      "s_cmp_lg_u32 s41, s43\n\t"
      "s_cbranch_scc1 .Lloop_%=\n\t"
      "s_andn2_b32 s76, 4, s76\n\t"
      "s_or_b32 s76, s76, s64\n\t"
      "v_mov_b32_e32 v240, s76\n\t"
      "s_mov_b64 exec, 1\n\t"
      "buffer_store_dword v240, v253, s[24:27], 0 offen sc1\n\t"
      "s_mov_b64 exec, -1\n\t"
      "s_branch .Lloop_%=\n\t"
      ".Lstop2_%=:\n\t"
      "s_cmp_eq_u32 s94, 0\n\t"
      "s_cbranch_scc1 .Llt1_%=\n\t"
      "ds_read_b128 v[226:229], v238 offset:36864\n\t"
      "ds_read_b128 v[230:233], v238 offset:37888\n\t"
      "ds_read_b128 v[234:237], v238 offset:38912\n\t"
      "v_readlane_b32 s68, v249, 0\n\t"
      "s_nop 4\n\t"
      "s_waitcnt lgkmcnt(2)\n\t"
      "buffer_store_dwordx4 v[226:229], v209, s[28:31], s68 offen sc1\n\t"
      "s_waitcnt lgkmcnt(1)\n\t"
      "buffer_store_dwordx4 v[230:233], v209, s[28:31], s68 offen offset:1024 sc1\n\t"
      "s_waitcnt lgkmcnt(0)\n\t"
      "buffer_store_dwordx4 v[234:237], v209, s[28:31], s68 offen offset:2048 sc1\n\t"
      ".Llt1_%=:\n\t"
      "s_cmp_lg_u32 s41, s43\n\t"
      "s_cbranch_scc1 .Lloop_%=\n\t"
      "buffer_load_dword v255, v253, s[24:27], 0 offen sc1\n\t"
      "s_branch .Lloop_%=\n\t"
      ".LslpP3_%=:\n\t"
      "s_sleep 1\n\t"
      "s_sub_u32 s83, s83, 3\n\t"
      "s_cmp_gt_i32 s83, 1\n\t"
      "s_cbranch_scc1 .LslpP3_%=\n\t"
      "s_branch .LretP3_%=\n\t"
      ".LslpP4_%=:\n\t"
      "s_sleep 1\n\t"
      "s_sub_u32 s83, s83, 3\n\t"
      "s_cmp_gt_i32 s83, 1\n\t"
      "s_cbranch_scc1 .LslpP4_%=\n\t"
      "s_branch .LretP4_%=\n\t"
      ".LslpP5_%=:\n\t"
      "s_sleep 1\n\t"
      "s_sub_u32 s83, s83, 3\n\t"
      "s_cmp_gt_i32 s83, 1\n\t"
      "s_cbranch_scc1 .LslpP5_%=\n\t"
      "s_branch .LretP5_%=\n\t"
      ".LslpA_%=:\n\t"
      "s_sleep 1\n\t"
      "s_sub_u32 s83, s83, 3\n\t"
      "s_cmp_gt_i32 s83, 1\n\t"
      "s_cbranch_scc1 .LslpA_%=\n\t"
      "s_branch .LretA_%=\n\t"
      ".LslpB_%=:\n\t"
      "s_sleep 1\n\t"
      "s_sub_u32 s83, s83, 3\n\t"
      "s_cmp_gt_i32 s83, 1\n\t"
      "s_cbranch_scc1 .LslpB_%=\n\t"
      "s_branch .LretB_%=\n\t"
      ".Lsec_%=:\n\t"
      "s_mov_b32 s62, 1\n\t"
      "v_lshl_add_u32 v240, v251, 1, s66\n\t"
      "v_sub_u32_e32 v248, v240, v248\n\t"
      "v_sub_u32_e32 v249, v240, v249\n\t"
      "v_cmp_eq_u32_e32 vcc, 0, v252\n\t"
      "v_mov_b32_e32 v240, 0x6c00000\n\t"
      "s_nop 1\n\t"
      "v_cndmask_b32_e32 v248, v248, v240, vcc\n\t"
      "v_cndmask_b32_e32 v249, v249, v240, vcc\n\t"
      "s_add_u32 s44, s94, 3\n\t"
      "s_add_u32 s85, s44, 1\n\t"
      "s_mov_b32 s83, 0\n\t"
      "s_waitcnt vmcnt(0) lgkmcnt(0)\n\t"
      "s_barrier\n\t"
      "v_readlane_b32 s73, v248, s85\n\t"
      "s_mov_b32 m0, s46\n\t"
      "s_nop 4\n\t"
      "buffer_load_dwordx4 v208, s[20:23], s73 offen lds\n\t"
      "buffer_load_dwordx4 v208, s[20:23], s73 offen offset:1024 lds\n\t"
      "buffer_load_dwordx4 v208, s[20:23], s73 offen offset:2048 lds\n\t"
      "v_readlane_b32 s73, v249, s85\n\t"
      "s_add_u32 m0, s46, 0x3000\n\t"
      "s_nop 4\n\t"
      "buffer_load_dwordx4 v209, s[20:23], s73 offen lds\n\t"
      "buffer_load_dwordx4 v209, s[20:23], s73 offen offset:1024 lds\n\t"
      "buffer_load_dwordx4 v209, s[20:23], s73 offen offset:2048 lds\n\t"
      "s_add_u32 s85, s85, 1\n\t"
      "s_nop 4\n\t"
      "v_readlane_b32 s73, v248, s85\n\t"
      "s_mov_b32 m0, s47\n\t"
      "s_nop 4\n\t"
      "buffer_load_dwordx4 v208, s[20:23], s73 offen lds\n\t"
      "buffer_load_dwordx4 v208, s[20:23], s73 offen offset:1024 lds\n\t"
      "buffer_load_dwordx4 v208, s[20:23], s73 offen offset:2048 lds\n\t"
      "v_readlane_b32 s73, v249, s85\n\t"
      "s_add_u32 m0, s47, 0x3000\n\t"
      "s_nop 4\n\t"
      "buffer_load_dwordx4 v209, s[20:23], s73 offen lds\n\t"
      "buffer_load_dwordx4 v209, s[20:23], s73 offen offset:1024 lds\n\t"
      "buffer_load_dwordx4 v209, s[20:23], s73 offen offset:2048 lds\n\t"
      "s_add_u32 s85, s85, 1\n\t"
      "s_nop 4\n\t"
      "v_readlane_b32 s73, v248, s85\n\t"
      "s_mov_b32 m0, s45\n\t"
      "s_nop 4\n\t"
      "buffer_load_dwordx4 v208, s[20:23], s73 offen lds\n\t"
      "buffer_load_dwordx4 v208, s[20:23], s73 offen offset:1024 lds\n\t"
      "buffer_load_dwordx4 v208, s[20:23], s73 offen offset:2048 lds\n\t"
      "s_waitcnt vmcnt(0)\n\t"
      "s_barrier\n\t"
      "ds_read_b128 v[32:35], v196 offset:0\n\t"
      "ds_read_b128 v[36:39], v197 offset:0\n\t"
      "ds_read_b128 v[40:43], v198 offset:0\n\t"
      "ds_read_b128 v[44:47], v199 offset:0\n\t"
      "ds_read_b128 v[48:51], v197 offset:256\n\t"
      "ds_read_b128 v[52:55], v198 offset:256\n\t"
      "ds_read_b128 v[56:59], v199 offset:256\n\t"
      "ds_read_b128 v[60:63], v196 offset:256\n\t"
      "ds_read_b128 v[64:67], v198 offset:512\n\t"
      "ds_read_b128 v[68:71], v199 offset:512\n\t"
      "ds_read_b128 v[72:75], v196 offset:512\n\t"
      "ds_read_b128 v[76:79], v197 offset:512\n\t"
      "s_waitcnt lgkmcnt(0)\n\t"
      "s_branch .Lloop_%=\n\t"
      ".Lgs_%=:\n\t"
      "s_sleep 8\n\t"
      "buffer_load_dwordx2 v[240:241], v245, s[24:27], 0 offen sc1\n\t"
      "s_memrealtime s[76:77]\n\t"
      "s_waitcnt vmcnt(0) lgkmcnt(0)\n\t"
      "v_subrev_u32_e32 v242, s63, v240\n\t"
      "v_xor_b32_e32 v241, s53, v241\n\t"
      "v_lshrrev_b32_e32 v240, 13, v242\n\t"
      "v_or_b32_e32 v240, v240, v241\n\t"
      "v_cmp_ne_u32_e32 vcc, 0, v240\n\t"
      "v_and_b32_e32 v242, 15, v242\n\t"
      "s_cbranch_vccz .Lgr_%=\n\t"
      "s_sub_u32 s76, s76, s52\n\t"
      "s_cmp_lt_u32 s76, 2000\n\t"
      "s_cbranch_scc1 .Lgs_%=\n\t"
      "v_mov_b32_e32 v242, 64\n\t"
      "s_branch .Lgr_%=\n\t"
      ".Lpb_%=:\n\t"
      "s_mov_b64 vcc, -1\n\t"
      "s_branch .Lp2_%=\n\t"
      ".Lwsb_%=:\n\t"
      "s_mov_b64 vcc, 0\n\t"
      "s_waitcnt vmcnt(12)\n\t"
      "v_lshlrev_b32_e32 v8, 16, v226\n\t"
      "v_and_b32_e32 v9, 0xffff0000, v226\n\t"
      "v_lshlrev_b32_e32 v10, 16, v227\n\t"
      "v_and_b32_e32 v11, 0xffff0000, v227\n\t"
      "v_lshlrev_b32_e32 v12, 16, v228\n\t"
      "v_and_b32_e32 v13, 0xffff0000, v228\n\t"
      "v_lshlrev_b32_e32 v14, 16, v229\n\t"
      "v_and_b32_e32 v15, 0xffff0000, v229\n\t"
      "v_lshlrev_b32_e32 v16, 16, v230\n\t"
      "v_and_b32_e32 v17, 0xffff0000, v230\n\t"
      "v_lshlrev_b32_e32 v18, 16, v231\n\t"
      "v_and_b32_e32 v19, 0xffff0000, v231\n\t"
      "v_lshlrev_b32_e32 v20, 16, v232\n\t"
      "v_and_b32_e32 v21, 0xffff0000, v232\n\t"
      "v_lshlrev_b32_e32 v22, 16, v233\n\t"
      "v_and_b32_e32 v23, 0xffff0000, v233\n\t"
      "v_lshlrev_b32_e32 v24, 16, v234\n\t"
      "v_and_b32_e32 v25, 0xffff0000, v234\n\t"
      "v_lshlrev_b32_e32 v26, 16, v235\n\t"
      "v_and_b32_e32 v27, 0xffff0000, v235\n\t"
      "v_lshlrev_b32_e32 v28, 16, v236\n\t"
      "v_and_b32_e32 v29, 0xffff0000, v236\n\t"
      "v_lshlrev_b32_e32 v30, 16, v237\n\t"
      "v_and_b32_e32 v31, 0xffff0000, v237\n\t"
      "s_add_u32 m0, s41, 0x1e000\n\t"
      "s_mov_b32 s51, 0x0\n\t"
      "buffer_load_dwordx4 v211, s[16:19], s51 offen lds\n\t"
      "buffer_load_dwordx4 v212, s[16:19], s51 offen offset:1024 lds\n\t"
      "buffer_load_dwordx4 v213, s[16:19], s51 offen offset:2048 lds\n\t"
      "s_add_u32 m0, s41, 0x21000\n\t"
      "s_mov_b32 s51, 0xc00\n\t"
      "buffer_load_dwordx4 v211, s[16:19], s51 offen lds\n\t"
      "buffer_load_dwordx4 v212, s[16:19], s51 offen offset:1024 lds\n\t"
      "buffer_load_dwordx4 v213, s[16:19], s51 offen offset:2048 lds\n\t"
      "s_add_u32 m0, s41, 0x24000\n\t"
      "s_mov_b32 s51, 0x6000\n\t"
      "buffer_load_dwordx4 v211, s[16:19], s51 offen lds\n\t"
      "buffer_load_dwordx4 v212, s[16:19], s51 offen offset:1024 lds\n\t"
      "buffer_load_dwordx4 v213, s[16:19], s51 offen offset:2048 lds\n\t"
      "s_add_u32 m0, s41, 0x12000\n\t"
      "s_mov_b32 s51, 0x6c00\n\t"
      "buffer_load_dwordx4 v211, s[16:19], s51 offen lds\n\t"
      "buffer_load_dwordx4 v212, s[16:19], s51 offen offset:1024 lds\n\t"
      "buffer_load_dwordx4 v213, s[16:19], s51 offen offset:2048 lds\n\t"
      "s_add_u32 m0, s41, 0x15000\n\t"
      "s_mov_b32 s51, 0xc000\n\t"
      "buffer_load_dwordx4 v211, s[16:19], s51 offen lds\n\t"
      "buffer_load_dwordx4 v212, s[16:19], s51 offen offset:1024 lds\n\t"
      "buffer_load_dwordx4 v213, s[16:19], s51 offen offset:2048 lds\n\t"
      "s_mov_b32 s44, 1\n\t"
      "s_mov_b32 s85, 4\n\t"
      "s_add_u32 s45, s41, 0x6000\n\t"
      "s_add_u32 s46, s41, 0xc000\n\t"
      "s_mov_b32 s47, s41\n\t"
      "s_waitcnt vmcnt(12)\n\t"
      "s_barrier\n\t"
      "ds_read_b64_tr_b16 a[0:1], v214 offset:49152\n\t"
      "ds_read_b64_tr_b16 a[4:5], v214 offset:49160\n\t"
      "ds_read_b64_tr_b16 a[8:9], v214 offset:49216\n\t"
      "ds_read_b64_tr_b16 a[12:13], v214 offset:49224\n\t"
      "ds_read_b64_tr_b16 a[16:17], v214 offset:49280\n\t"
      "ds_read_b64_tr_b16 a[20:21], v214 offset:49288\n\t"
      "ds_read_b128 v[156:159], v196 offset:0\n\t"
      "ds_read_b128 v[160:163], v196 offset:12288\n\t"
      "ds_read_b128 v[128:131], v196 offset:24576\n\t"
      "s_add_u32 m0, s41, 0x18000\n\t"
      "s_mov_b32 s51, 0xcc00\n\t"
      "buffer_load_dwordx4 v211, s[16:19], s51 offen lds\n\t"
      "buffer_load_dwordx4 v212, s[16:19], s51 offen offset:1024 lds\n\t"
      "buffer_load_dwordx4 v213, s[16:19], s51 offen offset:2048 lds\n\t"
      "s_waitcnt vmcnt(12)\n\t"
      "ds_read_b64_tr_b16 a[2:3], v221 offset:0\n\t"
      "ds_read_b64_tr_b16 a[6:7], v221 offset:8\n\t"
      "ds_read_b64_tr_b16 a[10:11], v221 offset:64\n\t"
      "ds_read_b64_tr_b16 a[14:15], v221 offset:72\n\t"
      "ds_read_b64_tr_b16 a[18:19], v221 offset:128\n\t"
      "ds_read_b64_tr_b16 a[22:23], v221 offset:136\n\t"
      "s_add_u32 m0, s41, 0x1b000\n\t"
      "s_mov_b32 s51, 0x12000\n\t"
      "buffer_load_dwordx4 v211, s[16:19], s51 offen lds\n\t"
      "buffer_load_dwordx4 v212, s[16:19], s51 offen offset:1024 lds\n\t"
      "buffer_load_dwordx4 v213, s[16:19], s51 offen offset:2048 lds\n\t"
      "s_waitcnt lgkmcnt(0)\n\t"
      "v_mfma_f32_16x16x32_bf16 v[32:35], a[0:3], v[156:159], v[8:11]\n\t"
      "v_mfma_f32_16x16x32_bf16 v[36:39], a[4:7], v[156:159], v[12:15]\n\t"
      "s_waitcnt vmcnt(12)\n\t"
      "ds_read_b64_tr_b16 a[24:25], v221 offset:12288\n\t"
      "v_mfma_f32_16x16x32_bf16 v[40:43], a[8:11], v[156:159], v[16:19]\n\t"
      "ds_read_b64_tr_b16 a[28:29], v221 offset:12296\n\t"
      "v_mfma_f32_16x16x32_bf16 v[44:47], a[12:15], v[156:159], v[20:23]\n\t"
      "ds_read_b64_tr_b16 a[32:33], v221 offset:12352\n\t"
      "v_mfma_f32_16x16x32_bf16 v[48:51], a[16:19], v[156:159], v[24:27]\n\t"
      "ds_read_b64_tr_b16 a[36:37], v221 offset:12360\n\t"
      "ds_read_b128 v[136:139], v196 offset:36864\n\t"
      "v_mfma_f32_16x16x32_bf16 v[52:55], a[20:23], v[156:159], v[28:31]\n\t"
      "ds_read_b64_tr_b16 a[40:41], v221 offset:12416\n\t"
      "v_mfma_f32_16x16x32_bf16 v[56:59], a[0:3], v[160:163], v[8:11]\n\t"
      "ds_read_b64_tr_b16 a[44:45], v221 offset:12424\n\t"
      "v_mfma_f32_16x16x32_bf16 v[60:63], a[4:7], v[160:163], v[12:15]\n\t"
      "v_mfma_f32_16x16x32_bf16 v[64:67], a[8:11], v[160:163], v[16:19]\n\t"
      "s_waitcnt vmcnt(9)\n\t"
      "ds_read_b64_tr_b16 a[26:27], v214 offset:0\n\t"
      "v_mfma_f32_16x16x32_bf16 v[68:71], a[12:15], v[160:163], v[20:23]\n\t"
      "ds_read_b64_tr_b16 a[30:31], v214 offset:8\n\t"
      "v_mfma_f32_16x16x32_bf16 v[72:75], a[16:19], v[160:163], v[24:27]\n\t"
      "ds_read_b64_tr_b16 a[34:35], v214 offset:64\n\t"
      "ds_read_b128 v[140:143], v197 offset:0\n\t"
      "v_mfma_f32_16x16x32_bf16 v[76:79], a[20:23], v[160:163], v[28:31]\n\t"
      "ds_read_b64_tr_b16 a[38:39], v214 offset:72\n\t"
      "v_mfma_f32_16x16x32_bf16 v[80:83], a[0:3], v[128:131], v[8:11]\n\t"
      "ds_read_b64_tr_b16 a[42:43], v214 offset:128\n\t"
      "s_add_u32 m0, s41, 0x1e000\n\t"
      "s_mov_b32 s51, 0x12c00\n\t"
      "buffer_load_dwordx4 v211, s[16:19], s51 offen lds\n\t"
      "v_mfma_f32_16x16x32_bf16 v[84:87], a[4:7], v[128:131], v[12:15]\n\t"
      "ds_read_b64_tr_b16 a[46:47], v214 offset:136\n\t"
      "v_mfma_f32_16x16x32_bf16 v[88:91], a[8:11], v[128:131], v[16:19]\n\t"
      "v_mfma_f32_16x16x32_bf16 v[92:95], a[12:15], v[128:131], v[20:23]\n\t"
      "buffer_load_dwordx4 v212, s[16:19], s51 offen offset:1024 lds\n\t"
      "v_mfma_f32_16x16x32_bf16 v[96:99], a[16:19], v[128:131], v[24:27]\n\t"
      "ds_read_b128 v[144:147], v197 offset:12288\n\t"
      "v_mfma_f32_16x16x32_bf16 v[100:103], a[20:23], v[128:131], v[28:31]\n\t"
      "s_waitcnt lgkmcnt(10)\n\t"
      "v_mfma_f32_16x16x32_bf16 v[104:107], a[0:3], v[136:139], v[8:11]\n\t"
      "buffer_load_dwordx4 v213, s[16:19], s51 offen offset:2048 lds\n\t"
      "v_mfma_f32_16x16x32_bf16 v[108:111], a[4:7], v[136:139], v[12:15]\n\t"
      "v_mfma_f32_16x16x32_bf16 v[112:115], a[8:11], v[136:139], v[16:19]\n\t"
      "s_waitcnt vmcnt(9)\n\t"
      "ds_read_b64_tr_b16 a[48:49], v214 offset:12288\n\t"
      "v_mfma_f32_16x16x32_bf16 v[116:119], a[12:15], v[136:139], v[20:23]\n\t"
      "ds_read_b64_tr_b16 a[52:53], v214 offset:12296\n\t"
      "s_add_u32 m0, s41, 0x21000\n\t"
      "s_mov_b32 s51, 0x18000\n\t"
      "buffer_load_dwordx4 v211, s[16:19], s51 offen lds\n\t"
      "v_mfma_f32_16x16x32_bf16 v[120:123], a[16:19], v[136:139], v[24:27]\n\t"
      "ds_read_b64_tr_b16 a[56:57], v214 offset:12352\n\t"
      "ds_read_b128 v[148:151], v197 offset:24576\n\t"
      "v_mfma_f32_16x16x32_bf16 v[132:135], a[20:23], v[136:139], v[28:31]\n\t"
      "ds_read_b64_tr_b16 a[60:61], v214 offset:12360\n\t"
      "s_waitcnt lgkmcnt(6)\n\t"
      "v_mfma_f32_16x16x32_bf16 v[32:35], a[24:27], v[140:143], v[32:35]\n\t"
      "ds_read_b64_tr_b16 a[64:65], v214 offset:12416\n\t"
      "buffer_load_dwordx4 v212, s[16:19], s51 offen offset:1024 lds\n\t"
      "v_mfma_f32_16x16x32_bf16 v[36:39], a[28:31], v[140:143], v[36:39]\n\t"
      "ds_read_b64_tr_b16 a[68:69], v214 offset:12424\n\t"
      "v_mfma_f32_16x16x32_bf16 v[40:43], a[32:35], v[140:143], v[40:43]\n\t"
      "v_mfma_f32_16x16x32_bf16 v[44:47], a[36:39], v[140:143], v[44:47]\n\t"
      "buffer_load_dwordx4 v213, s[16:19], s51 offen offset:2048 lds\n\t"
      "v_mfma_f32_16x16x32_bf16 v[48:51], a[40:43], v[140:143], v[48:51]\n\t"
      "ds_read_b128 v[152:155], v197 offset:36864\n\t"
      "v_mfma_f32_16x16x32_bf16 v[52:55], a[44:47], v[140:143], v[52:55]\n\t"
      "s_waitcnt lgkmcnt(8)\n\t"
      "v_mfma_f32_16x16x32_bf16 v[56:59], a[24:27], v[144:147], v[56:59]\n\t"
      "s_add_u32 m0, s41, 0x24000\n\t"
      "s_mov_b32 s51, 0x18c00\n\t"
      "buffer_load_dwordx4 v211, s[16:19], s51 offen lds\n\t"
      "v_mfma_f32_16x16x32_bf16 v[60:63], a[28:31], v[144:147], v[60:63]\n\t"
      "v_mfma_f32_16x16x32_bf16 v[64:67], a[32:35], v[144:147], v[64:67]\n\t"
      "s_waitcnt vmcnt(10)\n\t"
      "ds_read_b64_tr_b16 a[50:51], v214 offset:24576\n\t"
      "v_mfma_f32_16x16x32_bf16 v[68:71], a[36:39], v[144:147], v[68:71]\n\t"
      "ds_read_b64_tr_b16 a[54:55], v214 offset:24584\n\t"
      "buffer_load_dwordx4 v212, s[16:19], s51 offen offset:1024 lds\n\t"
      "v_mfma_f32_16x16x32_bf16 v[72:75], a[40:43], v[144:147], v[72:75]\n\t"
      "ds_read_b64_tr_b16 a[58:59], v214 offset:24640\n\t"
      "ds_read_b128 v[156:159], v198 offset:0\n\t"
      "v_mfma_f32_16x16x32_bf16 v[76:79], a[44:47], v[144:147], v[76:79]\n\t"
      "ds_read_b64_tr_b16 a[62:63], v214 offset:24648\n\t"
      "s_waitcnt lgkmcnt(9)\n\t"
      "v_mfma_f32_16x16x32_bf16 v[80:83], a[24:27], v[148:151], v[80:83]\n\t"
      "ds_read_b64_tr_b16 a[66:67], v214 offset:24704\n\t"
      "buffer_load_dwordx4 v213, s[16:19], s51 offen offset:2048 lds\n\t"
      "v_mfma_f32_16x16x32_bf16 v[84:87], a[28:31], v[148:151], v[84:87]\n\t"
      "ds_read_b64_tr_b16 a[70:71], v214 offset:24712\n\t"
      "v_mfma_f32_16x16x32_bf16 v[88:91], a[32:35], v[148:151], v[88:91]\n\t"
      "v_mfma_f32_16x16x32_bf16 v[92:95], a[36:39], v[148:151], v[92:95]\n\t"
      "s_add_u32 m0, s41, 0x12000\n\t"
      "s_mov_b32 s51, 0x1e000\n\t"
      "buffer_load_dwordx4 v211, s[16:19], s51 offen lds\n\t"
      "v_mfma_f32_16x16x32_bf16 v[96:99], a[40:43], v[148:151], v[96:99]\n\t"
      "ds_read_b128 v[160:163], v198 offset:12288\n\t"
      "v_mfma_f32_16x16x32_bf16 v[100:103], a[44:47], v[148:151], v[100:103]\n\t"
      "s_waitcnt lgkmcnt(8)\n\t"
      "v_mfma_f32_16x16x32_bf16 v[104:107], a[24:27], v[152:155], v[104:107]\n\t"
      "buffer_load_dwordx4 v212, s[16:19], s51 offen offset:1024 lds\n\t"
      "v_mfma_f32_16x16x32_bf16 v[108:111], a[28:31], v[152:155], v[108:111]\n\t"
      "v_mfma_f32_16x16x32_bf16 v[112:115], a[32:35], v[152:155], v[112:115]\n\t"
      "s_waitcnt vmcnt(11)\n\t"
      "ds_read_b64_tr_b16 a[72:73], v214 offset:36864\n\t"
      "v_mfma_f32_16x16x32_bf16 v[116:119], a[36:39], v[152:155], v[116:119]\n\t"
      "ds_read_b64_tr_b16 a[76:77], v214 offset:36872\n\t"
      "buffer_load_dwordx4 v213, s[16:19], s51 offen offset:2048 lds\n\t"
      "v_mfma_f32_16x16x32_bf16 v[120:123], a[40:43], v[152:155], v[120:123]\n\t"
      "ds_read_b64_tr_b16 a[80:81], v214 offset:36928\n\t"
      "ds_read_b128 v[128:131], v198 offset:24576\n\t"
      "v_mfma_f32_16x16x32_bf16 v[132:135], a[44:47], v[152:155], v[132:135]\n\t"
      "ds_read_b64_tr_b16 a[84:85], v214 offset:36936\n\t"
      "s_waitcnt lgkmcnt(6)\n\t"
      "v_mfma_f32_16x16x32_bf16 v[32:35], a[48:51], v[156:159], v[32:35]\n\t"
      "ds_read_b64_tr_b16 a[88:89], v214 offset:36992\n\t"
      "s_add_u32 m0, s41, 0x15000\n\t"
      "s_mov_b32 s51, 0x1ec00\n\t"
      "buffer_load_dwordx4 v211, s[16:19], s51 offen lds\n\t"
      "v_mfma_f32_16x16x32_bf16 v[36:39], a[52:55], v[156:159], v[36:39]\n\t"
      "ds_read_b64_tr_b16 a[92:93], v214 offset:37000\n\t"
      "v_mfma_f32_16x16x32_bf16 v[40:43], a[56:59], v[156:159], v[40:43]\n\t"
      "v_mfma_f32_16x16x32_bf16 v[44:47], a[60:63], v[156:159], v[44:47]\n\t"
      "buffer_load_dwordx4 v212, s[16:19], s51 offen offset:1024 lds\n\t"
      "v_mfma_f32_16x16x32_bf16 v[48:51], a[64:67], v[156:159], v[48:51]\n\t"
      "ds_read_b128 v[136:139], v198 offset:36864\n\t"
      "v_mfma_f32_16x16x32_bf16 v[52:55], a[68:71], v[156:159], v[52:55]\n\t"
      "s_waitcnt lgkmcnt(8)\n\t"
      "v_mfma_f32_16x16x32_bf16 v[56:59], a[48:51], v[160:163], v[56:59]\n\t"
      "buffer_load_dwordx4 v213, s[16:19], s51 offen offset:2048 lds\n\t"
      "v_mfma_f32_16x16x32_bf16 v[60:63], a[52:55], v[160:163], v[60:63]\n\t"
      "v_mfma_f32_16x16x32_bf16 v[64:67], a[56:59], v[160:163], v[64:67]\n\t"
      "s_waitcnt vmcnt(12)\n\t"
      "ds_read_b64_tr_b16 a[74:75], v214 offset:49152\n\t"
      "v_mfma_f32_16x16x32_bf16 v[68:71], a[60:63], v[160:163], v[68:71]\n\t"
      "ds_read_b64_tr_b16 a[78:79], v214 offset:49160\n\t"
      "s_add_u32 m0, s41, 0x18000\n\t"
      "s_mov_b32 s51, 0x24000\n\t"
      "buffer_load_dwordx4 v211, s[16:19], s51 offen lds\n\t"
      "v_mfma_f32_16x16x32_bf16 v[72:75], a[64:67], v[160:163], v[72:75]\n\t"
      "ds_read_b64_tr_b16 a[82:83], v214 offset:49216\n\t"
      "ds_read_b128 v[140:143], v199 offset:0\n\t"
      "v_mfma_f32_16x16x32_bf16 v[76:79], a[68:71], v[160:163], v[76:79]\n\t"
      "ds_read_b64_tr_b16 a[86:87], v214 offset:49224\n\t"
      "s_waitcnt lgkmcnt(9)\n\t"
      "v_mfma_f32_16x16x32_bf16 v[80:83], a[48:51], v[128:131], v[80:83]\n\t"
      "ds_read_b64_tr_b16 a[90:91], v214 offset:49280\n\t"
      "buffer_load_dwordx4 v212, s[16:19], s51 offen offset:1024 lds\n\t"
      "v_mfma_f32_16x16x32_bf16 v[84:87], a[52:55], v[128:131], v[84:87]\n\t"
      "ds_read_b64_tr_b16 a[94:95], v214 offset:49288\n\t"
      "v_mfma_f32_16x16x32_bf16 v[88:91], a[56:59], v[128:131], v[88:91]\n\t"
      "v_mfma_f32_16x16x32_bf16 v[92:95], a[60:63], v[128:131], v[92:95]\n\t"
      "buffer_load_dwordx4 v213, s[16:19], s51 offen offset:2048 lds\n\t"
      "v_mfma_f32_16x16x32_bf16 v[96:99], a[64:67], v[128:131], v[96:99]\n\t"
      "ds_read_b128 v[144:147], v199 offset:12288\n\t"
      "v_mfma_f32_16x16x32_bf16 v[100:103], a[68:71], v[128:131], v[100:103]\n\t"
      "s_waitcnt lgkmcnt(8)\n\t"
      "v_mfma_f32_16x16x32_bf16 v[104:107], a[48:51], v[136:139], v[104:107]\n\t"
      "s_add_u32 m0, s41, 0x1b000\n\t"
      "s_mov_b32 s51, 0x24c00\n\t"
      "buffer_load_dwordx4 v211, s[16:19], s51 offen lds\n\t"
      "v_mfma_f32_16x16x32_bf16 v[108:111], a[52:55], v[136:139], v[108:111]\n\t"
      "v_mfma_f32_16x16x32_bf16 v[112:115], a[56:59], v[136:139], v[112:115]\n\t"
      "s_waitcnt vmcnt(13)\n\t"
      "ds_read_b64_tr_b16 a[96:97], v221 offset:0\n\t"
      "v_mfma_f32_16x16x32_bf16 v[116:119], a[60:63], v[136:139], v[116:119]\n\t"
      "ds_read_b64_tr_b16 a[100:101], v221 offset:8\n\t"
      "buffer_load_dwordx4 v212, s[16:19], s51 offen offset:1024 lds\n\t"
      "v_mfma_f32_16x16x32_bf16 v[120:123], a[64:67], v[136:139], v[120:123]\n\t"
      "ds_read_b64_tr_b16 a[104:105], v221 offset:64\n\t"
      "ds_read_b128 v[148:151], v199 offset:24576\n\t"
      "v_mfma_f32_16x16x32_bf16 v[132:135], a[68:71], v[136:139], v[132:135]\n\t"
      "ds_read_b64_tr_b16 a[108:109], v221 offset:72\n\t"
      "s_waitcnt lgkmcnt(6)\n\t"
      "v_mfma_f32_16x16x32_bf16 v[32:35], a[72:75], v[140:143], v[32:35]\n\t"
      "ds_read_b64_tr_b16 a[112:113], v221 offset:128\n\t"
      "buffer_load_dwordx4 v213, s[16:19], s51 offen offset:2048 lds\n\t"
      "v_mfma_f32_16x16x32_bf16 v[36:39], a[76:79], v[140:143], v[36:39]\n\t"
      "ds_read_b64_tr_b16 a[116:117], v221 offset:136\n\t"
      "v_mfma_f32_16x16x32_bf16 v[40:43], a[80:83], v[140:143], v[40:43]\n\t"
      "v_mfma_f32_16x16x32_bf16 v[44:47], a[84:87], v[140:143], v[44:47]\n\t"
      "s_add_u32 m0, s41, 0x1e000\n\t"
      "s_mov_b32 s51, 0x2a000\n\t"
      "buffer_load_dwordx4 v211, s[16:19], s51 offen lds\n\t"
      "v_mfma_f32_16x16x32_bf16 v[48:51], a[88:91], v[140:143], v[48:51]\n\t"
      "ds_read_b128 v[152:155], v199 offset:36864\n\t"
      "v_mfma_f32_16x16x32_bf16 v[52:55], a[92:95], v[140:143], v[52:55]\n\t"
      "s_waitcnt lgkmcnt(8)\n\t"
      "v_mfma_f32_16x16x32_bf16 v[56:59], a[72:75], v[144:147], v[56:59]\n\t"
      "buffer_load_dwordx4 v212, s[16:19], s51 offen offset:1024 lds\n\t"
      "v_mfma_f32_16x16x32_bf16 v[60:63], a[76:79], v[144:147], v[60:63]\n\t"
      "v_mfma_f32_16x16x32_bf16 v[64:67], a[80:83], v[144:147], v[64:67]\n\t"
      "s_waitcnt vmcnt(14)\n\t"
      "ds_read_b64_tr_b16 a[98:99], v221 offset:12288\n\t"
      "v_mfma_f32_16x16x32_bf16 v[68:71], a[84:87], v[144:147], v[68:71]\n\t"
      "ds_read_b64_tr_b16 a[102:103], v221 offset:12296\n\t"
      "buffer_load_dwordx4 v213, s[16:19], s51 offen offset:2048 lds\n\t"
      "v_mfma_f32_16x16x32_bf16 v[72:75], a[88:91], v[144:147], v[72:75]\n\t"
      "ds_read_b64_tr_b16 a[106:107], v221 offset:12352\n\t"
      "ds_read_b128 v[156:159], v197 offset:256\n\t"
      "v_mfma_f32_16x16x32_bf16 v[76:79], a[92:95], v[144:147], v[76:79]\n\t"
      "ds_read_b64_tr_b16 a[110:111], v221 offset:12360\n\t"
      "s_waitcnt lgkmcnt(9)\n\t"
      "v_mfma_f32_16x16x32_bf16 v[80:83], a[72:75], v[148:151], v[80:83]\n\t"
      "ds_read_b64_tr_b16 a[114:115], v221 offset:12416\n\t"
      "s_waitcnt lgkmcnt(7)\n\t"
      "s_add_u32 m0, s41, 0x21000\n\t"
      "s_mov_b32 s51, 0x2ac00\n\t"
      "buffer_load_dwordx4 v211, s[16:19], s51 offen lds\n\t"
      "v_mfma_f32_16x16x32_bf16 v[84:87], a[76:79], v[148:151], v[84:87]\n\t"
      "ds_read_b64_tr_b16 a[118:119], v221 offset:12424\n\t"
      "v_mfma_f32_16x16x32_bf16 v[88:91], a[80:83], v[148:151], v[88:91]\n\t"
      "v_mfma_f32_16x16x32_bf16 v[92:95], a[84:87], v[148:151], v[92:95]\n\t"
      "buffer_load_dwordx4 v212, s[16:19], s51 offen offset:1024 lds\n\t"
      "v_mfma_f32_16x16x32_bf16 v[96:99], a[88:91], v[148:151], v[96:99]\n\t"
      "ds_read_b128 v[160:163], v197 offset:12544\n\t"
      "v_mfma_f32_16x16x32_bf16 v[100:103], a[92:95], v[148:151], v[100:103]\n\t"
      "s_waitcnt lgkmcnt(8)\n\t"
      "v_mfma_f32_16x16x32_bf16 v[104:107], a[72:75], v[152:155], v[104:107]\n\t"
      "buffer_load_dwordx4 v213, s[16:19], s51 offen offset:2048 lds\n\t"
      "v_mfma_f32_16x16x32_bf16 v[108:111], a[76:79], v[152:155], v[108:111]\n\t"
      "v_mfma_f32_16x16x32_bf16 v[112:115], a[80:83], v[152:155], v[112:115]\n\t"
      "s_waitcnt vmcnt(15)\n\t"
      "ds_read_b64_tr_b16 a[120:121], v214 offset:0\n\t"
      "v_mfma_f32_16x16x32_bf16 v[116:119], a[84:87], v[152:155], v[116:119]\n\t"
      "ds_read_b64_tr_b16 a[124:125], v214 offset:8\n\t"
      "s_waitcnt lgkmcnt(3)\n\t"
      "s_add_u32 m0, s41, 0x24000\n\t"
      "s_mov_b32 s51, 0x30000\n\t"
      "buffer_load_dwordx4 v211, s[16:19], s51 offen lds\n\t"
      "v_mfma_f32_16x16x32_bf16 v[120:123], a[88:91], v[152:155], v[120:123]\n\t"
      "ds_read_b64_tr_b16 a[128:129], v214 offset:64\n\t"
      "ds_read_b128 v[128:131], v197 offset:24832\n\t"
      "v_mfma_f32_16x16x32_bf16 v[132:135], a[92:95], v[152:155], v[132:135]\n\t"
      "ds_read_b64_tr_b16 a[132:133], v214 offset:72\n\t"
      "v_mfma_f32_16x16x32_bf16 v[32:35], a[96:99], v[156:159], v[32:35]\n\t"
      "ds_read_b64_tr_b16 a[136:137], v214 offset:128\n\t"
      "buffer_load_dwordx4 v212, s[16:19], s51 offen offset:1024 lds\n\t"
      "v_mfma_f32_16x16x32_bf16 v[36:39], a[100:103], v[156:159], v[36:39]\n\t"
      "ds_read_b64_tr_b16 a[140:141], v214 offset:136\n\t"
      "v_mfma_f32_16x16x32_bf16 v[40:43], a[104:107], v[156:159], v[40:43]\n\t"
      "v_mfma_f32_16x16x32_bf16 v[44:47], a[108:111], v[156:159], v[44:47]\n\t"
      "buffer_load_dwordx4 v213, s[16:19], s51 offen offset:2048 lds\n\t"
      "v_mfma_f32_16x16x32_bf16 v[48:51], a[112:115], v[156:159], v[48:51]\n\t"
      "ds_read_b128 v[136:139], v197 offset:37120\n\t"
      "v_mfma_f32_16x16x32_bf16 v[52:55], a[116:119], v[156:159], v[52:55]\n\t"
      "s_waitcnt lgkmcnt(8)\n\t"
      "v_mfma_f32_16x16x32_bf16 v[56:59], a[96:99], v[160:163], v[56:59]\n\t"
      "v_mfma_f32_16x16x32_bf16 v[60:63], a[100:103], v[160:163], v[60:63]\n\t"
      "v_mfma_f32_16x16x32_bf16 v[64:67], a[104:107], v[160:163], v[64:67]\n\t"
      "s_waitcnt vmcnt(15)\n\t"
      "ds_read_b64_tr_b16 a[122:123], v214 offset:12288\n\t"
      "s_waitcnt lgkmcnt(2)\n\t"
      "s_add_u32 m0, s41, 0x12000\n\t"
      "s_mov_b32 s51, 0x30c00\n\t"
      "buffer_load_dwordx4 v211, s[16:19], s51 offen lds\n\t"
      "v_mfma_f32_16x16x32_bf16 v[68:71], a[108:111], v[160:163], v[68:71]\n\t"
      "ds_read_b64_tr_b16 a[126:127], v214 offset:12296\n\t"
      "v_mfma_f32_16x16x32_bf16 v[72:75], a[112:115], v[160:163], v[72:75]\n\t"
      "ds_read_b64_tr_b16 a[130:131], v214 offset:12352\n\t"
      "ds_read_b128 v[140:143], v198 offset:256\n\t"
      "v_mfma_f32_16x16x32_bf16 v[76:79], a[116:119], v[160:163], v[76:79]\n\t"
      "ds_read_b64_tr_b16 a[134:135], v214 offset:12360\n\t"
      "buffer_load_dwordx4 v212, s[16:19], s51 offen offset:1024 lds\n\t"
      "v_mfma_f32_16x16x32_bf16 v[80:83], a[96:99], v[128:131], v[80:83]\n\t"
      "ds_read_b64_tr_b16 a[138:139], v214 offset:12416\n\t"
      "v_mfma_f32_16x16x32_bf16 v[84:87], a[100:103], v[128:131], v[84:87]\n\t"
      "ds_read_b64_tr_b16 a[142:143], v214 offset:12424\n\t"
      "v_mfma_f32_16x16x32_bf16 v[88:91], a[104:107], v[128:131], v[88:91]\n\t"
      "buffer_load_dwordx4 v213, s[16:19], s51 offen offset:2048 lds\n\t"
      "v_mfma_f32_16x16x32_bf16 v[92:95], a[108:111], v[128:131], v[92:95]\n\t"
      "v_mfma_f32_16x16x32_bf16 v[96:99], a[112:115], v[128:131], v[96:99]\n\t"
      "ds_read_b128 v[144:147], v198 offset:12544\n\t"
      "v_mfma_f32_16x16x32_bf16 v[100:103], a[116:119], v[128:131], v[100:103]\n\t"
      "s_waitcnt lgkmcnt(8)\n\t"
      "v_mfma_f32_16x16x32_bf16 v[104:107], a[96:99], v[136:139], v[104:107]\n\t"
      "v_mfma_f32_16x16x32_bf16 v[108:111], a[100:103], v[136:139], v[108:111]\n\t"
      "v_mfma_f32_16x16x32_bf16 v[112:115], a[104:107], v[136:139], v[112:115]\n\t"
      "s_waitcnt vmcnt(15)\n\t"
      "ds_read_b64_tr_b16 a[144:145], v214 offset:24576\n\t"
      "s_waitcnt lgkmcnt(2)\n\t"
      "s_add_u32 m0, s41, 0x15000\n\t"
      "s_mov_b32 s51, 0x36000\n\t"
      "buffer_load_dwordx4 v211, s[16:19], s51 offen lds\n\t"
      "v_mfma_f32_16x16x32_bf16 v[116:119], a[108:111], v[136:139], v[116:119]\n\t"
      "ds_read_b64_tr_b16 a[148:149], v214 offset:24584\n\t"
      "v_mfma_f32_16x16x32_bf16 v[120:123], a[112:115], v[136:139], v[120:123]\n\t"
      "ds_read_b64_tr_b16 a[152:153], v214 offset:24640\n\t"
      "ds_read_b128 v[148:151], v198 offset:24832\n\t"
      "v_mfma_f32_16x16x32_bf16 v[132:135], a[116:119], v[136:139], v[132:135]\n\t"
      "ds_read_b64_tr_b16 a[156:157], v214 offset:24648\n\t"
      "buffer_load_dwordx4 v212, s[16:19], s51 offen offset:1024 lds\n\t"
      "v_mfma_f32_16x16x32_bf16 v[32:35], a[120:123], v[140:143], v[32:35]\n\t"
      "ds_read_b64_tr_b16 a[160:161], v214 offset:24704\n\t"
      "v_mfma_f32_16x16x32_bf16 v[36:39], a[124:127], v[140:143], v[36:39]\n\t"
      "ds_read_b64_tr_b16 a[164:165], v214 offset:24712\n\t"
      "v_mfma_f32_16x16x32_bf16 v[40:43], a[128:131], v[140:143], v[40:43]\n\t"
      "buffer_load_dwordx4 v213, s[16:19], s51 offen offset:2048 lds\n\t"
      "v_mfma_f32_16x16x32_bf16 v[44:47], a[132:135], v[140:143], v[44:47]\n\t"
      "v_mfma_f32_16x16x32_bf16 v[48:51], a[136:139], v[140:143], v[48:51]\n\t"
      "ds_read_b128 v[152:155], v198 offset:37120\n\t"
      "v_mfma_f32_16x16x32_bf16 v[52:55], a[140:143], v[140:143], v[52:55]\n\t"
      "s_waitcnt lgkmcnt(8)\n\t"
      "v_mfma_f32_16x16x32_bf16 v[56:59], a[120:123], v[144:147], v[56:59]\n\t"
      "v_mfma_f32_16x16x32_bf16 v[60:63], a[124:127], v[144:147], v[60:63]\n\t"
      "v_mfma_f32_16x16x32_bf16 v[64:67], a[128:131], v[144:147], v[64:67]\n\t"
      "s_waitcnt vmcnt(15)\n\t"
      "ds_read_b64_tr_b16 a[146:147], v214 offset:36864\n\t"
      "s_waitcnt lgkmcnt(2)\n\t"
      "s_add_u32 m0, s41, 0x18000\n\t"
      "s_mov_b32 s51, 0x36c00\n\t"
      "buffer_load_dwordx4 v211, s[16:19], s51 offen lds\n\t"
      "v_mfma_f32_16x16x32_bf16 v[68:71], a[132:135], v[144:147], v[68:71]\n\t"
      "ds_read_b64_tr_b16 a[150:151], v214 offset:36872\n\t"
      "v_mfma_f32_16x16x32_bf16 v[72:75], a[136:139], v[144:147], v[72:75]\n\t"
      "ds_read_b64_tr_b16 a[154:155], v214 offset:36928\n\t"
      "ds_read_b128 v[156:159], v199 offset:256\n\t"
      "v_mfma_f32_16x16x32_bf16 v[76:79], a[140:143], v[144:147], v[76:79]\n\t"
      "ds_read_b64_tr_b16 a[158:159], v214 offset:36936\n\t"
      "buffer_load_dwordx4 v212, s[16:19], s51 offen offset:1024 lds\n\t"
      "v_mfma_f32_16x16x32_bf16 v[80:83], a[120:123], v[148:151], v[80:83]\n\t"
      "ds_read_b64_tr_b16 a[162:163], v214 offset:36992\n\t"
      "v_mfma_f32_16x16x32_bf16 v[84:87], a[124:127], v[148:151], v[84:87]\n\t"
      "ds_read_b64_tr_b16 a[166:167], v214 offset:37000\n\t"
      "v_mfma_f32_16x16x32_bf16 v[88:91], a[128:131], v[148:151], v[88:91]\n\t"
      "buffer_load_dwordx4 v213, s[16:19], s51 offen offset:2048 lds\n\t"
      "v_mfma_f32_16x16x32_bf16 v[92:95], a[132:135], v[148:151], v[92:95]\n\t"
      "v_mfma_f32_16x16x32_bf16 v[96:99], a[136:139], v[148:151], v[96:99]\n\t"
      "ds_read_b128 v[160:163], v199 offset:12544\n\t"
      "v_mfma_f32_16x16x32_bf16 v[100:103], a[140:143], v[148:151], v[100:103]\n\t"
      "s_waitcnt lgkmcnt(8)\n\t"
      "v_mfma_f32_16x16x32_bf16 v[104:107], a[120:123], v[152:155], v[104:107]\n\t"
      "v_mfma_f32_16x16x32_bf16 v[108:111], a[124:127], v[152:155], v[108:111]\n\t"
      "v_mfma_f32_16x16x32_bf16 v[112:115], a[128:131], v[152:155], v[112:115]\n\t"
      "s_waitcnt vmcnt(15)\n\t"
      "ds_read_b64_tr_b16 a[168:169], v214 offset:49152\n\t"
      "s_waitcnt lgkmcnt(2)\n\t"
      "s_add_u32 m0, s41, 0x1b000\n\t"
      "s_mov_b32 s51, 0x3c000\n\t"
      "buffer_load_dwordx4 v211, s[16:19], s51 offen lds\n\t"
      "v_mfma_f32_16x16x32_bf16 v[116:119], a[132:135], v[152:155], v[116:119]\n\t"
      "ds_read_b64_tr_b16 a[172:173], v214 offset:49160\n\t"
      "v_mfma_f32_16x16x32_bf16 v[120:123], a[136:139], v[152:155], v[120:123]\n\t"
      "ds_read_b64_tr_b16 a[176:177], v214 offset:49216\n\t"
      "ds_read_b128 v[128:131], v199 offset:24832\n\t"
      "v_mfma_f32_16x16x32_bf16 v[132:135], a[140:143], v[152:155], v[132:135]\n\t"
      "ds_read_b64_tr_b16 a[180:181], v214 offset:49224\n\t"
      "buffer_load_dwordx4 v212, s[16:19], s51 offen offset:1024 lds\n\t"
      "v_mfma_f32_16x16x32_bf16 v[32:35], a[144:147], v[156:159], v[32:35]\n\t"
      "ds_read_b64_tr_b16 a[184:185], v214 offset:49280\n\t"
      "v_mfma_f32_16x16x32_bf16 v[36:39], a[148:151], v[156:159], v[36:39]\n\t"
      "ds_read_b64_tr_b16 a[188:189], v214 offset:49288\n\t"
      "v_mfma_f32_16x16x32_bf16 v[40:43], a[152:155], v[156:159], v[40:43]\n\t"
      "buffer_load_dwordx4 v213, s[16:19], s51 offen offset:2048 lds\n\t"
      "v_mfma_f32_16x16x32_bf16 v[44:47], a[156:159], v[156:159], v[44:47]\n\t"
      "v_mfma_f32_16x16x32_bf16 v[48:51], a[160:163], v[156:159], v[48:51]\n\t"
      "ds_read_b128 v[136:139], v199 offset:37120\n\t"
      "v_mfma_f32_16x16x32_bf16 v[52:55], a[164:167], v[156:159], v[52:55]\n\t"
      "s_waitcnt lgkmcnt(8)\n\t"
      "v_mfma_f32_16x16x32_bf16 v[56:59], a[144:147], v[160:163], v[56:59]\n\t"
      "v_mfma_f32_16x16x32_bf16 v[60:63], a[148:151], v[160:163], v[60:63]\n\t"
      "v_mfma_f32_16x16x32_bf16 v[64:67], a[152:155], v[160:163], v[64:67]\n\t"
      "s_waitcnt vmcnt(15)\n\t"
      "ds_read_b64_tr_b16 a[170:171], v221 offset:0\n\t"
      "s_waitcnt lgkmcnt(2)\n\t"
      "s_add_u32 m0, s41, 0x1e000\n\t"
      "s_mov_b32 s51, 0x3cc00\n\t"
      "buffer_load_dwordx4 v211, s[16:19], s51 offen lds\n\t"
      "v_mfma_f32_16x16x32_bf16 v[68:71], a[156:159], v[160:163], v[68:71]\n\t"
      "ds_read_b64_tr_b16 a[174:175], v221 offset:8\n\t"
      "v_mfma_f32_16x16x32_bf16 v[72:75], a[160:163], v[160:163], v[72:75]\n\t"
      "ds_read_b64_tr_b16 a[178:179], v221 offset:64\n\t"
      "ds_read_b128 v[140:143], v196 offset:256\n\t"
      "v_mfma_f32_16x16x32_bf16 v[76:79], a[164:167], v[160:163], v[76:79]\n\t"
      "ds_read_b64_tr_b16 a[182:183], v221 offset:72\n\t"
      "buffer_load_dwordx4 v212, s[16:19], s51 offen offset:1024 lds\n\t"
      "v_mfma_f32_16x16x32_bf16 v[80:83], a[144:147], v[128:131], v[80:83]\n\t"
      "ds_read_b64_tr_b16 a[186:187], v221 offset:128\n\t"
      "v_mfma_f32_16x16x32_bf16 v[84:87], a[148:151], v[128:131], v[84:87]\n\t"
      "ds_read_b64_tr_b16 a[190:191], v221 offset:136\n\t"
      "v_mfma_f32_16x16x32_bf16 v[88:91], a[152:155], v[128:131], v[88:91]\n\t"
      "buffer_load_dwordx4 v213, s[16:19], s51 offen offset:2048 lds\n\t"
      "v_mfma_f32_16x16x32_bf16 v[92:95], a[156:159], v[128:131], v[92:95]\n\t"
      "v_mfma_f32_16x16x32_bf16 v[96:99], a[160:163], v[128:131], v[96:99]\n\t"
      "ds_read_b128 v[144:147], v196 offset:12544\n\t"
      "v_mfma_f32_16x16x32_bf16 v[100:103], a[164:167], v[128:131], v[100:103]\n\t"
      "s_waitcnt lgkmcnt(8)\n\t"
      "v_mfma_f32_16x16x32_bf16 v[104:107], a[144:147], v[136:139], v[104:107]\n\t"
      "v_mfma_f32_16x16x32_bf16 v[108:111], a[148:151], v[136:139], v[108:111]\n\t"
      "v_mfma_f32_16x16x32_bf16 v[112:115], a[152:155], v[136:139], v[112:115]\n\t"
      "s_waitcnt vmcnt(15)\n\t"
      "ds_read_b64_tr_b16 a[192:193], v221 offset:12288\n\t"
      "s_waitcnt lgkmcnt(2)\n\t"
      "s_add_u32 m0, s41, 0x21000\n\t"
      "s_mov_b32 s51, 0x42000\n\t"
      "buffer_load_dwordx4 v211, s[16:19], s51 offen lds\n\t"
      "v_mfma_f32_16x16x32_bf16 v[116:119], a[156:159], v[136:139], v[116:119]\n\t"
      "ds_read_b64_tr_b16 a[196:197], v221 offset:12296\n\t"
      "v_mfma_f32_16x16x32_bf16 v[120:123], a[160:163], v[136:139], v[120:123]\n\t"
      "ds_read_b64_tr_b16 a[200:201], v221 offset:12352\n\t"
      "ds_read_b128 v[148:151], v196 offset:24832\n\t"
      "v_mfma_f32_16x16x32_bf16 v[132:135], a[164:167], v[136:139], v[132:135]\n\t"
      "ds_read_b64_tr_b16 a[204:205], v221 offset:12360\n\t"
      "buffer_load_dwordx4 v212, s[16:19], s51 offen offset:1024 lds\n\t"
      "v_mfma_f32_16x16x32_bf16 v[32:35], a[168:171], v[140:143], v[32:35]\n\t"
      "ds_read_b64_tr_b16 a[208:209], v221 offset:12416\n\t"
      "v_mfma_f32_16x16x32_bf16 v[36:39], a[172:175], v[140:143], v[36:39]\n\t"
      "ds_read_b64_tr_b16 a[212:213], v221 offset:12424\n\t"
      "v_mfma_f32_16x16x32_bf16 v[40:43], a[176:179], v[140:143], v[40:43]\n\t"
      "buffer_load_dwordx4 v213, s[16:19], s51 offen offset:2048 lds\n\t"
      "v_mfma_f32_16x16x32_bf16 v[44:47], a[180:183], v[140:143], v[44:47]\n\t"
      "v_mfma_f32_16x16x32_bf16 v[48:51], a[184:187], v[140:143], v[48:51]\n\t"
      "ds_read_b128 v[152:155], v196 offset:37120\n\t"
      "v_mfma_f32_16x16x32_bf16 v[52:55], a[188:191], v[140:143], v[52:55]\n\t"
      "s_waitcnt lgkmcnt(8)\n\t"
      "v_mfma_f32_16x16x32_bf16 v[56:59], a[168:171], v[144:147], v[56:59]\n\t"
      "v_mfma_f32_16x16x32_bf16 v[60:63], a[172:175], v[144:147], v[60:63]\n\t"
      "v_mfma_f32_16x16x32_bf16 v[64:67], a[176:179], v[144:147], v[64:67]\n\t"
      "s_waitcnt vmcnt(15)\n\t"
      "ds_read_b64_tr_b16 a[194:195], v214 offset:0\n\t"
      "s_waitcnt lgkmcnt(2)\n\t"
      "s_add_u32 m0, s41, 0x24000\n\t"
      "s_mov_b32 s51, 0x42c00\n\t"
      "buffer_load_dwordx4 v211, s[16:19], s51 offen lds\n\t"
      "v_mfma_f32_16x16x32_bf16 v[68:71], a[180:183], v[144:147], v[68:71]\n\t"
      "ds_read_b64_tr_b16 a[198:199], v214 offset:8\n\t"
      "v_mfma_f32_16x16x32_bf16 v[72:75], a[184:187], v[144:147], v[72:75]\n\t"
      "ds_read_b64_tr_b16 a[202:203], v214 offset:64\n\t"
      "ds_read_b128 v[156:159], v198 offset:512\n\t"
      "v_mfma_f32_16x16x32_bf16 v[76:79], a[188:191], v[144:147], v[76:79]\n\t"
      "ds_read_b64_tr_b16 a[206:207], v214 offset:72\n\t"
      "buffer_load_dwordx4 v212, s[16:19], s51 offen offset:1024 lds\n\t"
      "v_mfma_f32_16x16x32_bf16 v[80:83], a[168:171], v[148:151], v[80:83]\n\t"
      "ds_read_b64_tr_b16 a[210:211], v214 offset:128\n\t"
      "v_mfma_f32_16x16x32_bf16 v[84:87], a[172:175], v[148:151], v[84:87]\n\t"
      "ds_read_b64_tr_b16 a[214:215], v214 offset:136\n\t"
      "v_mfma_f32_16x16x32_bf16 v[88:91], a[176:179], v[148:151], v[88:91]\n\t"
      "buffer_load_dwordx4 v213, s[16:19], s51 offen offset:2048 lds\n\t"
      "v_mfma_f32_16x16x32_bf16 v[92:95], a[180:183], v[148:151], v[92:95]\n\t"
      "v_mfma_f32_16x16x32_bf16 v[96:99], a[184:187], v[148:151], v[96:99]\n\t"
      "ds_read_b128 v[160:163], v198 offset:12800\n\t"
      "v_mfma_f32_16x16x32_bf16 v[100:103], a[188:191], v[148:151], v[100:103]\n\t"
      "s_waitcnt lgkmcnt(8)\n\t"
      "v_mfma_f32_16x16x32_bf16 v[104:107], a[168:171], v[152:155], v[104:107]\n\t"
      "s_add_u32 m0, s41, 0xc000\n\t"
      "s_add_u32 s51, s40, 0xc00000\n\t"
      "buffer_load_dwordx4 v208, s[20:23], s51 offen lds\n\t"
      "v_mfma_f32_16x16x32_bf16 v[108:111], a[172:175], v[152:155], v[108:111]\n\t"
      "v_mfma_f32_16x16x32_bf16 v[112:115], a[176:179], v[152:155], v[112:115]\n\t"
      "s_waitcnt vmcnt(16)\n\t"
      "ds_read_b64_tr_b16 a[216:217], v214 offset:12288\n\t"
      "buffer_load_dwordx4 v208, s[20:23], s51 offen offset:1024 lds\n\t"
      "v_mfma_f32_16x16x32_bf16 v[116:119], a[180:183], v[152:155], v[116:119]\n\t"
      "ds_read_b64_tr_b16 a[220:221], v214 offset:12296\n\t"
      "v_mfma_f32_16x16x32_bf16 v[120:123], a[184:187], v[152:155], v[120:123]\n\t"
      "ds_read_b64_tr_b16 a[224:225], v214 offset:12352\n\t"
      "ds_read_b128 v[128:131], v198 offset:25088\n\t"
      "buffer_load_dwordx4 v208, s[20:23], s51 offen offset:2048 lds\n\t"
      "v_mfma_f32_16x16x32_bf16 v[132:135], a[188:191], v[152:155], v[132:135]\n\t"
      "ds_read_b64_tr_b16 a[228:229], v214 offset:12360\n\t"
      "s_waitcnt lgkmcnt(6)\n\t"
      "v_mfma_f32_16x16x32_bf16 v[32:35], a[192:195], v[156:159], v[32:35]\n\t"
      "ds_read_b64_tr_b16 a[232:233], v214 offset:12416\n\t"
      "s_add_u32 m0, s41, 0xf000\n\t"
      "s_add_u32 s51, s40, 0xc00000\n\t"
      "buffer_load_dwordx4 v209, s[20:23], s51 offen lds\n\t"
      "v_mfma_f32_16x16x32_bf16 v[36:39], a[196:199], v[156:159], v[36:39]\n\t"
      "ds_read_b64_tr_b16 a[236:237], v214 offset:12424\n\t"
      "v_mfma_f32_16x16x32_bf16 v[40:43], a[200:203], v[156:159], v[40:43]\n\t"
      "buffer_load_dwordx4 v209, s[20:23], s51 offen offset:1024 lds\n\t"
      "v_mfma_f32_16x16x32_bf16 v[44:47], a[204:207], v[156:159], v[44:47]\n\t"
      "v_mfma_f32_16x16x32_bf16 v[48:51], a[208:211], v[156:159], v[48:51]\n\t"
      "ds_read_b128 v[136:139], v198 offset:37376\n\t"
      "buffer_load_dwordx4 v209, s[20:23], s51 offen offset:2048 lds\n\t"
      "v_mfma_f32_16x16x32_bf16 v[52:55], a[212:215], v[156:159], v[52:55]\n\t"
      "s_waitcnt lgkmcnt(8)\n\t"
      "v_mfma_f32_16x16x32_bf16 v[56:59], a[192:195], v[160:163], v[56:59]\n\t"
      "v_mfma_f32_16x16x32_bf16 v[60:63], a[196:199], v[160:163], v[60:63]\n\t"
      "v_mfma_f32_16x16x32_bf16 v[64:67], a[200:203], v[160:163], v[64:67]\n\t"
      "s_waitcnt vmcnt(18)\n\t"
      "ds_read_b64_tr_b16 a[218:219], v214 offset:24576\n\t"
      "v_mfma_f32_16x16x32_bf16 v[68:71], a[204:207], v[160:163], v[68:71]\n\t"
      "ds_read_b64_tr_b16 a[222:223], v214 offset:24584\n\t"
      "v_mfma_f32_16x16x32_bf16 v[72:75], a[208:211], v[160:163], v[72:75]\n\t"
      "ds_read_b64_tr_b16 a[226:227], v214 offset:24640\n\t"
      "ds_read_b128 v[140:143], v199 offset:512\n\t"
      "v_mfma_f32_16x16x32_bf16 v[76:79], a[212:215], v[160:163], v[76:79]\n\t"
      "ds_read_b64_tr_b16 a[230:231], v214 offset:24648\n\t"
      "s_waitcnt lgkmcnt(9)\n\t"
      "v_mfma_f32_16x16x32_bf16 v[80:83], a[192:195], v[128:131], v[80:83]\n\t"
      "ds_read_b64_tr_b16 a[234:235], v214 offset:24704\n\t"
      "v_mfma_f32_16x16x32_bf16 v[84:87], a[196:199], v[128:131], v[84:87]\n\t"
      "ds_read_b64_tr_b16 a[238:239], v214 offset:24712\n\t"
      "v_mfma_f32_16x16x32_bf16 v[88:91], a[200:203], v[128:131], v[88:91]\n\t"
      "v_mfma_f32_16x16x32_bf16 v[92:95], a[204:207], v[128:131], v[92:95]\n\t"
      "v_mfma_f32_16x16x32_bf16 v[96:99], a[208:211], v[128:131], v[96:99]\n\t"
      "ds_read_b128 v[144:147], v199 offset:12800\n\t"
      "v_mfma_f32_16x16x32_bf16 v[100:103], a[212:215], v[128:131], v[100:103]\n\t"
      "s_waitcnt lgkmcnt(8)\n\t"
      "v_mfma_f32_16x16x32_bf16 v[104:107], a[192:195], v[136:139], v[104:107]\n\t"
      "v_mfma_f32_16x16x32_bf16 v[108:111], a[196:199], v[136:139], v[108:111]\n\t"
      "v_mfma_f32_16x16x32_bf16 v[112:115], a[200:203], v[136:139], v[112:115]\n\t"
      "s_waitcnt vmcnt(15)\n\t"
      "ds_read_b64_tr_b16 a[240:241], v214 offset:36864\n\t"
      "v_mfma_f32_16x16x32_bf16 v[116:119], a[204:207], v[136:139], v[116:119]\n\t"
      "ds_read_b64_tr_b16 a[244:245], v214 offset:36872\n\t"
      "v_mfma_f32_16x16x32_bf16 v[120:123], a[208:211], v[136:139], v[120:123]\n\t"
      "ds_read_b64_tr_b16 a[248:249], v214 offset:36928\n\t"
      "ds_read_b128 v[148:151], v199 offset:25088\n\t"
      "v_mfma_f32_16x16x32_bf16 v[132:135], a[212:215], v[136:139], v[132:135]\n\t"
      "ds_read_b64_tr_b16 a[252:253], v214 offset:36936\n\t"
      "s_waitcnt lgkmcnt(6)\n\t"
      "v_mfma_f32_16x16x32_bf16 v[32:35], a[216:219], v[140:143], v[32:35]\n\t"
      "ds_read_b64_tr_b16 v[164:165], v214 offset:36992\n\t"
      "v_mfma_f32_16x16x32_bf16 v[36:39], a[220:223], v[140:143], v[36:39]\n\t"
      "ds_read_b64_tr_b16 v[168:169], v214 offset:37000\n\t"
      "v_mfma_f32_16x16x32_bf16 v[40:43], a[224:227], v[140:143], v[40:43]\n\t"
      "s_waitcnt vmcnt(12)\n\t"
      "ds_read_b64_tr_b16 a[242:243], v214 offset:49152\n\t"
      "v_mfma_f32_16x16x32_bf16 v[44:47], a[228:231], v[140:143], v[44:47]\n\t"
      "ds_read_b64_tr_b16 a[246:247], v214 offset:49160\n\t"
      "v_mfma_f32_16x16x32_bf16 v[48:51], a[232:235], v[140:143], v[48:51]\n\t"
      "ds_read_b64_tr_b16 a[250:251], v214 offset:49216\n\t"
      "ds_read_b128 v[152:155], v199 offset:37376\n\t"
      "v_mfma_f32_16x16x32_bf16 v[52:55], a[236:239], v[140:143], v[52:55]\n\t"
      "ds_read_b64_tr_b16 a[254:255], v214 offset:49224\n\t"
      "s_waitcnt lgkmcnt(12)\n\t"
      "v_mfma_f32_16x16x32_bf16 v[56:59], a[216:219], v[144:147], v[56:59]\n\t"
      "ds_read_b64_tr_b16 v[166:167], v214 offset:49280\n\t"
      "v_mfma_f32_16x16x32_bf16 v[60:63], a[220:223], v[144:147], v[60:63]\n\t"
      "ds_read_b64_tr_b16 v[170:171], v214 offset:49288\n\t"
      "v_mfma_f32_16x16x32_bf16 v[64:67], a[224:227], v[144:147], v[64:67]\n\t"
      "s_waitcnt vmcnt(9)\n\t"
      "ds_read_b64_tr_b16 v[172:173], v221 offset:0\n\t"
      "v_mfma_f32_16x16x32_bf16 v[68:71], a[228:231], v[144:147], v[68:71]\n\t"
      "ds_read_b64_tr_b16 v[176:177], v221 offset:8\n\t"
      "v_mfma_f32_16x16x32_bf16 v[72:75], a[232:235], v[144:147], v[72:75]\n\t"
      "ds_read_b64_tr_b16 v[180:181], v221 offset:64\n\t"
      "ds_read_b128 v[156:159], v196 offset:512\n\t"
      "v_mfma_f32_16x16x32_bf16 v[76:79], a[236:239], v[144:147], v[76:79]\n\t"
      "ds_read_b64_tr_b16 v[184:185], v221 offset:72\n\t"
      "s_waitcnt lgkmcnt(14)\n\t"
      "v_mfma_f32_16x16x32_bf16 v[80:83], a[216:219], v[148:151], v[80:83]\n\t"
      "ds_read_b64_tr_b16 v[188:189], v221 offset:128\n\t"
      "v_mfma_f32_16x16x32_bf16 v[84:87], a[220:223], v[148:151], v[84:87]\n\t"
      "ds_read_b64_tr_b16 v[192:193], v221 offset:136\n\t"
      "v_mfma_f32_16x16x32_bf16 v[88:91], a[224:227], v[148:151], v[88:91]\n\t"
      "v_mfma_f32_16x16x32_bf16 v[92:95], a[228:231], v[148:151], v[92:95]\n\t"
      "v_mfma_f32_16x16x32_bf16 v[96:99], a[232:235], v[148:151], v[96:99]\n\t"
      "ds_read_b128 v[160:163], v196 offset:12800\n\t"
      "v_mfma_f32_16x16x32_bf16 v[100:103], a[236:239], v[148:151], v[100:103]\n\t"
      "s_waitcnt lgkmcnt(11)\n\t"
      "v_mfma_f32_16x16x32_bf16 v[104:107], a[216:219], v[152:155], v[104:107]\n\t"
      "v_mfma_f32_16x16x32_bf16 v[108:111], a[220:223], v[152:155], v[108:111]\n\t"
      "v_mfma_f32_16x16x32_bf16 v[112:115], a[224:227], v[152:155], v[112:115]\n\t"
      "s_waitcnt vmcnt(6)\n\t"
      "ds_read_b64_tr_b16 v[174:175], v221 offset:12288\n\t"
      "buffer_load_dwordx2 v[240:241], v245, s[24:27], 0 offen sc1\n\t"
      "v_mfma_f32_16x16x32_bf16 v[116:119], a[228:231], v[152:155], v[116:119]\n\t"
      "ds_read_b64_tr_b16 v[178:179], v221 offset:12296\n\t"
      "s_cmp_eq_u32 s41, s43\n\t"
      "s_cbranch_scc0 .Lc4_%=\n\t"
      "buffer_load_dword v254, v253, s[24:27], 0 offen sc1\n\t"
      ".Lc4_%=:\n\t"
      "v_mfma_f32_16x16x32_bf16 v[120:123], a[232:235], v[152:155], v[120:123]\n\t"
      "ds_read_b64_tr_b16 v[182:183], v221 offset:12352\n\t"
      "ds_read_b128 v[140:143], v197 offset:512\n\t"
      "v_mfma_f32_16x16x32_bf16 v[132:135], a[236:239], v[152:155], v[132:135]\n\t"
      "ds_read_b64_tr_b16 v[186:187], v221 offset:12360\n\t"
      "s_waitcnt lgkmcnt(9)\n\t"
      "v_mfma_f32_16x16x32_bf16 v[32:35], a[240:243], v[156:159], v[32:35]\n\t"
      "ds_read_b64_tr_b16 v[190:191], v221 offset:12416\n\t"
      "v_mfma_f32_16x16x32_bf16 v[36:39], a[244:247], v[156:159], v[36:39]\n\t"
      "ds_read_b64_tr_b16 v[194:195], v221 offset:12424\n\t"
      "v_mfma_f32_16x16x32_bf16 v[40:43], a[248:251], v[156:159], v[40:43]\n\t"
      "v_mfma_f32_16x16x32_bf16 v[44:47], a[252:255], v[156:159], v[44:47]\n\t"
      "v_mfma_f32_16x16x32_bf16 v[48:51], v[164:167], v[156:159], v[48:51]\n\t"
      "ds_read_b128 v[144:147], v197 offset:12800\n\t"
      "v_mfma_f32_16x16x32_bf16 v[52:55], v[168:171], v[156:159], v[52:55]\n\t"
      "s_waitcnt lgkmcnt(8)\n\t"
      "v_mfma_f32_16x16x32_bf16 v[56:59], a[240:243], v[160:163], v[56:59]\n\t"
      "v_mfma_f32_16x16x32_bf16 v[60:63], a[244:247], v[160:163], v[60:63]\n\t"
      "v_mfma_f32_16x16x32_bf16 v[64:67], a[248:251], v[160:163], v[64:67]\n\t"
      "v_mfma_f32_16x16x32_bf16 v[68:71], a[252:255], v[160:163], v[68:71]\n\t"
      "v_mfma_f32_16x16x32_bf16 v[72:75], v[164:167], v[160:163], v[72:75]\n\t"
      "ds_read_b128 v[128:131], v196 offset:25088\n\t"
      "v_mfma_f32_16x16x32_bf16 v[76:79], v[168:171], v[160:163], v[76:79]\n\t"
      "s_waitcnt lgkmcnt(2)\n\t"
      "v_mfma_f32_16x16x32_bf16 v[32:35], v[172:175], v[140:143], v[32:35]\n\t"
      "v_mfma_f32_16x16x32_bf16 v[36:39], v[176:179], v[140:143], v[36:39]\n\t"
      "v_mfma_f32_16x16x32_bf16 v[40:43], v[180:183], v[140:143], v[40:43]\n\t"
      "v_mfma_f32_16x16x32_bf16 v[44:47], v[184:187], v[140:143], v[44:47]\n\t"
      "v_mfma_f32_16x16x32_bf16 v[48:51], v[188:191], v[140:143], v[48:51]\n\t"
      "ds_read_b128 v[136:139], v196 offset:37376\n\t"
      "v_mfma_f32_16x16x32_bf16 v[52:55], v[192:195], v[140:143], v[52:55]\n\t"
      "s_waitcnt lgkmcnt(2)\n\t"
      "v_mfma_f32_16x16x32_bf16 v[56:59], v[172:175], v[144:147], v[56:59]\n\t"
      "v_mfma_f32_16x16x32_bf16 v[60:63], v[176:179], v[144:147], v[60:63]\n\t"
      "s_waitcnt vmcnt(4) lgkmcnt(2)\n\t"
      "s_barrier\n\t"
      "v_mfma_f32_16x16x32_bf16 v[64:67], v[180:183], v[144:147], v[64:67]\n\t"
      "v_cvt_pk_bf16_f32 v32, v32, v33\n\t"
      "v_cvt_pk_bf16_f32 v33, v34, v35\n\t"
      "v_mfma_f32_16x16x32_bf16 v[68:71], v[184:187], v[144:147], v[68:71]\n\t"
      "v_cvt_pk_bf16_f32 v34, v36, v37\n\t"
      "v_cvt_pk_bf16_f32 v35, v38, v39\n\t"
      "v_mfma_f32_16x16x32_bf16 v[72:75], v[188:191], v[144:147], v[72:75]\n\t"
      "ds_read_b128 v[148:151], v197 offset:25088\n\t"
      "ds_write_b128 v217, v[32:35] offset:24576\n\t"
      "v_cvt_pk_bf16_f32 v40, v40, v41\n\t"
      "v_mfma_f32_16x16x32_bf16 v[76:79], v[192:195], v[144:147], v[76:79]\n\t"
      "v_cvt_pk_bf16_f32 v41, v42, v43\n\t"
      "v_cvt_pk_bf16_f32 v42, v44, v45\n\t"
      "s_mov_b32 m0, s41\n\t"
      "s_add_u32 s51, s40, 0x1200000\n\t"
      "buffer_load_dwordx4 v208, s[20:23], s51 offen lds\n\t"
      "s_waitcnt lgkmcnt(3)\n\t"
      "v_mfma_f32_16x16x32_bf16 v[80:83], a[240:243], v[128:131], v[80:83]\n\t"
      "v_cvt_pk_bf16_f32 v43, v46, v47\n\t"
      "ds_write_b128 v218, v[40:43] offset:24576\n\t"
      "ds_read_b128 v[32:35], v196 offset:49152\n\t"
      "v_mfma_f32_16x16x32_bf16 v[84:87], a[244:247], v[128:131], v[84:87]\n\t"
      "v_cvt_pk_bf16_f32 v48, v48, v49\n\t"
      "v_cvt_pk_bf16_f32 v49, v50, v51\n\t"
      "v_mfma_f32_16x16x32_bf16 v[88:91], a[248:251], v[128:131], v[88:91]\n\t"
      "v_cvt_pk_bf16_f32 v50, v52, v53\n\t"
      "v_cvt_pk_bf16_f32 v51, v54, v55\n\t"
      "buffer_load_dwordx4 v208, s[20:23], s51 offen offset:1024 lds\n\t"
      "v_mfma_f32_16x16x32_bf16 v[92:95], a[252:255], v[128:131], v[92:95]\n\t"
      "ds_write_b128 v219, v[48:51] offset:24576\n\t"
      "ds_read_b128 v[36:39], v197 offset:49152\n\t"
      "v_cvt_pk_bf16_f32 v56, v56, v57\n\t"
      "v_mfma_f32_16x16x32_bf16 v[96:99], v[164:167], v[128:131], v[96:99]\n\t"
      "ds_read_b128 v[152:155], v197 offset:37376\n\t"
      "v_cvt_pk_bf16_f32 v57, v58, v59\n\t"
      "v_cvt_pk_bf16_f32 v58, v60, v61\n\t"
      "v_mfma_f32_16x16x32_bf16 v[100:103], v[168:171], v[128:131], v[100:103]\n\t"
      "v_cvt_pk_bf16_f32 v59, v62, v63\n\t"
      "ds_write_b128 v217, v[56:59] offset:36864\n\t"
      "ds_read_b128 v[40:43], v198 offset:49152\n\t"
      "s_waitcnt lgkmcnt(9)\n\t"
      "v_mfma_f32_16x16x32_bf16 v[104:107], a[240:243], v[136:139], v[104:107]\n\t"
      "v_cvt_pk_bf16_f32 v64, v64, v65\n\t"
      "v_cvt_pk_bf16_f32 v65, v66, v67\n\t"
      "buffer_load_dwordx4 v208, s[20:23], s51 offen offset:2048 lds\n\t"
      "v_mfma_f32_16x16x32_bf16 v[108:111], a[244:247], v[136:139], v[108:111]\n\t"
      "v_cvt_pk_bf16_f32 v66, v68, v69\n\t"
      "v_cvt_pk_bf16_f32 v67, v70, v71\n\t"
      "v_mfma_f32_16x16x32_bf16 v[112:115], a[248:251], v[136:139], v[112:115]\n\t"
      "ds_write_b128 v218, v[64:67] offset:36864\n\t"
      "ds_read_b128 v[44:47], v199 offset:49152\n\t"
      "v_cvt_pk_bf16_f32 v72, v72, v73\n\t"
      "v_mfma_f32_16x16x32_bf16 v[116:119], a[252:255], v[136:139], v[116:119]\n\t"
      "v_cvt_pk_bf16_f32 v73, v74, v75\n\t"
      "v_cvt_pk_bf16_f32 v74, v76, v77\n\t"
      "v_mfma_f32_16x16x32_bf16 v[120:123], v[164:167], v[136:139], v[120:123]\n\t"
      "v_cvt_pk_bf16_f32 v75, v78, v79\n\t"
      "ds_write_b128 v219, v[72:75] offset:36864\n\t"
      "ds_read_b128 v[48:51], v197 offset:49408\n\t"
      "v_mfma_f32_16x16x32_bf16 v[132:135], v[168:171], v[136:139], v[132:135]\n\t"
      "s_add_u32 m0, s41, 0x3000\n\t"
      "s_add_u32 s51, s40, 0x1200000\n\t"
      "buffer_load_dwordx4 v209, s[20:23], s51 offen lds\n\t"
      "ds_read_b128 v[52:55], v198 offset:49408\n\t"
      "s_waitcnt lgkmcnt(13)\n\t"
      "v_mfma_f32_16x16x32_bf16 v[80:83], v[172:175], v[148:151], v[80:83]\n\t"
      "ds_read_b128 v[56:59], v199 offset:49408\n\t"
      "ds_read_b128 v[60:63], v196 offset:49408\n\t"
      "v_mfma_f32_16x16x32_bf16 v[84:87], v[176:179], v[148:151], v[84:87]\n\t"
      "ds_read_b128 v[64:67], v198 offset:49664\n\t"
      "s_memrealtime s[80:81]\n\t"
      "v_mfma_f32_16x16x32_bf16 v[88:91], v[180:183], v[148:151], v[88:91]\n\t"
      "buffer_load_dwordx4 v209, s[20:23], s51 offen offset:1024 lds\n\t"
      "ds_read_b128 v[68:71], v199 offset:49664\n\t"
      "v_mfma_f32_16x16x32_bf16 v[92:95], v[184:187], v[148:151], v[92:95]\n\t"
      "ds_read_b128 v[72:75], v196 offset:49664\n\t"
      "ds_read_b128 v[76:79], v197 offset:49664\n\t"
      "v_mfma_f32_16x16x32_bf16 v[96:99], v[188:191], v[148:151], v[96:99]\n\t"
      "v_mfma_f32_16x16x32_bf16 v[100:103], v[192:195], v[148:151], v[100:103]\n\t"
      "buffer_load_dwordx4 v209, s[20:23], s51 offen offset:2048 lds\n\t"
      "s_waitcnt lgkmcnt(13)\n\t"
      "v_mfma_f32_16x16x32_bf16 v[104:107], v[172:175], v[152:155], v[104:107]\n\t"
      "v_mfma_f32_16x16x32_bf16 v[108:111], v[176:179], v[152:155], v[108:111]\n\t"
      "v_mfma_f32_16x16x32_bf16 v[112:115], v[180:183], v[152:155], v[112:115]\n\t"
      "v_mfma_f32_16x16x32_bf16 v[116:119], v[184:187], v[152:155], v[116:119]\n\t"
      "v_mfma_f32_16x16x32_bf16 v[120:123], v[188:191], v[152:155], v[120:123]\n\t"
      "v_mfma_f32_16x16x32_bf16 v[132:135], v[192:195], v[152:155], v[132:135]\n\t"
      "v_add_u32_e32 v196, s46, v200\n\t"
      "v_add_u32_e32 v197, s46, v201\n\t"
      "v_add_u32_e32 v198, s46, v202\n\t"
      "v_add_u32_e32 v199, s46, v203\n\t"
      "s_mov_b32 s86, 76\n\t"
      "s_mov_b32 s84, 96\n\t"
      "s_branch .Lp2_%=\n\t"
      ".Lend_%=:\n\t"
      ".Lconv_end:\n\t"
      :
      : "{s[16:31]}"(d16), "{s[36:39]}"(db), "{s[52:53]}"(rs), "{s40}"(t0), "{s41}"(p0), "{s43}"(lds0), "{s94}"(ty), "{s95}"(tres), "{s[60:61]}"(pres),
      "{s69}"(pre1), "{s74}"(plm), "{s75}"(plb), "{s66}"(sst ? 24576u : 0u - 24576u), "{s89}"((blk >> 4) % 5u),
      "{s71}"(wv == 0u ? 568645u + ((blk >> 4) % 5u) * 1057u : ty ? 17733u + ((blk >> 4) % 5u) * 33u : 17u), "{v208}"(ga), "{v209}"(gb),
      "{v211}"(woff[0]), "{v212}"(woff[1]), "{v213}"(woff[2]), "{v248}"(ta), "{v249}"(ta), "{v250}"(tp), "{v251}"(t1), "{v252}"(mk), "{v245}"(vgv),
      "{v246}"(vxw), "{v247}"(vxr), "{v253}"(vcell), "{v200}"(cfa[0]), "{v201}"(cfa[1]), "{v202}"(cfa[2]), "{v203}"(cfa[3]), "{v204}"(csw[0]),
      "{v205}"(csw[1]), "{v206}"(csw[2]), "{v207}"(csr), "{v214}"(tbase + 73728u), "{v221}"(tbase + 135168u), "{v[226:229]}"(br0), "{v[230:233]}"(br1),
      "{v[234:237]}"(br2), "{v6}"(cpo)
      : "memory", "scc", "vcc",
      "s42", "s44", "s45", "s46", "s47", "s48", "s49", "s50", "s51", "s54", "s55", "s56", "s57", "s58", "s59", "s62", "s63", "s64", "s65", "s67",
      "s68", "s70", "s72", "s73", "s76", "s77", "s78", "s79", "s80", "s81", "s82", "s83", "s84", "s85", "s86", "s87", "s88", "s90", "s91", "s92",
      "s93", "s96", "s97", "s98", "s99",
      "v1", "v2", "v3", "v4", "v5", "v7", "v8", "v9", "v10", "v11", "v12", "v13", "v14", "v15", "v16", "v17", "v18", "v19", "v20", "v21", "v22", "v23",
      "v24", "v25", "v26", "v27", "v28", "v29", "v30", "v31", "v32", "v33", "v34", "v35", "v36", "v37", "v38", "v39", "v40", "v41", "v42", "v43",
      "v44", "v45", "v46", "v47", "v48", "v49", "v50", "v51", "v52", "v53", "v54", "v55", "v56", "v57", "v58", "v59", "v60", "v61", "v62", "v63",
      "v64", "v65", "v66", "v67", "v68", "v69", "v70", "v71", "v72", "v73", "v74", "v75", "v76", "v77", "v78", "v79", "v80", "v81", "v82", "v83",
      "v84", "v85", "v86", "v87", "v88", "v89", "v90", "v91", "v92", "v93", "v94", "v95", "v96", "v97", "v98", "v99", "v100", "v101", "v102", "v103",
      "v104", "v105", "v106", "v107", "v108", "v109", "v110", "v111", "v112", "v113", "v114", "v115", "v116", "v117", "v118", "v119", "v120", "v121",
      "v122", "v123", "v124", "v125", "v126", "v127", "v128", "v129", "v130", "v131", "v132", "v133", "v134", "v135", "v136", "v137", "v138", "v139",
      "v140", "v141", "v142", "v143", "v144", "v145", "v146", "v147", "v148", "v149", "v150", "v151", "v152", "v153", "v154", "v155", "v156", "v157",
      "v158", "v159", "v160", "v161", "v162", "v163", "v164", "v165", "v166", "v167", "v168", "v169", "v170", "v171", "v172", "v173", "v174", "v175",
      "v176", "v177", "v178", "v179", "v180", "v181", "v182", "v183", "v184", "v185", "v186", "v187", "v188", "v189", "v190", "v191", "v192", "v193",
      "v194", "v195", "v196", "v197", "v198", "v199", "v210", "v215", "v216", "v217", "v218", "v219", "v220", "v222", "v223", "v224", "v225", "v238",
      "v239", "v240", "v241", "v242", "v243", "v244", "v254", "v255",
      "a0", "a1", "a2", "a3", "a4", "a5", "a6", "a7", "a8", "a9", "a10", "a11", "a12", "a13", "a14", "a15", "a16", "a17", "a18", "a19", "a20", "a21",
      "a22", "a23", "a24", "a25", "a26", "a27", "a28", "a29", "a30", "a31", "a32", "a33", "a34", "a35", "a36", "a37", "a38", "a39", "a40", "a41",
      "a42", "a43", "a44", "a45", "a46", "a47", "a48", "a49", "a50", "a51", "a52", "a53", "a54", "a55", "a56", "a57", "a58", "a59", "a60", "a61",
      "a62", "a63", "a64", "a65", "a66", "a67", "a68", "a69", "a70", "a71", "a72", "a73", "a74", "a75", "a76", "a77", "a78", "a79", "a80", "a81",
      "a82", "a83", "a84", "a85", "a86", "a87", "a88", "a89", "a90", "a91", "a92", "a93", "a94", "a95", "a96", "a97", "a98", "a99", "a100", "a101",
      "a102", "a103", "a104", "a105", "a106", "a107", "a108", "a109", "a110", "a111", "a112", "a113", "a114", "a115", "a116", "a117", "a118", "a119",
      "a120", "a121", "a122", "a123", "a124", "a125", "a126", "a127", "a128", "a129", "a130", "a131", "a132", "a133", "a134", "a135", "a136", "a137",
      "a138", "a139", "a140", "a141", "a142", "a143", "a144", "a145", "a146", "a147", "a148", "a149", "a150", "a151", "a152", "a153", "a154", "a155",
      "a156", "a157", "a158", "a159", "a160", "a161", "a162", "a163", "a164", "a165", "a166", "a167", "a168", "a169", "a170", "a171", "a172", "a173",
      "a174", "a175", "a176", "a177", "a178", "a179", "a180", "a181", "a182", "a183", "a184", "a185", "a186", "a187", "a188", "a189", "a190", "a191",
      "a192", "a193", "a194", "a195", "a196", "a197", "a198", "a199", "a200", "a201", "a202", "a203", "a204", "a205", "a206", "a207", "a208", "a209",
      "a210", "a211", "a212", "a213", "a214", "a215", "a216", "a217", "a218", "a219", "a220", "a221", "a222", "a223", "a224", "a225", "a226", "a227",
      "a228", "a229", "a230", "a231", "a232", "a233", "a234", "a235", "a236", "a237", "a238", "a239", "a240", "a241", "a242", "a243", "a244", "a245",
      "a246", "a247", "a248", "a249", "a250", "a251", "a252", "a253", "a254", "a255");
}

extern "C" void kernel_launch(void* const* d_in, const int* in_sizes, int n_in,
                              void* d_out, int out_size, void* d_ws, size_t ws_size,
                              hipStream_t stream) {
  conv1x1_kernel<<<256, 256, 0, stream>>>(d_in[1], 0x0002000000048000ull, d_in[0], 0x0002000006c00000ull, d_ws, 0x0002000000000c00ull, d_out, d_in[2]);
}
```

```cpp
#include <hip/hip_runtime.h>
#include <stdint.h>

#define LAS __attribute__((address_space(3)))
typedef __attribute__((ext_vector_type(16))) unsigned u32x16;
typedef __attribute__((ext_vector_type(4))) unsigned u32x4;
typedef unsigned long long u64;

__global__ __launch_bounds__(256, 1) void conv1x1_kernel(const void* w, u64 cw, const void* x, u64 cx, void* ws, u64 cs, void* out, const void* bias) {
  __shared__ __attribute__((aligned(1024))) unsigned char lds[160768];

  u32x16 d16;
  u32x4 db;
  u64 rs;
  asm volatile(
      "s_load_dwordx16 s[16:31], %3, 0x0\n\t"
      "s_memrealtime s[52:53]\n\t"
      "s_movk_i32 s38, 0x300\n\t"
      "s_mov_b32 s39, 0x20000\n\t"
      : "={s[16:31]}"(d16), "={s[36:39]}"(db), "={s[52:53]}"(rs)
      : "s"(__builtin_amdgcn_kernarg_segment_ptr())
      : "memory");
  unsigned cpo = ((blockIdx.x >> 3) * 256u + threadIdx.x) * 4u;
  asm volatile(
      "s_getpc_b64 s[92:93]\n\t"
      ".Lconv_cp:\n\t"
      "v_min_u32_e32 v6, .Lconv_end-.Lconv_cp-4, v6\n\t"
      "global_load_dword v6, v6, s[92:93]\n\t"
      : "+{v6}"(cpo)
      :
      : "memory", "s92", "s93");
  __builtin_amdgcn_sched_barrier(0);
  unsigned tid  = threadIdx.x;
  unsigned lane = tid & 63;
  unsigned wv   = __builtin_amdgcn_readfirstlane(tid >> 6);
  const unsigned lds0 = (unsigned)(size_t)(LAS unsigned char*)lds;
  unsigned blk  = blockIdx.x;
  const unsigned p0   = lds0 + 3072u * wv;
  const unsigned lq = lane >> 4, ph = (lane >> 2) & 3, pl = lane & 3;
  const unsigned gs = 256u * lq + 64u * ((ph - lq) & 3) + 16u * (pl ^ ((4u - wv) & 3));
  const unsigned ga = gs + 3072u * (wv < 3u ? wv : 5u);
  const unsigned gb = gs + 3072u * (wv < 2u ? wv + 6u : wv + 1u);
  const unsigned boff = 192u * wv + 16u * lq;
  const unsigned role = (blk >> 2) & 1u;
  unsigned pk = (blk & 3u) * 32u + (blk >> 3);
  const unsigned sst  = (role ^ (blk >> 4)) & 1u;
  const unsigned t0   = pk * 49152u + sst * 24576u;
  const unsigned pv = blk >> 7, ps = ((blk & 7u) | ((blk >> 1) & 56u)) ^ (4u * pv), pgr = (ps >> 2) & 7u, pi = (ps & 3u) * 2u + pv;
  const unsigned ppg = min(pgr * 8u, 52u) + (pi & (0x13777777u >> (4u * pgr)) & 7u);
  const unsigned pop = (ppg << 21) + ((ps & 3u) * 4u + pv) * 0x1c080u + (ps >> 5) * 0x38000u + 0x24080u;
  unsigned pfire = ((role ^ ppg) & 1u) * 2u;
  const unsigned vpub = lane == 0u ? 0u + (pv * 64u + ps) * 8u : 0x40000000u;
  const unsigned pfo = (blk >> 4) * 18432u;
  const unsigned pfv = tid * 16u;
  const unsigned ty = (blk >> 3) & 1u;
  const unsigned tidx = (blk >> 3) * 4u + wv, tsel = tidx >> 6;
  const unsigned toff = (tidx & 63u) < 54u ? (tidx & 63u) << 21 : 113246204u;
  unsigned tres, pre1;
  u64 pres;
  u32x4 br0, br1, br2;
  asm volatile(
      "s_add_u32 m0, s43, 0x27000\n\t"
      "s_waitcnt lgkmcnt(0)\n\t"
      "s_mov_b64 s[36:37], s[30:31]\n\t"
      "s_mov_b64 s[30:31], s[22:23]\n\t"
      "s_cmp_eq_u32 s94, 1\n\t"
      "s_cbranch_scc0 .Lc1_%=\n\t"
      "s_cmp_eq_u32 s41, s43\n\t"
      "s_cbranch_scc0 .Lc0_%=\n\t"
      "s_load_dword s69, s[20:21], s76 offset:0x0\n\t"
      "s_load_dwordx2 s[60:61], s[20:21], s76 offset:0x3300\n\t"
      ".Lc0_%=:\n\t"
      "buffer_load_dwordx4 v216, s[16:19], s42 offen lds\n\t"
      "s_add_u32 s51, s42, 0x1000\n\t"
      "buffer_load_dwordx4 v216, s[16:19], s51 offen lds\n\t"
      "s_add_u32 s51, s42, 0x2000\n\t"
      "buffer_load_dwordx4 v216, s[16:19], s51 offen lds\n\t"
      "s_add_u32 s51, s42, 0x3000\n\t"
      "buffer_load_dwordx4 v216, s[16:19], s51 offen lds\n\t"
      "s_add_u32 s51, s42, 0x4000\n\t"
      "buffer_load_dwordx4 v216, s[16:19], s51 offen lds\n\t"
      ".Lc1_%=:\n\t"
      "buffer_load_dwordx4 v[226:229], v215, s[36:39], 0 offen offset:0\n\t"
      "buffer_load_dwordx4 v[230:233], v215, s[36:39], 0 offen offset:64\n\t"
      "buffer_load_dwordx4 v[234:237], v215, s[36:39], 0 offen offset:128\n\t"
      "s_mov_b32 m0, s41\n\t"
      "s_nop 0\n\t"
      "buffer_load_dwordx4 v208, s[20:23], s40 offen lds\n\t"
      "buffer_load_dwordx4 v208, s[20:23], s40 offen offset:1024 lds\n\t"
      "buffer_load_dwordx4 v208, s[20:23], s40 offen offset:2048 lds\n\t"
      "s_cmp_eq_u32 s94, 0\n\t"
      "s_cbranch_scc0 .Lvb0_%=\n\t"
      "s_add_u32 m0, s41, 0x3000\n\t"
      "s_nop 0\n\t"
      "buffer_load_dwordx4 v209, s[20:23], s40 offen lds\n\t"
      "buffer_load_dwordx4 v209, s[20:23], s40 offen offset:1024 lds\n\t"
      "buffer_load_dwordx4 v209, s[20:23], s40 offen offset:2048 lds\n\t"
      "s_cmp_eq_u32 s99, 0\n\t"
      "s_cselect_b64 s[96:97], s[20:21], s[28:29]\n\t"
      "s_load_dword s95, s[96:97], s98\n\t"
      "s_branch .Lvb1_%=\n\t"
      ".Lvb0_%=:\n\t"
      "s_cmp_eq_u32 s41, s43\n\t"
      "s_cbranch_scc1 .Lev0_%=\n\t"
      "s_cmp_eq_u32 s99, 0\n\t"
      "s_cselect_b64 s[96:97], s[20:21], s[28:29]\n\t"
      "s_load_dword s95, s[96:97], s98\n\t"
      "s_cmp_eq_u32 s94, 0\n\t"
      "s_cbranch_scc1 .Lev1_%=\n\t"
      "s_sleep 8\n\t"
      "s_branch .Lev1_%=\n\t"
      ".Lev0_%=:\n\t"
      "s_andn2_b32 s79, s52, 15\n\t"
      "v_mov_b32_e32 v241, s53\n\t"
      "s_waitcnt lgkmcnt(2)\n\t"
      "s_nop 7\n\t"
      "s_getreg_b32 s78, hwreg(HW_REG_IB_STS, 8, 4)\n\t"
      "s_cmp_eq_u32 s78, 1\n\t"
      "s_cselect_b32 s51, 2, 0\n\t"
      "s_xor_b32 s77, s77, s51\n\t"
      "s_cmp_lg_u32 s78, 0\n\t"
      "s_cselect_b32 s77, s77, 1\n\t"
      "s_or_b32 s79, s79, s77\n\t"
      "v_mov_b32_e32 v240, s79\n\t"
      "buffer_store_dwordx2 v[240:241], v244, s[24:27], 0 offen sc1\n\t"
      "s_cmp_eq_u32 s99, 0\n\t"
      "s_cselect_b64 s[96:97], s[20:21], s[28:29]\n\t"
      "s_load_dword s95, s[96:97], s98\n\t"
      ".Lev1_%=:\n\t"
      "s_add_u32 m0, s41, 0x3000\n\t"
      "s_nop 0\n\t"
      "buffer_load_dwordx4 v209, s[20:23], s40 offen lds\n\t"
      "buffer_load_dwordx4 v209, s[20:23], s40 offen offset:1024 lds\n\t"
      "buffer_load_dwordx4 v209, s[20:23], s40 offen offset:2048 lds\n\t"
      "s_add_u32 m0, s41, 0x6000\n\t"
      "s_add_u32 s51, s40, 0x600000\n\t"
      "buffer_load_dwordx4 v208, s[20:23], s51 offen lds\n\t"
      "buffer_load_dwordx4 v208, s[20:23], s51 offen offset:1024 lds\n\t"
      "buffer_load_dwordx4 v208, s[20:23], s51 offen offset:2048 lds\n\t"
      "s_add_u32 m0, s41, 0x9000\n\t"
      "s_add_u32 s51, s40, 0x600000\n\t"
      "buffer_load_dwordx4 v209, s[20:23], s51 offen lds\n\t"
      "buffer_load_dwordx4 v209, s[20:23], s51 offen offset:1024 lds\n\t"
      "buffer_load_dwordx4 v209, s[20:23], s51 offen offset:2048 lds\n\t"
      ".Lvb1_%=:\n\t"
      : "={s95}"(tres), "={s[60:61]}"(pres), "={s69}"(pre1), "={v[226:229]}"(br0), "={v[230:233]}"(br1), "={v[234:237]}"(br2),
        "+{s[16:31]}"(d16), "+{s[36:39]}"(db), "+v"(lane), "+{s77}"(pfire), "+{v6}"(cpo)
      : "{s40}"(t0), "{s41}"(p0), "{s43}"(lds0), "{s94}"(ty),
        "{v208}"(ga), "{v209}"(gb), "{v215}"(boff), "{v216}"(pfv),
        "{s42}"(pfo), "{s98}"(toff), "{s99}"(tsel), "{s76}"(pop), "{s[52:53]}"(rs), "{v244}"(vpub)
      : "memory", "scc", "s51", "s92", "s93", "s96", "s97", "s78", "s79", "v240", "v241");
  asm volatile("" : "+s"(wv), "+s"(pk));
  __builtin_amdgcn_sched_barrier(0);
  const unsigned vcell = lane < 2u ? 2048u + pk * 8u + 4u * (role ^ lane) : 0x40000000u;
  const unsigned ob = (unsigned)(((lane < 12u ? 0x434ec32ca418820ull >> (5u * lane) : 0x23053d2eull >> (5u * ((lane - 12u) & 7u)))) & 31u) * 6291456u + pk * 49152u;
  const unsigned pg = (ob + 24576u) >> 21;
  const unsigned ta = lane < 18u ? ob + 24576u * sst : 113246208u, t1 = ob + 24576u * (sst ^ 1u);
  const unsigned tq = (lane >= 9u ? 80u : 77u) - (blk & 1u) * 2u + (lane < 4u ? 3u : 0u), tp = lane >= 14u ? 0u : tq;
  const unsigned mk = lane >= 4u + ty && lane < 18u ? 1u << (pg < 52u ? (pg >> 3) * 4u : 28u) : 0u;
  const unsigned dyn = (0x3fff0u << ty) & 0x3ffffu;
  const unsigned plm = (unsigned)__builtin_amdgcn_ballot_w64(((pg ^ role ^ sst) & 1u) != 0u) & dyn, plb = plm ^ (wv < 2u ? 0u : dyn);
  const unsigned vgv = 0u + (wv & 1u) * 512u + (lane ^ ((wv & 2u) << 4)) * 8u;
  const unsigned vxr = lds0 + 159744u + lane * 16u, vxw = vxr + wv * 4u;
  const unsigned li = lane & 15, lg = lane >> 4, rl = li & 3, hi = li >> 2;
  const unsigned rb = 768u * li + 16u * (lg ^ ((4u - hi) & 3)) - 3072u * wv;
  unsigned cfa[4], csw[3];
#pragma unroll
  for (unsigned u = 0; u < 4; ++u) cfa[u] = rb + 64u * ((u - rl) & 3);
#pragma unroll
  for (unsigned j = 0; j < 3; ++j) {
    const unsigned e = 3u * wv + j;
    csw[j] = rb + 256u * (e >> 2) + 64u * (((e & 3) + (e >> 2) - rl) & 3);
  }
  const unsigned csr = 16u * lane;
  const unsigned tbase = p0 + (4u * lg + (li >> 2)) * 192u + 16u * (li & 3);
  unsigned woff[3];
#pragma unroll
  for (unsigned j = 0; j < 3; ++j) {
    const unsigned rr = (__umul24(lane, 171u) + 10944u * j) >> 11;
    woff[j] = __umul24(rr, 576u) + __umul24(rr & 12u, 768u) + (16u * lane + 192u * wv);
  }
  asm volatile(
      "s_cmp_eq_u32 s94, 0\n\t"
      "s_cbranch_scc0 .Lc2_%=\n\t"
      "s_add_u32 m0, s41, 0x1e000\n\t"
      "s_mov_b32 s51, 0x0\n\t"
      "buffer_load_dwordx4 v211, s[16:19], s51 offen lds\n\t"
      "buffer_load_dwordx4 v212, s[16:19], s51 offen offset:1024 lds\n\t"
      "buffer_load_dwordx4 v213, s[16:19], s51 offen offset:2048 lds\n\t"
      "s_add_u32 m0, s41, 0x21000\n\t"
      "s_mov_b32 s51, 0xc00\n\t"
      "buffer_load_dwordx4 v211, s[16:19], s51 offen lds\n\t"
      "buffer_load_dwordx4 v212, s[16:19], s51 offen offset:1024 lds\n\t"
      "buffer_load_dwordx4 v213, s[16:19], s51 offen offset:2048 lds\n\t"
      "s_add_u32 m0, s41, 0x24000\n\t"
      "s_mov_b32 s51, 0x6000\n\t"
      "buffer_load_dwordx4 v211, s[16:19], s51 offen lds\n\t"
      "buffer_load_dwordx4 v212, s[16:19], s51 offen offset:1024 lds\n\t"
      "buffer_load_dwordx4 v213, s[16:19], s51 offen offset:2048 lds\n\t"
      "s_add_u32 m0, s41, 0x12000\n\t"
      "s_mov_b32 s51, 0x6c00\n\t"
      "buffer_load_dwordx4 v211, s[16:19], s51 offen lds\n\t"
      "buffer_load_dwordx4 v212, s[16:19], s51 offen offset:1024 lds\n\t"
      "buffer_load_dwordx4 v213, s[16:19], s51 offen offset:2048 lds\n\t"
      ".Lc2_%=:\n\t"
      "s_mov_b32 s62, 0\n\t"
      "s_add_u32 s50, s41, 0x12000\n\t"
      "s_add_u32 s51, s41, 0x12000\n\t"
      "s_sub_u32 s63, s52, 800\n\t"
      "s_andn2_b32 s63, s63, 15\n\t"
      "v_add_u32_e32 v196, s41, v200\n\t"
      "v_add_u32_e32 v197, s41, v201\n\t"
      "v_add_u32_e32 v198, s41, v202\n\t"
      "v_add_u32_e32 v199, s41, v203\n\t"
      "v_add_u32_e32 v217, s51, v204\n\t"
      "v_add_u32_e32 v218, s51, v205\n\t"
      "v_add_u32_e32 v219, s51, v206\n\t"
      "v_add_u32_e32 v220, s51, v207\n\t"
      "v_add_u32_e32 v225, s51, v204\n\t"
      "v_add_u32_e32 v239, s51, v205\n\t"
      "v_add_u32_e32 v210, s51, v206\n\t"
      "v_add_u32_e32 v238, s51, v207\n\t"
      "s_and_b32 s49, s71, 31\n\t"
      "s_lshr_b32 s71, s71, 5\n\t"
      "s_cmp_eq_u32 s94, 0\n\t"
      "s_cbranch_scc0 .Lpb_%=\n\t"
      "s_add_u32 m0, s41, 0x15000\n\t"
      "s_mov_b32 s51, 0xc000\n\t"
      "buffer_load_dwordx4 v211, s[16:19], s51 offen lds\n\t"
      "buffer_load_dwordx4 v212, s[16:19], s51 offen offset:1024 lds\n\t"
      "buffer_load_dwordx4 v213, s[16:19], s51 offen offset:2048 lds\n\t"
      "s_waitcnt vmcnt(21)\n\t"
      "v_lshlrev_b32_e32 v8, 16, v226\n\t"
      "v_and_b32_e32 v9, 0xffff0000, v226\n\t"
      "v_lshlrev_b32_e32 v10, 16, v227\n\t"
      "v_and_b32_e32 v11, 0xffff0000, v227\n\t"
      "v_lshlrev_b32_e32 v12, 16, v228\n\t"
      "v_and_b32_e32 v13, 0xffff0000, v228\n\t"
      "v_lshlrev_b32_e32 v14, 16, v229\n\t"
      "v_and_b32_e32 v15, 0xffff0000, v229\n\t"
      "v_lshlrev_b32_e32 v16, 16, v230\n\t"
      "v_and_b32_e32 v17, 0xffff0000, v230\n\t"
      "v_lshlrev_b32_e32 v18, 16, v231\n\t"
      "v_and_b32_e32 v19, 0xffff0000, v231\n\t"
      "v_lshlrev_b32_e32 v20, 16, v232\n\t"
      "v_and_b32_e32 v21, 0xffff0000, v232\n\t"
      "v_lshlrev_b32_e32 v22, 16, v233\n\t"
      "v_and_b32_e32 v23, 0xffff0000, v233\n\t"
      "v_lshlrev_b32_e32 v24, 16, v234\n\t"
      "v_and_b32_e32 v25, 0xffff0000, v234\n\t"
      "v_lshlrev_b32_e32 v26, 16, v235\n\t"
      "v_and_b32_e32 v27, 0xffff0000, v235\n\t"
      "v_lshlrev_b32_e32 v28, 16, v236\n\t"
      "v_and_b32_e32 v29, 0xffff0000, v236\n\t"
      "v_lshlrev_b32_e32 v30, 16, v237\n\t"
      "v_and_b32_e32 v31, 0xffff0000, v237\n\t"
      "s_add_u32 m0, s41, 0x18000\n\t"
      "s_mov_b32 s51, 0xcc00\n\t"
      "buffer_load_dwordx4 v211, s[16:19], s51 offen lds\n\t"
      "buffer_load_dwordx4 v212, s[16:19], s51 offen offset:1024 lds\n\t"
      "buffer_load_dwordx4 v213, s[16:19], s51 offen offset:2048 lds\n\t"
      "s_add_u32 m0, s41, 0x1b000\n\t"
      "s_mov_b32 s51, 0x12000\n\t"
      "buffer_load_dwordx4 v211, s[16:19], s51 offen lds\n\t"
      "buffer_load_dwordx4 v212, s[16:19], s51 offen offset:1024 lds\n\t"
      "buffer_load_dwordx4 v213, s[16:19], s51 offen offset:2048 lds\n\t"
      "s_mov_b32 s44, 0\n\t"
      "s_mov_b32 s85, 3\n\t"
      "s_mov_b32 s45, s41\n\t"
      "s_add_u32 s46, s41, 0x6000\n\t"
      "s_add_u32 s47, s41, 0xc000\n\t"
      "s_mov_b64 vcc, 0\n\t"
      "s_waitcnt vmcnt(18)\n\t"
      "s_barrier\n\t"
      "ds_read_b64_tr_b16 a[0:1], v214 offset:49152\n\t"
      "ds_read_b64_tr_b16 a[4:5], v214 offset:49160\n\t"
      "ds_read_b64_tr_b16 a[8:9], v214 offset:49216\n\t"
      "ds_read_b64_tr_b16 a[12:13], v214 offset:49224\n\t"
      "ds_read_b64_tr_b16 a[16:17], v214 offset:49280\n\t"
      "ds_read_b64_tr_b16 a[20:21], v214 offset:49288\n\t"
      "ds_read_b128 v[140:143], v196 offset:0\n\t"
      "s_waitcnt vmcnt(15)\n\t"
      "ds_read_b64_tr_b16 a[2:3], v221 offset:0\n\t"
      "ds_read_b64_tr_b16 a[6:7], v221 offset:8\n\t"
      "ds_read_b64_tr_b16 a[10:11], v221 offset:64\n\t"
      "ds_read_b64_tr_b16 a[14:15], v221 offset:72\n\t"
      "ds_read_b64_tr_b16 a[18:19], v221 offset:128\n\t"
      "ds_read_b64_tr_b16 a[22:23], v221 offset:136\n\t"
      "ds_read_b128 v[152:155], v196 offset:12288\n\t"
      "s_waitcnt lgkmcnt(8)\n\t"
      "s_add_u32 m0, s41, 0x1e000\n\t"
      "s_mov_b32 s51, 0x12c00\n\t"
      "buffer_load_dwordx4 v211, s[16:19], s51 offen lds\n\t"
      "buffer_load_dwordx4 v212, s[16:19], s51 offen offset:1024 lds\n\t"
      "buffer_load_dwordx4 v213, s[16:19], s51 offen offset:2048 lds\n\t"
      "s_waitcnt vmcnt(15)\n\t"
      "ds_read_b64_tr_b16 a[24:25], v221 offset:12288\n\t"
      "ds_read_b64_tr_b16 a[28:29], v221 offset:12296\n\t"
      "ds_read_b64_tr_b16 a[32:33], v221 offset:12352\n\t"
      "ds_read_b64_tr_b16 a[36:37], v221 offset:12360\n\t"
      "ds_read_b64_tr_b16 a[40:41], v221 offset:12416\n\t"
      "ds_read_b64_tr_b16 a[44:45], v221 offset:12424\n\t"
      "ds_read_b128 v[144:147], v197 offset:0\n\t"
      "s_waitcnt lgkmcnt(8)\n\t"
      "v_mfma_f32_16x16x32_bf16 v[80:83], a[0:3], v[140:143], v[8:11]\n\t"
      "s_add_u32 m0, s41, 0x21000\n\t"
      "s_mov_b32 s51, 0x18000\n\t"
      "buffer_load_dwordx4 v211, s[16:19], s51 offen lds\n\t"
      "v_mfma_f32_16x16x32_bf16 v[84:87], a[4:7], v[140:143], v[12:15]\n\t"
      "buffer_load_dwordx4 v212, s[16:19], s51 offen offset:1024 lds\n\t"
      "v_mfma_f32_16x16x32_bf16 v[88:91], a[8:11], v[140:143], v[16:19]\n\t"
      "buffer_load_dwordx4 v213, s[16:19], s51 offen offset:2048 lds\n\t"
      "s_waitcnt vmcnt(15)\n\t"
      "ds_read_b64_tr_b16 a[26:27], v214 offset:0\n\t"
      "v_mfma_f32_16x16x32_bf16 v[92:95], a[12:15], v[140:143], v[20:23]\n\t"
      "ds_read_b64_tr_b16 a[30:31], v214 offset:8\n\t"
      "ds_read_b64_tr_b16 a[34:35], v214 offset:64\n\t"
      "v_mfma_f32_16x16x32_bf16 v[96:99], a[16:19], v[140:143], v[24:27]\n\t"
      "ds_read_b64_tr_b16 a[38:39], v214 offset:72\n\t"
      "ds_read_b64_tr_b16 a[42:43], v214 offset:128\n\t"
      "v_mfma_f32_16x16x32_bf16 v[100:103], a[20:23], v[140:143], v[28:31]\n\t"
      "ds_read_b64_tr_b16 a[46:47], v214 offset:136\n\t"
      "ds_read_b128 v[156:159], v197 offset:12288\n\t"
      "s_waitcnt lgkmcnt(8)\n\t"
      "v_mfma_f32_16x16x32_bf16 v[104:107], a[0:3], v[152:155], v[8:11]\n\t"
      "s_add_u32 m0, s41, 0x24000\n\t"
      "s_mov_b32 s51, 0x18c00\n\t"
      "buffer_load_dwordx4 v211, s[16:19], s51 offen lds\n\t"
      "v_mfma_f32_16x16x32_bf16 v[108:111], a[4:7], v[152:155], v[12:15]\n\t"
      "buffer_load_dwordx4 v212, s[16:19], s51 offen offset:1024 lds\n\t"
      "v_mfma_f32_16x16x32_bf16 v[112:115], a[8:11], v[152:155], v[16:19]\n\t"
      "buffer_load_dwordx4 v213, s[16:19], s51 offen offset:2048 lds\n\t"
      "s_waitcnt vmcnt(15)\n\t"
      "ds_read_b64_tr_b16 a[48:49], v214 offset:12288\n\t"
      "v_mfma_f32_16x16x32_bf16 v[116:119], a[12:15], v[152:155], v[20:23]\n\t"
      "ds_read_b64_tr_b16 a[52:53], v214 offset:12296\n\t"
      "ds_read_b64_tr_b16 a[56:57], v214 offset:12352\n\t"
      "v_mfma_f32_16x16x32_bf16 v[120:123], a[16:19], v[152:155], v[24:27]\n\t"
      "ds_read_b64_tr_b16 a[60:61], v214 offset:12360\n\t"
      "ds_read_b64_tr_b16 a[64:65], v214 offset:12416\n\t"
      "v_mfma_f32_16x16x32_bf16 v[132:135], a[20:23], v[152:155], v[28:31]\n\t"
      "ds_read_b64_tr_b16 a[68:69], v214 offset:12424\n\t"
      "ds_read_b128 v[148:151], v198 offset:0\n\t"
      "s_waitcnt lgkmcnt(8)\n\t"
      "v_mfma_f32_16x16x32_bf16 v[80:83], a[24:27], v[144:147], v[80:83]\n\t"
      "s_add_u32 m0, s41, 0x12000\n\t"
      "s_mov_b32 s51, 0x1e000\n\t"
      "buffer_load_dwordx4 v211, s[16:19], s51 offen lds\n\t"
      "v_mfma_f32_16x16x32_bf16 v[84:87], a[28:31], v[144:147], v[84:87]\n\t"
      "buffer_load_dwordx4 v212, s[16:19], s51 offen offset:1024 lds\n\t"
      "v_mfma_f32_16x16x32_bf16 v[88:91], a[32:35], v[144:147], v[88:91]\n\t"
      "buffer_load_dwordx4 v213, s[16:19], s51 offen offset:2048 lds\n\t"
      "s_waitcnt vmcnt(15)\n\t"
      "ds_read_b64_tr_b16 a[50:51], v214 offset:24576\n\t"
      "v_mfma_f32_16x16x32_bf16 v[92:95], a[36:39], v[144:147], v[92:95]\n\t"
      "ds_read_b64_tr_b16 a[54:55], v214 offset:24584\n\t"
      "ds_read_b64_tr_b16 a[58:59], v214 offset:24640\n\t"
      "v_mfma_f32_16x16x32_bf16 v[96:99], a[40:43], v[144:147], v[96:99]\n\t"
      "ds_read_b64_tr_b16 a[62:63], v214 offset:24648\n\t"
      "ds_read_b64_tr_b16 a[66:67], v214 offset:24704\n\t"
      "v_mfma_f32_16x16x32_bf16 v[100:103], a[44:47], v[144:147], v[100:103]\n\t"
      "ds_read_b64_tr_b16 a[70:71], v214 offset:24712\n\t"
      "ds_read_b128 v[160:163], v198 offset:12288\n\t"
      "s_waitcnt lgkmcnt(8)\n\t"
      "v_mfma_f32_16x16x32_bf16 v[104:107], a[24:27], v[156:159], v[104:107]\n\t"
      "s_add_u32 m0, s41, 0x15000\n\t"
      "s_mov_b32 s51, 0x1ec00\n\t"
      "buffer_load_dwordx4 v211, s[16:19], s51 offen lds\n\t"
      "v_mfma_f32_16x16x32_bf16 v[108:111], a[28:31], v[156:159], v[108:111]\n\t"
      "buffer_load_dwordx4 v212, s[16:19], s51 offen offset:1024 lds\n\t"
      "v_mfma_f32_16x16x32_bf16 v[112:115], a[32:35], v[156:159], v[112:115]\n\t"
      "buffer_load_dwordx4 v213, s[16:19], s51 offen offset:2048 lds\n\t"
      "s_waitcnt vmcnt(15)\n\t"
      "ds_read_b64_tr_b16 a[72:73], v214 offset:36864\n\t"
      "v_mfma_f32_16x16x32_bf16 v[116:119], a[36:39], v[156:159], v[116:119]\n\t"
      "ds_read_b64_tr_b16 a[76:77], v214 offset:36872\n\t"
      "ds_read_b64_tr_b16 a[80:81], v214 offset:36928\n\t"
      "v_mfma_f32_16x16x32_bf16 v[120:123], a[40:43], v[156:159], v[120:123]\n\t"
      "ds_read_b64_tr_b16 a[84:85], v214 offset:36936\n\t"
      "ds_read_b64_tr_b16 a[88:89], v214 offset:36992\n\t"
      "v_mfma_f32_16x16x32_bf16 v[132:135], a[44:47], v[156:159], v[132:135]\n\t"
      "ds_read_b64_tr_b16 a[92:93], v214 offset:37000\n\t"
      "ds_read_b128 v[140:143], v199 offset:0\n\t"
      "s_waitcnt lgkmcnt(8)\n\t"
      "v_mfma_f32_16x16x32_bf16 v[80:83], a[48:51], v[148:151], v[80:83]\n\t"
      "s_add_u32 m0, s41, 0x18000\n\t"
      "s_mov_b32 s51, 0x24000\n\t"
      "buffer_load_dwordx4 v211, s[16:19], s51 offen lds\n\t"
      "v_mfma_f32_16x16x32_bf16 v[84:87], a[52:55], v[148:151], v[84:87]\n\t"
      "buffer_load_dwordx4 v212, s[16:19], s51 offen offset:1024 lds\n\t"
      "v_mfma_f32_16x16x32_bf16 v[88:91], a[56:59], v[148:151], v[88:91]\n\t"
      "buffer_load_dwordx4 v213, s[16:19], s51 offen offset:2048 lds\n\t"
      "s_waitcnt vmcnt(15)\n\t"
      "ds_read_b64_tr_b16 a[74:75], v214 offset:49152\n\t"
      "v_mfma_f32_16x16x32_bf16 v[92:95], a[60:63], v[148:151], v[92:95]\n\t"
      "ds_read_b64_tr_b16 a[78:79], v214 offset:49160\n\t"
      "ds_read_b64_tr_b16 a[82:83], v214 offset:49216\n\t"
      "v_mfma_f32_16x16x32_bf16 v[96:99], a[64:67], v[148:151], v[96:99]\n\t"
      "ds_read_b64_tr_b16 a[86:87], v214 offset:49224\n\t"
      "ds_read_b64_tr_b16 a[90:91], v214 offset:49280\n\t"
      "v_mfma_f32_16x16x32_bf16 v[100:103], a[68:71], v[148:151], v[100:103]\n\t"
      "ds_read_b64_tr_b16 a[94:95], v214 offset:49288\n\t"
      "ds_read_b128 v[152:155], v199 offset:12288\n\t"
      "s_waitcnt lgkmcnt(8)\n\t"
      "v_mfma_f32_16x16x32_bf16 v[104:107], a[48:51], v[160:163], v[104:107]\n\t"
      "s_add_u32 m0, s41, 0x1b000\n\t"
      "s_mov_b32 s51, 0x24c00\n\t"
      "buffer_load_dwordx4 v211, s[16:19], s51 offen lds\n\t"
      "v_mfma_f32_16x16x32_bf16 v[108:111], a[52:55], v[160:163], v[108:111]\n\t"
      "buffer_load_dwordx4 v212, s[16:19], s51 offen offset:1024 lds\n\t"
      "v_mfma_f32_16x16x32_bf16 v[112:115], a[56:59], v[160:163], v[112:115]\n\t"
      "buffer_load_dwordx4 v213, s[16:19], s51 offen offset:2048 lds\n\t"
      "s_waitcnt vmcnt(15)\n\t"
      "ds_read_b64_tr_b16 a[96:97], v221 offset:0\n\t"
      "v_mfma_f32_16x16x32_bf16 v[116:119], a[60:63], v[160:163], v[116:119]\n\t"
      "ds_read_b64_tr_b16 a[100:101], v221 offset:8\n\t"
      "ds_read_b64_tr_b16 a[104:105], v221 offset:64\n\t"
      "v_mfma_f32_16x16x32_bf16 v[120:123], a[64:67], v[160:163], v[120:123]\n\t"
      "ds_read_b64_tr_b16 a[108:109], v221 offset:72\n\t"
      "ds_read_b64_tr_b16 a[112:113], v221 offset:128\n\t"
      "v_mfma_f32_16x16x32_bf16 v[132:135], a[68:71], v[160:163], v[132:135]\n\t"
      "ds_read_b64_tr_b16 a[116:117], v221 offset:136\n\t"
      "ds_read_b128 v[144:147], v197 offset:256\n\t"
      "s_waitcnt lgkmcnt(8)\n\t"
      "v_mfma_f32_16x16x32_bf16 v[80:83], a[72:75], v[140:143], v[80:83]\n\t"
      "s_add_u32 m0, s41, 0x1e000\n\t"
      "s_mov_b32 s51, 0x2a000\n\t"
      "buffer_load_dwordx4 v211, s[16:19], s51 offen lds\n\t"
      "v_mfma_f32_16x16x32_bf16 v[84:87], a[76:79], v[140:143], v[84:87]\n\t"
      "buffer_load_dwordx4 v212, s[16:19], s51 offen offset:1024 lds\n\t"
      "v_mfma_f32_16x16x32_bf16 v[88:91], a[80:83], v[140:143], v[88:91]\n\t"
      "buffer_load_dwordx4 v213, s[16:19], s51 offen offset:2048 lds\n\t"
      "s_waitcnt vmcnt(15)\n\t"
      "ds_read_b64_tr_b16 a[98:99], v221 offset:12288\n\t"
      "v_mfma_f32_16x16x32_bf16 v[92:95], a[84:87], v[140:143], v[92:95]\n\t"
      "ds_read_b64_tr_b16 a[102:103], v221 offset:12296\n\t"
      "ds_read_b64_tr_b16 a[106:107], v221 offset:12352\n\t"
      "v_mfma_f32_16x16x32_bf16 v[96:99], a[88:91], v[140:143], v[96:99]\n\t"
      "ds_read_b64_tr_b16 a[110:111], v221 offset:12360\n\t"
      "ds_read_b64_tr_b16 a[114:115], v221 offset:12416\n\t"
      "v_mfma_f32_16x16x32_bf16 v[100:103], a[92:95], v[140:143], v[100:103]\n\t"
      "ds_read_b64_tr_b16 a[118:119], v221 offset:12424\n\t"
      "ds_read_b128 v[156:159], v197 offset:12544\n\t"
      "s_waitcnt lgkmcnt(8)\n\t"
      "v_mfma_f32_16x16x32_bf16 v[104:107], a[72:75], v[152:155], v[104:107]\n\t"
      "s_add_u32 m0, s41, 0x21000\n\t"
      "s_mov_b32 s51, 0x2ac00\n\t"
      "buffer_load_dwordx4 v211, s[16:19], s51 offen lds\n\t"
      "v_mfma_f32_16x16x32_bf16 v[108:111], a[76:79], v[152:155], v[108:111]\n\t"
      "buffer_load_dwordx4 v212, s[16:19], s51 offen offset:1024 lds\n\t"
      "v_mfma_f32_16x16x32_bf16 v[112:115], a[80:83], v[152:155], v[112:115]\n\t"
      "buffer_load_dwordx4 v213, s[16:19], s51 offen offset:2048 lds\n\t"
      "s_waitcnt vmcnt(15)\n\t"
      "ds_read_b64_tr_b16 a[120:121], v214 offset:0\n\t"
      "v_mfma_f32_16x16x32_bf16 v[116:119], a[84:87], v[152:155], v[116:119]\n\t"
      "ds_read_b64_tr_b16 a[124:125], v214 offset:8\n\t"
      "ds_read_b64_tr_b16 a[128:129], v214 offset:64\n\t"
      "v_mfma_f32_16x16x32_bf16 v[120:123], a[88:91], v[152:155], v[120:123]\n\t"
      "ds_read_b64_tr_b16 a[132:133], v214 offset:72\n\t"
      "ds_read_b64_tr_b16 a[136:137], v214 offset:128\n\t"
      "v_mfma_f32_16x16x32_bf16 v[132:135], a[92:95], v[152:155], v[132:135]\n\t"
      "ds_read_b64_tr_b16 a[140:141], v214 offset:136\n\t"
      "ds_read_b128 v[148:151], v198 offset:256\n\t"
      "s_waitcnt lgkmcnt(8)\n\t"
      "v_mfma_f32_16x16x32_bf16 v[80:83], a[96:99], v[144:147], v[80:83]\n\t"
      "s_add_u32 m0, s41, 0x24000\n\t"
      "s_mov_b32 s51, 0x30000\n\t"
      "buffer_load_dwordx4 v211, s[16:19], s51 offen lds\n\t"
      "v_mfma_f32_16x16x32_bf16 v[84:87], a[100:103], v[144:147], v[84:87]\n\t"
      "buffer_load_dwordx4 v212, s[16:19], s51 offen offset:1024 lds\n\t"
      "v_mfma_f32_16x16x32_bf16 v[88:91], a[104:107], v[144:147], v[88:91]\n\t"
      "buffer_load_dwordx4 v213, s[16:19], s51 offen offset:2048 lds\n\t"
      "s_waitcnt vmcnt(15)\n\t"
      "ds_read_b64_tr_b16 a[122:123], v214 offset:12288\n\t"
      "v_mfma_f32_16x16x32_bf16 v[92:95], a[108:111], v[144:147], v[92:95]\n\t"
      "ds_read_b64_tr_b16 a[126:127], v214 offset:12296\n\t"
      "ds_read_b64_tr_b16 a[130:131], v214 offset:12352\n\t"
      "v_mfma_f32_16x16x32_bf16 v[96:99], a[112:115], v[144:147], v[96:99]\n\t"
      "ds_read_b64_tr_b16 a[134:135], v214 offset:12360\n\t"
      "ds_read_b64_tr_b16 a[138:139], v214 offset:12416\n\t"
      "v_mfma_f32_16x16x32_bf16 v[100:103], a[116:119], v[144:147], v[100:103]\n\t"
      "ds_read_b64_tr_b16 a[142:143], v214 offset:12424\n\t"
      "ds_read_b128 v[160:163], v198 offset:12544\n\t"
      "s_waitcnt lgkmcnt(8)\n\t"
      "v_mfma_f32_16x16x32_bf16 v[104:107], a[96:99], v[156:159], v[104:107]\n\t"
      "s_add_u32 m0, s41, 0x12000\n\t"
      "s_mov_b32 s51, 0x30c00\n\t"
      "buffer_load_dwordx4 v211, s[16:19], s51 offen lds\n\t"
      "v_mfma_f32_16x16x32_bf16 v[108:111], a[100:103], v[156:159], v[108:111]\n\t"
      "buffer_load_dwordx4 v212, s[16:19], s51 offen offset:1024 lds\n\t"
      "v_mfma_f32_16x16x32_bf16 v[112:115], a[104:107], v[156:159], v[112:115]\n\t"
      "buffer_load_dwordx4 v213, s[16:19], s51 offen offset:2048 lds\n\t"
      "s_waitcnt vmcnt(15)\n\t"
      "ds_read_b64_tr_b16 a[144:145], v214 offset:24576\n\t"
      "v_mfma_f32_16x16x32_bf16 v[116:119], a[108:111], v[156:159], v[116:119]\n\t"
      "ds_read_b64_tr_b16 a[148:149], v214 offset:24584\n\t"
      "ds_read_b64_tr_b16 a[152:153], v214 offset:24640\n\t"
      "v_mfma_f32_16x16x32_bf16 v[120:123], a[112:115], v[156:159], v[120:123]\n\t"
      "ds_read_b64_tr_b16 a[156:157], v214 offset:24648\n\t"
      "ds_read_b64_tr_b16 a[160:161], v214 offset:24704\n\t"
      "v_mfma_f32_16x16x32_bf16 v[132:135], a[116:119], v[156:159], v[132:135]\n\t"
      "ds_read_b64_tr_b16 a[164:165], v214 offset:24712\n\t"
      "ds_read_b128 v[140:143], v199 offset:256\n\t"
      "s_waitcnt lgkmcnt(8)\n\t"
      "v_mfma_f32_16x16x32_bf16 v[80:83], a[120:123], v[148:151], v[80:83]\n\t"
      "s_add_u32 m0, s41, 0x15000\n\t"
      "s_mov_b32 s51, 0x36000\n\t"
      "buffer_load_dwordx4 v211, s[16:19], s51 offen lds\n\t"
      "v_mfma_f32_16x16x32_bf16 v[84:87], a[124:127], v[148:151], v[84:87]\n\t"
      "buffer_load_dwordx4 v212, s[16:19], s51 offen offset:1024 lds\n\t"
      "v_mfma_f32_16x16x32_bf16 v[88:91], a[128:131], v[148:151], v[88:91]\n\t"
      "buffer_load_dwordx4 v213, s[16:19], s51 offen offset:2048 lds\n\t"
      "s_waitcnt vmcnt(15)\n\t"
      "ds_read_b64_tr_b16 a[146:147], v214 offset:36864\n\t"
      "v_mfma_f32_16x16x32_bf16 v[92:95], a[132:135], v[148:151], v[92:95]\n\t"
      "ds_read_b64_tr_b16 a[150:151], v214 offset:36872\n\t"
      "ds_read_b64_tr_b16 a[154:155], v214 offset:36928\n\t"
      "v_mfma_f32_16x16x32_bf16 v[96:99], a[136:139], v[148:151], v[96:99]\n\t"
      "ds_read_b64_tr_b16 a[158:159], v214 offset:36936\n\t"
      "ds_read_b64_tr_b16 a[162:163], v214 offset:36992\n\t"
      "v_mfma_f32_16x16x32_bf16 v[100:103], a[140:143], v[148:151], v[100:103]\n\t"
      "ds_read_b64_tr_b16 a[166:167], v214 offset:37000\n\t"
      "ds_read_b128 v[152:155], v199 offset:12544\n\t"
      "s_waitcnt lgkmcnt(8)\n\t"
      "v_mfma_f32_16x16x32_bf16 v[104:107], a[120:123], v[160:163], v[104:107]\n\t"
      "s_add_u32 m0, s41, 0x18000\n\t"
      "s_mov_b32 s51, 0x36c00\n\t"
      "buffer_load_dwordx4 v211, s[16:19], s51 offen lds\n\t"
      "v_mfma_f32_16x16x32_bf16 v[108:111], a[124:127], v[160:163], v[108:111]\n\t"
      "buffer_load_dwordx4 v212, s[16:19], s51 offen offset:1024 lds\n\t"
      "v_mfma_f32_16x16x32_bf16 v[112:115], a[128:131], v[160:163], v[112:115]\n\t"
      "buffer_load_dwordx4 v213, s[16:19], s51 offen offset:2048 lds\n\t"
      "s_waitcnt vmcnt(15)\n\t"
      "ds_read_b64_tr_b16 a[168:169], v214 offset:49152\n\t"
      "v_mfma_f32_16x16x32_bf16 v[116:119], a[132:135], v[160:163], v[116:119]\n\t"
      "ds_read_b64_tr_b16 a[172:173], v214 offset:49160\n\t"
      "ds_read_b64_tr_b16 a[176:177], v214 offset:49216\n\t"
      "v_mfma_f32_16x16x32_bf16 v[120:123], a[136:139], v[160:163], v[120:123]\n\t"
      "ds_read_b64_tr_b16 a[180:181], v214 offset:49224\n\t"
      "ds_read_b64_tr_b16 a[184:185], v214 offset:49280\n\t"
      "v_mfma_f32_16x16x32_bf16 v[132:135], a[140:143], v[160:163], v[132:135]\n\t"
      "ds_read_b64_tr_b16 a[188:189], v214 offset:49288\n\t"
      "ds_read_b128 v[144:147], v196 offset:256\n\t"
      "s_waitcnt lgkmcnt(8)\n\t"
      "v_mfma_f32_16x16x32_bf16 v[80:83], a[144:147], v[140:143], v[80:83]\n\t"
      "s_add_u32 m0, s41, 0x1b000\n\t"
      "s_mov_b32 s51, 0x3c000\n\t"
      "buffer_load_dwordx4 v211, s[16:19], s51 offen lds\n\t"
      "v_mfma_f32_16x16x32_bf16 v[84:87], a[148:151], v[140:143], v[84:87]\n\t"
      "buffer_load_dwordx4 v212, s[16:19], s51 offen offset:1024 lds\n\t"
      "v_mfma_f32_16x16x32_bf16 v[88:91], a[152:155], v[140:143], v[88:91]\n\t"
      "buffer_load_dwordx4 v213, s[16:19], s51 offen offset:2048 lds\n\t"
      "s_waitcnt vmcnt(15)\n\t"
      "ds_read_b64_tr_b16 a[170:171], v221 offset:0\n\t"
      "v_mfma_f32_16x16x32_bf16 v[92:95], a[156:159], v[140:143], v[92:95]\n\t"
      "ds_read_b64_tr_b16 a[174:175], v221 offset:8\n\t"
      "ds_read_b64_tr_b16 a[178:179], v221 offset:64\n\t"
      "v_mfma_f32_16x16x32_bf16 v[96:99], a[160:163], v[140:143], v[96:99]\n\t"
      "ds_read_b64_tr_b16 a[182:183], v221 offset:72\n\t"
      "ds_read_b64_tr_b16 a[186:187], v221 offset:128\n\t"
      "v_mfma_f32_16x16x32_bf16 v[100:103], a[164:167], v[140:143], v[100:103]\n\t"
      "ds_read_b64_tr_b16 a[190:191], v221 offset:136\n\t"
      "ds_read_b128 v[156:159], v196 offset:12544\n\t"
      "s_waitcnt lgkmcnt(8)\n\t"
      "v_mfma_f32_16x16x32_bf16 v[104:107], a[144:147], v[152:155], v[104:107]\n\t"
      "s_add_u32 m0, s41, 0x1e000\n\t"
      "s_mov_b32 s51, 0x3cc00\n\t"
      "buffer_load_dwordx4 v211, s[16:19], s51 offen lds\n\t"
      "v_mfma_f32_16x16x32_bf16 v[108:111], a[148:151], v[152:155], v[108:111]\n\t"
      "buffer_load_dwordx4 v212, s[16:19], s51 offen offset:1024 lds\n\t"
      "v_mfma_f32_16x16x32_bf16 v[112:115], a[152:155], v[152:155], v[112:115]\n\t"
      "buffer_load_dwordx4 v213, s[16:19], s51 offen offset:2048 lds\n\t"
      "s_waitcnt vmcnt(15)\n\t"
      "ds_read_b64_tr_b16 a[192:193], v221 offset:12288\n\t"
      "v_mfma_f32_16x16x32_bf16 v[116:119], a[156:159], v[152:155], v[116:119]\n\t"
      "ds_read_b64_tr_b16 a[196:197], v221 offset:12296\n\t"
      "ds_read_b64_tr_b16 a[200:201], v221 offset:12352\n\t"
      "v_mfma_f32_16x16x32_bf16 v[120:123], a[160:163], v[152:155], v[120:123]\n\t"
      "ds_read_b64_tr_b16 a[204:205], v221 offset:12360\n\t"
      "ds_read_b64_tr_b16 a[208:209], v221 offset:12416\n\t"
      "v_mfma_f32_16x16x32_bf16 v[132:135], a[164:167], v[152:155], v[132:135]\n\t"
      "ds_read_b64_tr_b16 a[212:213], v221 offset:12424\n\t"
      "ds_read_b128 v[148:151], v198 offset:512\n\t"
      "s_waitcnt lgkmcnt(8)\n\t"
      "v_mfma_f32_16x16x32_bf16 v[80:83], a[168:171], v[144:147], v[80:83]\n\t"
      "s_add_u32 m0, s41, 0x21000\n\t"
      "s_mov_b32 s51, 0x42000\n\t"
      "buffer_load_dwordx4 v211, s[16:19], s51 offen lds\n\t"
      "v_mfma_f32_16x16x32_bf16 v[84:87], a[172:175], v[144:147], v[84:87]\n\t"
      "buffer_load_dwordx4 v212, s[16:19], s51 offen offset:1024 lds\n\t"
      "v_mfma_f32_16x16x32_bf16 v[88:91], a[176:179], v[144:147], v[88:91]\n\t"
      "buffer_load_dwordx4 v213, s[16:19], s51 offen offset:2048 lds\n\t"
      "s_waitcnt vmcnt(15)\n\t"
      "ds_read_b64_tr_b16 a[194:195], v214 offset:0\n\t"
      "v_mfma_f32_16x16x32_bf16 v[92:95], a[180:183], v[144:147], v[92:95]\n\t"
      "ds_read_b64_tr_b16 a[198:199], v214 offset:8\n\t"
      "ds_read_b64_tr_b16 a[202:203], v214 offset:64\n\t"
      "v_mfma_f32_16x16x32_bf16 v[96:99], a[184:187], v[144:147], v[96:99]\n\t"
      "ds_read_b64_tr_b16 a[206:207], v214 offset:72\n\t"
      "ds_read_b64_tr_b16 a[210:211], v214 offset:128\n\t"
      "v_mfma_f32_16x16x32_bf16 v[100:103], a[188:191], v[144:147], v[100:103]\n\t"
      "ds_read_b64_tr_b16 a[214:215], v214 offset:136\n\t"
      "ds_read_b128 v[160:163], v198 offset:12800\n\t"
      "s_waitcnt lgkmcnt(8)\n\t"
      "v_mfma_f32_16x16x32_bf16 v[104:107], a[168:171], v[156:159], v[104:107]\n\t"
      "s_add_u32 m0, s41, 0x24000\n\t"
      "s_mov_b32 s51, 0x42c00\n\t"
      "buffer_load_dwordx4 v211, s[16:19], s51 offen lds\n\t"
      "v_mfma_f32_16x16x32_bf16 v[108:111], a[172:175], v[156:159], v[108:111]\n\t"
      "buffer_load_dwordx4 v212, s[16:19], s51 offen offset:1024 lds\n\t"
      "v_mfma_f32_16x16x32_bf16 v[112:115], a[176:179], v[156:159], v[112:115]\n\t"
      "buffer_load_dwordx4 v213, s[16:19], s51 offen offset:2048 lds\n\t"
      "s_waitcnt vmcnt(15)\n\t"
      "ds_read_b64_tr_b16 a[216:217], v214 offset:12288\n\t"
      "v_mfma_f32_16x16x32_bf16 v[116:119], a[180:183], v[156:159], v[116:119]\n\t"
      "ds_read_b64_tr_b16 a[220:221], v214 offset:12296\n\t"
      "ds_read_b64_tr_b16 a[224:225], v214 offset:12352\n\t"
      "v_mfma_f32_16x16x32_bf16 v[120:123], a[184:187], v[156:159], v[120:123]\n\t"
      "ds_read_b64_tr_b16 a[228:229], v214 offset:12360\n\t"
      "ds_read_b64_tr_b16 a[232:233], v214 offset:12416\n\t"
      "v_mfma_f32_16x16x32_bf16 v[132:135], a[188:191], v[156:159], v[132:135]\n\t"
      "ds_read_b64_tr_b16 a[236:237], v214 offset:12424\n\t"
      "ds_read_b128 v[140:143], v199 offset:512\n\t"
      "s_waitcnt lgkmcnt(8)\n\t"
      "v_mfma_f32_16x16x32_bf16 v[80:83], a[192:195], v[148:151], v[80:83]\n\t"
      "s_add_u32 m0, s41, 0x6000\n\t"
      "s_add_u32 s51, s40, 0x600000\n\t"
      "buffer_load_dwordx4 v208, s[20:23], s51 offen lds\n\t"
      "v_mfma_f32_16x16x32_bf16 v[84:87], a[196:199], v[148:151], v[84:87]\n\t"
      "buffer_load_dwordx4 v208, s[20:23], s51 offen offset:1024 lds\n\t"
      "v_mfma_f32_16x16x32_bf16 v[88:91], a[200:203], v[148:151], v[88:91]\n\t"
      "buffer_load_dwordx4 v208, s[20:23], s51 offen offset:2048 lds\n\t"
      "s_waitcnt vmcnt(15)\n\t"
      "ds_read_b64_tr_b16 a[218:219], v214 offset:24576\n\t"
      "v_mfma_f32_16x16x32_bf16 v[92:95], a[204:207], v[148:151], v[92:95]\n\t"
      "ds_read_b64_tr_b16 a[222:223], v214 offset:24584\n\t"
      "ds_read_b64_tr_b16 a[226:227], v214 offset:24640\n\t"
      "v_mfma_f32_16x16x32_bf16 v[96:99], a[208:211], v[148:151], v[96:99]\n\t"
      "ds_read_b64_tr_b16 a[230:231], v214 offset:24648\n\t"
      "ds_read_b64_tr_b16 a[234:235], v214 offset:24704\n\t"
      "v_mfma_f32_16x16x32_bf16 v[100:103], a[212:215], v[148:151], v[100:103]\n\t"
      "ds_read_b64_tr_b16 a[238:239], v214 offset:24712\n\t"
      "ds_read_b128 v[152:155], v199 offset:12800\n\t"
      "s_waitcnt lgkmcnt(14)\n\t"
      "v_mfma_f32_16x16x32_bf16 v[104:107], a[192:195], v[160:163], v[104:107]\n\t"
      "s_add_u32 m0, s41, 0x9000\n\t"
      "s_add_u32 s51, s40, 0x600000\n\t"
      "buffer_load_dwordx4 v209, s[20:23], s51 offen lds\n\t"
      "v_mfma_f32_16x16x32_bf16 v[108:111], a[196:199], v[160:163], v[108:111]\n\t"
      "buffer_load_dwordx4 v209, s[20:23], s51 offen offset:1024 lds\n\t"
      "v_mfma_f32_16x16x32_bf16 v[112:115], a[200:203], v[160:163], v[112:115]\n\t"
      "buffer_load_dwordx4 v209, s[20:23], s51 offen offset:2048 lds\n\t"
      "s_waitcnt vmcnt(15)\n\t"
      "ds_read_b64_tr_b16 a[240:241], v214 offset:36864\n\t"
      "v_mfma_f32_16x16x32_bf16 v[116:119], a[204:207], v[160:163], v[116:119]\n\t"
      "ds_read_b64_tr_b16 a[244:245], v214 offset:36872\n\t"
      "ds_read_b64_tr_b16 a[248:249], v214 offset:36928\n\t"
      "v_mfma_f32_16x16x32_bf16 v[120:123], a[208:211], v[160:163], v[120:123]\n\t"
      "ds_read_b64_tr_b16 a[252:253], v214 offset:36936\n\t"
      "ds_read_b64_tr_b16 v[164:165], v214 offset:36992\n\t"
      "v_mfma_f32_16x16x32_bf16 v[132:135], a[212:215], v[160:163], v[132:135]\n\t"
      "ds_read_b64_tr_b16 v[168:169], v214 offset:37000\n\t"
      "ds_read_b128 v[144:147], v196 offset:512\n\t"
      "s_waitcnt lgkmcnt(8)\n\t"
      "v_mfma_f32_16x16x32_bf16 v[80:83], a[216:219], v[140:143], v[80:83]\n\t"
      "s_add_u32 m0, s41, 0xc000\n\t"
      "s_add_u32 s51, s40, 0xc00000\n\t"
      "buffer_load_dwordx4 v208, s[20:23], s51 offen lds\n\t"
      "v_mfma_f32_16x16x32_bf16 v[84:87], a[220:223], v[140:143], v[84:87]\n\t"
      "buffer_load_dwordx4 v208, s[20:23], s51 offen offset:1024 lds\n\t"
      "v_mfma_f32_16x16x32_bf16 v[88:91], a[224:227], v[140:143], v[88:91]\n\t"
      "buffer_load_dwordx4 v208, s[20:23], s51 offen offset:2048 lds\n\t"
      "s_waitcnt vmcnt(15)\n\t"
      "ds_read_b64_tr_b16 a[242:243], v214 offset:49152\n\t"
      "v_mfma_f32_16x16x32_bf16 v[92:95], a[228:231], v[140:143], v[92:95]\n\t"
      "ds_read_b64_tr_b16 a[246:247], v214 offset:49160\n\t"
      "ds_read_b64_tr_b16 a[250:251], v214 offset:49216\n\t"
      "v_mfma_f32_16x16x32_bf16 v[96:99], a[232:235], v[140:143], v[96:99]\n\t"
      "ds_read_b64_tr_b16 a[254:255], v214 offset:49224\n\t"
      "ds_read_b64_tr_b16 v[166:167], v214 offset:49280\n\t"
      "v_mfma_f32_16x16x32_bf16 v[100:103], a[236:239], v[140:143], v[100:103]\n\t"
      "ds_read_b64_tr_b16 v[170:171], v214 offset:49288\n\t"
      "ds_read_b128 v[156:159], v196 offset:12800\n\t"
      "s_waitcnt lgkmcnt(14)\n\t"
      "v_mfma_f32_16x16x32_bf16 v[104:107], a[216:219], v[152:155], v[104:107]\n\t"
      "s_add_u32 m0, s41, 0xf000\n\t"
      "s_add_u32 s51, s40, 0xc00000\n\t"
      "buffer_load_dwordx4 v209, s[20:23], s51 offen lds\n\t"
      "v_mfma_f32_16x16x32_bf16 v[108:111], a[220:223], v[152:155], v[108:111]\n\t"
      "buffer_load_dwordx4 v209, s[20:23], s51 offen offset:1024 lds\n\t"
      "v_mfma_f32_16x16x32_bf16 v[112:115], a[224:227], v[152:155], v[112:115]\n\t"
      "buffer_load_dwordx4 v209, s[20:23], s51 offen offset:2048 lds\n\t"
      "s_waitcnt vmcnt(15)\n\t"
      "ds_read_b64_tr_b16 v[172:173], v221 offset:0\n\t"
      "v_mfma_f32_16x16x32_bf16 v[116:119], a[228:231], v[152:155], v[116:119]\n\t"
      "ds_read_b64_tr_b16 v[176:177], v221 offset:8\n\t"
      "ds_read_b64_tr_b16 v[180:181], v221 offset:64\n\t"
      "ds_read_b64_tr_b16 v[184:185], v221 offset:72\n\t"
      "v_mfma_f32_16x16x32_bf16 v[120:123], a[232:235], v[152:155], v[120:123]\n\t"
      "ds_read_b64_tr_b16 v[188:189], v221 offset:128\n\t"
      "ds_read_b64_tr_b16 v[192:193], v221 offset:136\n\t"
      "v_mfma_f32_16x16x32_bf16 v[132:135], a[236:239], v[152:155], v[132:135]\n\t"
      "ds_read_b128 v[148:151], v197 offset:512\n\t"
      "ds_read_b128 v[160:163], v197 offset:12800\n\t"
      "s_waitcnt lgkmcnt(9)\n\t"
      "v_mfma_f32_16x16x32_bf16 v[80:83], a[240:243], v[144:147], v[80:83]\n\t"
      "buffer_load_dwordx2 v[240:241], v245, s[24:27], 0 offen sc1\n\t"
      "v_mfma_f32_16x16x32_bf16 v[84:87], a[244:247], v[144:147], v[84:87]\n\t"
      "s_cmp_eq_u32 s41, s43\n\t"
      "s_cbranch_scc0 .Lc3_%=\n\t"
      "buffer_load_dword v254, v253, s[24:27], 0 offen sc1\n\t"
      ".Lc3_%=:\n\t"
      "v_mfma_f32_16x16x32_bf16 v[88:91], a[248:251], v[144:147], v[88:91]\n\t"
      "s_waitcnt vmcnt(13)\n\t"
      "ds_read_b64_tr_b16 v[174:175], v221 offset:12288\n\t"
      "v_mfma_f32_16x16x32_bf16 v[92:95], a[252:255], v[144:147], v[92:95]\n\t"
      "ds_read_b64_tr_b16 v[178:179], v221 offset:12296\n\t"
      "ds_read_b64_tr_b16 v[182:183], v221 offset:12352\n\t"
      "v_mfma_f32_16x16x32_bf16 v[96:99], v[164:167], v[144:147], v[96:99]\n\t"
      "ds_read_b64_tr_b16 v[186:187], v221 offset:12360\n\t"
      "ds_read_b64_tr_b16 v[190:191], v221 offset:12416\n\t"
      "v_mfma_f32_16x16x32_bf16 v[100:103], v[168:171], v[144:147], v[100:103]\n\t"
      "ds_read_b64_tr_b16 v[194:195], v221 offset:12424\n\t"
      "s_memrealtime s[80:81]\n\t"
      "v_add_u32_e32 v196, s46, v200\n\t"
      "v_add_u32_e32 v197, s46, v201\n\t"
      "v_add_u32_e32 v198, s46, v202\n\t"
      "v_add_u32_e32 v199, s46, v203\n\t"
      "s_waitcnt vmcnt(10)\n\t"
      "s_waitcnt lgkmcnt(6)\n\t"
      "s_barrier\n\t"
      "v_mfma_f32_16x16x32_bf16 v[104:107], a[240:243], v[156:159], v[104:107]\n\t"
      "ds_read_b128 v[32:35], v196 offset:0\n\t"
      "v_mfma_f32_16x16x32_bf16 v[108:111], a[244:247], v[156:159], v[108:111]\n\t"
      "ds_read_b128 v[36:39], v197 offset:0\n\t"
      "v_mfma_f32_16x16x32_bf16 v[112:115], a[248:251], v[156:159], v[112:115]\n\t"
      "ds_read_b128 v[40:43], v198 offset:0\n\t"
      "v_mfma_f32_16x16x32_bf16 v[116:119], a[252:255], v[156:159], v[116:119]\n\t"
      "ds_read_b128 v[44:47], v199 offset:0\n\t"
      "v_mfma_f32_16x16x32_bf16 v[120:123], v[164:167], v[156:159], v[120:123]\n\t"
      "ds_read_b128 v[48:51], v197 offset:256\n\t"
      "v_mfma_f32_16x16x32_bf16 v[132:135], v[168:171], v[156:159], v[132:135]\n\t"
      "s_waitcnt lgkmcnt(5)\n\t"
      "s_mov_b32 s86, 77\n\t"
      "s_mov_b32 s84, 100\n\t"
      "v_mfma_f32_16x16x32_bf16 v[80:83], v[172:175], v[148:151], v[80:83]\n\t"
      "ds_read_b128 v[52:55], v198 offset:256\n\t"
      "v_mfma_f32_16x16x32_bf16 v[84:87], v[176:179], v[148:151], v[84:87]\n\t"
      "ds_read_b128 v[56:59], v199 offset:256\n\t"
      "v_mfma_f32_16x16x32_bf16 v[88:91], v[180:183], v[148:151], v[88:91]\n\t"
      "ds_read_b128 v[60:63], v196 offset:256\n\t"
      "v_mfma_f32_16x16x32_bf16 v[92:95], v[184:187], v[148:151], v[92:95]\n\t"
      "ds_read_b128 v[64:67], v198 offset:512\n\t"
      "v_mfma_f32_16x16x32_bf16 v[96:99], v[188:191], v[148:151], v[96:99]\n\t"
      "ds_read_b128 v[68:71], v199 offset:512\n\t"
      "v_mfma_f32_16x16x32_bf16 v[100:103], v[192:195], v[148:151], v[100:103]\n\t"
      "ds_read_b128 v[72:75], v196 offset:512\n\t"
      "v_mfma_f32_16x16x32_bf16 v[104:107], v[172:175], v[160:163], v[104:107]\n\t"
      "ds_read_b128 v[76:79], v197 offset:512\n\t"
      "v_mfma_f32_16x16x32_bf16 v[108:111], v[176:179], v[160:163], v[108:111]\n\t"
      "v_mfma_f32_16x16x32_bf16 v[112:115], v[180:183], v[160:163], v[112:115]\n\t"
      "v_mfma_f32_16x16x32_bf16 v[116:119], v[184:187], v[160:163], v[116:119]\n\t"
      "v_mfma_f32_16x16x32_bf16 v[120:123], v[188:191], v[160:163], v[120:123]\n\t"
      "v_mfma_f32_16x16x32_bf16 v[132:135], v[192:195], v[160:163], v[132:135]\n\t"
      ".Lp2_%=:\n\t"
      "s_cbranch_vccnz .Lwsb_%=\n\t"
      "s_waitcnt lgkmcnt(8)\n\t"
      "v_mfma_f32_16x16x32_bf16 v[128:131], a[0:3], v[32:35], v[8:11]\n\t"
      "v_cvt_pk_bf16_f32 v136, v120, v121\n\t"
      "v_mfma_f32_16x16x32_bf16 v[128:131], a[24:27], v[36:39], v[128:131]\n\t"
      "v_cvt_pk_bf16_f32 v137, v122, v123\n\t"
      "v_mfma_f32_16x16x32_bf16 v[128:131], a[48:51], v[40:43], v[128:131]\n\t"
      "v_cvt_pk_bf16_f32 v138, v132, v133\n\t"
      "v_mfma_f32_16x16x32_bf16 v[128:131], a[72:75], v[44:47], v[128:131]\n\t"
      "v_cvt_pk_bf16_f32 v139, v134, v135\n\t"
      "s_waitcnt lgkmcnt(4)\n\t"
      "v_mfma_f32_16x16x32_bf16 v[128:131], a[96:99], v[48:51], v[128:131]\n\t"
      "ds_write_b128 v219, v[136:139] offset:12288\n\t"
      "v_mfma_f32_16x16x32_bf16 v[128:131], a[120:123], v[52:55], v[128:131]\n\t"
      "v_cvt_pk_bf16_f32 v80, v80, v81\n\t"
      "v_mfma_f32_16x16x32_bf16 v[128:131], a[144:147], v[56:59], v[128:131]\n\t"
      "v_cvt_pk_bf16_f32 v81, v82, v83\n\t"
      "v_mfma_f32_16x16x32_bf16 v[128:131], a[168:171], v[60:63], v[128:131]\n\t"
      "v_cvt_pk_bf16_f32 v82, v84, v85\n\t"
      "s_waitcnt lgkmcnt(1)\n\t"
      "v_mfma_f32_16x16x32_bf16 v[128:131], a[192:195], v[64:67], v[128:131]\n\t"
      "v_cvt_pk_bf16_f32 v83, v86, v87\n\t"
      "v_mfma_f32_16x16x32_bf16 v[128:131], a[216:219], v[68:71], v[128:131]\n\t"
      "ds_write_b128 v225, v[80:83] offset:0\n\t"
      "v_mfma_f32_16x16x32_bf16 v[128:131], a[240:243], v[72:75], v[128:131]\n\t"
      "v_cvt_pk_bf16_f32 v88, v88, v89\n\t"
      "v_mfma_f32_16x16x32_bf16 v[128:131], v[172:175], v[76:79], v[128:131]\n\t"
      "v_mfma_f32_16x16x32_bf16 v[132:135], a[4:7], v[32:35], v[12:15]\n\t"
      "v_cvt_pk_bf16_f32 v89, v90, v91\n\t"
      "v_mfma_f32_16x16x32_bf16 v[132:135], a[28:31], v[36:39], v[132:135]\n\t"
      "v_cvt_pk_bf16_f32 v90, v92, v93\n\t"
      "v_mfma_f32_16x16x32_bf16 v[132:135], a[52:55], v[40:43], v[132:135]\n\t"
      "v_cvt_pk_bf16_f32 v136, v128, v129\n\t"
      "v_cvt_pk_bf16_f32 v91, v94, v95\n\t"
      "v_mfma_f32_16x16x32_bf16 v[132:135], a[76:79], v[44:47], v[132:135]\n\t"
      "v_cvt_pk_bf16_f32 v137, v130, v131\n\t"
      "ds_write_b128 v239, v[88:91] offset:0\n\t"
      "v_mfma_f32_16x16x32_bf16 v[132:135], a[100:103], v[48:51], v[132:135]\n\t"
      "v_cvt_pk_bf16_f32 v96, v96, v97\n\t"
      "s_add_u32 s48, s47, 0x6000\n\t"
      "v_mfma_f32_16x16x32_bf16 v[132:135], a[124:127], v[52:55], v[132:135]\n\t"
      "s_cmp_eq_u32 s48, s50\n\t"
      "s_cselect_b32 s48, s41, s48\n\t"
      "v_mfma_f32_16x16x32_bf16 v[132:135], a[148:151], v[56:59], v[132:135]\n\t"
      "v_cvt_pk_bf16_f32 v97, v98, v99\n\t"
      "v_mfma_f32_16x16x32_bf16 v[132:135], a[172:175], v[60:63], v[132:135]\n\t"
      "v_cvt_pk_bf16_f32 v98, v100, v101\n\t"
      "v_add_u32_e32 v217, s46, v204\n\t"
      "v_mfma_f32_16x16x32_bf16 v[132:135], a[196:199], v[64:67], v[132:135]\n\t"
      "v_cvt_pk_bf16_f32 v99, v102, v103\n\t"
      "v_add_u32_e32 v218, s46, v205\n\t"
      "v_mfma_f32_16x16x32_bf16 v[132:135], a[220:223], v[68:71], v[132:135]\n\t"
      "v_readlane_b32 s73, v248, s85\n\t"
      "v_add_u32_e32 v219, s46, v206\n\t"
      "v_mfma_f32_16x16x32_bf16 v[132:135], a[244:247], v[72:75], v[132:135]\n\t"
      "s_mov_b32 m0, s45\n\t"
      "v_mfma_f32_16x16x32_bf16 v[132:135], v[176:179], v[76:79], v[132:135]\n\t"
      "s_waitcnt lgkmcnt(0)\n\t"
      "s_barrier\n\t"
      "v_mfma_f32_16x16x32_bf16 v[128:131], a[8:11], v[32:35], v[16:19]\n\t"
      "ds_write_b128 v210, v[96:99] offset:0\n\t"
      "s_mov_b32 s82, s80\n\t"
      "v_mfma_f32_16x16x32_bf16 v[128:131], a[32:35], v[36:39], v[128:131]\n\t"
      "v_cvt_pk_bf16_f32 v104, v104, v105\n\t"
      "s_memrealtime s[80:81]\n\t"
      "v_mfma_f32_16x16x32_bf16 v[128:131], a[56:59], v[40:43], v[128:131]\n\t"
      "v_cvt_pk_bf16_f32 v138, v132, v133\n\t"
      "v_cvt_pk_bf16_f32 v105, v106, v107\n\t"
      "v_mfma_f32_16x16x32_bf16 v[128:131], a[80:83], v[44:47], v[128:131]\n\t"
      "v_cvt_pk_bf16_f32 v139, v134, v135\n\t"
      "v_cvt_pk_bf16_f32 v106, v108, v109\n\t"
      "v_mfma_f32_16x16x32_bf16 v[128:131], a[104:107], v[48:51], v[128:131]\n\t"
      "ds_write_b128 v217, v[136:139] offset:0\n\t"
      "v_mfma_f32_16x16x32_bf16 v[128:131], a[128:131], v[52:55], v[128:131]\n\t"
      "buffer_load_dwordx4 v208, s[20:23], s73 offen lds\n\t"
      "v_mfma_f32_16x16x32_bf16 v[128:131], a[152:155], v[56:59], v[128:131]\n\t"
      "v_cvt_pk_bf16_f32 v107, v110, v111\n\t"
      "v_mfma_f32_16x16x32_bf16 v[128:131], a[176:179], v[60:63], v[128:131]\n\t"
      "ds_write_b128 v225, v[104:107] offset:12288\n\t"
      "v_mfma_f32_16x16x32_bf16 v[128:131], a[200:203], v[64:67], v[128:131]\n\t"
      "v_cvt_pk_bf16_f32 v112, v112, v113\n\t"
      "v_mfma_f32_16x16x32_bf16 v[128:131], a[224:227], v[68:71], v[128:131]\n\t"
      "v_cvt_pk_bf16_f32 v113, v114, v115\n\t"
      "v_mfma_f32_16x16x32_bf16 v[128:131], a[248:251], v[72:75], v[128:131]\n\t"
      "v_cvt_pk_bf16_f32 v114, v116, v117\n\t"
      "v_mfma_f32_16x16x32_bf16 v[128:131], v[180:183], v[76:79], v[128:131]\n\t"
      "v_mfma_f32_16x16x32_bf16 v[132:135], a[12:15], v[32:35], v[20:23]\n\t"
      "v_cvt_pk_bf16_f32 v115, v118, v119\n\t"
      "v_mfma_f32_16x16x32_bf16 v[132:135], a[36:39], v[36:39], v[132:135]\n\t"
      "buffer_load_dwordx4 v208, s[20:23], s73 offen offset:1024 lds\n\t"
      "v_mfma_f32_16x16x32_bf16 v[132:135], a[60:63], v[40:43], v[132:135]\n\t"
      "v_cvt_pk_bf16_f32 v136, v128, v129\n\t"
      "ds_write_b128 v239, v[112:115] offset:12288\n\t"
      "v_mfma_f32_16x16x32_bf16 v[132:135], a[84:87], v[44:47], v[132:135]\n\t"
      "v_cvt_pk_bf16_f32 v137, v130, v131\n\t"
      "v_mfma_f32_16x16x32_bf16 v[132:135], a[108:111], v[48:51], v[132:135]\n\t"
      "v_mfma_f32_16x16x32_bf16 v[132:135], a[132:135], v[52:55], v[132:135]\n\t"
      "v_mfma_f32_16x16x32_bf16 v[132:135], a[156:159], v[56:59], v[132:135]\n\t"
      "v_mfma_f32_16x16x32_bf16 v[132:135], a[180:183], v[60:63], v[132:135]\n\t"
      "v_mfma_f32_16x16x32_bf16 v[132:135], a[204:207], v[64:67], v[132:135]\n\t"
      "v_mfma_f32_16x16x32_bf16 v[132:135], a[228:231], v[68:71], v[132:135]\n\t"
      "buffer_load_dwordx4 v208, s[20:23], s73 offen offset:2048 lds\n\t"
      "v_mfma_f32_16x16x32_bf16 v[132:135], a[252:255], v[72:75], v[132:135]\n\t"
      "v_readlane_b32 s73, v249, s85\n\t"
      "s_add_u32 m0, s45, 0x3000\n\t"
      "v_mfma_f32_16x16x32_bf16 v[132:135], v[184:187], v[76:79], v[132:135]\n\t"
      "s_waitcnt vmcnt(10)\n\t"
      "s_barrier\n\t"
      "v_mfma_f32_16x16x32_bf16 v[128:131], a[16:19], v[32:35], v[24:27]\n\t"
      "ds_read_b128 v[80:83], v196 offset:12288\n\t"
      "v_mfma_f32_16x16x32_bf16 v[128:131], a[40:43], v[36:39], v[128:131]\n\t"
      "v_mfma_f32_16x16x32_bf16 v[128:131], a[64:67], v[40:43], v[128:131]\n\t"
      "ds_read_b128 v[84:87], v197 offset:12288\n\t"
      "v_cvt_pk_bf16_f32 v138, v132, v133\n\t"
      "v_mfma_f32_16x16x32_bf16 v[128:131], a[88:91], v[44:47], v[128:131]\n\t"
      "ds_read_b128 v[88:91], v198 offset:12288\n\t"
      "v_cvt_pk_bf16_f32 v139, v134, v135\n\t"
      "v_mfma_f32_16x16x32_bf16 v[128:131], a[112:115], v[48:51], v[128:131]\n\t"
      "ds_write_b128 v218, v[136:139] offset:0\n\t"
      "v_mfma_f32_16x16x32_bf16 v[128:131], a[136:139], v[52:55], v[128:131]\n\t"
      "buffer_load_dwordx4 v209, s[20:23], s73 offen lds\n\t"
      "s_waitcnt vmcnt(10)\n\t"
      "s_cmp_eq_u32 s94, 0\n\t"
      "s_cbranch_scc0 .Lwgvw_0_%=\n\t"
      "s_waitcnt vmcnt(4)\n\t"
      ".Lwgvw_0_%=:\n\t"
      "v_subrev_u32_e32 v242, s63, v240\n\t"
      "v_xor_b32_e32 v241, s53, v241\n\t"
      "v_mfma_f32_16x16x32_bf16 v[128:131], a[160:163], v[56:59], v[128:131]\n\t"
      "ds_read_b128 v[92:95], v199 offset:12288\n\t"
      "v_lshrrev_b32_e32 v240, 13, v242\n\t"
      "v_or_b32_e32 v240, v240, v241\n\t"
      "v_mfma_f32_16x16x32_bf16 v[128:131], a[184:187], v[60:63], v[128:131]\n\t"
      "ds_read_b128 v[96:99], v197 offset:12544\n\t"
      "v_mfma_f32_16x16x32_bf16 v[128:131], a[208:211], v[64:67], v[128:131]\n\t"
      "v_cmp_ne_u32_e32 vcc, 0, v240\n\t"
      "v_and_b32_e32 v242, 15, v242\n\t"
      "v_mfma_f32_16x16x32_bf16 v[128:131], a[232:235], v[68:71], v[128:131]\n\t"
      "v_mfma_f32_16x16x32_bf16 v[128:131], v[164:167], v[72:75], v[128:131]\n\t"
      "ds_read_b128 v[100:103], v198 offset:12544\n\t"
      "s_cbranch_vccnz .Lgs_%=\n\t"
      ".Lgr_%=:\n\t"
      "ds_write_b32 v246, v242\n\t"
      "v_mfma_f32_16x16x32_bf16 v[128:131], v[188:191], v[76:79], v[128:131]\n\t"
      "ds_read_b128 v[104:107], v199 offset:12544\n\t"
      "v_mfma_f32_16x16x32_bf16 v[132:135], a[20:23], v[32:35], v[28:31]\n\t"
      "ds_read_b128 v[108:111], v196 offset:12544\n\t"
      "v_mfma_f32_16x16x32_bf16 v[132:135], a[44:47], v[36:39], v[132:135]\n\t"
      "buffer_load_dwordx4 v209, s[20:23], s73 offen offset:1024 lds\n\t"
      "v_mfma_f32_16x16x32_bf16 v[132:135], a[68:71], v[40:43], v[132:135]\n\t"
      "ds_read_b128 v[112:115], v198 offset:12800\n\t"
      "v_cvt_pk_bf16_f32 v136, v128, v129\n\t"
      "v_add_u32_e32 v220, s46, v207\n\t"
      "v_mfma_f32_16x16x32_bf16 v[132:135], a[92:95], v[44:47], v[132:135]\n\t"
      "ds_read_b128 v[116:119], v199 offset:12800\n\t"
      "v_cvt_pk_bf16_f32 v137, v130, v131\n\t"
      "s_sub_u32 s84, s84, 4\n\t"
      "s_max_i32 s84, s84, s86\n\t"
      "v_mfma_f32_16x16x32_bf16 v[132:135], a[116:119], v[48:51], v[132:135]\n\t"
      "s_sub_u32 s83, s82, s80\n\t"
      "v_mfma_f32_16x16x32_bf16 v[132:135], a[140:143], v[52:55], v[132:135]\n\t"
      "s_max_i32 s83, s83, 0\n\t"
      "s_add_u32 s82, s80, s83\n\t"
      "v_mfma_f32_16x16x32_bf16 v[132:135], a[164:167], v[56:59], v[132:135]\n\t"
      "ds_read_b128 v[120:123], v196 offset:12800\n\t"
      "v_mfma_f32_16x16x32_bf16 v[132:135], a[188:191], v[60:63], v[132:135]\n\t"
      "ds_read_b128 v[124:127], v197 offset:12800\n\t"
      "v_mfma_f32_16x16x32_bf16 v[132:135], a[212:215], v[64:67], v[132:135]\n\t"
      "v_mfma_f32_16x16x32_bf16 v[132:135], a[236:239], v[68:71], v[132:135]\n\t"
      "s_add_u32 s82, s82, s84\n\t"
      "v_mfma_f32_16x16x32_bf16 v[132:135], v[168:171], v[72:75], v[132:135]\n\t"
      "s_add_u32 s44, s44, 1\n\t"
      "s_add_u32 s85, s85, 1\n\t"
      "v_mfma_f32_16x16x32_bf16 v[132:135], v[192:195], v[76:79], v[132:135]\n\t"
      "s_waitcnt lgkmcnt(9)\n\t"
      "v_mfma_f32_16x16x32_bf16 v[128:131], a[0:3], v[80:83], v[8:11]\n\t"
      "v_mfma_f32_16x16x32_bf16 v[128:131], a[24:27], v[84:87], v[128:131]\n\t"
      "v_readlane_b32 s72, v248, s44\n\t"
      "v_mfma_f32_16x16x32_bf16 v[128:131], a[48:51], v[88:91], v[128:131]\n\t"
      "v_cvt_pk_bf16_f32 v138, v132, v133\n\t"
      "v_mfma_f32_16x16x32_bf16 v[128:131], a[72:75], v[92:95], v[128:131]\n\t"
      "v_cvt_pk_bf16_f32 v139, v134, v135\n\t"
      "s_waitcnt lgkmcnt(4)\n\t"
      "v_mfma_f32_16x16x32_bf16 v[128:131], a[96:99], v[96:99], v[128:131]\n\t"
      "ds_write_b128 v219, v[136:139] offset:0\n\t"
      "v_mfma_f32_16x16x32_bf16 v[128:131], a[120:123], v[100:103], v[128:131]\n\t"
      "v_mfma_f32_16x16x32_bf16 v[128:131], a[144:147], v[104:107], v[128:131]\n\t"
      "buffer_load_dwordx4 v209, s[20:23], s73 offen offset:2048 lds\n\t"
      "v_mfma_f32_16x16x32_bf16 v[128:131], a[168:171], v[108:111], v[128:131]\n\t"
      "v_add_u32_e32 v196, s47, v200\n\t"
      "s_waitcnt lgkmcnt(1)\n\t"
      "v_mfma_f32_16x16x32_bf16 v[128:131], a[192:195], v[112:115], v[128:131]\n\t"
      "v_add_u32_e32 v197, s47, v201\n\t"
      "v_mfma_f32_16x16x32_bf16 v[128:131], a[216:219], v[116:119], v[128:131]\n\t"
      "v_add_u32_e32 v198, s47, v202\n\t"
      "v_mfma_f32_16x16x32_bf16 v[128:131], a[240:243], v[120:123], v[128:131]\n\t"
      "v_add_u32_e32 v199, s47, v203\n\t"
      "v_mfma_f32_16x16x32_bf16 v[128:131], v[172:175], v[124:127], v[128:131]\n\t"
      "s_cmp_gt_i32 s83, 1\n\t"
      "s_cbranch_scc1 .LslpP3_%=\n\t"
      ".LretP3_%=:\n\t"
      "s_cmp_eq_u32 s94, 1\n\t"
      "s_cbranch_scc0 .LwP3barv_1_%=\n\t"
      "s_waitcnt vmcnt(9)\n\t"
      ".LwP3barv_1_%=:\n\t"
      "s_waitcnt lgkmcnt(0)\n\t"
      "s_barrier\n\t"
      "v_mfma_f32_16x16x32_bf16 v[132:135], a[4:7], v[80:83], v[12:15]\n\t"
      "ds_read_b128 v[140:143], v220 offset:0\n\t"
      "v_mfma_f32_16x16x32_bf16 v[132:135], a[28:31], v[84:87], v[132:135]\n\t"
      "ds_read_b128 v[240:243], v247 offset:0\n\t"
      "v_mfma_f32_16x16x32_bf16 v[132:135], a[52:55], v[88:91], v[132:135]\n\t"
      "v_cvt_pk_bf16_f32 v136, v128, v129\n\t"
      "v_mfma_f32_16x16x32_bf16 v[132:135], a[76:79], v[92:95], v[132:135]\n\t"
      "ds_read_b128 v[32:35], v196 offset:0\n\t"
      "v_cvt_pk_bf16_f32 v137, v130, v131\n\t"
      "v_mfma_f32_16x16x32_bf16 v[132:135], a[100:103], v[96:99], v[132:135]\n\t"
      "v_readlane_b32 s68, v248, s94\n\t"
      "v_mfma_f32_16x16x32_bf16 v[132:135], a[124:127], v[100:103], v[132:135]\n\t"
      "ds_read_b128 v[152:155], v238 offset:0\n\t"
      "v_mfma_f32_16x16x32_bf16 v[132:135], a[148:151], v[104:107], v[132:135]\n\t"
      "ds_read_b128 v[36:39], v197 offset:0\n\t"
      "v_mfma_f32_16x16x32_bf16 v[132:135], a[172:175], v[108:111], v[132:135]\n\t"
      "v_mfma_f32_16x16x32_bf16 v[132:135], a[196:199], v[112:115], v[132:135]\n\t"
      "s_waitcnt lgkmcnt(3)\n\t"
      "v_add3_u32 v240, v240, v241, v242\n\t"
      "v_mfma_f32_16x16x32_bf16 v[132:135], a[220:223], v[116:119], v[132:135]\n\t"
      "ds_read_b128 v[40:43], v198 offset:0\n\t"
      "v_mfma_f32_16x16x32_bf16 v[132:135], a[244:247], v[120:123], v[132:135]\n\t"
      "v_add_u32_e32 v240, v240, v243\n\t"
      "v_mfma_f32_16x16x32_bf16 v[132:135], v[176:179], v[124:127], v[132:135]\n\t"
      "buffer_store_dwordx4 v[140:143], v208, s[28:31], s72 offen sc1\n\t"
      "v_mfma_f32_16x16x32_bf16 v[128:131], a[8:11], v[80:83], v[16:19]\n\t"
      "ds_read_b128 v[44:47], v199 offset:0\n\t"
      "v_mfma_f32_16x16x32_bf16 v[128:131], a[32:35], v[84:87], v[128:131]\n\t"
      "v_add_u32_dpp v241, v240, v240 quad_perm:[1,0,3,2] row_mask:0xf bank_mask:0xf\n\t"
      "s_memrealtime s[80:81]\n\t"
      "v_mfma_f32_16x16x32_bf16 v[128:131], a[56:59], v[88:91], v[128:131]\n\t"
      "v_cvt_pk_bf16_f32 v138, v132, v133\n\t"
      "v_mfma_f32_16x16x32_bf16 v[128:131], a[80:83], v[92:95], v[128:131]\n\t"
      "v_cvt_pk_bf16_f32 v139, v134, v135\n\t"
      "v_mfma_f32_16x16x32_bf16 v[128:131], a[104:107], v[96:99], v[128:131]\n\t"
      "ds_write_b128 v217, v[136:139] offset:12288\n\t"
      "v_add_u32_dpp v240, v241, v241 quad_perm:[2,3,0,1] row_mask:0xf bank_mask:0xf\n\t"
      "v_mfma_f32_16x16x32_bf16 v[128:131], a[128:131], v[100:103], v[128:131]\n\t"
      "s_mov_b32 m0, s46\n\t"
      "v_mfma_f32_16x16x32_bf16 v[128:131], a[152:155], v[104:107], v[128:131]\n\t"
      "s_waitcnt lgkmcnt(4)\n\t"
      "buffer_store_dwordx4 v[152:155], v208, s[28:31], s68 offen sc1\n\t"
      "v_cmp_lt_u32_e64 s[76:77], 16, v240\n\t"
      "v_cmp_lt_u32_e32 vcc, 63, v240\n\t"
      "v_mfma_f32_16x16x32_bf16 v[128:131], a[176:179], v[108:111], v[128:131]\n\t"
      "ds_read_b128 v[48:51], v197 offset:256\n\t"
      "v_mfma_f32_16x16x32_bf16 v[128:131], a[200:203], v[112:115], v[128:131]\n\t"
      "v_mfma_f32_16x16x32_bf16 v[128:131], a[224:227], v[116:119], v[128:131]\n\t"
      "ds_read_b128 v[52:55], v198 offset:256\n\t"
      "v_mfma_f32_16x16x32_bf16 v[128:131], a[248:251], v[120:123], v[128:131]\n\t"
      "ds_read_b128 v[156:159], v238 offset:1024\n\t"
      "s_and_b32 s64, s76, 0x11111111\n\t"
      "s_cmp_lg_u64 vcc, 0\n\t"
      "s_cselect_b32 s64, 0, s64\n\t"
      "v_mfma_f32_16x16x32_bf16 v[128:131], v[180:183], v[124:127], v[128:131]\n\t"
      "v_mfma_f32_16x16x32_bf16 v[132:135], a[12:15], v[80:83], v[20:23]\n\t"
      "ds_read_b128 v[56:59], v199 offset:256\n\t"
      "v_mfma_f32_16x16x32_bf16 v[132:135], a[36:39], v[84:87], v[132:135]\n\t"
      "v_and_b32_e32 v240, s64, v252\n\t"
      "v_mfma_f32_16x16x32_bf16 v[132:135], a[60:63], v[88:91], v[132:135]\n\t"
      "v_cvt_pk_bf16_f32 v136, v128, v129\n\t"
      "v_mfma_f32_16x16x32_bf16 v[132:135], a[84:87], v[92:95], v[132:135]\n\t"
      "v_cvt_pk_bf16_f32 v137, v130, v131\n\t"
      "ds_read_b128 v[144:147], v220 offset:1024\n\t"
      "v_mfma_f32_16x16x32_bf16 v[132:135], a[108:111], v[96:99], v[132:135]\n\t"
      "v_cmp_ne_u32_e64 s[76:77], 0, v240\n\t"
      "v_mfma_f32_16x16x32_bf16 v[132:135], a[132:135], v[100:103], v[132:135]\n\t"
      "s_waitcnt lgkmcnt(2)\n\t"
      "buffer_store_dwordx4 v[156:159], v208, s[28:31], s68 offen offset:1024 sc1\n\t"
      "v_mfma_f32_16x16x32_bf16 v[132:135], a[156:159], v[104:107], v[132:135]\n\t"
      "ds_read_b128 v[60:63], v196 offset:256\n\t"
      "v_mfma_f32_16x16x32_bf16 v[132:135], a[180:183], v[108:111], v[132:135]\n\t"
      "s_xor_b32 s74, s74, s76\n\t"
      "s_xor_b32 s76, s75, s76\n\t"
      "v_mfma_f32_16x16x32_bf16 v[132:135], a[204:207], v[112:115], v[132:135]\n\t"
      "v_cndmask_b32_e64 v249, v249, v251, s[76:77]\n\t"
      "v_mfma_f32_16x16x32_bf16 v[132:135], a[228:231], v[116:119], v[132:135]\n\t"
      "ds_read_b128 v[64:67], v198 offset:512\n\t"
      "v_mfma_f32_16x16x32_bf16 v[132:135], a[252:255], v[120:123], v[132:135]\n\t"
      "s_mov_b32 s76, s74\n\t"
      "v_cndmask_b32_e64 v248, v248, v251, s[76:77]\n\t"
      "v_mfma_f32_16x16x32_bf16 v[132:135], v[184:187], v[124:127], v[132:135]\n\t"
      "s_waitcnt lgkmcnt(2)\n\t"
      "buffer_store_dwordx4 v[144:147], v208, s[28:31], s72 offen offset:1024 sc1\n\t"
      "v_mfma_f32_16x16x32_bf16 v[128:131], a[16:19], v[80:83], v[24:27]\n\t"
      "ds_read_b128 v[68:71], v199 offset:512\n\t"
      "v_mfma_f32_16x16x32_bf16 v[128:131], a[40:43], v[84:87], v[128:131]\n\t"
      "v_mfma_f32_16x16x32_bf16 v[128:131], a[64:67], v[88:91], v[128:131]\n\t"
      "v_cvt_pk_bf16_f32 v138, v132, v133\n\t"
      "v_readlane_b32 s73, v248, s85\n\t"
      "v_mfma_f32_16x16x32_bf16 v[128:131], a[88:91], v[92:95], v[128:131]\n\t"
      "v_cvt_pk_bf16_f32 v139, v134, v135\n\t"
      "ds_read_b128 v[160:163], v238 offset:2048\n\t"
      "v_mfma_f32_16x16x32_bf16 v[128:131], a[112:115], v[96:99], v[128:131]\n\t"
      "ds_write_b128 v218, v[136:139] offset:12288\n\t"
      "v_mfma_f32_16x16x32_bf16 v[128:131], a[136:139], v[100:103], v[128:131]\n\t"
      "v_mfma_f32_16x16x32_bf16 v[128:131], a[160:163], v[104:107], v[128:131]\n\t"
      "v_mfma_f32_16x16x32_bf16 v[128:131], a[184:187], v[108:111], v[128:131]\n\t"
      "ds_read_b128 v[148:151], v220 offset:2048\n\t"
      "v_mfma_f32_16x16x32_bf16 v[128:131], a[208:211], v[112:115], v[128:131]\n\t"
      "buffer_load_dwordx4 v208, s[20:23], s73 offen lds\n\t"
      "v_mfma_f32_16x16x32_bf16 v[128:131], a[232:235], v[116:119], v[128:131]\n\t"
      "ds_read_b128 v[72:75], v196 offset:512\n\t"
      "v_mfma_f32_16x16x32_bf16 v[128:131], v[164:167], v[120:123], v[128:131]\n\t"
      "v_readlane_b32 s86, v250, s44\n\t"
      "v_mfma_f32_16x16x32_bf16 v[128:131], v[188:191], v[124:127], v[128:131]\n\t"
      "v_mfma_f32_16x16x32_bf16 v[132:135], a[20:23], v[80:83], v[28:31]\n\t"
      "ds_read_b128 v[76:79], v197 offset:512\n\t"
      "v_mfma_f32_16x16x32_bf16 v[132:135], a[44:47], v[84:87], v[132:135]\n\t"
      "buffer_load_dwordx4 v208, s[20:23], s73 offen offset:1024 lds\n\t"
      "v_mfma_f32_16x16x32_bf16 v[132:135], a[68:71], v[88:91], v[132:135]\n\t"
      "v_cvt_pk_bf16_f32 v136, v128, v129\n\t"
      "v_mfma_f32_16x16x32_bf16 v[132:135], a[92:95], v[92:95], v[132:135]\n\t"
      "v_cvt_pk_bf16_f32 v137, v130, v131\n\t"
      "s_sub_u32 s84, s84, 4\n\t"
      "s_max_i32 s84, s84, s86\n\t"
      "v_mfma_f32_16x16x32_bf16 v[132:135], a[116:119], v[96:99], v[132:135]\n\t"
      "s_sub_u32 s83, s82, s80\n\t"
      "v_mfma_f32_16x16x32_bf16 v[132:135], a[140:143], v[100:103], v[132:135]\n\t"
      "s_waitcnt lgkmcnt(4)\n\t"
      "buffer_store_dwordx4 v[160:163], v208, s[28:31], s68 offen offset:2048 sc1\n\t"
      "s_max_i32 s83, s83, 0\n\t"
      "s_add_u32 s82, s80, s83\n\t"
      "v_mfma_f32_16x16x32_bf16 v[132:135], a[164:167], v[104:107], v[132:135]\n\t"
      "s_mov_b32 s45, s46\n\t"
      "v_mfma_f32_16x16x32_bf16 v[132:135], a[188:191], v[108:111], v[132:135]\n\t"
      "s_mov_b32 s46, s47\n\t"
      "v_mfma_f32_16x16x32_bf16 v[132:135], a[212:215], v[112:115], v[132:135]\n\t"
      "s_mov_b32 s47, s48\n\t"
      "v_mfma_f32_16x16x32_bf16 v[132:135], a[236:239], v[116:119], v[132:135]\n\t"
      "s_add_u32 s82, s82, s84\n\t"
      "v_mfma_f32_16x16x32_bf16 v[132:135], v[168:171], v[120:123], v[132:135]\n\t"
      "s_cmp_lg_u32 s44, s49\n\t"
      "v_mfma_f32_16x16x32_bf16 v[132:135], v[192:195], v[124:127], v[132:135]\n\t"
      "s_waitcnt lgkmcnt(2)\n\t"
      "buffer_store_dwordx4 v[148:151], v208, s[28:31], s72 offen offset:2048 sc1\n\t"
      "v_mfma_f32_16x16x32_bf16 v[128:131], a[0:3], v[32:35], v[8:11]\n\t"
      "v_mfma_f32_16x16x32_bf16 v[128:131], a[24:27], v[36:39], v[128:131]\n\t"
      "v_readlane_b32 s72, v249, s44\n\t"
      "v_mfma_f32_16x16x32_bf16 v[128:131], a[48:51], v[40:43], v[128:131]\n\t"
      "v_cvt_pk_bf16_f32 v138, v132, v133\n\t"
      "v_mfma_f32_16x16x32_bf16 v[128:131], a[72:75], v[44:47], v[128:131]\n\t"
      "v_cvt_pk_bf16_f32 v139, v134, v135\n\t"
      "v_mfma_f32_16x16x32_bf16 v[128:131], a[96:99], v[48:51], v[128:131]\n\t"
      "ds_write_b128 v219, v[136:139] offset:12288\n\t"
      "v_mfma_f32_16x16x32_bf16 v[128:131], a[120:123], v[52:55], v[128:131]\n\t"
      "v_mfma_f32_16x16x32_bf16 v[128:131], a[144:147], v[56:59], v[128:131]\n\t"
      "buffer_load_dwordx4 v208, s[20:23], s73 offen offset:2048 lds\n\t"
      "v_mfma_f32_16x16x32_bf16 v[128:131], a[168:171], v[60:63], v[128:131]\n\t"
      "s_waitcnt lgkmcnt(1)\n\t"
      "v_mfma_f32_16x16x32_bf16 v[128:131], a[192:195], v[64:67], v[128:131]\n\t"
      "v_add_u32_e32 v217, s46, v204\n\t"
      "v_mfma_f32_16x16x32_bf16 v[128:131], a[216:219], v[68:71], v[128:131]\n\t"
      "v_add_u32_e32 v218, s46, v205\n\t"
      "v_mfma_f32_16x16x32_bf16 v[128:131], a[240:243], v[72:75], v[128:131]\n\t"
      "v_add_u32_e32 v219, s46, v206\n\t"
      "v_mfma_f32_16x16x32_bf16 v[128:131], v[172:175], v[76:79], v[128:131]\n\t"
      "s_cmp_gt_i32 s83, 1\n\t"
      "s_cbranch_scc1 .LslpP4_%=\n\t"
      ".LretP4_%=:\n\t"
      "s_waitcnt vmcnt(15) lgkmcnt(0)\n\t"
      "s_barrier\n\t"
      "v_mfma_f32_16x16x32_bf16 v[132:135], a[4:7], v[32:35], v[12:15]\n\t"
      "ds_read_b128 v[140:143], v220 offset:12288\n\t"
      "v_mfma_f32_16x16x32_bf16 v[132:135], a[28:31], v[36:39], v[132:135]\n\t"
      "v_mfma_f32_16x16x32_bf16 v[132:135], a[52:55], v[40:43], v[132:135]\n\t"
      "v_cvt_pk_bf16_f32 v136, v128, v129\n\t"
      "v_mfma_f32_16x16x32_bf16 v[132:135], a[76:79], v[44:47], v[132:135]\n\t"
      "ds_read_b128 v[80:83], v196 offset:12288\n\t"
      "v_cvt_pk_bf16_f32 v137, v130, v131\n\t"
      "v_mfma_f32_16x16x32_bf16 v[132:135], a[100:103], v[48:51], v[132:135]\n\t"
      "v_readlane_b32 s68, v248, s94\n\t"
      "s_add_u32 s48, s47, 0x6000\n\t"
      "v_mfma_f32_16x16x32_bf16 v[132:135], a[124:127], v[52:55], v[132:135]\n\t"
      "ds_read_b128 v[152:155], v238 offset:12288\n\t"
      "s_cmp_eq_u32 s48, s50\n\t"
      "s_cselect_b32 s48, s41, s48\n\t"
      "v_mfma_f32_16x16x32_bf16 v[132:135], a[148:151], v[56:59], v[132:135]\n\t"
      "ds_read_b128 v[84:87], v197 offset:12288\n\t"
      "v_mfma_f32_16x16x32_bf16 v[132:135], a[172:175], v[60:63], v[132:135]\n\t"
      "v_readlane_b32 s73, v249, s85\n\t"
      "v_mfma_f32_16x16x32_bf16 v[132:135], a[196:199], v[64:67], v[132:135]\n\t"
      "v_mfma_f32_16x16x32_bf16 v[132:135], a[220:223], v[68:71], v[132:135]\n\t"
      "ds_read_b128 v[88:91], v198 offset:12288\n\t"
      "v_mfma_f32_16x16x32_bf16 v[132:135], a[244:247], v[72:75], v[132:135]\n\t"
      "v_mfma_f32_16x16x32_bf16 v[132:135], v[176:179], v[76:79], v[132:135]\n\t"
      "s_waitcnt lgkmcnt(4)\n\t"
      "buffer_store_dwordx4 v[140:143], v209, s[28:31], s72 offen sc1\n\t"
      "v_mfma_f32_16x16x32_bf16 v[128:131], a[8:11], v[32:35], v[16:19]\n\t"
      "ds_read_b128 v[92:95], v199 offset:12288\n\t"
      "v_mfma_f32_16x16x32_bf16 v[128:131], a[32:35], v[36:39], v[128:131]\n\t"
      "s_memrealtime s[80:81]\n\t"
      "v_mfma_f32_16x16x32_bf16 v[128:131], a[56:59], v[40:43], v[128:131]\n\t"
      "v_cvt_pk_bf16_f32 v138, v132, v133\n\t"
      "v_mfma_f32_16x16x32_bf16 v[128:131], a[80:83], v[44:47], v[128:131]\n\t"
      "v_cvt_pk_bf16_f32 v139, v134, v135\n\t"
      "v_mfma_f32_16x16x32_bf16 v[128:131], a[104:107], v[48:51], v[128:131]\n\t"
      "ds_write_b128 v217, v[136:139] offset:0\n\t"
      "v_mfma_f32_16x16x32_bf16 v[128:131], a[128:131], v[52:55], v[128:131]\n\t"
      "s_add_u32 m0, s45, 0x3000\n\t"
      "v_mfma_f32_16x16x32_bf16 v[128:131], a[152:155], v[56:59], v[128:131]\n\t"
      "s_waitcnt lgkmcnt(4)\n\t"
      "buffer_store_dwordx4 v[152:155], v209, s[28:31], s68 offen sc1\n\t"
      "v_mfma_f32_16x16x32_bf16 v[128:131], a[176:179], v[60:63], v[128:131]\n\t"
      "ds_read_b128 v[96:99], v197 offset:12544\n\t"
      "v_mfma_f32_16x16x32_bf16 v[128:131], a[200:203], v[64:67], v[128:131]\n\t"
      "v_mfma_f32_16x16x32_bf16 v[128:131], a[224:227], v[68:71], v[128:131]\n\t"
      "ds_read_b128 v[100:103], v198 offset:12544\n\t"
      "v_mfma_f32_16x16x32_bf16 v[128:131], a[248:251], v[72:75], v[128:131]\n\t"
      "ds_read_b128 v[156:159], v238 offset:13312\n\t"
      "v_mfma_f32_16x16x32_bf16 v[128:131], v[180:183], v[76:79], v[128:131]\n\t"
      "buffer_load_dwordx4 v209, s[20:23], s73 offen lds\n\t"
      "v_mfma_f32_16x16x32_bf16 v[132:135], a[12:15], v[32:35], v[20:23]\n\t"
      "ds_read_b128 v[104:107], v199 offset:12544\n\t"
      "v_mfma_f32_16x16x32_bf16 v[132:135], a[36:39], v[36:39], v[132:135]\n\t"
      "v_mfma_f32_16x16x32_bf16 v[132:135], a[60:63], v[40:43], v[132:135]\n\t"
      "v_cvt_pk_bf16_f32 v136, v128, v129\n\t"
      "v_mfma_f32_16x16x32_bf16 v[132:135], a[84:87], v[44:47], v[132:135]\n\t"
      "v_cvt_pk_bf16_f32 v137, v130, v131\n\t"
      "ds_read_b128 v[144:147], v220 offset:13312\n\t"
      "v_mfma_f32_16x16x32_bf16 v[132:135], a[108:111], v[48:51], v[132:135]\n\t"
      "v_mfma_f32_16x16x32_bf16 v[132:135], a[132:135], v[52:55], v[132:135]\n\t"
      "s_waitcnt lgkmcnt(2)\n\t"
      "buffer_store_dwordx4 v[156:159], v209, s[28:31], s68 offen offset:1024 sc1\n\t"
      "v_mfma_f32_16x16x32_bf16 v[132:135], a[156:159], v[56:59], v[132:135]\n\t"
      "ds_read_b128 v[108:111], v196 offset:12544\n\t"
      "v_mfma_f32_16x16x32_bf16 v[132:135], a[180:183], v[60:63], v[132:135]\n\t"
      "v_mfma_f32_16x16x32_bf16 v[132:135], a[204:207], v[64:67], v[132:135]\n\t"
      "v_mfma_f32_16x16x32_bf16 v[132:135], a[228:231], v[68:71], v[132:135]\n\t"
      "ds_read_b128 v[112:115], v198 offset:12800\n\t"
      "v_mfma_f32_16x16x32_bf16 v[132:135], a[252:255], v[72:75], v[132:135]\n\t"
      "v_mfma_f32_16x16x32_bf16 v[132:135], v[184:187], v[76:79], v[132:135]\n\t"
      "s_waitcnt lgkmcnt(2)\n\t"
      "buffer_store_dwordx4 v[144:147], v209, s[28:31], s72 offen offset:1024 sc1\n\t"
      "v_mfma_f32_16x16x32_bf16 v[128:131], a[16:19], v[32:35], v[24:27]\n\t"
      "ds_read_b128 v[116:119], v199 offset:12800\n\t"
      "v_mfma_f32_16x16x32_bf16 v[128:131], a[40:43], v[36:39], v[128:131]\n\t"
      "v_mfma_f32_16x16x32_bf16 v[128:131], a[64:67], v[40:43], v[128:131]\n\t"
      "v_cvt_pk_bf16_f32 v138, v132, v133\n\t"
      "v_mfma_f32_16x16x32_bf16 v[128:131], a[88:91], v[44:47], v[128:131]\n\t"
      "v_cvt_pk_bf16_f32 v139, v134, v135\n\t"
      "ds_read_b128 v[160:163], v238 offset:14336\n\t"
      "v_mfma_f32_16x16x32_bf16 v[128:131], a[112:115], v[48:51], v[128:131]\n\t"
      "ds_write_b128 v218, v[136:139] offset:0\n\t"
      "v_mfma_f32_16x16x32_bf16 v[128:131], a[136:139], v[52:55], v[128:131]\n\t"
      "v_mfma_f32_16x16x32_bf16 v[128:131], a[160:163], v[56:59], v[128:131]\n\t"
      "v_mfma_f32_16x16x32_bf16 v[128:131], a[184:187], v[60:63], v[128:131]\n\t"
      "ds_read_b128 v[148:151], v220 offset:14336\n\t"
      "v_mfma_f32_16x16x32_bf16 v[128:131], a[208:211], v[64:67], v[128:131]\n\t"
      "v_mfma_f32_16x16x32_bf16 v[128:131], a[232:235], v[68:71], v[128:131]\n\t"
      "ds_read_b128 v[120:123], v196 offset:12800\n\t"
      "v_mfma_f32_16x16x32_bf16 v[128:131], v[164:167], v[72:75], v[128:131]\n\t"
      "v_mfma_f32_16x16x32_bf16 v[128:131], v[188:191], v[76:79], v[128:131]\n\t"
      "buffer_load_dwordx4 v209, s[20:23], s73 offen offset:1024 lds\n\t"
      "v_mfma_f32_16x16x32_bf16 v[132:135], a[20:23], v[32:35], v[28:31]\n\t"
      "ds_read_b128 v[124:127], v197 offset:12800\n\t"
      "v_mfma_f32_16x16x32_bf16 v[132:135], a[44:47], v[36:39], v[132:135]\n\t"
      "v_mfma_f32_16x16x32_bf16 v[132:135], a[68:71], v[40:43], v[132:135]\n\t"
      "v_cvt_pk_bf16_f32 v136, v128, v129\n\t"
      "v_add_u32_e32 v220, s46, v207\n\t"
      "v_mfma_f32_16x16x32_bf16 v[132:135], a[92:95], v[44:47], v[132:135]\n\t"
      "v_cvt_pk_bf16_f32 v137, v130, v131\n\t"
      "s_sub_u32 s84, s84, 4\n\t"
      "s_max_i32 s84, s84, s86\n\t"
      "v_mfma_f32_16x16x32_bf16 v[132:135], a[116:119], v[48:51], v[132:135]\n\t"
      "s_sub_u32 s83, s82, s80\n\t"
      "v_mfma_f32_16x16x32_bf16 v[132:135], a[140:143], v[52:55], v[132:135]\n\t"
      "s_waitcnt lgkmcnt(4)\n\t"
      "buffer_store_dwordx4 v[160:163], v209, s[28:31], s68 offen offset:2048 sc1\n\t"
      "s_max_i32 s83, s83, 0\n\t"
      "s_add_u32 s82, s80, s83\n\t"
      "v_mfma_f32_16x16x32_bf16 v[132:135], a[164:167], v[56:59], v[132:135]\n\t"
      "v_mfma_f32_16x16x32_bf16 v[132:135], a[188:191], v[60:63], v[132:135]\n\t"
      "v_mfma_f32_16x16x32_bf16 v[132:135], a[212:215], v[64:67], v[132:135]\n\t"
      "s_add_u32 s44, s44, 1\n\t"
      "v_mfma_f32_16x16x32_bf16 v[132:135], a[236:239], v[68:71], v[132:135]\n\t"
      "s_add_u32 s82, s82, s84\n\t"
      "v_mfma_f32_16x16x32_bf16 v[132:135], v[168:171], v[72:75], v[132:135]\n\t"
      "s_add_u32 s85, s85, 1\n\t"
      "v_mfma_f32_16x16x32_bf16 v[132:135], v[192:195], v[76:79], v[132:135]\n\t"
      "s_waitcnt lgkmcnt(2)\n\t"
      "buffer_store_dwordx4 v[148:151], v209, s[28:31], s72 offen offset:2048 sc1\n\t"
      "v_mfma_f32_16x16x32_bf16 v[128:131], a[0:3], v[80:83], v[8:11]\n\t"
      "v_mfma_f32_16x16x32_bf16 v[128:131], a[24:27], v[84:87], v[128:131]\n\t"
      "v_readlane_b32 s72, v248, s44\n\t"
      "v_mfma_f32_16x16x32_bf16 v[128:131], a[48:51], v[88:91], v[128:131]\n\t"
      "v_cvt_pk_bf16_f32 v138, v132, v133\n\t"
      "v_mfma_f32_16x16x32_bf16 v[128:131], a[72:75], v[92:95], v[128:131]\n\t"
      "v_cvt_pk_bf16_f32 v139, v134, v135\n\t"
      "v_mfma_f32_16x16x32_bf16 v[128:131], a[96:99], v[96:99], v[128:131]\n\t"
      "ds_write_b128 v219, v[136:139] offset:0\n\t"
      "v_mfma_f32_16x16x32_bf16 v[128:131], a[120:123], v[100:103], v[128:131]\n\t"
      "v_mfma_f32_16x16x32_bf16 v[128:131], a[144:147], v[104:107], v[128:131]\n\t"
      "buffer_load_dwordx4 v209, s[20:23], s73 offen offset:2048 lds\n\t"
      "v_mfma_f32_16x16x32_bf16 v[128:131], a[168:171], v[108:111], v[128:131]\n\t"
      "v_add_u32_e32 v196, s47, v200\n\t"
      "s_waitcnt lgkmcnt(1)\n\t"
      "v_mfma_f32_16x16x32_bf16 v[128:131], a[192:195], v[112:115], v[128:131]\n\t"
      "v_add_u32_e32 v197, s47, v201\n\t"
      "v_mfma_f32_16x16x32_bf16 v[128:131], a[216:219], v[116:119], v[128:131]\n\t"
      "v_add_u32_e32 v198, s47, v202\n\t"
      "v_mfma_f32_16x16x32_bf16 v[128:131], a[240:243], v[120:123], v[128:131]\n\t"
      "v_add_u32_e32 v199, s47, v203\n\t"
      "v_mfma_f32_16x16x32_bf16 v[128:131], v[172:175], v[124:127], v[128:131]\n\t"
      "s_cmp_gt_i32 s83, 1\n\t"
      "s_cbranch_scc1 .LslpP5_%=\n\t"
      ".LretP5_%=:\n\t"
      "s_waitcnt vmcnt(21) lgkmcnt(0)\n\t"
      "s_barrier\n\t"
      "v_mfma_f32_16x16x32_bf16 v[132:135], a[4:7], v[80:83], v[12:15]\n\t"
      "ds_read_b128 v[140:143], v220 offset:0\n\t"
      "v_mfma_f32_16x16x32_bf16 v[132:135], a[28:31], v[84:87], v[132:135]\n\t"
      "v_mfma_f32_16x16x32_bf16 v[132:135], a[52:55], v[88:91], v[132:135]\n\t"
      "v_cvt_pk_bf16_f32 v136, v128, v129\n\t"
      "v_mfma_f32_16x16x32_bf16 v[132:135], a[76:79], v[92:95], v[132:135]\n\t"
      "ds_read_b128 v[32:35], v196 offset:0\n\t"
      "v_cvt_pk_bf16_f32 v137, v130, v131\n\t"
      "v_mfma_f32_16x16x32_bf16 v[132:135], a[100:103], v[96:99], v[132:135]\n\t"
      "v_mfma_f32_16x16x32_bf16 v[132:135], a[124:127], v[100:103], v[132:135]\n\t"
      "v_mfma_f32_16x16x32_bf16 v[132:135], a[148:151], v[104:107], v[132:135]\n\t"
      "ds_read_b128 v[36:39], v197 offset:0\n\t"
      "v_mfma_f32_16x16x32_bf16 v[132:135], a[172:175], v[108:111], v[132:135]\n\t"
      "v_readlane_b32 s73, v248, s85\n\t"
      "v_mfma_f32_16x16x32_bf16 v[132:135], a[196:199], v[112:115], v[132:135]\n\t"
      "v_mfma_f32_16x16x32_bf16 v[132:135], a[220:223], v[116:119], v[132:135]\n\t"
      "ds_read_b128 v[40:43], v198 offset:0\n\t"
      "v_mfma_f32_16x16x32_bf16 v[132:135], a[244:247], v[120:123], v[132:135]\n\t"
      "v_mfma_f32_16x16x32_bf16 v[132:135], v[176:179], v[124:127], v[132:135]\n\t"
      "s_waitcnt lgkmcnt(3)\n\t"
      "buffer_store_dwordx4 v[140:143], v208, s[28:31], s72 offen sc1\n\t"
      "v_mfma_f32_16x16x32_bf16 v[128:131], a[8:11], v[80:83], v[16:19]\n\t"
      "ds_read_b128 v[44:47], v199 offset:0\n\t"
      "v_mfma_f32_16x16x32_bf16 v[128:131], a[32:35], v[84:87], v[128:131]\n\t"
      "s_memrealtime s[80:81]\n\t"
      "v_mfma_f32_16x16x32_bf16 v[128:131], a[56:59], v[88:91], v[128:131]\n\t"
      "v_cvt_pk_bf16_f32 v138, v132, v133\n\t"
      "v_mfma_f32_16x16x32_bf16 v[128:131], a[80:83], v[92:95], v[128:131]\n\t"
      "v_cvt_pk_bf16_f32 v139, v134, v135\n\t"
      "v_mfma_f32_16x16x32_bf16 v[128:131], a[104:107], v[96:99], v[128:131]\n\t"
      "ds_write_b128 v217, v[136:139] offset:12288\n\t"
      "v_mfma_f32_16x16x32_bf16 v[128:131], a[128:131], v[100:103], v[128:131]\n\t"
      "s_mov_b32 m0, s46\n\t"
      "v_mfma_f32_16x16x32_bf16 v[128:131], a[152:155], v[104:107], v[128:131]\n\t"
      "v_mfma_f32_16x16x32_bf16 v[128:131], a[176:179], v[108:111], v[128:131]\n\t"
      "ds_read_b128 v[48:51], v197 offset:256\n\t"
      "v_mfma_f32_16x16x32_bf16 v[128:131], a[200:203], v[112:115], v[128:131]\n\t"
      "v_mfma_f32_16x16x32_bf16 v[128:131], a[224:227], v[116:119], v[128:131]\n\t"
      "ds_read_b128 v[52:55], v198 offset:256\n\t"
      "v_mfma_f32_16x16x32_bf16 v[128:131], a[248:251], v[120:123], v[128:131]\n\t"
      "v_mfma_f32_16x16x32_bf16 v[128:131], v[180:183], v[124:127], v[128:131]\n\t"
      "buffer_load_dwordx4 v208, s[20:23], s73 offen lds\n\t"
      "v_mfma_f32_16x16x32_bf16 v[132:135], a[12:15], v[80:83], v[20:23]\n\t"
      "ds_read_b128 v[56:59], v199 offset:256\n\t"
      "v_mfma_f32_16x16x32_bf16 v[132:135], a[36:39], v[84:87], v[132:135]\n\t"
      "v_mfma_f32_16x16x32_bf16 v[132:135], a[60:63], v[88:91], v[132:135]\n\t"
      "v_cvt_pk_bf16_f32 v136, v128, v129\n\t"
      "v_mfma_f32_16x16x32_bf16 v[132:135], a[84:87], v[92:95], v[132:135]\n\t"
      "v_cvt_pk_bf16_f32 v137, v130, v131\n\t"
      "ds_read_b128 v[144:147], v220 offset:1024\n\t"
      "v_mfma_f32_16x16x32_bf16 v[132:135], a[108:111], v[96:99], v[132:135]\n\t"
      "v_mfma_f32_16x16x32_bf16 v[132:135], a[132:135], v[100:103], v[132:135]\n\t"
      "v_mfma_f32_16x16x32_bf16 v[132:135], a[156:159], v[104:107], v[132:135]\n\t"
      "ds_read_b128 v[60:63], v196 offset:256\n\t"
      "v_mfma_f32_16x16x32_bf16 v[132:135], a[180:183], v[108:111], v[132:135]\n\t"
      "v_mfma_f32_16x16x32_bf16 v[132:135], a[204:207], v[112:115], v[132:135]\n\t"
      "v_mfma_f32_16x16x32_bf16 v[132:135], a[228:231], v[116:119], v[132:135]\n\t"
      "ds_read_b128 v[64:67], v198 offset:512\n\t"
      "v_mfma_f32_16x16x32_bf16 v[132:135], a[252:255], v[120:123], v[132:135]\n\t"
      "v_mfma_f32_16x16x32_bf16 v[132:135], v[184:187], v[124:127], v[132:135]\n\t"
      "s_waitcnt lgkmcnt(2)\n\t"
      "buffer_store_dwordx4 v[144:147], v208, s[28:31], s72 offen offset:1024 sc1\n\t"
      "v_mfma_f32_16x16x32_bf16 v[128:131], a[16:19], v[80:83], v[24:27]\n\t"
      "ds_read_b128 v[68:71], v199 offset:512\n\t"
      "v_mfma_f32_16x16x32_bf16 v[128:131], a[40:43], v[84:87], v[128:131]\n\t"
      "v_readlane_b32 s86, v250, s44\n\t"
      "v_mfma_f32_16x16x32_bf16 v[128:131], a[64:67], v[88:91], v[128:131]\n\t"
      "v_cvt_pk_bf16_f32 v138, v132, v133\n\t"
      "v_mfma_f32_16x16x32_bf16 v[128:131], a[88:91], v[92:95], v[128:131]\n\t"
      "v_cvt_pk_bf16_f32 v139, v134, v135\n\t"
      "v_mfma_f32_16x16x32_bf16 v[128:131], a[112:115], v[96:99], v[128:131]\n\t"
      "ds_write_b128 v218, v[136:139] offset:12288\n\t"
      "v_mfma_f32_16x16x32_bf16 v[128:131], a[136:139], v[100:103], v[128:131]\n\t"
      "v_mfma_f32_16x16x32_bf16 v[128:131], a[160:163], v[104:107], v[128:131]\n\t"
      "v_mfma_f32_16x16x32_bf16 v[128:131], a[184:187], v[108:111], v[128:131]\n\t"
      "ds_read_b128 v[148:151], v220 offset:2048\n\t"
      "v_mfma_f32_16x16x32_bf16 v[128:131], a[208:211], v[112:115], v[128:131]\n\t"
      "v_mfma_f32_16x16x32_bf16 v[128:131], a[232:235], v[116:119], v[128:131]\n\t"
      "ds_read_b128 v[72:75], v196 offset:512\n\t"
      "v_mfma_f32_16x16x32_bf16 v[128:131], v[164:167], v[120:123], v[128:131]\n\t"
      "v_mfma_f32_16x16x32_bf16 v[128:131], v[188:191], v[124:127], v[128:131]\n\t"
      "buffer_load_dwordx4 v208, s[20:23], s73 offen offset:1024 lds\n\t"
      "v_mfma_f32_16x16x32_bf16 v[132:135], a[20:23], v[80:83], v[28:31]\n\t"
      "ds_read_b128 v[76:79], v197 offset:512\n\t"
      "v_mfma_f32_16x16x32_bf16 v[132:135], a[44:47], v[84:87], v[132:135]\n\t"
      "v_mfma_f32_16x16x32_bf16 v[132:135], a[68:71], v[88:91], v[132:135]\n\t"
      "v_cvt_pk_bf16_f32 v136, v128, v129\n\t"
      "v_mfma_f32_16x16x32_bf16 v[132:135], a[92:95], v[92:95], v[132:135]\n\t"
      "v_cvt_pk_bf16_f32 v137, v130, v131\n\t"
      "s_sub_u32 s84, s84, 4\n\t"
      "s_max_i32 s84, s84, s86\n\t"
      "v_mfma_f32_16x16x32_bf16 v[132:135], a[116:119], v[96:99], v[132:135]\n\t"
      "s_sub_u32 s83, s82, s80\n\t"
      "v_mfma_f32_16x16x32_bf16 v[132:135], a[140:143], v[100:103], v[132:135]\n\t"
      "s_max_i32 s83, s83, 0\n\t"
      "s_add_u32 s82, s80, s83\n\t"
      "v_mfma_f32_16x16x32_bf16 v[132:135], a[164:167], v[104:107], v[132:135]\n\t"
      "s_mov_b32 s45, s46\n\t"
      "v_mfma_f32_16x16x32_bf16 v[132:135], a[188:191], v[108:111], v[132:135]\n\t"
      "s_mov_b32 s46, s47\n\t"
      "v_mfma_f32_16x16x32_bf16 v[132:135], a[212:215], v[112:115], v[132:135]\n\t"
      "s_mov_b32 s47, s48\n\t"
      "v_mfma_f32_16x16x32_bf16 v[132:135], a[236:239], v[116:119], v[132:135]\n\t"
      "s_add_u32 s82, s82, s84\n\t"
      "v_mfma_f32_16x16x32_bf16 v[132:135], v[168:171], v[120:123], v[132:135]\n\t"
      "s_cmp_lg_u32 s44, s49\n\t"
      "v_mfma_f32_16x16x32_bf16 v[132:135], v[192:195], v[124:127], v[132:135]\n\t"
      "s_waitcnt lgkmcnt(2)\n\t"
      "buffer_store_dwordx4 v[148:151], v208, s[28:31], s72 offen offset:2048 sc1\n\t"
      ".p2align 6\n\t"
      ".Lloop_%=:\n\t"
      "s_waitcnt lgkmcnt(12)\n\t"
      "v_mfma_f32_16x16x32_bf16 v[128:131], a[0:3], v[32:35], v[8:11]\n\t"
      "v_mfma_f32_16x16x32_bf16 v[128:131], a[24:27], v[36:39], v[128:131]\n\t"
      "v_readlane_b32 s72, v249, s44\n\t"
      "v_mfma_f32_16x16x32_bf16 v[128:131], a[48:51], v[40:43], v[128:131]\n\t"
      "v_cvt_pk_bf16_f32 v138, v132, v133\n\t"
      "v_mfma_f32_16x16x32_bf16 v[128:131], a[72:75], v[44:47], v[128:131]\n\t"
      "v_cvt_pk_bf16_f32 v139, v134, v135\n\t"
      "s_waitcnt lgkmcnt(6)\n\t"
      "v_mfma_f32_16x16x32_bf16 v[128:131], a[96:99], v[48:51], v[128:131]\n\t"
      "ds_write_b128 v219, v[136:139] offset:12288\n\t"
      "v_mfma_f32_16x16x32_bf16 v[128:131], a[120:123], v[52:55], v[128:131]\n\t"
      "v_mfma_f32_16x16x32_bf16 v[128:131], a[144:147], v[56:59], v[128:131]\n\t"
      "buffer_load_dwordx4 v208, s[20:23], s73 offen offset:2048 lds\n\t"
      "v_mfma_f32_16x16x32_bf16 v[128:131], a[168:171], v[60:63], v[128:131]\n\t"
      "s_waitcnt lgkmcnt(1)\n\t"
      "v_mfma_f32_16x16x32_bf16 v[128:131], a[192:195], v[64:67], v[128:131]\n\t"
      "v_add_u32_e32 v217, s46, v204\n\t"
      "v_mfma_f32_16x16x32_bf16 v[128:131], a[216:219], v[68:71], v[128:131]\n\t"
      "v_add_u32_e32 v218, s46, v205\n\t"
      "v_mfma_f32_16x16x32_bf16 v[128:131], a[240:243], v[72:75], v[128:131]\n\t"
      "v_add_u32_e32 v219, s46, v206\n\t"
      "v_mfma_f32_16x16x32_bf16 v[128:131], v[172:175], v[76:79], v[128:131]\n\t"
      "s_cmp_gt_i32 s83, 1\n\t"
      "s_cbranch_scc1 .LslpA_%=\n\t"
      ".LretA_%=:\n\t"
      "s_waitcnt vmcnt(18) lgkmcnt(0)\n\t"
      "s_barrier\n\t"
      "v_mfma_f32_16x16x32_bf16 v[132:135], a[4:7], v[32:35], v[12:15]\n\t"
      "ds_read_b128 v[140:143], v220 offset:12288\n\t"
      "v_mfma_f32_16x16x32_bf16 v[132:135], a[28:31], v[36:39], v[132:135]\n\t"
      "v_mfma_f32_16x16x32_bf16 v[132:135], a[52:55], v[40:43], v[132:135]\n\t"
      "v_cvt_pk_bf16_f32 v136, v128, v129\n\t"
      "v_mfma_f32_16x16x32_bf16 v[132:135], a[76:79], v[44:47], v[132:135]\n\t"
      "ds_read_b128 v[80:83], v196 offset:12288\n\t"
      "v_cvt_pk_bf16_f32 v137, v130, v131\n\t"
      "v_mfma_f32_16x16x32_bf16 v[132:135], a[100:103], v[48:51], v[132:135]\n\t"
      "s_add_u32 s48, s47, 0x6000\n\t"
      "v_mfma_f32_16x16x32_bf16 v[132:135], a[124:127], v[52:55], v[132:135]\n\t"
      "s_cmp_eq_u32 s48, s50\n\t"
      "s_cselect_b32 s48, s41, s48\n\t"
      "v_mfma_f32_16x16x32_bf16 v[132:135], a[148:151], v[56:59], v[132:135]\n\t"
      "ds_read_b128 v[84:87], v197 offset:12288\n\t"
      "v_mfma_f32_16x16x32_bf16 v[132:135], a[172:175], v[60:63], v[132:135]\n\t"
      "v_readlane_b32 s73, v249, s85\n\t"
      "v_mfma_f32_16x16x32_bf16 v[132:135], a[196:199], v[64:67], v[132:135]\n\t"
      "v_mfma_f32_16x16x32_bf16 v[132:135], a[220:223], v[68:71], v[132:135]\n\t"
      "ds_read_b128 v[88:91], v198 offset:12288\n\t"
      "v_mfma_f32_16x16x32_bf16 v[132:135], a[244:247], v[72:75], v[132:135]\n\t"
      "v_mfma_f32_16x16x32_bf16 v[132:135], v[176:179], v[76:79], v[132:135]\n\t"
      "s_waitcnt lgkmcnt(3)\n\t"
      "buffer_store_dwordx4 v[140:143], v209, s[28:31], s72 offen sc1\n\t"
      "v_mfma_f32_16x16x32_bf16 v[128:131], a[8:11], v[32:35], v[16:19]\n\t"
      "ds_read_b128 v[92:95], v199 offset:12288\n\t"
      "v_mfma_f32_16x16x32_bf16 v[128:131], a[32:35], v[36:39], v[128:131]\n\t"
      "s_memrealtime s[80:81]\n\t"
      "v_mfma_f32_16x16x32_bf16 v[128:131], a[56:59], v[40:43], v[128:131]\n\t"
      "v_cvt_pk_bf16_f32 v138, v132, v133\n\t"
      "v_mfma_f32_16x16x32_bf16 v[128:131], a[80:83], v[44:47], v[128:131]\n\t"
      "v_cvt_pk_bf16_f32 v139, v134, v135\n\t"
      "v_mfma_f32_16x16x32_bf16 v[128:131], a[104:107], v[48:51], v[128:131]\n\t"
      "ds_write_b128 v217, v[136:139] offset:0\n\t"
      "v_mfma_f32_16x16x32_bf16 v[128:131], a[128:131], v[52:55], v[128:131]\n\t"
      "s_add_u32 m0, s45, 0x3000\n\t"
      "v_mfma_f32_16x16x32_bf16 v[128:131], a[152:155], v[56:59], v[128:131]\n\t"
      "v_mfma_f32_16x16x32_bf16 v[128:131], a[176:179], v[60:63], v[128:131]\n\t"
      "ds_read_b128 v[96:99], v197 offset:12544\n\t"
      "v_mfma_f32_16x16x32_bf16 v[128:131], a[200:203], v[64:67], v[128:131]\n\t"
      "v_mfma_f32_16x16x32_bf16 v[128:131], a[224:227], v[68:71], v[128:131]\n\t"
      "ds_read_b128 v[100:103], v198 offset:12544\n\t"
      "v_mfma_f32_16x16x32_bf16 v[128:131], a[248:251], v[72:75], v[128:131]\n\t"
      "v_mfma_f32_16x16x32_bf16 v[128:131], v[180:183], v[76:79], v[128:131]\n\t"
      "buffer_load_dwordx4 v209, s[20:23], s73 offen lds\n\t"
      "v_mfma_f32_16x16x32_bf16 v[132:135], a[12:15], v[32:35], v[20:23]\n\t"
      "ds_read_b128 v[104:107], v199 offset:12544\n\t"
      "v_mfma_f32_16x16x32_bf16 v[132:135], a[36:39], v[36:39], v[132:135]\n\t"
      "v_mfma_f32_16x16x32_bf16 v[132:135], a[60:63], v[40:43], v[132:135]\n\t"
      "v_cvt_pk_bf16_f32 v136, v128, v129\n\t"
      "v_mfma_f32_16x16x32_bf16 v[132:135], a[84:87], v[44:47], v[132:135]\n\t"
      "v_cvt_pk_bf16_f32 v137, v130, v131\n\t"
      "ds_read_b128 v[144:147], v220 offset:13312\n\t"
      "v_mfma_f32_16x16x32_bf16 v[132:135], a[108:111], v[48:51], v[132:135]\n\t"
      "v_mfma_f32_16x16x32_bf16 v[132:135], a[132:135], v[52:55], v[132:135]\n\t"
      "v_mfma_f32_16x16x32_bf16 v[132:135], a[156:159], v[56:59], v[132:135]\n\t"
      "ds_read_b128 v[108:111], v196 offset:12544\n\t"
      "v_mfma_f32_16x16x32_bf16 v[132:135], a[180:183], v[60:63], v[132:135]\n\t"
      "v_mfma_f32_16x16x32_bf16 v[132:135], a[204:207], v[64:67], v[132:135]\n\t"
      "v_mfma_f32_16x16x32_bf16 v[132:135], a[228:231], v[68:71], v[132:135]\n\t"
      "ds_read_b128 v[112:115], v198 offset:12800\n\t"
      "v_mfma_f32_16x16x32_bf16 v[132:135], a[252:255], v[72:75], v[132:135]\n\t"
      "v_mfma_f32_16x16x32_bf16 v[132:135], v[184:187], v[76:79], v[132:135]\n\t"
      "s_waitcnt lgkmcnt(2)\n\t"
      "buffer_store_dwordx4 v[144:147], v209, s[28:31], s72 offen offset:1024 sc1\n\t"
      "v_mfma_f32_16x16x32_bf16 v[128:131], a[16:19], v[32:35], v[24:27]\n\t"
      "ds_read_b128 v[116:119], v199 offset:12800\n\t"
      "v_mfma_f32_16x16x32_bf16 v[128:131], a[40:43], v[36:39], v[128:131]\n\t"
      "v_mfma_f32_16x16x32_bf16 v[128:131], a[64:67], v[40:43], v[128:131]\n\t"
      "v_cvt_pk_bf16_f32 v138, v132, v133\n\t"
      "v_mfma_f32_16x16x32_bf16 v[128:131], a[88:91], v[44:47], v[128:131]\n\t"
      "v_cvt_pk_bf16_f32 v139, v134, v135\n\t"
      "v_mfma_f32_16x16x32_bf16 v[128:131], a[112:115], v[48:51], v[128:131]\n\t"
      "ds_write_b128 v218, v[136:139] offset:0\n\t"
      "v_mfma_f32_16x16x32_bf16 v[128:131], a[136:139], v[52:55], v[128:131]\n\t"
      "v_mfma_f32_16x16x32_bf16 v[128:131], a[160:163], v[56:59], v[128:131]\n\t"
      "v_mfma_f32_16x16x32_bf16 v[128:131], a[184:187], v[60:63], v[128:131]\n\t"
      "ds_read_b128 v[148:151], v220 offset:14336\n\t"
      "v_mfma_f32_16x16x32_bf16 v[128:131], a[208:211], v[64:67], v[128:131]\n\t"
      "v_mfma_f32_16x16x32_bf16 v[128:131], a[232:235], v[68:71], v[128:131]\n\t"
      "ds_read_b128 v[120:123], v196 offset:12800\n\t"
      "v_mfma_f32_16x16x32_bf16 v[128:131], v[164:167], v[72:75], v[128:131]\n\t"
      "v_mfma_f32_16x16x32_bf16 v[128:131], v[188:191], v[76:79], v[128:131]\n\t"
      "buffer_load_dwordx4 v209, s[20:23], s73 offen offset:1024 lds\n\t"
      "v_mfma_f32_16x16x32_bf16 v[132:135], a[20:23], v[32:35], v[28:31]\n\t"
      "ds_read_b128 v[124:127], v197 offset:12800\n\t"
      "v_mfma_f32_16x16x32_bf16 v[132:135], a[44:47], v[36:39], v[132:135]\n\t"
      "v_mfma_f32_16x16x32_bf16 v[132:135], a[68:71], v[40:43], v[132:135]\n\t"
      "v_cvt_pk_bf16_f32 v136, v128, v129\n\t"
      "v_add_u32_e32 v220, s46, v207\n\t"
      "v_mfma_f32_16x16x32_bf16 v[132:135], a[92:95], v[44:47], v[132:135]\n\t"
      "v_cvt_pk_bf16_f32 v137, v130, v131\n\t"
      "s_sub_u32 s84, s84, 4\n\t"
      "s_max_i32 s84, s84, s86\n\t"
      "v_mfma_f32_16x16x32_bf16 v[132:135], a[116:119], v[48:51], v[132:135]\n\t"
      "s_sub_u32 s83, s82, s80\n\t"
      "v_mfma_f32_16x16x32_bf16 v[132:135], a[140:143], v[52:55], v[132:135]\n\t"
      "s_max_i32 s83, s83, 0\n\t"
      "s_add_u32 s82, s80, s83\n\t"
      "v_mfma_f32_16x16x32_bf16 v[132:135], a[164:167], v[56:59], v[132:135]\n\t"
      "v_mfma_f32_16x16x32_bf16 v[132:135], a[188:191], v[60:63], v[132:135]\n\t"
      "v_mfma_f32_16x16x32_bf16 v[132:135], a[212:215], v[64:67], v[132:135]\n\t"
      "s_add_u32 s44, s44, 1\n\t"
      "v_mfma_f32_16x16x32_bf16 v[132:135], a[236:239], v[68:71], v[132:135]\n\t"
      "s_add_u32 s82, s82, s84\n\t"
      "v_mfma_f32_16x16x32_bf16 v[132:135], v[168:171], v[72:75], v[132:135]\n\t"
      "s_add_u32 s85, s85, 1\n\t"
      "v_mfma_f32_16x16x32_bf16 v[132:135], v[192:195], v[76:79], v[132:135]\n\t"
      "s_waitcnt lgkmcnt(2)\n\t"
      "buffer_store_dwordx4 v[148:151], v209, s[28:31], s72 offen offset:2048 sc1\n\t"
      "s_waitcnt lgkmcnt(12)\n\t"
      "v_mfma_f32_16x16x32_bf16 v[128:131], a[0:3], v[80:83], v[8:11]\n\t"
      "v_mfma_f32_16x16x32_bf16 v[128:131], a[24:27], v[84:87], v[128:131]\n\t"
      "v_readlane_b32 s72, v248, s44\n\t"
      "v_mfma_f32_16x16x32_bf16 v[128:131], a[48:51], v[88:91], v[128:131]\n\t"
      "v_cvt_pk_bf16_f32 v138, v132, v133\n\t"
      "v_mfma_f32_16x16x32_bf16 v[128:131], a[72:75], v[92:95], v[128:131]\n\t"
      "v_cvt_pk_bf16_f32 v139, v134, v135\n\t"
      "s_waitcnt lgkmcnt(6)\n\t"
      "v_mfma_f32_16x16x32_bf16 v[128:131], a[96:99], v[96:99], v[128:131]\n\t"
      "ds_write_b128 v219, v[136:139] offset:0\n\t"
      "v_mfma_f32_16x16x32_bf16 v[128:131], a[120:123], v[100:103], v[128:131]\n\t"
      "v_mfma_f32_16x16x32_bf16 v[128:131], a[144:147], v[104:107], v[128:131]\n\t"
      "buffer_load_dwordx4 v209, s[20:23], s73 offen offset:2048 lds\n\t"
      "v_mfma_f32_16x16x32_bf16 v[128:131], a[168:171], v[108:111], v[128:131]\n\t"
      "v_add_u32_e32 v196, s47, v200\n\t"
      "s_waitcnt lgkmcnt(1)\n\t"
      "v_mfma_f32_16x16x32_bf16 v[128:131], a[192:195], v[112:115], v[128:131]\n\t"
      "v_add_u32_e32 v197, s47, v201\n\t"
      "v_mfma_f32_16x16x32_bf16 v[128:131], a[216:219], v[116:119], v[128:131]\n\t"
      "v_add_u32_e32 v198, s47, v202\n\t"
      "v_mfma_f32_16x16x32_bf16 v[128:131], a[240:243], v[120:123], v[128:131]\n\t"
      "v_add_u32_e32 v199, s47, v203\n\t"
      "v_mfma_f32_16x16x32_bf16 v[128:131], v[172:175], v[124:127], v[128:131]\n\t"
      "s_cmp_gt_i32 s83, 1\n\t"
      "s_cbranch_scc1 .LslpB_%=\n\t"
      ".LretB_%=:\n\t"
      "s_waitcnt vmcnt(18) lgkmcnt(0)\n\t"
      "s_barrier\n\t"
      "v_mfma_f32_16x16x32_bf16 v[132:135], a[4:7], v[80:83], v[12:15]\n\t"
      "ds_read_b128 v[140:143], v220 offset:0\n\t"
      "v_mfma_f32_16x16x32_bf16 v[132:135], a[28:31], v[84:87], v[132:135]\n\t"
      "v_mfma_f32_16x16x32_bf16 v[132:135], a[52:55], v[88:91], v[132:135]\n\t"
      "v_cvt_pk_bf16_f32 v136, v128, v129\n\t"
      "v_mfma_f32_16x16x32_bf16 v[132:135], a[76:79], v[92:95], v[132:135]\n\t"
      "ds_read_b128 v[32:35], v196 offset:0\n\t"
      "v_cvt_pk_bf16_f32 v137, v130, v131\n\t"
      "v_mfma_f32_16x16x32_bf16 v[132:135], a[100:103], v[96:99], v[132:135]\n\t"
      "v_mfma_f32_16x16x32_bf16 v[132:135], a[124:127], v[100:103], v[132:135]\n\t"
      "v_mfma_f32_16x16x32_bf16 v[132:135], a[148:151], v[104:107], v[132:135]\n\t"
      "ds_read_b128 v[36:39], v197 offset:0\n\t"
      "v_mfma_f32_16x16x32_bf16 v[132:135], a[172:175], v[108:111], v[132:135]\n\t"
      "v_readlane_b32 s73, v248, s85\n\t"
      "v_mfma_f32_16x16x32_bf16 v[132:135], a[196:199], v[112:115], v[132:135]\n\t"
      "v_mfma_f32_16x16x32_bf16 v[132:135], a[220:223], v[116:119], v[132:135]\n\t"
      "ds_read_b128 v[40:43], v198 offset:0\n\t"
      "v_mfma_f32_16x16x32_bf16 v[132:135], a[244:247], v[120:123], v[132:135]\n\t"
      "v_mfma_f32_16x16x32_bf16 v[132:135], v[176:179], v[124:127], v[132:135]\n\t"
      "s_waitcnt lgkmcnt(3)\n\t"
      "buffer_store_dwordx4 v[140:143], v208, s[28:31], s72 offen sc1\n\t"
      "v_mfma_f32_16x16x32_bf16 v[128:131], a[8:11], v[80:83], v[16:19]\n\t"
      "ds_read_b128 v[44:47], v199 offset:0\n\t"
      "v_mfma_f32_16x16x32_bf16 v[128:131], a[32:35], v[84:87], v[128:131]\n\t"
      "s_memrealtime s[80:81]\n\t"
      "v_mfma_f32_16x16x32_bf16 v[128:131], a[56:59], v[88:91], v[128:131]\n\t"
      "v_cvt_pk_bf16_f32 v138, v132, v133\n\t"
      "v_mfma_f32_16x16x32_bf16 v[128:131], a[80:83], v[92:95], v[128:131]\n\t"
      "v_cvt_pk_bf16_f32 v139, v134, v135\n\t"
      "v_mfma_f32_16x16x32_bf16 v[128:131], a[104:107], v[96:99], v[128:131]\n\t"
      "ds_write_b128 v217, v[136:139] offset:12288\n\t"
      "v_mfma_f32_16x16x32_bf16 v[128:131], a[128:131], v[100:103], v[128:131]\n\t"
      "s_mov_b32 m0, s46\n\t"
      "v_mfma_f32_16x16x32_bf16 v[128:131], a[152:155], v[104:107], v[128:131]\n\t"
      "v_mfma_f32_16x16x32_bf16 v[128:131], a[176:179], v[108:111], v[128:131]\n\t"
      "ds_read_b128 v[48:51], v197 offset:256\n\t"
      "v_mfma_f32_16x16x32_bf16 v[128:131], a[200:203], v[112:115], v[128:131]\n\t"
      "v_mfma_f32_16x16x32_bf16 v[128:131], a[224:227], v[116:119], v[128:131]\n\t"
      "ds_read_b128 v[52:55], v198 offset:256\n\t"
      "v_mfma_f32_16x16x32_bf16 v[128:131], a[248:251], v[120:123], v[128:131]\n\t"
      "v_mfma_f32_16x16x32_bf16 v[128:131], v[180:183], v[124:127], v[128:131]\n\t"
      "buffer_load_dwordx4 v208, s[20:23], s73 offen lds\n\t"
      "v_mfma_f32_16x16x32_bf16 v[132:135], a[12:15], v[80:83], v[20:23]\n\t"
      "ds_read_b128 v[56:59], v199 offset:256\n\t"
      "v_mfma_f32_16x16x32_bf16 v[132:135], a[36:39], v[84:87], v[132:135]\n\t"
      "v_mfma_f32_16x16x32_bf16 v[132:135], a[60:63], v[88:91], v[132:135]\n\t"
      "v_cvt_pk_bf16_f32 v136, v128, v129\n\t"
      "v_mfma_f32_16x16x32_bf16 v[132:135], a[84:87], v[92:95], v[132:135]\n\t"
      "v_cvt_pk_bf16_f32 v137, v130, v131\n\t"
      "ds_read_b128 v[144:147], v220 offset:1024\n\t"
      "v_mfma_f32_16x16x32_bf16 v[132:135], a[108:111], v[96:99], v[132:135]\n\t"
      "v_mfma_f32_16x16x32_bf16 v[132:135], a[132:135], v[100:103], v[132:135]\n\t"
      "v_mfma_f32_16x16x32_bf16 v[132:135], a[156:159], v[104:107], v[132:135]\n\t"
      "ds_read_b128 v[60:63], v196 offset:256\n\t"
      "v_mfma_f32_16x16x32_bf16 v[132:135], a[180:183], v[108:111], v[132:135]\n\t"
      "v_mfma_f32_16x16x32_bf16 v[132:135], a[204:207], v[112:115], v[132:135]\n\t"
      "v_mfma_f32_16x16x32_bf16 v[132:135], a[228:231], v[116:119], v[132:135]\n\t"
      "ds_read_b128 v[64:67], v198 offset:512\n\t"
      "v_mfma_f32_16x16x32_bf16 v[132:135], a[252:255], v[120:123], v[132:135]\n\t"
      "v_mfma_f32_16x16x32_bf16 v[132:135], v[184:187], v[124:127], v[132:135]\n\t"
      "s_waitcnt lgkmcnt(2)\n\t"
      "buffer_store_dwordx4 v[144:147], v208, s[28:31], s72 offen offset:1024 sc1\n\t"
      "v_mfma_f32_16x16x32_bf16 v[128:131], a[16:19], v[80:83], v[24:27]\n\t"
      "ds_read_b128 v[68:71], v199 offset:512\n\t"
      "v_mfma_f32_16x16x32_bf16 v[128:131], a[40:43], v[84:87], v[128:131]\n\t"
      "v_readlane_b32 s86, v250, s44\n\t"
      "v_mfma_f32_16x16x32_bf16 v[128:131], a[64:67], v[88:91], v[128:131]\n\t"
      "v_cvt_pk_bf16_f32 v138, v132, v133\n\t"
      "v_mfma_f32_16x16x32_bf16 v[128:131], a[88:91], v[92:95], v[128:131]\n\t"
      "v_cvt_pk_bf16_f32 v139, v134, v135\n\t"
      "v_mfma_f32_16x16x32_bf16 v[128:131], a[112:115], v[96:99], v[128:131]\n\t"
      "ds_write_b128 v218, v[136:139] offset:12288\n\t"
      "v_mfma_f32_16x16x32_bf16 v[128:131], a[136:139], v[100:103], v[128:131]\n\t"
      "v_mfma_f32_16x16x32_bf16 v[128:131], a[160:163], v[104:107], v[128:131]\n\t"
      "v_mfma_f32_16x16x32_bf16 v[128:131], a[184:187], v[108:111], v[128:131]\n\t"
      "ds_read_b128 v[148:151], v220 offset:2048\n\t"
      "v_mfma_f32_16x16x32_bf16 v[128:131], a[208:211], v[112:115], v[128:131]\n\t"
      "v_mfma_f32_16x16x32_bf16 v[128:131], a[232:235], v[116:119], v[128:131]\n\t"
      "ds_read_b128 v[72:75], v196 offset:512\n\t"
      "v_mfma_f32_16x16x32_bf16 v[128:131], v[164:167], v[120:123], v[128:131]\n\t"
      "v_mfma_f32_16x16x32_bf16 v[128:131], v[188:191], v[124:127], v[128:131]\n\t"
      "buffer_load_dwordx4 v208, s[20:23], s73 offen offset:1024 lds\n\t"
      "v_mfma_f32_16x16x32_bf16 v[132:135], a[20:23], v[80:83], v[28:31]\n\t"
      "ds_read_b128 v[76:79], v197 offset:512\n\t"
      "v_mfma_f32_16x16x32_bf16 v[132:135], a[44:47], v[84:87], v[132:135]\n\t"
      "v_mfma_f32_16x16x32_bf16 v[132:135], a[68:71], v[88:91], v[132:135]\n\t"
      "v_cvt_pk_bf16_f32 v136, v128, v129\n\t"
      "v_mfma_f32_16x16x32_bf16 v[132:135], a[92:95], v[92:95], v[132:135]\n\t"
      "v_cvt_pk_bf16_f32 v137, v130, v131\n\t"
      "s_sub_u32 s84, s84, 4\n\t"
      "s_max_i32 s84, s84, s86\n\t"
      "v_mfma_f32_16x16x32_bf16 v[132:135], a[116:119], v[96:99], v[132:135]\n\t"
      "s_sub_u32 s83, s82, s80\n\t"
      "v_mfma_f32_16x16x32_bf16 v[132:135], a[140:143], v[100:103], v[132:135]\n\t"
      "s_max_i32 s83, s83, 0\n\t"
      "s_add_u32 s82, s80, s83\n\t"
      "v_mfma_f32_16x16x32_bf16 v[132:135], a[164:167], v[104:107], v[132:135]\n\t"
      "s_mov_b32 s45, s46\n\t"
      "v_mfma_f32_16x16x32_bf16 v[132:135], a[188:191], v[108:111], v[132:135]\n\t"
      "s_mov_b32 s46, s47\n\t"
      "v_mfma_f32_16x16x32_bf16 v[132:135], a[212:215], v[112:115], v[132:135]\n\t"
      "s_mov_b32 s47, s48\n\t"
      "v_mfma_f32_16x16x32_bf16 v[132:135], a[236:239], v[116:119], v[132:135]\n\t"
      "s_add_u32 s82, s82, s84\n\t"
      "v_mfma_f32_16x16x32_bf16 v[132:135], v[168:171], v[120:123], v[132:135]\n\t"
      "s_cmp_lg_u32 s44, s49\n\t"
      "v_mfma_f32_16x16x32_bf16 v[132:135], v[192:195], v[124:127], v[132:135]\n\t"
      "s_waitcnt lgkmcnt(2)\n\t"
      "buffer_store_dwordx4 v[148:151], v208, s[28:31], s72 offen offset:2048 sc1\n\t"
      "s_cbranch_scc1 .Lloop_%=\n\t"
      "s_cmp_lg_u32 s44, 17\n\t"
      "s_cbranch_scc1 .Lstop_%=\n\t"
      "v_readlane_b32 s72, v249, s44\n\t"
      "ds_read_b32 v241, v247\n\t"
      "s_nop 2\n\t"
      "v_cvt_pk_bf16_f32 v138, v132, v133\n\t"
      "v_cvt_pk_bf16_f32 v139, v134, v135\n\t"
      "ds_write_b128 v219, v[136:139] offset:12288\n\t"
      "s_waitcnt lgkmcnt(0)\n\t"
      "s_barrier\n\t"
      "ds_read_b128 v[32:35], v220 offset:12288\n\t"
      "ds_read_b128 v[36:39], v220 offset:13312\n\t"
      "ds_read_b128 v[40:43], v220 offset:14336\n\t"
      "v_cmp_ne_u32_e32 vcc, 0, v241\n\t"
      "s_cmp_lg_u32 s62, 0\n\t"
      "s_waitcnt lgkmcnt(2)\n\t"
      "buffer_store_dwordx4 v[32:35], v209, s[28:31], s72 offen sc1\n\t"
      "s_waitcnt lgkmcnt(1)\n\t"
      "buffer_store_dwordx4 v[36:39], v209, s[28:31], s72 offen offset:1024 sc1\n\t"
      "s_waitcnt lgkmcnt(0)\n\t"
      "buffer_store_dwordx4 v[40:43], v209, s[28:31], s72 offen offset:2048 sc1\n\t"
      "s_cbranch_scc1 .Lfin_%=\n\t"
      "s_cbranch_vccnz .Lsec_%=\n\t"
      ".Lfin_%=:\n\t"
      "s_endpgm\n\t"
      ".Lstop_%=:\n\t"
      "s_and_b32 s49, s71, 31\n\t"
      "s_lshr_b32 s71, s71, 5\n\t"
      "s_sub_u32 s78, s44, s89\n\t"
      "s_cmp_eq_u32 s78, 10\n\t"
      "s_cbranch_scc1 .Lstop2_%=\n\t"
      "s_cmp_eq_u32 s78, 5\n\t"
      "s_cbranch_scc1 .Lstop1_%=\n\t"
      "s_waitcnt vmcnt(12)\n\t"
      "v_readlane_b32 s76, v254, 1\n\t"
      "v_readlane_b32 s78, v255, 1\n\t"
      "s_andn2_b32 s76, 4, s76\n\t"
      "s_or_b32 s76, s76, s64\n\t"
      "s_xor_b32 s76, s76, s78\n\t"
      "v_mov_b32_e32 v240, s76\n\t"
      "ds_write_b32 v247, v240\n\t"
      "s_branch .Lloop_%=\n\t"
      ".Lstop1_%=:\n\t"
      "s_cmp_eq_u32 s94, 0\n\t"
      "s_cbranch_scc1 .Llt0_%=\n\t"
      "ds_read_b128 v[226:229], v238 offset:24576\n\t"
      "ds_read_b128 v[230:233], v238 offset:25600\n\t"
      "ds_read_b128 v[234:237], v238 offset:26624\n\t"
      "v_readlane_b32 s68, v248, 0\n\t"
      "s_nop 4\n\t"
      "s_waitcnt lgkmcnt(2)\n\t"
      "buffer_store_dwordx4 v[226:229], v208, s[28:31], s68 offen sc1\n\t"
      "s_waitcnt lgkmcnt(1)\n\t"
      "buffer_store_dwordx4 v[230:233], v208, s[28:31], s68 offen offset:1024 sc1\n\t"
      "s_waitcnt lgkmcnt(0)\n\t"
      "buffer_store_dwordx4 v[234:237], v208, s[28:31], s68 offen offset:2048 sc1\n\t"
      ".Llt0_%=:\n\t"
      "v_readlane_b32 s76, v254, 0\n\t"
      "s_cmp_lg_u32 s41, s43\n\t"
      "s_cbranch_scc1 .Lloop_%=\n\t"
      "s_andn2_b32 s76, 4, s76\n\t"
      "s_or_b32 s76, s76, s64\n\t"
      "v_mov_b32_e32 v240, s76\n\t"
      "s_mov_b64 exec, 1\n\t"
      "buffer_store_dword v240, v253, s[24:27], 0 offen sc1\n\t"
      "s_mov_b64 exec, -1\n\t"
      "s_branch .Lloop_%=\n\t"
      ".Lstop2_%=:\n\t"
      "s_cmp_eq_u32 s94, 0\n\t"
      "s_cbranch_scc1 .Llt1_%=\n\t"
      "ds_read_b128 v[226:229], v238 offset:36864\n\t"
      "ds_read_b128 v[230:233], v238 offset:37888\n\t"
      "ds_read_b128 v[234:237], v238 offset:38912\n\t"
      "v_readlane_b32 s68, v249, 0\n\t"
      "s_nop 4\n\t"
      "s_waitcnt lgkmcnt(2)\n\t"
      "buffer_store_dwordx4 v[226:229], v209, s[28:31], s68 offen sc1\n\t"
      "s_waitcnt lgkmcnt(1)\n\t"
      "buffer_store_dwordx4 v[230:233], v209, s[28:31], s68 offen offset:1024 sc1\n\t"
      "s_waitcnt lgkmcnt(0)\n\t"
      "buffer_store_dwordx4 v[234:237], v209, s[28:31], s68 offen offset:2048 sc1\n\t"
      ".Llt1_%=:\n\t"
      "s_cmp_lg_u32 s41, s43\n\t"
      "s_cbranch_scc1 .Lloop_%=\n\t"
      "buffer_load_dword v255, v253, s[24:27], 0 offen sc1\n\t"
      "s_branch .Lloop_%=\n\t"
      ".LslpP3_%=:\n\t"
      "s_sleep 1\n\t"
      "s_sub_u32 s83, s83, 3\n\t"
      "s_cmp_gt_i32 s83, 1\n\t"
      "s_cbranch_scc1 .LslpP3_%=\n\t"
      "s_branch .LretP3_%=\n\t"
      ".LslpP4_%=:\n\t"
      "s_sleep 1\n\t"
      "s_sub_u32 s83, s83, 3\n\t"
      "s_cmp_gt_i32 s83, 1\n\t"
      "s_cbranch_scc1 .LslpP4_%=\n\t"
      "s_branch .LretP4_%=\n\t"
      ".LslpP5_%=:\n\t"
      "s_sleep 1\n\t"
      "s_sub_u32 s83, s83, 3\n\t"
      "s_cmp_gt_i32 s83, 1\n\t"
      "s_cbranch_scc1 .LslpP5_%=\n\t"
      "s_branch .LretP5_%=\n\t"
      ".LslpA_%=:\n\t"
      "s_sleep 1\n\t"
      "s_sub_u32 s83, s83, 3\n\t"
      "s_cmp_gt_i32 s83, 1\n\t"
      "s_cbranch_scc1 .LslpA_%=\n\t"
      "s_branch .LretA_%=\n\t"
      ".LslpB_%=:\n\t"
      "s_sleep 1\n\t"
      "s_sub_u32 s83, s83, 3\n\t"
      "s_cmp_gt_i32 s83, 1\n\t"
      "s_cbranch_scc1 .LslpB_%=\n\t"
      "s_branch .LretB_%=\n\t"
      ".Lsec_%=:\n\t"
      "s_mov_b32 s62, 1\n\t"
      "v_lshl_add_u32 v240, v251, 1, s66\n\t"
      "v_sub_u32_e32 v248, v240, v248\n\t"
      "v_sub_u32_e32 v249, v240, v249\n\t"
      "v_cmp_eq_u32_e32 vcc, 0, v252\n\t"
      "v_mov_b32_e32 v240, 0x6c00000\n\t"
      "s_nop 1\n\t"
      "v_cndmask_b32_e32 v248, v248, v240, vcc\n\t"
      "v_cndmask_b32_e32 v249, v249, v240, vcc\n\t"
      "s_add_u32 s44, s94, 3\n\t"
      "s_add_u32 s85, s44, 1\n\t"
      "s_mov_b32 s83, 0\n\t"
      "s_waitcnt vmcnt(0) lgkmcnt(0)\n\t"
      "s_barrier\n\t"
      "v_readlane_b32 s73, v248, s85\n\t"
      "s_mov_b32 m0, s46\n\t"
      "s_nop 4\n\t"
      "buffer_load_dwordx4 v208, s[20:23], s73 offen lds\n\t"
      "buffer_load_dwordx4 v208, s[20:23], s73 offen offset:1024 lds\n\t"
      "buffer_load_dwordx4 v208, s[20:23], s73 offen offset:2048 lds\n\t"
      "v_readlane_b32 s73, v249, s85\n\t"
      "s_add_u32 m0, s46, 0x3000\n\t"
      "s_nop 4\n\t"
      "buffer_load_dwordx4 v209, s[20:23], s73 offen lds\n\t"
      "buffer_load_dwordx4 v209, s[20:23], s73 offen offset:1024 lds\n\t"
      "buffer_load_dwordx4 v209, s[20:23], s73 offen offset:2048 lds\n\t"
      "s_add_u32 s85, s85, 1\n\t"
      "s_nop 4\n\t"
      "v_readlane_b32 s73, v248, s85\n\t"
      "s_mov_b32 m0, s47\n\t"
      "s_nop 4\n\t"
      "buffer_load_dwordx4 v208, s[20:23], s73 offen lds\n\t"
      "buffer_load_dwordx4 v208, s[20:23], s73 offen offset:1024 lds\n\t"
      "buffer_load_dwordx4 v208, s[20:23], s73 offen offset:2048 lds\n\t"
      "v_readlane_b32 s73, v249, s85\n\t"
      "s_add_u32 m0, s47, 0x3000\n\t"
      "s_nop 4\n\t"
      "buffer_load_dwordx4 v209, s[20:23], s73 offen lds\n\t"
      "buffer_load_dwordx4 v209, s[20:23], s73 offen offset:1024 lds\n\t"
      "buffer_load_dwordx4 v209, s[20:23], s73 offen offset:2048 lds\n\t"
      "s_add_u32 s85, s85, 1\n\t"
      "s_nop 4\n\t"
      "v_readlane_b32 s73, v248, s85\n\t"
      "s_mov_b32 m0, s45\n\t"
      "s_nop 4\n\t"
      "buffer_load_dwordx4 v208, s[20:23], s73 offen lds\n\t"
      "buffer_load_dwordx4 v208, s[20:23], s73 offen offset:1024 lds\n\t"
      "buffer_load_dwordx4 v208, s[20:23], s73 offen offset:2048 lds\n\t"
      "s_waitcnt vmcnt(0)\n\t"
      "s_barrier\n\t"
      "ds_read_b128 v[32:35], v196 offset:0\n\t"
      "ds_read_b128 v[36:39], v197 offset:0\n\t"
      "ds_read_b128 v[40:43], v198 offset:0\n\t"
      "ds_read_b128 v[44:47], v199 offset:0\n\t"
      "ds_read_b128 v[48:51], v197 offset:256\n\t"
      "ds_read_b128 v[52:55], v198 offset:256\n\t"
      "ds_read_b128 v[56:59], v199 offset:256\n\t"
      "ds_read_b128 v[60:63], v196 offset:256\n\t"
      "ds_read_b128 v[64:67], v198 offset:512\n\t"
      "ds_read_b128 v[68:71], v199 offset:512\n\t"
      "ds_read_b128 v[72:75], v196 offset:512\n\t"
      "ds_read_b128 v[76:79], v197 offset:512\n\t"
      "s_waitcnt lgkmcnt(0)\n\t"
      "s_branch .Lloop_%=\n\t"
      ".Lgs_%=:\n\t"
      "s_sleep 8\n\t"
      "buffer_load_dwordx2 v[240:241], v245, s[24:27], 0 offen sc1\n\t"
      "s_memrealtime s[76:77]\n\t"
      "s_waitcnt vmcnt(0) lgkmcnt(0)\n\t"
      "v_subrev_u32_e32 v242, s63, v240\n\t"
      "v_xor_b32_e32 v241, s53, v241\n\t"
      "v_lshrrev_b32_e32 v240, 13, v242\n\t"
      "v_or_b32_e32 v240, v240, v241\n\t"
      "v_cmp_ne_u32_e32 vcc, 0, v240\n\t"
      "v_and_b32_e32 v242, 15, v242\n\t"
      "s_cbranch_vccz .Lgr_%=\n\t"
      "s_sub_u32 s76, s76, s52\n\t"
      "s_cmp_lt_u32 s76, 2000\n\t"
      "s_cbranch_scc1 .Lgs_%=\n\t"
      "v_mov_b32_e32 v242, 64\n\t"
      "s_branch .Lgr_%=\n\t"
      ".Lpb_%=:\n\t"
      "s_mov_b64 vcc, -1\n\t"
      "s_branch .Lp2_%=\n\t"
      ".Lwsb_%=:\n\t"
      "s_mov_b64 vcc, 0\n\t"
      "s_waitcnt vmcnt(12)\n\t"
      "v_lshlrev_b32_e32 v8, 16, v226\n\t"
      "v_and_b32_e32 v9, 0xffff0000, v226\n\t"
      "v_lshlrev_b32_e32 v10, 16, v227\n\t"
      "v_and_b32_e32 v11, 0xffff0000, v227\n\t"
      "v_lshlrev_b32_e32 v12, 16, v228\n\t"
      "v_and_b32_e32 v13, 0xffff0000, v228\n\t"
      "v_lshlrev_b32_e32 v14, 16, v229\n\t"
      "v_and_b32_e32 v15, 0xffff0000, v229\n\t"
      "v_lshlrev_b32_e32 v16, 16, v230\n\t"
      "v_and_b32_e32 v17, 0xffff0000, v230\n\t"
      "v_lshlrev_b32_e32 v18, 16, v231\n\t"
      "v_and_b32_e32 v19, 0xffff0000, v231\n\t"
      "v_lshlrev_b32_e32 v20, 16, v232\n\t"
      "v_and_b32_e32 v21, 0xffff0000, v232\n\t"
      "v_lshlrev_b32_e32 v22, 16, v233\n\t"
      "v_and_b32_e32 v23, 0xffff0000, v233\n\t"
      "v_lshlrev_b32_e32 v24, 16, v234\n\t"
      "v_and_b32_e32 v25, 0xffff0000, v234\n\t"
      "v_lshlrev_b32_e32 v26, 16, v235\n\t"
      "v_and_b32_e32 v27, 0xffff0000, v235\n\t"
      "v_lshlrev_b32_e32 v28, 16, v236\n\t"
      "v_and_b32_e32 v29, 0xffff0000, v236\n\t"
      "v_lshlrev_b32_e32 v30, 16, v237\n\t"
      "v_and_b32_e32 v31, 0xffff0000, v237\n\t"
      "s_add_u32 m0, s41, 0x1e000\n\t"
      "s_mov_b32 s51, 0x0\n\t"
      "buffer_load_dwordx4 v211, s[16:19], s51 offen lds\n\t"
      "buffer_load_dwordx4 v212, s[16:19], s51 offen offset:1024 lds\n\t"
      "buffer_load_dwordx4 v213, s[16:19], s51 offen offset:2048 lds\n\t"
      "s_add_u32 m0, s41, 0x21000\n\t"
      "s_mov_b32 s51, 0xc00\n\t"
      "buffer_load_dwordx4 v211, s[16:19], s51 offen lds\n\t"
      "buffer_load_dwordx4 v212, s[16:19], s51 offen offset:1024 lds\n\t"
      "buffer_load_dwordx4 v213, s[16:19], s51 offen offset:2048 lds\n\t"
      "s_add_u32 m0, s41, 0x24000\n\t"
      "s_mov_b32 s51, 0x6000\n\t"
      "buffer_load_dwordx4 v211, s[16:19], s51 offen lds\n\t"
      "buffer_load_dwordx4 v212, s[16:19], s51 offen offset:1024 lds\n\t"
      "buffer_load_dwordx4 v213, s[16:19], s51 offen offset:2048 lds\n\t"
      "s_add_u32 m0, s41, 0x12000\n\t"
      "s_mov_b32 s51, 0x6c00\n\t"
      "buffer_load_dwordx4 v211, s[16:19], s51 offen lds\n\t"
      "buffer_load_dwordx4 v212, s[16:19], s51 offen offset:1024 lds\n\t"
      "buffer_load_dwordx4 v213, s[16:19], s51 offen offset:2048 lds\n\t"
      "s_add_u32 m0, s41, 0x15000\n\t"
      "s_mov_b32 s51, 0xc000\n\t"
      "buffer_load_dwordx4 v211, s[16:19], s51 offen lds\n\t"
      "buffer_load_dwordx4 v212, s[16:19], s51 offen offset:1024 lds\n\t"
      "buffer_load_dwordx4 v213, s[16:19], s51 offen offset:2048 lds\n\t"
      "s_mov_b32 s44, 1\n\t"
      "s_mov_b32 s85, 4\n\t"
      "s_add_u32 s45, s41, 0x6000\n\t"
      "s_add_u32 s46, s41, 0xc000\n\t"
      "s_mov_b32 s47, s41\n\t"
      "s_waitcnt vmcnt(12)\n\t"
      "s_barrier\n\t"
      "ds_read_b64_tr_b16 a[0:1], v214 offset:49152\n\t"
      "ds_read_b64_tr_b16 a[4:5], v214 offset:49160\n\t"
      "ds_read_b64_tr_b16 a[8:9], v214 offset:49216\n\t"
      "ds_read_b64_tr_b16 a[12:13], v214 offset:49224\n\t"
      "ds_read_b64_tr_b16 a[16:17], v214 offset:49280\n\t"
      "ds_read_b64_tr_b16 a[20:21], v214 offset:49288\n\t"
      "ds_read_b128 v[156:159], v196 offset:0\n\t"
      "ds_read_b128 v[160:163], v196 offset:12288\n\t"
      "ds_read_b128 v[128:131], v196 offset:24576\n\t"
      "s_add_u32 m0, s41, 0x18000\n\t"
      "s_mov_b32 s51, 0xcc00\n\t"
      "buffer_load_dwordx4 v211, s[16:19], s51 offen lds\n\t"
      "buffer_load_dwordx4 v212, s[16:19], s51 offen offset:1024 lds\n\t"
      "buffer_load_dwordx4 v213, s[16:19], s51 offen offset:2048 lds\n\t"
      "s_waitcnt vmcnt(12)\n\t"
      "ds_read_b64_tr_b16 a[2:3], v221 offset:0\n\t"
      "ds_read_b64_tr_b16 a[6:7], v221 offset:8\n\t"
      "ds_read_b64_tr_b16 a[10:11], v221 offset:64\n\t"
      "ds_read_b64_tr_b16 a[14:15], v221 offset:72\n\t"
      "ds_read_b64_tr_b16 a[18:19], v221 offset:128\n\t"
      "ds_read_b64_tr_b16 a[22:23], v221 offset:136\n\t"
      "s_add_u32 m0, s41, 0x1b000\n\t"
      "s_mov_b32 s51, 0x12000\n\t"
      "buffer_load_dwordx4 v211, s[16:19], s51 offen lds\n\t"
      "buffer_load_dwordx4 v212, s[16:19], s51 offen offset:1024 lds\n\t"
      "buffer_load_dwordx4 v213, s[16:19], s51 offen offset:2048 lds\n\t"
      "s_waitcnt lgkmcnt(0)\n\t"
      "v_mfma_f32_16x16x32_bf16 v[32:35], a[0:3], v[156:159], v[8:11]\n\t"
      "v_mfma_f32_16x16x32_bf16 v[36:39], a[4:7], v[156:159], v[12:15]\n\t"
      "s_waitcnt vmcnt(12)\n\t"
      "ds_read_b64_tr_b16 a[24:25], v221 offset:12288\n\t"
      "v_mfma_f32_16x16x32_bf16 v[40:43], a[8:11], v[156:159], v[16:19]\n\t"
      "ds_read_b64_tr_b16 a[28:29], v221 offset:12296\n\t"
      "v_mfma_f32_16x16x32_bf16 v[44:47], a[12:15], v[156:159], v[20:23]\n\t"
      "ds_read_b64_tr_b16 a[32:33], v221 offset:12352\n\t"
      "v_mfma_f32_16x16x32_bf16 v[48:51], a[16:19], v[156:159], v[24:27]\n\t"
      "ds_read_b64_tr_b16 a[36:37], v221 offset:12360\n\t"
      "ds_read_b128 v[136:139], v196 offset:36864\n\t"
      "v_mfma_f32_16x16x32_bf16 v[52:55], a[20:23], v[156:159], v[28:31]\n\t"
      "ds_read_b64_tr_b16 a[40:41], v221 offset:12416\n\t"
      "v_mfma_f32_16x16x32_bf16 v[56:59], a[0:3], v[160:163], v[8:11]\n\t"
      "ds_read_b64_tr_b16 a[44:45], v221 offset:12424\n\t"
      "v_mfma_f32_16x16x32_bf16 v[60:63], a[4:7], v[160:163], v[12:15]\n\t"
      "v_mfma_f32_16x16x32_bf16 v[64:67], a[8:11], v[160:163], v[16:19]\n\t"
      "s_waitcnt vmcnt(9)\n\t"
      "ds_read_b64_tr_b16 a[26:27], v214 offset:0\n\t"
      "v_mfma_f32_16x16x32_bf16 v[68:71], a[12:15], v[160:163], v[20:23]\n\t"
      "ds_read_b64_tr_b16 a[30:31], v214 offset:8\n\t"
      "v_mfma_f32_16x16x32_bf16 v[72:75], a[16:19], v[160:163], v[24:27]\n\t"
      "ds_read_b64_tr_b16 a[34:35], v214 offset:64\n\t"
      "ds_read_b128 v[140:143], v197 offset:0\n\t"
      "v_mfma_f32_16x16x32_bf16 v[76:79], a[20:23], v[160:163], v[28:31]\n\t"
      "ds_read_b64_tr_b16 a[38:39], v214 offset:72\n\t"
      "v_mfma_f32_16x16x32_bf16 v[80:83], a[0:3], v[128:131], v[8:11]\n\t"
      "ds_read_b64_tr_b16 a[42:43], v214 offset:128\n\t"
      "s_add_u32 m0, s41, 0x1e000\n\t"
      "s_mov_b32 s51, 0x12c00\n\t"
      "buffer_load_dwordx4 v211, s[16:19], s51 offen lds\n\t"
      "v_mfma_f32_16x16x32_bf16 v[84:87], a[4:7], v[128:131], v[12:15]\n\t"
      "ds_read_b64_tr_b16 a[46:47], v214 offset:136\n\t"
      "v_mfma_f32_16x16x32_bf16 v[88:91], a[8:11], v[128:131], v[16:19]\n\t"
      "v_mfma_f32_16x16x32_bf16 v[92:95], a[12:15], v[128:131], v[20:23]\n\t"
      "buffer_load_dwordx4 v212, s[16:19], s51 offen offset:1024 lds\n\t"
      "v_mfma_f32_16x16x32_bf16 v[96:99], a[16:19], v[128:131], v[24:27]\n\t"
      "ds_read_b128 v[144:147], v197 offset:12288\n\t"
      "v_mfma_f32_16x16x32_bf16 v[100:103], a[20:23], v[128:131], v[28:31]\n\t"
      "s_waitcnt lgkmcnt(10)\n\t"
      "v_mfma_f32_16x16x32_bf16 v[104:107], a[0:3], v[136:139], v[8:11]\n\t"
      "buffer_load_dwordx4 v213, s[16:19], s51 offen offset:2048 lds\n\t"
      "v_mfma_f32_16x16x32_bf16 v[108:111], a[4:7], v[136:139], v[12:15]\n\t"
      "v_mfma_f32_16x16x32_bf16 v[112:115], a[8:11], v[136:139], v[16:19]\n\t"
      "s_waitcnt vmcnt(9)\n\t"
      "ds_read_b64_tr_b16 a[48:49], v214 offset:12288\n\t"
      "v_mfma_f32_16x16x32_bf16 v[116:119], a[12:15], v[136:139], v[20:23]\n\t"
      "ds_read_b64_tr_b16 a[52:53], v214 offset:12296\n\t"
      "s_add_u32 m0, s41, 0x21000\n\t"
      "s_mov_b32 s51, 0x18000\n\t"
      "buffer_load_dwordx4 v211, s[16:19], s51 offen lds\n\t"
      "v_mfma_f32_16x16x32_bf16 v[120:123], a[16:19], v[136:139], v[24:27]\n\t"
      "ds_read_b64_tr_b16 a[56:57], v214 offset:12352\n\t"
      "ds_read_b128 v[148:151], v197 offset:24576\n\t"
      "v_mfma_f32_16x16x32_bf16 v[132:135], a[20:23], v[136:139], v[28:31]\n\t"
      "ds_read_b64_tr_b16 a[60:61], v214 offset:12360\n\t"
      "s_waitcnt lgkmcnt(6)\n\t"
      "v_mfma_f32_16x16x32_bf16 v[32:35], a[24:27], v[140:143], v[32:35]\n\t"
      "ds_read_b64_tr_b16 a[64:65], v214 offset:12416\n\t"
      "buffer_load_dwordx4 v212, s[16:19], s51 offen offset:1024 lds\n\t"
      "v_mfma_f32_16x16x32_bf16 v[36:39], a[28:31], v[140:143], v[36:39]\n\t"
      "ds_read_b64_tr_b16 a[68:69], v214 offset:12424\n\t"
      "v_mfma_f32_16x16x32_bf16 v[40:43], a[32:35], v[140:143], v[40:43]\n\t"
      "v_mfma_f32_16x16x32_bf16 v[44:47], a[36:39], v[140:143], v[44:47]\n\t"
      "buffer_load_dwordx4 v213, s[16:19], s51 offen offset:2048 lds\n\t"
      "v_mfma_f32_16x16x32_bf16 v[48:51], a[40:43], v[140:143], v[48:51]\n\t"
      "ds_read_b128 v[152:155], v197 offset:36864\n\t"
      "v_mfma_f32_16x16x32_bf16 v[52:55], a[44:47], v[140:143], v[52:55]\n\t"
      "s_waitcnt lgkmcnt(8)\n\t"
      "v_mfma_f32_16x16x32_bf16 v[56:59], a[24:27], v[144:147], v[56:59]\n\t"
      "s_add_u32 m0, s41, 0x24000\n\t"
      "s_mov_b32 s51, 0x18c00\n\t"
      "buffer_load_dwordx4 v211, s[16:19], s51 offen lds\n\t"
      "v_mfma_f32_16x16x32_bf16 v[60:63], a[28:31], v[144:147], v[60:63]\n\t"
      "v_mfma_f32_16x16x32_bf16 v[64:67], a[32:35], v[144:147], v[64:67]\n\t"
      "s_waitcnt vmcnt(10)\n\t"
      "ds_read_b64_tr_b16 a[50:51], v214 offset:24576\n\t"
      "v_mfma_f32_16x16x32_bf16 v[68:71], a[36:39], v[144:147], v[68:71]\n\t"
      "ds_read_b64_tr_b16 a[54:55], v214 offset:24584\n\t"
      "buffer_load_dwordx4 v212, s[16:19], s51 offen offset:1024 lds\n\t"
      "v_mfma_f32_16x16x32_bf16 v[72:75], a[40:43], v[144:147], v[72:75]\n\t"
      "ds_read_b64_tr_b16 a[58:59], v214 offset:24640\n\t"
      "ds_read_b128 v[156:159], v198 offset:0\n\t"
      "v_mfma_f32_16x16x32_bf16 v[76:79], a[44:47], v[144:147], v[76:79]\n\t"
      "ds_read_b64_tr_b16 a[62:63], v214 offset:24648\n\t"
      "s_waitcnt lgkmcnt(9)\n\t"
      "v_mfma_f32_16x16x32_bf16 v[80:83], a[24:27], v[148:151], v[80:83]\n\t"
      "ds_read_b64_tr_b16 a[66:67], v214 offset:24704\n\t"
      "buffer_load_dwordx4 v213, s[16:19], s51 offen offset:2048 lds\n\t"
      "v_mfma_f32_16x16x32_bf16 v[84:87], a[28:31], v[148:151], v[84:87]\n\t"
      "ds_read_b64_tr_b16 a[70:71], v214 offset:24712\n\t"
      "v_mfma_f32_16x16x32_bf16 v[88:91], a[32:35], v[148:151], v[88:91]\n\t"
      "v_mfma_f32_16x16x32_bf16 v[92:95], a[36:39], v[148:151], v[92:95]\n\t"
      "s_add_u32 m0, s41, 0x12000\n\t"
      "s_mov_b32 s51, 0x1e000\n\t"
      "buffer_load_dwordx4 v211, s[16:19], s51 offen lds\n\t"
      "v_mfma_f32_16x16x32_bf16 v[96:99], a[40:43], v[148:151], v[96:99]\n\t"
      "ds_read_b128 v[160:163], v198 offset:12288\n\t"
      "v_mfma_f32_16x16x32_bf16 v[100:103], a[44:47], v[148:151], v[100:103]\n\t"
      "s_waitcnt lgkmcnt(8)\n\t"
      "v_mfma_f32_16x16x32_bf16 v[104:107], a[24:27], v[152:155], v[104:107]\n\t"
      "buffer_load_dwordx4 v212, s[16:19], s51 offen offset:1024 lds\n\t"
      "v_mfma_f32_16x16x32_bf16 v[108:111], a[28:31], v[152:155], v[108:111]\n\t"
      "v_mfma_f32_16x16x32_bf16 v[112:115], a[32:35], v[152:155], v[112:115]\n\t"
      "s_waitcnt vmcnt(11)\n\t"
      "ds_read_b64_tr_b16 a[72:73], v214 offset:36864\n\t"
      "v_mfma_f32_16x16x32_bf16 v[116:119], a[36:39], v[152:155], v[116:119]\n\t"
      "ds_read_b64_tr_b16 a[76:77], v214 offset:36872\n\t"
      "buffer_load_dwordx4 v213, s[16:19], s51 offen offset:2048 lds\n\t"
      "v_mfma_f32_16x16x32_bf16 v[120:123], a[40:43], v[152:155], v[120:123]\n\t"
      "ds_read_b64_tr_b16 a[80:81], v214 offset:36928\n\t"
      "ds_read_b128 v[128:131], v198 offset:24576\n\t"
      "v_mfma_f32_16x16x32_bf16 v[132:135], a[44:47], v[152:155], v[132:135]\n\t"
      "ds_read_b64_tr_b16 a[84:85], v214 offset:36936\n\t"
      "s_waitcnt lgkmcnt(6)\n\t"
      "v_mfma_f32_16x16x32_bf16 v[32:35], a[48:51], v[156:159], v[32:35]\n\t"
      "ds_read_b64_tr_b16 a[88:89], v214 offset:36992\n\t"
      "s_add_u32 m0, s41, 0x15000\n\t"
      "s_mov_b32 s51, 0x1ec00\n\t"
      "buffer_load_dwordx4 v211, s[16:19], s51 offen lds\n\t"
      "v_mfma_f32_16x16x32_bf16 v[36:39], a[52:55], v[156:159], v[36:39]\n\t"
      "ds_read_b64_tr_b16 a[92:93], v214 offset:37000\n\t"
      "v_mfma_f32_16x16x32_bf16 v[40:43], a[56:59], v[156:159], v[40:43]\n\t"
      "v_mfma_f32_16x16x32_bf16 v[44:47], a[60:63], v[156:159], v[44:47]\n\t"
      "buffer_load_dwordx4 v212, s[16:19], s51 offen offset:1024 lds\n\t"
      "v_mfma_f32_16x16x32_bf16 v[48:51], a[64:67], v[156:159], v[48:51]\n\t"
      "ds_read_b128 v[136:139], v198 offset:36864\n\t"
      "v_mfma_f32_16x16x32_bf16 v[52:55], a[68:71], v[156:159], v[52:55]\n\t"
      "s_waitcnt lgkmcnt(8)\n\t"
      "v_mfma_f32_16x16x32_bf16 v[56:59], a[48:51], v[160:163], v[56:59]\n\t"
      "buffer_load_dwordx4 v213, s[16:19], s51 offen offset:2048 lds\n\t"
      "v_mfma_f32_16x16x32_bf16 v[60:63], a[52:55], v[160:163], v[60:63]\n\t"
      "v_mfma_f32_16x16x32_bf16 v[64:67], a[56:59], v[160:163], v[64:67]\n\t"
      "s_waitcnt vmcnt(12)\n\t"
      "ds_read_b64_tr_b16 a[74:75], v214 offset:49152\n\t"
      "v_mfma_f32_16x16x32_bf16 v[68:71], a[60:63], v[160:163], v[68:71]\n\t"
      "ds_read_b64_tr_b16 a[78:79], v214 offset:49160\n\t"
      "s_add_u32 m0, s41, 0x18000\n\t"
      "s_mov_b32 s51, 0x24000\n\t"
      "buffer_load_dwordx4 v211, s[16:19], s51 offen lds\n\t"
      "v_mfma_f32_16x16x32_bf16 v[72:75], a[64:67], v[160:163], v[72:75]\n\t"
      "ds_read_b64_tr_b16 a[82:83], v214 offset:49216\n\t"
      "ds_read_b128 v[140:143], v199 offset:0\n\t"
      "v_mfma_f32_16x16x32_bf16 v[76:79], a[68:71], v[160:163], v[76:79]\n\t"
      "ds_read_b64_tr_b16 a[86:87], v214 offset:49224\n\t"
      "s_waitcnt lgkmcnt(9)\n\t"
      "v_mfma_f32_16x16x32_bf16 v[80:83], a[48:51], v[128:131], v[80:83]\n\t"
      "ds_read_b64_tr_b16 a[90:91], v214 offset:49280\n\t"
      "buffer_load_dwordx4 v212, s[16:19], s51 offen offset:1024 lds\n\t"
      "v_mfma_f32_16x16x32_bf16 v[84:87], a[52:55], v[128:131], v[84:87]\n\t"
      "ds_read_b64_tr_b16 a[94:95], v214 offset:49288\n\t"
      "v_mfma_f32_16x16x32_bf16 v[88:91], a[56:59], v[128:131], v[88:91]\n\t"
      "v_mfma_f32_16x16x32_bf16 v[92:95], a[60:63], v[128:131], v[92:95]\n\t"
      "buffer_load_dwordx4 v213, s[16:19], s51 offen offset:2048 lds\n\t"
      "v_mfma_f32_16x16x32_bf16 v[96:99], a[64:67], v[128:131], v[96:99]\n\t"
      "ds_read_b128 v[144:147], v199 offset:12288\n\t"
      "v_mfma_f32_16x16x32_bf16 v[100:103], a[68:71], v[128:131], v[100:103]\n\t"
      "s_waitcnt lgkmcnt(8)\n\t"
      "v_mfma_f32_16x16x32_bf16 v[104:107], a[48:51], v[136:139], v[104:107]\n\t"
      "s_add_u32 m0, s41, 0x1b000\n\t"
      "s_mov_b32 s51, 0x24c00\n\t"
      "buffer_load_dwordx4 v211, s[16:19], s51 offen lds\n\t"
      "v_mfma_f32_16x16x32_bf16 v[108:111], a[52:55], v[136:139], v[108:111]\n\t"
      "v_mfma_f32_16x16x32_bf16 v[112:115], a[56:59], v[136:139], v[112:115]\n\t"
      "s_waitcnt vmcnt(13)\n\t"
      "ds_read_b64_tr_b16 a[96:97], v221 offset:0\n\t"
      "v_mfma_f32_16x16x32_bf16 v[116:119], a[60:63], v[136:139], v[116:119]\n\t"
      "ds_read_b64_tr_b16 a[100:101], v221 offset:8\n\t"
      "buffer_load_dwordx4 v212, s[16:19], s51 offen offset:1024 lds\n\t"
      "v_mfma_f32_16x16x32_bf16 v[120:123], a[64:67], v[136:139], v[120:123]\n\t"
      "ds_read_b64_tr_b16 a[104:105], v221 offset:64\n\t"
      "ds_read_b128 v[148:151], v199 offset:24576\n\t"
      "v_mfma_f32_16x16x32_bf16 v[132:135], a[68:71], v[136:139], v[132:135]\n\t"
      "ds_read_b64_tr_b16 a[108:109], v221 offset:72\n\t"
      "s_waitcnt lgkmcnt(6)\n\t"
      "v_mfma_f32_16x16x32_bf16 v[32:35], a[72:75], v[140:143], v[32:35]\n\t"
      "ds_read_b64_tr_b16 a[112:113], v221 offset:128\n\t"
      "buffer_load_dwordx4 v213, s[16:19], s51 offen offset:2048 lds\n\t"
      "v_mfma_f32_16x16x32_bf16 v[36:39], a[76:79], v[140:143], v[36:39]\n\t"
      "ds_read_b64_tr_b16 a[116:117], v221 offset:136\n\t"
      "v_mfma_f32_16x16x32_bf16 v[40:43], a[80:83], v[140:143], v[40:43]\n\t"
      "v_mfma_f32_16x16x32_bf16 v[44:47], a[84:87], v[140:143], v[44:47]\n\t"
      "s_add_u32 m0, s41, 0x1e000\n\t"
      "s_mov_b32 s51, 0x2a000\n\t"
      "buffer_load_dwordx4 v211, s[16:19], s51 offen lds\n\t"
      "v_mfma_f32_16x16x32_bf16 v[48:51], a[88:91], v[140:143], v[48:51]\n\t"
      "ds_read_b128 v[152:155], v199 offset:36864\n\t"
      "v_mfma_f32_16x16x32_bf16 v[52:55], a[92:95], v[140:143], v[52:55]\n\t"
      "s_waitcnt lgkmcnt(8)\n\t"
      "v_mfma_f32_16x16x32_bf16 v[56:59], a[72:75], v[144:147], v[56:59]\n\t"
      "buffer_load_dwordx4 v212, s[16:19], s51 offen offset:1024 lds\n\t"
      "v_mfma_f32_16x16x32_bf16 v[60:63], a[76:79], v[144:147], v[60:63]\n\t"
      "v_mfma_f32_16x16x32_bf16 v[64:67], a[80:83], v[144:147], v[64:67]\n\t"
      "s_waitcnt vmcnt(14)\n\t"
      "ds_read_b64_tr_b16 a[98:99], v221 offset:12288\n\t"
      "v_mfma_f32_16x16x32_bf16 v[68:71], a[84:87], v[144:147], v[68:71]\n\t"
      "ds_read_b64_tr_b16 a[102:103], v221 offset:12296\n\t"
      "buffer_load_dwordx4 v213, s[16:19], s51 offen offset:2048 lds\n\t"
      "v_mfma_f32_16x16x32_bf16 v[72:75], a[88:91], v[144:147], v[72:75]\n\t"
      "ds_read_b64_tr_b16 a[106:107], v221 offset:12352\n\t"
      "ds_read_b128 v[156:159], v197 offset:256\n\t"
      "v_mfma_f32_16x16x32_bf16 v[76:79], a[92:95], v[144:147], v[76:79]\n\t"
      "ds_read_b64_tr_b16 a[110:111], v221 offset:12360\n\t"
      "s_waitcnt lgkmcnt(9)\n\t"
      "v_mfma_f32_16x16x32_bf16 v[80:83], a[72:75], v[148:151], v[80:83]\n\t"
      "ds_read_b64_tr_b16 a[114:115], v221 offset:12416\n\t"
      "s_waitcnt lgkmcnt(7)\n\t"
      "s_add_u32 m0, s41, 0x21000\n\t"
      "s_mov_b32 s51, 0x2ac00\n\t"
      "buffer_load_dwordx4 v211, s[16:19], s51 offen lds\n\t"
      "v_mfma_f32_16x16x32_bf16 v[84:87], a[76:79], v[148:151], v[84:87]\n\t"
      "ds_read_b64_tr_b16 a[118:119], v221 offset:12424\n\t"
      "v_mfma_f32_16x16x32_bf16 v[88:91], a[80:83], v[148:151], v[88:91]\n\t"
      "v_mfma_f32_16x16x32_bf16 v[92:95], a[84:87], v[148:151], v[92:95]\n\t"
      "buffer_load_dwordx4 v212, s[16:19], s51 offen offset:1024 lds\n\t"
      "v_mfma_f32_16x16x32_bf16 v[96:99], a[88:91], v[148:151], v[96:99]\n\t"
      "ds_read_b128 v[160:163], v197 offset:12544\n\t"
      "v_mfma_f32_16x16x32_bf16 v[100:103], a[92:95], v[148:151], v[100:103]\n\t"
      "s_waitcnt lgkmcnt(8)\n\t"
      "v_mfma_f32_16x16x32_bf16 v[104:107], a[72:75], v[152:155], v[104:107]\n\t"
      "buffer_load_dwordx4 v213, s[16:19], s51 offen offset:2048 lds\n\t"
      "v_mfma_f32_16x16x32_bf16 v[108:111], a[76:79], v[152:155], v[108:111]\n\t"
      "v_mfma_f32_16x16x32_bf16 v[112:115], a[80:83], v[152:155], v[112:115]\n\t"
      "s_waitcnt vmcnt(15)\n\t"
      "ds_read_b64_tr_b16 a[120:121], v214 offset:0\n\t"
      "v_mfma_f32_16x16x32_bf16 v[116:119], a[84:87], v[152:155], v[116:119]\n\t"
      "ds_read_b64_tr_b16 a[124:125], v214 offset:8\n\t"
      "s_waitcnt lgkmcnt(3)\n\t"
      "s_add_u32 m0, s41, 0x24000\n\t"
      "s_mov_b32 s51, 0x30000\n\t"
      "buffer_load_dwordx4 v211, s[16:19], s51 offen lds\n\t"
      "v_mfma_f32_16x16x32_bf16 v[120:123], a[88:91], v[152:155], v[120:123]\n\t"
      "ds_read_b64_tr_b16 a[128:129], v214 offset:64\n\t"
      "ds_read_b128 v[128:131], v197 offset:24832\n\t"
      "v_mfma_f32_16x16x32_bf16 v[132:135], a[92:95], v[152:155], v[132:135]\n\t"
      "ds_read_b64_tr_b16 a[132:133], v214 offset:72\n\t"
      "v_mfma_f32_16x16x32_bf16 v[32:35], a[96:99], v[156:159], v[32:35]\n\t"
      "ds_read_b64_tr_b16 a[136:137], v214 offset:128\n\t"
      "buffer_load_dwordx4 v212, s[16:19], s51 offen offset:1024 lds\n\t"
      "v_mfma_f32_16x16x32_bf16 v[36:39], a[100:103], v[156:159], v[36:39]\n\t"
      "ds_read_b64_tr_b16 a[140:141], v214 offset:136\n\t"
      "v_mfma_f32_16x16x32_bf16 v[40:43], a[104:107], v[156:159], v[40:43]\n\t"
      "v_mfma_f32_16x16x32_bf16 v[44:47], a[108:111], v[156:159], v[44:47]\n\t"
      "buffer_load_dwordx4 v213, s[16:19], s51 offen offset:2048 lds\n\t"
      "v_mfma_f32_16x16x32_bf16 v[48:51], a[112:115], v[156:159], v[48:51]\n\t"
      "ds_read_b128 v[136:139], v197 offset:37120\n\t"
      "v_mfma_f32_16x16x32_bf16 v[52:55], a[116:119], v[156:159], v[52:55]\n\t"
      "s_waitcnt lgkmcnt(8)\n\t"
      "v_mfma_f32_16x16x32_bf16 v[56:59], a[96:99], v[160:163], v[56:59]\n\t"
      "v_mfma_f32_16x16x32_bf16 v[60:63], a[100:103], v[160:163], v[60:63]\n\t"
      "v_mfma_f32_16x16x32_bf16 v[64:67], a[104:107], v[160:163], v[64:67]\n\t"
      "s_waitcnt vmcnt(15)\n\t"
      "ds_read_b64_tr_b16 a[122:123], v214 offset:12288\n\t"
      "s_waitcnt lgkmcnt(2)\n\t"
      "s_add_u32 m0, s41, 0x12000\n\t"
      "s_mov_b32 s51, 0x30c00\n\t"
      "buffer_load_dwordx4 v211, s[16:19], s51 offen lds\n\t"
      "v_mfma_f32_16x16x32_bf16 v[68:71], a[108:111], v[160:163], v[68:71]\n\t"
      "ds_read_b64_tr_b16 a[126:127], v214 offset:12296\n\t"
      "v_mfma_f32_16x16x32_bf16 v[72:75], a[112:115], v[160:163], v[72:75]\n\t"
      "ds_read_b64_tr_b16 a[130:131], v214 offset:12352\n\t"
      "ds_read_b128 v[140:143], v198 offset:256\n\t"
      "v_mfma_f32_16x16x32_bf16 v[76:79], a[116:119], v[160:163], v[76:79]\n\t"
      "ds_read_b64_tr_b16 a[134:135], v214 offset:12360\n\t"
      "buffer_load_dwordx4 v212, s[16:19], s51 offen offset:1024 lds\n\t"
      "v_mfma_f32_16x16x32_bf16 v[80:83], a[96:99], v[128:131], v[80:83]\n\t"
      "ds_read_b64_tr_b16 a[138:139], v214 offset:12416\n\t"
      "v_mfma_f32_16x16x32_bf16 v[84:87], a[100:103], v[128:131], v[84:87]\n\t"
      "ds_read_b64_tr_b16 a[142:143], v214 offset:12424\n\t"
      "v_mfma_f32_16x16x32_bf16 v[88:91], a[104:107], v[128:131], v[88:91]\n\t"
      "buffer_load_dwordx4 v213, s[16:19], s51 offen offset:2048 lds\n\t"
      "v_mfma_f32_16x16x32_bf16 v[92:95], a[108:111], v[128:131], v[92:95]\n\t"
      "v_mfma_f32_16x16x32_bf16 v[96:99], a[112:115], v[128:131], v[96:99]\n\t"
      "ds_read_b128 v[144:147], v198 offset:12544\n\t"
      "v_mfma_f32_16x16x32_bf16 v[100:103], a[116:119], v[128:131], v[100:103]\n\t"
      "s_waitcnt lgkmcnt(8)\n\t"
      "v_mfma_f32_16x16x32_bf16 v[104:107], a[96:99], v[136:139], v[104:107]\n\t"
      "v_mfma_f32_16x16x32_bf16 v[108:111], a[100:103], v[136:139], v[108:111]\n\t"
      "v_mfma_f32_16x16x32_bf16 v[112:115], a[104:107], v[136:139], v[112:115]\n\t"
      "s_waitcnt vmcnt(15)\n\t"
      "ds_read_b64_tr_b16 a[144:145], v214 offset:24576\n\t"
      "s_waitcnt lgkmcnt(2)\n\t"
      "s_add_u32 m0, s41, 0x15000\n\t"
      "s_mov_b32 s51, 0x36000\n\t"
      "buffer_load_dwordx4 v211, s[16:19], s51 offen lds\n\t"
      "v_mfma_f32_16x16x32_bf16 v[116:119], a[108:111], v[136:139], v[116:119]\n\t"
      "ds_read_b64_tr_b16 a[148:149], v214 offset:24584\n\t"
      "v_mfma_f32_16x16x32_bf16 v[120:123], a[112:115], v[136:139], v[120:123]\n\t"
      "ds_read_b64_tr_b16 a[152:153], v214 offset:24640\n\t"
      "ds_read_b128 v[148:151], v198 offset:24832\n\t"
      "v_mfma_f32_16x16x32_bf16 v[132:135], a[116:119], v[136:139], v[132:135]\n\t"
      "ds_read_b64_tr_b16 a[156:157], v214 offset:24648\n\t"
      "buffer_load_dwordx4 v212, s[16:19], s51 offen offset:1024 lds\n\t"
      "v_mfma_f32_16x16x32_bf16 v[32:35], a[120:123], v[140:143], v[32:35]\n\t"
      "ds_read_b64_tr_b16 a[160:161], v214 offset:24704\n\t"
      "v_mfma_f32_16x16x32_bf16 v[36:39], a[124:127], v[140:143], v[36:39]\n\t"
      "ds_read_b64_tr_b16 a[164:165], v214 offset:24712\n\t"
      "v_mfma_f32_16x16x32_bf16 v[40:43], a[128:131], v[140:143], v[40:43]\n\t"
      "buffer_load_dwordx4 v213, s[16:19], s51 offen offset:2048 lds\n\t"
      "v_mfma_f32_16x16x32_bf16 v[44:47], a[132:135], v[140:143], v[44:47]\n\t"
      "v_mfma_f32_16x16x32_bf16 v[48:51], a[136:139], v[140:143], v[48:51]\n\t"
      "ds_read_b128 v[152:155], v198 offset:37120\n\t"
      "v_mfma_f32_16x16x32_bf16 v[52:55], a[140:143], v[140:143], v[52:55]\n\t"
      "s_waitcnt lgkmcnt(8)\n\t"
      "v_mfma_f32_16x16x32_bf16 v[56:59], a[120:123], v[144:147], v[56:59]\n\t"
      "v_mfma_f32_16x16x32_bf16 v[60:63], a[124:127], v[144:147], v[60:63]\n\t"
      "v_mfma_f32_16x16x32_bf16 v[64:67], a[128:131], v[144:147], v[64:67]\n\t"
      "s_waitcnt vmcnt(15)\n\t"
      "ds_read_b64_tr_b16 a[146:147], v214 offset:36864\n\t"
      "s_waitcnt lgkmcnt(2)\n\t"
      "s_add_u32 m0, s41, 0x18000\n\t"
      "s_mov_b32 s51, 0x36c00\n\t"
      "buffer_load_dwordx4 v211, s[16:19], s51 offen lds\n\t"
      "v_mfma_f32_16x16x32_bf16 v[68:71], a[132:135], v[144:147], v[68:71]\n\t"
      "ds_read_b64_tr_b16 a[150:151], v214 offset:36872\n\t"
      "v_mfma_f32_16x16x32_bf16 v[72:75], a[136:139], v[144:147], v[72:75]\n\t"
      "ds_read_b64_tr_b16 a[154:155], v214 offset:36928\n\t"
      "ds_read_b128 v[156:159], v199 offset:256\n\t"
      "v_mfma_f32_16x16x32_bf16 v[76:79], a[140:143], v[144:147], v[76:79]\n\t"
      "ds_read_b64_tr_b16 a[158:159], v214 offset:36936\n\t"
      "buffer_load_dwordx4 v212, s[16:19], s51 offen offset:1024 lds\n\t"
      "v_mfma_f32_16x16x32_bf16 v[80:83], a[120:123], v[148:151], v[80:83]\n\t"
      "ds_read_b64_tr_b16 a[162:163], v214 offset:36992\n\t"
      "v_mfma_f32_16x16x32_bf16 v[84:87], a[124:127], v[148:151], v[84:87]\n\t"
      "ds_read_b64_tr_b16 a[166:167], v214 offset:37000\n\t"
      "v_mfma_f32_16x16x32_bf16 v[88:91], a[128:131], v[148:151], v[88:91]\n\t"
      "buffer_load_dwordx4 v213, s[16:19], s51 offen offset:2048 lds\n\t"
      "v_mfma_f32_16x16x32_bf16 v[92:95], a[132:135], v[148:151], v[92:95]\n\t"
      "v_mfma_f32_16x16x32_bf16 v[96:99], a[136:139], v[148:151], v[96:99]\n\t"
      "ds_read_b128 v[160:163], v199 offset:12544\n\t"
      "v_mfma_f32_16x16x32_bf16 v[100:103], a[140:143], v[148:151], v[100:103]\n\t"
      "s_waitcnt lgkmcnt(8)\n\t"
      "v_mfma_f32_16x16x32_bf16 v[104:107], a[120:123], v[152:155], v[104:107]\n\t"
      "v_mfma_f32_16x16x32_bf16 v[108:111], a[124:127], v[152:155], v[108:111]\n\t"
      "v_mfma_f32_16x16x32_bf16 v[112:115], a[128:131], v[152:155], v[112:115]\n\t"
      "s_waitcnt vmcnt(15)\n\t"
      "ds_read_b64_tr_b16 a[168:169], v214 offset:49152\n\t"
      "s_waitcnt lgkmcnt(2)\n\t"
      "s_add_u32 m0, s41, 0x1b000\n\t"
      "s_mov_b32 s51, 0x3c000\n\t"
      "buffer_load_dwordx4 v211, s[16:19], s51 offen lds\n\t"
      "v_mfma_f32_16x16x32_bf16 v[116:119], a[132:135], v[152:155], v[116:119]\n\t"
      "ds_read_b64_tr_b16 a[172:173], v214 offset:49160\n\t"
      "v_mfma_f32_16x16x32_bf16 v[120:123], a[136:139], v[152:155], v[120:123]\n\t"
      "ds_read_b64_tr_b16 a[176:177], v214 offset:49216\n\t"
      "ds_read_b128 v[128:131], v199 offset:24832\n\t"
      "v_mfma_f32_16x16x32_bf16 v[132:135], a[140:143], v[152:155], v[132:135]\n\t"
      "ds_read_b64_tr_b16 a[180:181], v214 offset:49224\n\t"
      "buffer_load_dwordx4 v212, s[16:19], s51 offen offset:1024 lds\n\t"
      "v_mfma_f32_16x16x32_bf16 v[32:35], a[144:147], v[156:159], v[32:35]\n\t"
      "ds_read_b64_tr_b16 a[184:185], v214 offset:49280\n\t"
      "v_mfma_f32_16x16x32_bf16 v[36:39], a[148:151], v[156:159], v[36:39]\n\t"
      "ds_read_b64_tr_b16 a[188:189], v214 offset:49288\n\t"
      "v_mfma_f32_16x16x32_bf16 v[40:43], a[152:155], v[156:159], v[40:43]\n\t"
      "buffer_load_dwordx4 v213, s[16:19], s51 offen offset:2048 lds\n\t"
      "v_mfma_f32_16x16x32_bf16 v[44:47], a[156:159], v[156:159], v[44:47]\n\t"
      "v_mfma_f32_16x16x32_bf16 v[48:51], a[160:163], v[156:159], v[48:51]\n\t"
      "ds_read_b128 v[136:139], v199 offset:37120\n\t"
      "v_mfma_f32_16x16x32_bf16 v[52:55], a[164:167], v[156:159], v[52:55]\n\t"
      "s_waitcnt lgkmcnt(8)\n\t"
      "v_mfma_f32_16x16x32_bf16 v[56:59], a[144:147], v[160:163], v[56:59]\n\t"
      "v_mfma_f32_16x16x32_bf16 v[60:63], a[148:151], v[160:163], v[60:63]\n\t"
      "v_mfma_f32_16x16x32_bf16 v[64:67], a[152:155], v[160:163], v[64:67]\n\t"
      "s_waitcnt vmcnt(15)\n\t"
      "ds_read_b64_tr_b16 a[170:171], v221 offset:0\n\t"
      "s_waitcnt lgkmcnt(2)\n\t"
      "s_add_u32 m0, s41, 0x1e000\n\t"
      "s_mov_b32 s51, 0x3cc00\n\t"
      "buffer_load_dwordx4 v211, s[16:19], s51 offen lds\n\t"
      "v_mfma_f32_16x16x32_bf16 v[68:71], a[156:159], v[160:163], v[68:71]\n\t"
      "ds_read_b64_tr_b16 a[174:175], v221 offset:8\n\t"
      "v_mfma_f32_16x16x32_bf16 v[72:75], a[160:163], v[160:163], v[72:75]\n\t"
      "ds_read_b64_tr_b16 a[178:179], v221 offset:64\n\t"
      "ds_read_b128 v[140:143], v196 offset:256\n\t"
      "v_mfma_f32_16x16x32_bf16 v[76:79], a[164:167], v[160:163], v[76:79]\n\t"
      "ds_read_b64_tr_b16 a[182:183], v221 offset:72\n\t"
      "buffer_load_dwordx4 v212, s[16:19], s51 offen offset:1024 lds\n\t"
      "v_mfma_f32_16x16x32_bf16 v[80:83], a[144:147], v[128:131], v[80:83]\n\t"
      "ds_read_b64_tr_b16 a[186:187], v221 offset:128\n\t"
      "v_mfma_f32_16x16x32_bf16 v[84:87], a[148:151], v[128:131], v[84:87]\n\t"
      "ds_read_b64_tr_b16 a[190:191], v221 offset:136\n\t"
      "v_mfma_f32_16x16x32_bf16 v[88:91], a[152:155], v[128:131], v[88:91]\n\t"
      "buffer_load_dwordx4 v213, s[16:19], s51 offen offset:2048 lds\n\t"
      "v_mfma_f32_16x16x32_bf16 v[92:95], a[156:159], v[128:131], v[92:95]\n\t"
      "v_mfma_f32_16x16x32_bf16 v[96:99], a[160:163], v[128:131], v[96:99]\n\t"
      "ds_read_b128 v[144:147], v196 offset:12544\n\t"
      "v_mfma_f32_16x16x32_bf16 v[100:103], a[164:167], v[128:131], v[100:103]\n\t"
      "s_waitcnt lgkmcnt(8)\n\t"
      "v_mfma_f32_16x16x32_bf16 v[104:107], a[144:147], v[136:139], v[104:107]\n\t"
      "v_mfma_f32_16x16x32_bf16 v[108:111], a[148:151], v[136:139], v[108:111]\n\t"
      "v_mfma_f32_16x16x32_bf16 v[112:115], a[152:155], v[136:139], v[112:115]\n\t"
      "s_waitcnt vmcnt(15)\n\t"
      "ds_read_b64_tr_b16 a[192:193], v221 offset:12288\n\t"
      "s_waitcnt lgkmcnt(2)\n\t"
      "s_add_u32 m0, s41, 0x21000\n\t"
      "s_mov_b32 s51, 0x42000\n\t"
      "buffer_load_dwordx4 v211, s[16:19], s51 offen lds\n\t"
      "v_mfma_f32_16x16x32_bf16 v[116:119], a[156:159], v[136:139], v[116:119]\n\t"
      "ds_read_b64_tr_b16 a[196:197], v221 offset:12296\n\t"
      "v_mfma_f32_16x16x32_bf16 v[120:123], a[160:163], v[136:139], v[120:123]\n\t"
      "ds_read_b64_tr_b16 a[200:201], v221 offset:12352\n\t"
      "ds_read_b128 v[148:151], v196 offset:24832\n\t"
      "v_mfma_f32_16x16x32_bf16 v[132:135], a[164:167], v[136:139], v[132:135]\n\t"
      "ds_read_b64_tr_b16 a[204:205], v221 offset:12360\n\t"
      "buffer_load_dwordx4 v212, s[16:19], s51 offen offset:1024 lds\n\t"
      "v_mfma_f32_16x16x32_bf16 v[32:35], a[168:171], v[140:143], v[32:35]\n\t"
      "ds_read_b64_tr_b16 a[208:209], v221 offset:12416\n\t"
      "v_mfma_f32_16x16x32_bf16 v[36:39], a[172:175], v[140:143], v[36:39]\n\t"
      "ds_read_b64_tr_b16 a[212:213], v221 offset:12424\n\t"
      "v_mfma_f32_16x16x32_bf16 v[40:43], a[176:179], v[140:143], v[40:43]\n\t"
      "buffer_load_dwordx4 v213, s[16:19], s51 offen offset:2048 lds\n\t"
      "v_mfma_f32_16x16x32_bf16 v[44:47], a[180:183], v[140:143], v[44:47]\n\t"
      "v_mfma_f32_16x16x32_bf16 v[48:51], a[184:187], v[140:143], v[48:51]\n\t"
      "ds_read_b128 v[152:155], v196 offset:37120\n\t"
      "v_mfma_f32_16x16x32_bf16 v[52:55], a[188:191], v[140:143], v[52:55]\n\t"
      "s_waitcnt lgkmcnt(8)\n\t"
      "v_mfma_f32_16x16x32_bf16 v[56:59], a[168:171], v[144:147], v[56:59]\n\t"
      "v_mfma_f32_16x16x32_bf16 v[60:63], a[172:175], v[144:147], v[60:63]\n\t"
      "v_mfma_f32_16x16x32_bf16 v[64:67], a[176:179], v[144:147], v[64:67]\n\t"
      "s_waitcnt vmcnt(15)\n\t"
      "ds_read_b64_tr_b16 a[194:195], v214 offset:0\n\t"
      "s_waitcnt lgkmcnt(2)\n\t"
      "s_add_u32 m0, s41, 0x24000\n\t"
      "s_mov_b32 s51, 0x42c00\n\t"
      "buffer_load_dwordx4 v211, s[16:19], s51 offen lds\n\t"
      "v_mfma_f32_16x16x32_bf16 v[68:71], a[180:183], v[144:147], v[68:71]\n\t"
      "ds_read_b64_tr_b16 a[198:199], v214 offset:8\n\t"
      "v_mfma_f32_16x16x32_bf16 v[72:75], a[184:187], v[144:147], v[72:75]\n\t"
      "ds_read_b64_tr_b16 a[202:203], v214 offset:64\n\t"
      "ds_read_b128 v[156:159], v198 offset:512\n\t"
      "v_mfma_f32_16x16x32_bf16 v[76:79], a[188:191], v[144:147], v[76:79]\n\t"
      "ds_read_b64_tr_b16 a[206:207], v214 offset:72\n\t"
      "buffer_load_dwordx4 v212, s[16:19], s51 offen offset:1024 lds\n\t"
      "v_mfma_f32_16x16x32_bf16 v[80:83], a[168:171], v[148:151], v[80:83]\n\t"
      "ds_read_b64_tr_b16 a[210:211], v214 offset:128\n\t"
      "v_mfma_f32_16x16x32_bf16 v[84:87], a[172:175], v[148:151], v[84:87]\n\t"
      "ds_read_b64_tr_b16 a[214:215], v214 offset:136\n\t"
      "v_mfma_f32_16x16x32_bf16 v[88:91], a[176:179], v[148:151], v[88:91]\n\t"
      "buffer_load_dwordx4 v213, s[16:19], s51 offen offset:2048 lds\n\t"
      "v_mfma_f32_16x16x32_bf16 v[92:95], a[180:183], v[148:151], v[92:95]\n\t"
      "v_mfma_f32_16x16x32_bf16 v[96:99], a[184:187], v[148:151], v[96:99]\n\t"
      "ds_read_b128 v[160:163], v198 offset:12800\n\t"
      "v_mfma_f32_16x16x32_bf16 v[100:103], a[188:191], v[148:151], v[100:103]\n\t"
      "s_waitcnt lgkmcnt(8)\n\t"
      "v_mfma_f32_16x16x32_bf16 v[104:107], a[168:171], v[152:155], v[104:107]\n\t"
      "s_add_u32 m0, s41, 0xc000\n\t"
      "s_add_u32 s51, s40, 0xc00000\n\t"
      "buffer_load_dwordx4 v208, s[20:23], s51 offen lds\n\t"
      "v_mfma_f32_16x16x32_bf16 v[108:111], a[172:175], v[152:155], v[108:111]\n\t"
      "v_mfma_f32_16x16x32_bf16 v[112:115], a[176:179], v[152:155], v[112:115]\n\t"
      "s_waitcnt vmcnt(16)\n\t"
      "ds_read_b64_tr_b16 a[216:217], v214 offset:12288\n\t"
      "buffer_load_dwordx4 v208, s[20:23], s51 offen offset:1024 lds\n\t"
      "v_mfma_f32_16x16x32_bf16 v[116:119], a[180:183], v[152:155], v[116:119]\n\t"
      "ds_read_b64_tr_b16 a[220:221], v214 offset:12296\n\t"
      "v_mfma_f32_16x16x32_bf16 v[120:123], a[184:187], v[152:155], v[120:123]\n\t"
      "ds_read_b64_tr_b16 a[224:225], v214 offset:12352\n\t"
      "ds_read_b128 v[128:131], v198 offset:25088\n\t"
      "buffer_load_dwordx4 v208, s[20:23], s51 offen offset:2048 lds\n\t"
      "v_mfma_f32_16x16x32_bf16 v[132:135], a[188:191], v[152:155], v[132:135]\n\t"
      "ds_read_b64_tr_b16 a[228:229], v214 offset:12360\n\t"
      "s_waitcnt lgkmcnt(6)\n\t"
      "v_mfma_f32_16x16x32_bf16 v[32:35], a[192:195], v[156:159], v[32:35]\n\t"
      "ds_read_b64_tr_b16 a[232:233], v214 offset:12416\n\t"
      "s_add_u32 m0, s41, 0xf000\n\t"
      "s_add_u32 s51, s40, 0xc00000\n\t"
      "buffer_load_dwordx4 v209, s[20:23], s51 offen lds\n\t"
      "v_mfma_f32_16x16x32_bf16 v[36:39], a[196:199], v[156:159], v[36:39]\n\t"
      "ds_read_b64_tr_b16 a[236:237], v214 offset:12424\n\t"
      "v_mfma_f32_16x16x32_bf16 v[40:43], a[200:203], v[156:159], v[40:43]\n\t"
      "buffer_load_dwordx4 v209, s[20:23], s51 offen offset:1024 lds\n\t"
      "v_mfma_f32_16x16x32_bf16 v[44:47], a[204:207], v[156:159], v[44:47]\n\t"
      "v_mfma_f32_16x16x32_bf16 v[48:51], a[208:211], v[156:159], v[48:51]\n\t"
      "ds_read_b128 v[136:139], v198 offset:37376\n\t"
      "buffer_load_dwordx4 v209, s[20:23], s51 offen offset:2048 lds\n\t"
      "v_mfma_f32_16x16x32_bf16 v[52:55], a[212:215], v[156:159], v[52:55]\n\t"
      "s_waitcnt lgkmcnt(8)\n\t"
      "v_mfma_f32_16x16x32_bf16 v[56:59], a[192:195], v[160:163], v[56:59]\n\t"
      "v_mfma_f32_16x16x32_bf16 v[60:63], a[196:199], v[160:163], v[60:63]\n\t"
      "v_mfma_f32_16x16x32_bf16 v[64:67], a[200:203], v[160:163], v[64:67]\n\t"
      "s_waitcnt vmcnt(18)\n\t"
      "ds_read_b64_tr_b16 a[218:219], v214 offset:24576\n\t"
      "v_mfma_f32_16x16x32_bf16 v[68:71], a[204:207], v[160:163], v[68:71]\n\t"
      "ds_read_b64_tr_b16 a[222:223], v214 offset:24584\n\t"
      "v_mfma_f32_16x16x32_bf16 v[72:75], a[208:211], v[160:163], v[72:75]\n\t"
      "ds_read_b64_tr_b16 a[226:227], v214 offset:24640\n\t"
      "ds_read_b128 v[140:143], v199 offset:512\n\t"
      "v_mfma_f32_16x16x32_bf16 v[76:79], a[212:215], v[160:163], v[76:79]\n\t"
      "ds_read_b64_tr_b16 a[230:231], v214 offset:24648\n\t"
      "s_waitcnt lgkmcnt(9)\n\t"
      "v_mfma_f32_16x16x32_bf16 v[80:83], a[192:195], v[128:131], v[80:83]\n\t"
      "ds_read_b64_tr_b16 a[234:235], v214 offset:24704\n\t"
      "v_mfma_f32_16x16x32_bf16 v[84:87], a[196:199], v[128:131], v[84:87]\n\t"
      "ds_read_b64_tr_b16 a[238:239], v214 offset:24712\n\t"
      "v_mfma_f32_16x16x32_bf16 v[88:91], a[200:203], v[128:131], v[88:91]\n\t"
      "v_mfma_f32_16x16x32_bf16 v[92:95], a[204:207], v[128:131], v[92:95]\n\t"
      "v_mfma_f32_16x16x32_bf16 v[96:99], a[208:211], v[128:131], v[96:99]\n\t"
      "ds_read_b128 v[144:147], v199 offset:12800\n\t"
      "v_mfma_f32_16x16x32_bf16 v[100:103], a[212:215], v[128:131], v[100:103]\n\t"
      "s_waitcnt lgkmcnt(8)\n\t"
      "v_mfma_f32_16x16x32_bf16 v[104:107], a[192:195], v[136:139], v[104:107]\n\t"
      "v_mfma_f32_16x16x32_bf16 v[108:111], a[196:199], v[136:139], v[108:111]\n\t"
      "v_mfma_f32_16x16x32_bf16 v[112:115], a[200:203], v[136:139], v[112:115]\n\t"
      "s_waitcnt vmcnt(15)\n\t"
      "ds_read_b64_tr_b16 a[240:241], v214 offset:36864\n\t"
      "v_mfma_f32_16x16x32_bf16 v[116:119], a[204:207], v[136:139], v[116:119]\n\t"
      "ds_read_b64_tr_b16 a[244:245], v214 offset:36872\n\t"
      "v_mfma_f32_16x16x32_bf16 v[120:123], a[208:211], v[136:139], v[120:123]\n\t"
      "ds_read_b64_tr_b16 a[248:249], v214 offset:36928\n\t"
      "ds_read_b128 v[148:151], v199 offset:25088\n\t"
      "v_mfma_f32_16x16x32_bf16 v[132:135], a[212:215], v[136:139], v[132:135]\n\t"
      "ds_read_b64_tr_b16 a[252:253], v214 offset:36936\n\t"
      "s_waitcnt lgkmcnt(6)\n\t"
      "v_mfma_f32_16x16x32_bf16 v[32:35], a[216:219], v[140:143], v[32:35]\n\t"
      "ds_read_b64_tr_b16 v[164:165], v214 offset:36992\n\t"
      "v_mfma_f32_16x16x32_bf16 v[36:39], a[220:223], v[140:143], v[36:39]\n\t"
      "ds_read_b64_tr_b16 v[168:169], v214 offset:37000\n\t"
      "v_mfma_f32_16x16x32_bf16 v[40:43], a[224:227], v[140:143], v[40:43]\n\t"
      "s_waitcnt vmcnt(12)\n\t"
      "ds_read_b64_tr_b16 a[242:243], v214 offset:49152\n\t"
      "v_mfma_f32_16x16x32_bf16 v[44:47], a[228:231], v[140:143], v[44:47]\n\t"
      "ds_read_b64_tr_b16 a[246:247], v214 offset:49160\n\t"
      "v_mfma_f32_16x16x32_bf16 v[48:51], a[232:235], v[140:143], v[48:51]\n\t"
      "ds_read_b64_tr_b16 a[250:251], v214 offset:49216\n\t"
      "ds_read_b128 v[152:155], v199 offset:37376\n\t"
      "v_mfma_f32_16x16x32_bf16 v[52:55], a[236:239], v[140:143], v[52:55]\n\t"
      "ds_read_b64_tr_b16 a[254:255], v214 offset:49224\n\t"
      "s_waitcnt lgkmcnt(12)\n\t"
      "v_mfma_f32_16x16x32_bf16 v[56:59], a[216:219], v[144:147], v[56:59]\n\t"
      "ds_read_b64_tr_b16 v[166:167], v214 offset:49280\n\t"
      "v_mfma_f32_16x16x32_bf16 v[60:63], a[220:223], v[144:147], v[60:63]\n\t"
      "ds_read_b64_tr_b16 v[170:171], v214 offset:49288\n\t"
      "v_mfma_f32_16x16x32_bf16 v[64:67], a[224:227], v[144:147], v[64:67]\n\t"
      "s_waitcnt vmcnt(9)\n\t"
      "ds_read_b64_tr_b16 v[172:173], v221 offset:0\n\t"
      "v_mfma_f32_16x16x32_bf16 v[68:71], a[228:231], v[144:147], v[68:71]\n\t"
      "ds_read_b64_tr_b16 v[176:177], v221 offset:8\n\t"
      "v_mfma_f32_16x16x32_bf16 v[72:75], a[232:235], v[144:147], v[72:75]\n\t"
      "ds_read_b64_tr_b16 v[180:181], v221 offset:64\n\t"
      "ds_read_b128 v[156:159], v196 offset:512\n\t"
      "v_mfma_f32_16x16x32_bf16 v[76:79], a[236:239], v[144:147], v[76:79]\n\t"
      "ds_read_b64_tr_b16 v[184:185], v221 offset:72\n\t"
      "s_waitcnt lgkmcnt(14)\n\t"
      "v_mfma_f32_16x16x32_bf16 v[80:83], a[216:219], v[148:151], v[80:83]\n\t"
      "ds_read_b64_tr_b16 v[188:189], v221 offset:128\n\t"
      "v_mfma_f32_16x16x32_bf16 v[84:87], a[220:223], v[148:151], v[84:87]\n\t"
      "ds_read_b64_tr_b16 v[192:193], v221 offset:136\n\t"
      "v_mfma_f32_16x16x32_bf16 v[88:91], a[224:227], v[148:151], v[88:91]\n\t"
      "v_mfma_f32_16x16x32_bf16 v[92:95], a[228:231], v[148:151], v[92:95]\n\t"
      "v_mfma_f32_16x16x32_bf16 v[96:99], a[232:235], v[148:151], v[96:99]\n\t"
      "ds_read_b128 v[160:163], v196 offset:12800\n\t"
      "v_mfma_f32_16x16x32_bf16 v[100:103], a[236:239], v[148:151], v[100:103]\n\t"
      "s_waitcnt lgkmcnt(11)\n\t"
      "v_mfma_f32_16x16x32_bf16 v[104:107], a[216:219], v[152:155], v[104:107]\n\t"
      "v_mfma_f32_16x16x32_bf16 v[108:111], a[220:223], v[152:155], v[108:111]\n\t"
      "v_mfma_f32_16x16x32_bf16 v[112:115], a[224:227], v[152:155], v[112:115]\n\t"
      "s_waitcnt vmcnt(6)\n\t"
      "ds_read_b64_tr_b16 v[174:175], v221 offset:12288\n\t"
      "buffer_load_dwordx2 v[240:241], v245, s[24:27], 0 offen sc1\n\t"
      "v_mfma_f32_16x16x32_bf16 v[116:119], a[228:231], v[152:155], v[116:119]\n\t"
      "ds_read_b64_tr_b16 v[178:179], v221 offset:12296\n\t"
      "s_cmp_eq_u32 s41, s43\n\t"
      "s_cbranch_scc0 .Lc4_%=\n\t"
      "buffer_load_dword v254, v253, s[24:27], 0 offen sc1\n\t"
      ".Lc4_%=:\n\t"
      "v_mfma_f32_16x16x32_bf16 v[120:123], a[232:235], v[152:155], v[120:123]\n\t"
      "ds_read_b64_tr_b16 v[182:183], v221 offset:12352\n\t"
      "ds_read_b128 v[140:143], v197 offset:512\n\t"
      "v_mfma_f32_16x16x32_bf16 v[132:135], a[236:239], v[152:155], v[132:135]\n\t"
      "ds_read_b64_tr_b16 v[186:187], v221 offset:12360\n\t"
      "s_waitcnt lgkmcnt(9)\n\t"
      "v_mfma_f32_16x16x32_bf16 v[32:35], a[240:243], v[156:159], v[32:35]\n\t"
      "ds_read_b64_tr_b16 v[190:191], v221 offset:12416\n\t"
      "v_mfma_f32_16x16x32_bf16 v[36:39], a[244:247], v[156:159], v[36:39]\n\t"
      "ds_read_b64_tr_b16 v[194:195], v221 offset:12424\n\t"
      "v_mfma_f32_16x16x32_bf16 v[40:43], a[248:251], v[156:159], v[40:43]\n\t"
      "v_mfma_f32_16x16x32_bf16 v[44:47], a[252:255], v[156:159], v[44:47]\n\t"
      "v_mfma_f32_16x16x32_bf16 v[48:51], v[164:167], v[156:159], v[48:51]\n\t"
      "ds_read_b128 v[144:147], v197 offset:12800\n\t"
      "v_mfma_f32_16x16x32_bf16 v[52:55], v[168:171], v[156:159], v[52:55]\n\t"
      "s_waitcnt lgkmcnt(8)\n\t"
      "v_mfma_f32_16x16x32_bf16 v[56:59], a[240:243], v[160:163], v[56:59]\n\t"
      "v_mfma_f32_16x16x32_bf16 v[60:63], a[244:247], v[160:163], v[60:63]\n\t"
      "v_mfma_f32_16x16x32_bf16 v[64:67], a[248:251], v[160:163], v[64:67]\n\t"
      "v_mfma_f32_16x16x32_bf16 v[68:71], a[252:255], v[160:163], v[68:71]\n\t"
      "v_mfma_f32_16x16x32_bf16 v[72:75], v[164:167], v[160:163], v[72:75]\n\t"
      "ds_read_b128 v[128:131], v196 offset:25088\n\t"
      "v_mfma_f32_16x16x32_bf16 v[76:79], v[168:171], v[160:163], v[76:79]\n\t"
      "s_waitcnt lgkmcnt(2)\n\t"
      "v_mfma_f32_16x16x32_bf16 v[32:35], v[172:175], v[140:143], v[32:35]\n\t"
      "v_mfma_f32_16x16x32_bf16 v[36:39], v[176:179], v[140:143], v[36:39]\n\t"
      "v_mfma_f32_16x16x32_bf16 v[40:43], v[180:183], v[140:143], v[40:43]\n\t"
      "v_mfma_f32_16x16x32_bf16 v[44:47], v[184:187], v[140:143], v[44:47]\n\t"
      "v_mfma_f32_16x16x32_bf16 v[48:51], v[188:191], v[140:143], v[48:51]\n\t"
      "ds_read_b128 v[136:139], v196 offset:37376\n\t"
      "v_mfma_f32_16x16x32_bf16 v[52:55], v[192:195], v[140:143], v[52:55]\n\t"
      "s_waitcnt lgkmcnt(2)\n\t"
      "v_mfma_f32_16x16x32_bf16 v[56:59], v[172:175], v[144:147], v[56:59]\n\t"
      "v_mfma_f32_16x16x32_bf16 v[60:63], v[176:179], v[144:147], v[60:63]\n\t"
      "s_waitcnt vmcnt(4) lgkmcnt(2)\n\t"
      "s_barrier\n\t"
      "v_mfma_f32_16x16x32_bf16 v[64:67], v[180:183], v[144:147], v[64:67]\n\t"
      "v_cvt_pk_bf16_f32 v32, v32, v33\n\t"
      "v_cvt_pk_bf16_f32 v33, v34, v35\n\t"
      "v_mfma_f32_16x16x32_bf16 v[68:71], v[184:187], v[144:147], v[68:71]\n\t"
      "v_cvt_pk_bf16_f32 v34, v36, v37\n\t"
      "v_cvt_pk_bf16_f32 v35, v38, v39\n\t"
      "v_mfma_f32_16x16x32_bf16 v[72:75], v[188:191], v[144:147], v[72:75]\n\t"
      "ds_read_b128 v[148:151], v197 offset:25088\n\t"
      "ds_write_b128 v217, v[32:35] offset:24576\n\t"
      "v_cvt_pk_bf16_f32 v40, v40, v41\n\t"
      "v_mfma_f32_16x16x32_bf16 v[76:79], v[192:195], v[144:147], v[76:79]\n\t"
      "v_cvt_pk_bf16_f32 v41, v42, v43\n\t"
      "v_cvt_pk_bf16_f32 v42, v44, v45\n\t"
      "s_mov_b32 m0, s41\n\t"
      "s_add_u32 s51, s40, 0x1200000\n\t"
      "buffer_load_dwordx4 v208, s[20:23], s51 offen lds\n\t"
      "s_waitcnt lgkmcnt(3)\n\t"
      "v_mfma_f32_16x16x32_bf16 v[80:83], a[240:243], v[128:131], v[80:83]\n\t"
      "v_cvt_pk_bf16_f32 v43, v46, v47\n\t"
      "ds_write_b128 v218, v[40:43] offset:24576\n\t"
      "ds_read_b128 v[32:35], v196 offset:49152\n\t"
      "v_mfma_f32_16x16x32_bf16 v[84:87], a[244:247], v[128:131], v[84:87]\n\t"
      "v_cvt_pk_bf16_f32 v48, v48, v49\n\t"
      "v_cvt_pk_bf16_f32 v49, v50, v51\n\t"
      "v_mfma_f32_16x16x32_bf16 v[88:91], a[248:251], v[128:131], v[88:91]\n\t"
      "v_cvt_pk_bf16_f32 v50, v52, v53\n\t"
      "v_cvt_pk_bf16_f32 v51, v54, v55\n\t"
      "buffer_load_dwordx4 v208, s[20:23], s51 offen offset:1024 lds\n\t"
      "v_mfma_f32_16x16x32_bf16 v[92:95], a[252:255], v[128:131], v[92:95]\n\t"
      "ds_write_b128 v219, v[48:51] offset:24576\n\t"
      "ds_read_b128 v[36:39], v197 offset:49152\n\t"
      "v_cvt_pk_bf16_f32 v56, v56, v57\n\t"
      "v_mfma_f32_16x16x32_bf16 v[96:99], v[164:167], v[128:131], v[96:99]\n\t"
      "ds_read_b128 v[152:155], v197 offset:37376\n\t"
      "v_cvt_pk_bf16_f32 v57, v58, v59\n\t"
      "v_cvt_pk_bf16_f32 v58, v60, v61\n\t"
      "v_mfma_f32_16x16x32_bf16 v[100:103], v[168:171], v[128:131], v[100:103]\n\t"
      "v_cvt_pk_bf16_f32 v59, v62, v63\n\t"
      "ds_write_b128 v217, v[56:59] offset:36864\n\t"
      "ds_read_b128 v[40:43], v198 offset:49152\n\t"
      "s_waitcnt lgkmcnt(9)\n\t"
      "v_mfma_f32_16x16x32_bf16 v[104:107], a[240:243], v[136:139], v[104:107]\n\t"
      "v_cvt_pk_bf16_f32 v64, v64, v65\n\t"
      "v_cvt_pk_bf16_f32 v65, v66, v67\n\t"
      "buffer_load_dwordx4 v208, s[20:23], s51 offen offset:2048 lds\n\t"
      "v_mfma_f32_16x16x32_bf16 v[108:111], a[244:247], v[136:139], v[108:111]\n\t"
      "v_cvt_pk_bf16_f32 v66, v68, v69\n\t"
      "v_cvt_pk_bf16_f32 v67, v70, v71\n\t"
      "v_mfma_f32_16x16x32_bf16 v[112:115], a[248:251], v[136:139], v[112:115]\n\t"
      "ds_write_b128 v218, v[64:67] offset:36864\n\t"
      "ds_read_b128 v[44:47], v199 offset:49152\n\t"
      "v_cvt_pk_bf16_f32 v72, v72, v73\n\t"
      "v_mfma_f32_16x16x32_bf16 v[116:119], a[252:255], v[136:139], v[116:119]\n\t"
      "v_cvt_pk_bf16_f32 v73, v74, v75\n\t"
      "v_cvt_pk_bf16_f32 v74, v76, v77\n\t"
      "v_mfma_f32_16x16x32_bf16 v[120:123], v[164:167], v[136:139], v[120:123]\n\t"
      "v_cvt_pk_bf16_f32 v75, v78, v79\n\t"
      "ds_write_b128 v219, v[72:75] offset:36864\n\t"
      "ds_read_b128 v[48:51], v197 offset:49408\n\t"
      "v_mfma_f32_16x16x32_bf16 v[132:135], v[168:171], v[136:139], v[132:135]\n\t"
      "s_add_u32 m0, s41, 0x3000\n\t"
      "s_add_u32 s51, s40, 0x1200000\n\t"
      "buffer_load_dwordx4 v209, s[20:23], s51 offen lds\n\t"
      "ds_read_b128 v[52:55], v198 offset:49408\n\t"
      "s_waitcnt lgkmcnt(13)\n\t"
      "v_mfma_f32_16x16x32_bf16 v[80:83], v[172:175], v[148:151], v[80:83]\n\t"
      "ds_read_b128 v[56:59], v199 offset:49408\n\t"
      "ds_read_b128 v[60:63], v196 offset:49408\n\t"
      "v_mfma_f32_16x16x32_bf16 v[84:87], v[176:179], v[148:151], v[84:87]\n\t"
      "ds_read_b128 v[64:67], v198 offset:49664\n\t"
      "s_memrealtime s[80:81]\n\t"
      "v_mfma_f32_16x16x32_bf16 v[88:91], v[180:183], v[148:151], v[88:91]\n\t"
      "buffer_load_dwordx4 v209, s[20:23], s51 offen offset:1024 lds\n\t"
      "ds_read_b128 v[68:71], v199 offset:49664\n\t"
      "v_mfma_f32_16x16x32_bf16 v[92:95], v[184:187], v[148:151], v[92:95]\n\t"
      "ds_read_b128 v[72:75], v196 offset:49664\n\t"
      "ds_read_b128 v[76:79], v197 offset:49664\n\t"
      "v_mfma_f32_16x16x32_bf16 v[96:99], v[188:191], v[148:151], v[96:99]\n\t"
      "v_mfma_f32_16x16x32_bf16 v[100:103], v[192:195], v[148:151], v[100:103]\n\t"
      "buffer_load_dwordx4 v209, s[20:23], s51 offen offset:2048 lds\n\t"
      "s_waitcnt lgkmcnt(13)\n\t"
      "v_mfma_f32_16x16x32_bf16 v[104:107], v[172:175], v[152:155], v[104:107]\n\t"
      "v_mfma_f32_16x16x32_bf16 v[108:111], v[176:179], v[152:155], v[108:111]\n\t"
      "v_mfma_f32_16x16x32_bf16 v[112:115], v[180:183], v[152:155], v[112:115]\n\t"
      "v_mfma_f32_16x16x32_bf16 v[116:119], v[184:187], v[152:155], v[116:119]\n\t"
      "v_mfma_f32_16x16x32_bf16 v[120:123], v[188:191], v[152:155], v[120:123]\n\t"
      "v_mfma_f32_16x16x32_bf16 v[132:135], v[192:195], v[152:155], v[132:135]\n\t"
      "v_add_u32_e32 v196, s46, v200\n\t"
      "v_add_u32_e32 v197, s46, v201\n\t"
      "v_add_u32_e32 v198, s46, v202\n\t"
      "v_add_u32_e32 v199, s46, v203\n\t"
      "s_mov_b32 s86, 77\n\t"
      "s_mov_b32 s84, 96\n\t"
      "s_branch .Lp2_%=\n\t"
      ".Lend_%=:\n\t"
      ".Lconv_end:\n\t"
      :
      : "{s[16:31]}"(d16), "{s[36:39]}"(db), "{s[52:53]}"(rs), "{s40}"(t0), "{s41}"(p0), "{s43}"(lds0), "{s94}"(ty), "{s95}"(tres), "{s[60:61]}"(pres),
      "{s69}"(pre1), "{s74}"(plm), "{s75}"(plb), "{s66}"(sst ? 24576u : 0u - 24576u), "{s89}"((blk >> 4) % 5u),
      "{s71}"(wv == 0u ? 568645u + ((blk >> 4) % 5u) * 1057u : ty ? 17733u + ((blk >> 4) % 5u) * 33u : 17u), "{v208}"(ga), "{v209}"(gb),
      "{v211}"(woff[0]), "{v212}"(woff[1]), "{v213}"(woff[2]), "{v248}"(ta), "{v249}"(ta), "{v250}"(tp), "{v251}"(t1), "{v252}"(mk), "{v245}"(vgv),
      "{v246}"(vxw), "{v247}"(vxr), "{v253}"(vcell), "{v200}"(cfa[0]), "{v201}"(cfa[1]), "{v202}"(cfa[2]), "{v203}"(cfa[3]), "{v204}"(csw[0]),
      "{v205}"(csw[1]), "{v206}"(csw[2]), "{v207}"(csr), "{v214}"(tbase + 73728u), "{v221}"(tbase + 135168u), "{v[226:229]}"(br0), "{v[230:233]}"(br1),
      "{v[234:237]}"(br2), "{v6}"(cpo)
      : "memory", "scc", "vcc",
      "s42", "s44", "s45", "s46", "s47", "s48", "s49", "s50", "s51", "s54", "s55", "s56", "s57", "s58", "s59", "s62", "s63", "s64", "s65", "s67",
      "s68", "s70", "s72", "s73", "s76", "s77", "s78", "s79", "s80", "s81", "s82", "s83", "s84", "s85", "s86", "s87", "s88", "s90", "s91", "s92",
      "s93", "s96", "s97", "s98", "s99",
      "v1", "v2", "v3", "v4", "v5", "v7", "v8", "v9", "v10", "v11", "v12", "v13", "v14", "v15", "v16", "v17", "v18", "v19", "v20", "v21", "v22", "v23",
      "v24", "v25", "v26", "v27", "v28", "v29", "v30", "v31", "v32", "v33", "v34", "v35", "v36", "v37", "v38", "v39", "v40", "v41", "v42", "v43",
      "v44", "v45", "v46", "v47", "v48", "v49", "v50", "v51", "v52", "v53", "v54", "v55", "v56", "v57", "v58", "v59", "v60", "v61", "v62", "v63",
      "v64", "v65", "v66", "v67", "v68", "v69", "v70", "v71", "v72", "v73", "v74", "v75", "v76", "v77", "v78", "v79", "v80", "v81", "v82", "v83",
      "v84", "v85", "v86", "v87", "v88", "v89", "v90", "v91", "v92", "v93", "v94", "v95", "v96", "v97", "v98", "v99", "v100", "v101", "v102", "v103",
      "v104", "v105", "v106", "v107", "v108", "v109", "v110", "v111", "v112", "v113", "v114", "v115", "v116", "v117", "v118", "v119", "v120", "v121",
      "v122", "v123", "v124", "v125", "v126", "v127", "v128", "v129", "v130", "v131", "v132", "v133", "v134", "v135", "v136", "v137", "v138", "v139",
      "v140", "v141", "v142", "v143", "v144", "v145", "v146", "v147", "v148", "v149", "v150", "v151", "v152", "v153", "v154", "v155", "v156", "v157",
      "v158", "v159", "v160", "v161", "v162", "v163", "v164", "v165", "v166", "v167", "v168", "v169", "v170", "v171", "v172", "v173", "v174", "v175",
      "v176", "v177", "v178", "v179", "v180", "v181", "v182", "v183", "v184", "v185", "v186", "v187", "v188", "v189", "v190", "v191", "v192", "v193",
      "v194", "v195", "v196", "v197", "v198", "v199", "v210", "v215", "v216", "v217", "v218", "v219", "v220", "v222", "v223", "v224", "v225", "v238",
      "v239", "v240", "v241", "v242", "v243", "v244", "v254", "v255",
      "a0", "a1", "a2", "a3", "a4", "a5", "a6", "a7", "a8", "a9", "a10", "a11", "a12", "a13", "a14", "a15", "a16", "a17", "a18", "a19", "a20", "a21",
      "a22", "a23", "a24", "a25", "a26", "a27", "a28", "a29", "a30", "a31", "a32", "a33", "a34", "a35", "a36", "a37", "a38", "a39", "a40", "a41",
      "a42", "a43", "a44", "a45", "a46", "a47", "a48", "a49", "a50", "a51", "a52", "a53", "a54", "a55", "a56", "a57", "a58", "a59", "a60", "a61",
      "a62", "a63", "a64", "a65", "a66", "a67", "a68", "a69", "a70", "a71", "a72", "a73", "a74", "a75", "a76", "a77", "a78", "a79", "a80", "a81",
      "a82", "a83", "a84", "a85", "a86", "a87", "a88", "a89", "a90", "a91", "a92", "a93", "a94", "a95", "a96", "a97", "a98", "a99", "a100", "a101",
      "a102", "a103", "a104", "a105", "a106", "a107", "a108", "a109", "a110", "a111", "a112", "a113", "a114", "a115", "a116", "a117", "a118", "a119",
      "a120", "a121", "a122", "a123", "a124", "a125", "a126", "a127", "a128", "a129", "a130", "a131", "a132", "a133", "a134", "a135", "a136", "a137",
      "a138", "a139", "a140", "a141", "a142", "a143", "a144", "a145", "a146", "a147", "a148", "a149", "a150", "a151", "a152", "a153", "a154", "a155",
      "a156", "a157", "a158", "a159", "a160", "a161", "a162", "a163", "a164", "a165", "a166", "a167", "a168", "a169", "a170", "a171", "a172", "a173",
      "a174", "a175", "a176", "a177", "a178", "a179", "a180", "a181", "a182", "a183", "a184", "a185", "a186", "a187", "a188", "a189", "a190", "a191",
      "a192", "a193", "a194", "a195", "a196", "a197", "a198", "a199", "a200", "a201", "a202", "a203", "a204", "a205", "a206", "a207", "a208", "a209",
      "a210", "a211", "a212", "a213", "a214", "a215", "a216", "a217", "a218", "a219", "a220", "a221", "a222", "a223", "a224", "a225", "a226", "a227",
      "a228", "a229", "a230", "a231", "a232", "a233", "a234", "a235", "a236", "a237", "a238", "a239", "a240", "a241", "a242", "a243", "a244", "a245",
      "a246", "a247", "a248", "a249", "a250", "a251", "a252", "a253", "a254", "a255");
}

extern "C" void kernel_launch(void* const* d_in, const int* in_sizes, int n_in,
                              void* d_out, int out_size, void* d_ws, size_t ws_size,
                              hipStream_t stream) {
  conv1x1_kernel<<<256, 256, 0, stream>>>(d_in[1], 0x0002000000048000ull, d_in[0], 0x0002000006c00000ull, d_ws, 0x0002000000000c00ull, d_out, d_in[2]);
}
```

```cpp
#include <hip/hip_runtime.h>
#include <stdint.h>

#define LAS __attribute__((address_space(3)))
typedef __attribute__((ext_vector_type(16))) unsigned u32x16;
typedef __attribute__((ext_vector_type(4))) unsigned u32x4;
typedef unsigned long long u64;

__global__ __launch_bounds__(256, 1) void conv1x1_kernel(const void* w, u64 cw, const void* x, u64 cx, void* ws, u64 cs, void* out, const void* bias) {
  __shared__ __attribute__((aligned(1024))) unsigned char lds[160768];

  u32x16 d16;
  u32x4 db;
  u64 rs;
  asm volatile(
      "s_load_dwordx16 s[16:31], %3, 0x0\n\t"
      "s_memrealtime s[52:53]\n\t"
      "s_movk_i32 s38, 0x300\n\t"
      "s_mov_b32 s39, 0x20000\n\t"
      : "={s[16:31]}"(d16), "={s[36:39]}"(db), "={s[52:53]}"(rs)
      : "s"(__builtin_amdgcn_kernarg_segment_ptr())
      : "memory");
  unsigned cpo = ((blockIdx.x >> 3) * 256u + threadIdx.x) * 4u;
  asm volatile(
      "s_getpc_b64 s[92:93]\n\t"
      ".Lconv_cp:\n\t"
      "v_min_u32_e32 v6, .Lconv_end-.Lconv_cp-4, v6\n\t"
      "global_load_dword v6, v6, s[92:93]\n\t"
      : "+{v6}"(cpo)
      :
      : "memory", "s92", "s93");
  __builtin_amdgcn_sched_barrier(0);
  unsigned tid  = threadIdx.x;
  unsigned lane = tid & 63;
  unsigned wv   = __builtin_amdgcn_readfirstlane(tid >> 6);
  const unsigned lds0 = (unsigned)(size_t)(LAS unsigned char*)lds;
  unsigned blk  = blockIdx.x;
  const unsigned p0   = lds0 + 3072u * wv;
  const unsigned lq = lane >> 4, ph = (lane >> 2) & 3, pl = lane & 3;
  const unsigned gs = 256u * lq + 64u * ((ph - lq) & 3) + 16u * (pl ^ ((4u - wv) & 3));
  const unsigned ga = gs + 3072u * (wv < 3u ? wv : 5u);
  const unsigned gb = gs + 3072u * (wv < 2u ? wv + 6u : wv + 1u);
  const unsigned boff = 192u * wv + 16u * lq;
  const unsigned role = (blk >> 2) & 1u;
  unsigned pk = (blk & 3u) * 32u + (blk >> 3);
  const unsigned sst  = (role ^ (blk >> 4)) & 1u;
  const unsigned t0   = pk * 49152u + sst * 24576u;
  const unsigned pv = blk >> 7, ps = ((blk & 7u) | ((blk >> 1) & 56u)) ^ (4u * pv), pgr = (ps >> 2) & 7u, pi = (ps & 3u) * 2u + pv;
  const unsigned ppg = min(pgr * 8u, 52u) + (pi & (0x13777777u >> (4u * pgr)) & 7u);
  const unsigned pop = (ppg << 21) + ((ps & 3u) * 4u + pv) * 0x1c080u + 0x24080u;
  unsigned pfire = ((role ^ ppg) & 1u) * 2u, psel = ps >> 5;
  const unsigned vpub = lane == 0u ? 0u + (pv * 64u + ps) * 8u : 0x40000000u;
  const unsigned pfo = (blk >> 4) * 18432u;
  const unsigned pfv = tid * 16u;
  const unsigned ty = (blk >> 3) & 1u;
  const unsigned tidx = (blk >> 3) * 4u + wv, tsel = tidx >> 6;
  const unsigned toff = (tidx & 63u) < 54u ? (tidx & 63u) << 21 : 113246204u;
  unsigned tres, pre1;
  u64 pres;
  u32x4 br0, br1, br2;
  asm volatile(
      "s_add_u32 m0, s43, 0x27000\n\t"
      "s_waitcnt lgkmcnt(0)\n\t"
      "s_mov_b64 s[36:37], s[30:31]\n\t"
      "s_mov_b64 s[30:31], s[22:23]\n\t"
      "s_cmp_eq_u32 s94, 1\n\t"
      "s_cbranch_scc0 .Lc1_%=\n\t"
      "s_cmp_eq_u32 s41, s43\n\t"
      "s_cbranch_scc0 .Lc0_%=\n\t"
      "s_cmp_eq_u32 s78, 0\n\t"
      "s_cselect_b64 s[96:97], s[20:21], s[28:29]\n\t"
      "s_load_dword s69, s[96:97], s76 offset:0x0\n\t"
      "s_load_dwordx2 s[60:61], s[96:97], s76 offset:0x3300\n\t"
      ".Lc0_%=:\n\t"
      "buffer_load_dwordx4 v216, s[16:19], s42 offen lds\n\t"
      "s_add_u32 s51, s42, 0x1000\n\t"
      "buffer_load_dwordx4 v216, s[16:19], s51 offen lds\n\t"
      "s_add_u32 s51, s42, 0x2000\n\t"
      "buffer_load_dwordx4 v216, s[16:19], s51 offen lds\n\t"
      "s_add_u32 s51, s42, 0x3000\n\t"
      "buffer_load_dwordx4 v216, s[16:19], s51 offen lds\n\t"
      "s_add_u32 s51, s42, 0x4000\n\t"
      "buffer_load_dwordx4 v216, s[16:19], s51 offen lds\n\t"
      ".Lc1_%=:\n\t"
      "buffer_load_dwordx4 v[226:229], v215, s[36:39], 0 offen offset:0\n\t"
      "buffer_load_dwordx4 v[230:233], v215, s[36:39], 0 offen offset:64\n\t"
      "buffer_load_dwordx4 v[234:237], v215, s[36:39], 0 offen offset:128\n\t"
      "s_mov_b32 m0, s41\n\t"
      "s_nop 0\n\t"
      "buffer_load_dwordx4 v208, s[20:23], s40 offen lds\n\t"
      "buffer_load_dwordx4 v208, s[20:23], s40 offen offset:1024 lds\n\t"
      "buffer_load_dwordx4 v208, s[20:23], s40 offen offset:2048 lds\n\t"
      "s_cmp_eq_u32 s94, 0\n\t"
      "s_cbranch_scc0 .Lvb0_%=\n\t"
      "s_add_u32 m0, s41, 0x3000\n\t"
      "s_nop 0\n\t"
      "buffer_load_dwordx4 v209, s[20:23], s40 offen lds\n\t"
      "buffer_load_dwordx4 v209, s[20:23], s40 offen offset:1024 lds\n\t"
      "buffer_load_dwordx4 v209, s[20:23], s40 offen offset:2048 lds\n\t"
      "s_cmp_eq_u32 s99, 0\n\t"
      "s_cselect_b64 s[96:97], s[20:21], s[28:29]\n\t"
      "s_load_dword s95, s[96:97], s98\n\t"
      "s_branch .Lvb1_%=\n\t"
      ".Lvb0_%=:\n\t"
      "s_cmp_eq_u32 s41, s43\n\t"
      "s_cbranch_scc1 .Lev0_%=\n\t"
      "s_cmp_eq_u32 s99, 0\n\t"
      "s_cselect_b64 s[96:97], s[20:21], s[28:29]\n\t"
      "s_load_dword s95, s[96:97], s98\n\t"
      "s_cmp_eq_u32 s94, 0\n\t"
      "s_cbranch_scc1 .Lev1_%=\n\t"
      "s_sleep 8\n\t"
      "s_branch .Lev1_%=\n\t"
      ".Lev0_%=:\n\t"
      "s_andn2_b32 s79, s52, 15\n\t"
      "v_mov_b32_e32 v241, s53\n\t"
      "s_waitcnt lgkmcnt(2)\n\t"
      "s_nop 7\n\t"
      "s_getreg_b32 s78, hwreg(HW_REG_IB_STS, 8, 4)\n\t"
      "s_cmp_eq_u32 s78, 1\n\t"
      "s_cselect_b32 s51, 2, 0\n\t"
      "s_xor_b32 s77, s77, s51\n\t"
      "s_cmp_lg_u32 s78, 0\n\t"
      "s_cselect_b32 s77, s77, 1\n\t"
      "s_or_b32 s79, s79, s77\n\t"
      "v_mov_b32_e32 v240, s79\n\t"
      "buffer_store_dwordx2 v[240:241], v244, s[24:27], 0 offen sc1\n\t"
      "s_cmp_eq_u32 s99, 0\n\t"
      "s_cselect_b64 s[96:97], s[20:21], s[28:29]\n\t"
      "s_load_dword s95, s[96:97], s98\n\t"
      ".Lev1_%=:\n\t"
      "s_add_u32 m0, s41, 0x3000\n\t"
      "s_nop 0\n\t"
      "buffer_load_dwordx4 v209, s[20:23], s40 offen lds\n\t"
      "buffer_load_dwordx4 v209, s[20:23], s40 offen offset:1024 lds\n\t"
      "buffer_load_dwordx4 v209, s[20:23], s40 offen offset:2048 lds\n\t"
      "s_add_u32 m0, s41, 0x6000\n\t"
      "s_add_u32 s51, s40, 0x600000\n\t"
      "buffer_load_dwordx4 v208, s[20:23], s51 offen lds\n\t"
      "buffer_load_dwordx4 v208, s[20:23], s51 offen offset:1024 lds\n\t"
      "buffer_load_dwordx4 v208, s[20:23], s51 offen offset:2048 lds\n\t"
      "s_add_u32 m0, s41, 0x9000\n\t"
      "s_add_u32 s51, s40, 0x600000\n\t"
      "buffer_load_dwordx4 v209, s[20:23], s51 offen lds\n\t"
      "buffer_load_dwordx4 v209, s[20:23], s51 offen offset:1024 lds\n\t"
      "buffer_load_dwordx4 v209, s[20:23], s51 offen offset:2048 lds\n\t"
      ".Lvb1_%=:\n\t"
      : "={s95}"(tres), "={s[60:61]}"(pres), "={s69}"(pre1), "={v[226:229]}"(br0), "={v[230:233]}"(br1), "={v[234:237]}"(br2),
        "+{s[16:31]}"(d16), "+{s[36:39]}"(db), "+v"(lane), "+{s77}"(pfire), "+{s78}"(psel), "+{v6}"(cpo)
      : "{s40}"(t0), "{s41}"(p0), "{s43}"(lds0), "{s94}"(ty),
        "{v208}"(ga), "{v209}"(gb), "{v215}"(boff), "{v216}"(pfv),
        "{s42}"(pfo), "{s98}"(toff), "{s99}"(tsel), "{s76}"(pop), "{s[52:53]}"(rs), "{v244}"(vpub)
      : "memory", "scc", "s51", "s92", "s93", "s96", "s97", "s79", "v240", "v241");
  asm volatile("" : "+s"(wv), "+s"(pk));
  __builtin_amdgcn_sched_barrier(0);
  const unsigned vcell = lane < 2u ? 2048u + pk * 8u + 4u * (role ^ lane) : 0x40000000u;
  const unsigned ob = (unsigned)(((lane < 12u ? 0x434ec32ca418820ull >> (5u * lane) : 0x23053d2eull >> (5u * ((lane - 12u) & 7u)))) & 31u) * 6291456u + pk * 49152u;
  const unsigned pg = (ob + 24576u) >> 21;
  const unsigned ta = lane < 18u ? ob + 24576u * sst : 113246208u, t1 = ob + 24576u * (sst ^ 1u);
  const unsigned tq = (lane >= 9u ? 80u : 77u) - (blk & 1u) * 2u + (lane < 4u ? 3u : 0u), tp = lane >= 14u ? 0u : tq;
  const unsigned mk = lane >= 4u + ty && lane < 18u ? 1u << (pg < 52u ? (pg >> 3) * 4u : 28u) : 0u;
  const unsigned plm = (unsigned)__builtin_amdgcn_ballot_w64(((pg ^ role ^ sst) & 1u) != 0u), plb = wv < 2u ? plm : ~plm;
  const unsigned vgv = 0u + (wv & 1u) * 512u + lane * 8u;
  const unsigned vxr = lds0 + 159744u + lane * 16u, vxw = vxr + wv * 4u;
  const unsigned li = lane & 15, lg = lane >> 4, rl = li & 3, hi = li >> 2;
  const unsigned rb = 768u * li + 16u * (lg ^ ((4u - hi) & 3)) - 3072u * wv;
  unsigned cfa[4], csw[3];
#pragma unroll
  for (unsigned u = 0; u < 4; ++u) cfa[u] = rb + 64u * ((u - rl) & 3);
#pragma unroll
  for (unsigned j = 0; j < 3; ++j) {
    const unsigned e = 3u * wv + j;
    csw[j] = rb + 256u * (e >> 2) + 64u * (((e & 3) + (e >> 2) - rl) & 3);
  }
  const unsigned csr = 16u * lane;
  const unsigned tbase = p0 + (4u * lg + (li >> 2)) * 192u + 16u * (li & 3);
  unsigned woff[3];
#pragma unroll
  for (unsigned j = 0; j < 3; ++j) {
    const unsigned rr = (__umul24(lane, 171u) + 10944u * j) >> 11;
    woff[j] = __umul24(rr, 576u) + __umul24(rr & 12u, 768u) + (16u * lane + 192u * wv);
  }
  asm volatile(
      "s_cmp_eq_u32 s94, 0\n\t"
      "s_cbranch_scc0 .Lc2_%=\n\t"
      "s_add_u32 m0, s41, 0x1e000\n\t"
      "s_mov_b32 s51, 0x0\n\t"
      "buffer_load_dwordx4 v211, s[16:19], s51 offen lds\n\t"
      "buffer_load_dwordx4 v212, s[16:19], s51 offen offset:1024 lds\n\t"
      "buffer_load_dwordx4 v213, s[16:19], s51 offen offset:2048 lds\n\t"
      "s_add_u32 m0, s41, 0x21000\n\t"
      "s_mov_b32 s51, 0xc00\n\t"
      "buffer_load_dwordx4 v211, s[16:19], s51 offen lds\n\t"
      "buffer_load_dwordx4 v212, s[16:19], s51 offen offset:1024 lds\n\t"
      "buffer_load_dwordx4 v213, s[16:19], s51 offen offset:2048 lds\n\t"
      "s_add_u32 m0, s41, 0x24000\n\t"
      "s_mov_b32 s51, 0x6000\n\t"
      "buffer_load_dwordx4 v211, s[16:19], s51 offen lds\n\t"
      "buffer_load_dwordx4 v212, s[16:19], s51 offen offset:1024 lds\n\t"
      "buffer_load_dwordx4 v213, s[16:19], s51 offen offset:2048 lds\n\t"
      "s_add_u32 m0, s41, 0x12000\n\t"
      "s_mov_b32 s51, 0x6c00\n\t"
      "buffer_load_dwordx4 v211, s[16:19], s51 offen lds\n\t"
      "buffer_load_dwordx4 v212, s[16:19], s51 offen offset:1024 lds\n\t"
      "buffer_load_dwordx4 v213, s[16:19], s51 offen offset:2048 lds\n\t"
      ".Lc2_%=:\n\t"
      "s_mov_b32 s62, 0\n\t"
      "s_add_u32 s50, s41, 0x12000\n\t"
      "s_add_u32 s51, s41, 0x12000\n\t"
      "s_sub_u32 s63, s52, 800\n\t"
      "s_andn2_b32 s63, s63, 15\n\t"
      "v_add_u32_e32 v196, s41, v200\n\t"
      "v_add_u32_e32 v197, s41, v201\n\t"
      "v_add_u32_e32 v198, s41, v202\n\t"
      "v_add_u32_e32 v199, s41, v203\n\t"
      "v_add_u32_e32 v217, s51, v204\n\t"
      "v_add_u32_e32 v218, s51, v205\n\t"
      "v_add_u32_e32 v219, s51, v206\n\t"
      "v_add_u32_e32 v220, s51, v207\n\t"
      "v_add_u32_e32 v225, s51, v204\n\t"
      "v_add_u32_e32 v239, s51, v205\n\t"
      "v_add_u32_e32 v210, s51, v206\n\t"
      "v_add_u32_e32 v238, s51, v207\n\t"
      "s_and_b32 s49, s71, 31\n\t"
      "s_lshr_b32 s71, s71, 5\n\t"
      "s_cmp_eq_u32 s94, 0\n\t"
      "s_cbranch_scc0 .Lpb_%=\n\t"
      "s_add_u32 m0, s41, 0x15000\n\t"
      "s_mov_b32 s51, 0xc000\n\t"
      "buffer_load_dwordx4 v211, s[16:19], s51 offen lds\n\t"
      "buffer_load_dwordx4 v212, s[16:19], s51 offen offset:1024 lds\n\t"
      "buffer_load_dwordx4 v213, s[16:19], s51 offen offset:2048 lds\n\t"
      "s_waitcnt vmcnt(21)\n\t"
      "v_lshlrev_b32_e32 v8, 16, v226\n\t"
      "v_and_b32_e32 v9, 0xffff0000, v226\n\t"
      "v_lshlrev_b32_e32 v10, 16, v227\n\t"
      "v_and_b32_e32 v11, 0xffff0000, v227\n\t"
      "v_lshlrev_b32_e32 v12, 16, v228\n\t"
      "v_and_b32_e32 v13, 0xffff0000, v228\n\t"
      "v_lshlrev_b32_e32 v14, 16, v229\n\t"
      "v_and_b32_e32 v15, 0xffff0000, v229\n\t"
      "v_lshlrev_b32_e32 v16, 16, v230\n\t"
      "v_and_b32_e32 v17, 0xffff0000, v230\n\t"
      "v_lshlrev_b32_e32 v18, 16, v231\n\t"
      "v_and_b32_e32 v19, 0xffff0000, v231\n\t"
      "v_lshlrev_b32_e32 v20, 16, v232\n\t"
      "v_and_b32_e32 v21, 0xffff0000, v232\n\t"
      "v_lshlrev_b32_e32 v22, 16, v233\n\t"
      "v_and_b32_e32 v23, 0xffff0000, v233\n\t"
      "v_lshlrev_b32_e32 v24, 16, v234\n\t"
      "v_and_b32_e32 v25, 0xffff0000, v234\n\t"
      "v_lshlrev_b32_e32 v26, 16, v235\n\t"
      "v_and_b32_e32 v27, 0xffff0000, v235\n\t"
      "v_lshlrev_b32_e32 v28, 16, v236\n\t"
      "v_and_b32_e32 v29, 0xffff0000, v236\n\t"
      "v_lshlrev_b32_e32 v30, 16, v237\n\t"
      "v_and_b32_e32 v31, 0xffff0000, v237\n\t"
      "s_add_u32 m0, s41, 0x18000\n\t"
      "s_mov_b32 s51, 0xcc00\n\t"
      "buffer_load_dwordx4 v211, s[16:19], s51 offen lds\n\t"
      "buffer_load_dwordx4 v212, s[16:19], s51 offen offset:1024 lds\n\t"
      "buffer_load_dwordx4 v213, s[16:19], s51 offen offset:2048 lds\n\t"
      "s_add_u32 m0, s41, 0x1b000\n\t"
      "s_mov_b32 s51, 0x12000\n\t"
      "buffer_load_dwordx4 v211, s[16:19], s51 offen lds\n\t"
      "buffer_load_dwordx4 v212, s[16:19], s51 offen offset:1024 lds\n\t"
      "buffer_load_dwordx4 v213, s[16:19], s51 offen offset:2048 lds\n\t"
      "s_mov_b32 s44, 0\n\t"
      "s_mov_b32 s85, 3\n\t"
      "s_mov_b32 s45, s41\n\t"
      "s_add_u32 s46, s41, 0x6000\n\t"
      "s_add_u32 s47, s41, 0xc000\n\t"
      "s_mov_b64 vcc, 0\n\t"
      "s_waitcnt vmcnt(18)\n\t"
      "s_barrier\n\t"
      "ds_read_b64_tr_b16 a[0:1], v214 offset:49152\n\t"
      "ds_read_b64_tr_b16 a[4:5], v214 offset:49160\n\t"
      "ds_read_b64_tr_b16 a[8:9], v214 offset:49216\n\t"
      "ds_read_b64_tr_b16 a[12:13], v214 offset:49224\n\t"
      "ds_read_b64_tr_b16 a[16:17], v214 offset:49280\n\t"
      "ds_read_b64_tr_b16 a[20:21], v214 offset:49288\n\t"
      "ds_read_b128 v[140:143], v196 offset:0\n\t"
      "s_waitcnt vmcnt(15)\n\t"
      "ds_read_b64_tr_b16 a[2:3], v221 offset:0\n\t"
      "ds_read_b64_tr_b16 a[6:7], v221 offset:8\n\t"
      "ds_read_b64_tr_b16 a[10:11], v221 offset:64\n\t"
      "ds_read_b64_tr_b16 a[14:15], v221 offset:72\n\t"
      "ds_read_b64_tr_b16 a[18:19], v221 offset:128\n\t"
      "ds_read_b64_tr_b16 a[22:23], v221 offset:136\n\t"
      "ds_read_b128 v[152:155], v196 offset:12288\n\t"
      "s_waitcnt lgkmcnt(8)\n\t"
      "s_add_u32 m0, s41, 0x1e000\n\t"
      "s_mov_b32 s51, 0x12c00\n\t"
      "buffer_load_dwordx4 v211, s[16:19], s51 offen lds\n\t"
      "buffer_load_dwordx4 v212, s[16:19], s51 offen offset:1024 lds\n\t"
      "buffer_load_dwordx4 v213, s[16:19], s51 offen offset:2048 lds\n\t"
      "s_waitcnt vmcnt(15)\n\t"
      "ds_read_b64_tr_b16 a[24:25], v221 offset:12288\n\t"
      "ds_read_b64_tr_b16 a[28:29], v221 offset:12296\n\t"
      "ds_read_b64_tr_b16 a[32:33], v221 offset:12352\n\t"
      "ds_read_b64_tr_b16 a[36:37], v221 offset:12360\n\t"
      "ds_read_b64_tr_b16 a[40:41], v221 offset:12416\n\t"
      "ds_read_b64_tr_b16 a[44:45], v221 offset:12424\n\t"
      "ds_read_b128 v[144:147], v197 offset:0\n\t"
      "s_waitcnt lgkmcnt(8)\n\t"
      "v_mfma_f32_16x16x32_bf16 v[80:83], a[0:3], v[140:143], v[8:11]\n\t"
      "s_add_u32 m0, s41, 0x21000\n\t"
      "s_mov_b32 s51, 0x18000\n\t"
      "buffer_load_dwordx4 v211, s[16:19], s51 offen lds\n\t"
      "v_mfma_f32_16x16x32_bf16 v[84:87], a[4:7], v[140:143], v[12:15]\n\t"
      "buffer_load_dwordx4 v212, s[16:19], s51 offen offset:1024 lds\n\t"
      "v_mfma_f32_16x16x32_bf16 v[88:91], a[8:11], v[140:143], v[16:19]\n\t"
      "buffer_load_dwordx4 v213, s[16:19], s51 offen offset:2048 lds\n\t"
      "s_waitcnt vmcnt(15)\n\t"
      "ds_read_b64_tr_b16 a[26:27], v214 offset:0\n\t"
      "v_mfma_f32_16x16x32_bf16 v[92:95], a[12:15], v[140:143], v[20:23]\n\t"
      "ds_read_b64_tr_b16 a[30:31], v214 offset:8\n\t"
      "ds_read_b64_tr_b16 a[34:35], v214 offset:64\n\t"
      "v_mfma_f32_16x16x32_bf16 v[96:99], a[16:19], v[140:143], v[24:27]\n\t"
      "ds_read_b64_tr_b16 a[38:39], v214 offset:72\n\t"
      "ds_read_b64_tr_b16 a[42:43], v214 offset:128\n\t"
      "v_mfma_f32_16x16x32_bf16 v[100:103], a[20:23], v[140:143], v[28:31]\n\t"
      "ds_read_b64_tr_b16 a[46:47], v214 offset:136\n\t"
      "ds_read_b128 v[156:159], v197 offset:12288\n\t"
      "s_waitcnt lgkmcnt(8)\n\t"
      "v_mfma_f32_16x16x32_bf16 v[104:107], a[0:3], v[152:155], v[8:11]\n\t"
      "s_add_u32 m0, s41, 0x24000\n\t"
      "s_mov_b32 s51, 0x18c00\n\t"
      "buffer_load_dwordx4 v211, s[16:19], s51 offen lds\n\t"
      "v_mfma_f32_16x16x32_bf16 v[108:111], a[4:7], v[152:155], v[12:15]\n\t"
      "buffer_load_dwordx4 v212, s[16:19], s51 offen offset:1024 lds\n\t"
      "v_mfma_f32_16x16x32_bf16 v[112:115], a[8:11], v[152:155], v[16:19]\n\t"
      "buffer_load_dwordx4 v213, s[16:19], s51 offen offset:2048 lds\n\t"
      "s_waitcnt vmcnt(15)\n\t"
      "ds_read_b64_tr_b16 a[48:49], v214 offset:12288\n\t"
      "v_mfma_f32_16x16x32_bf16 v[116:119], a[12:15], v[152:155], v[20:23]\n\t"
      "ds_read_b64_tr_b16 a[52:53], v214 offset:12296\n\t"
      "ds_read_b64_tr_b16 a[56:57], v214 offset:12352\n\t"
      "v_mfma_f32_16x16x32_bf16 v[120:123], a[16:19], v[152:155], v[24:27]\n\t"
      "ds_read_b64_tr_b16 a[60:61], v214 offset:12360\n\t"
      "ds_read_b64_tr_b16 a[64:65], v214 offset:12416\n\t"
      "v_mfma_f32_16x16x32_bf16 v[132:135], a[20:23], v[152:155], v[28:31]\n\t"
      "ds_read_b64_tr_b16 a[68:69], v214 offset:12424\n\t"
      "ds_read_b128 v[148:151], v198 offset:0\n\t"
      "s_waitcnt lgkmcnt(8)\n\t"
      "v_mfma_f32_16x16x32_bf16 v[80:83], a[24:27], v[144:147], v[80:83]\n\t"
      "s_add_u32 m0, s41, 0x12000\n\t"
      "s_mov_b32 s51, 0x1e000\n\t"
      "buffer_load_dwordx4 v211, s[16:19], s51 offen lds\n\t"
      "v_mfma_f32_16x16x32_bf16 v[84:87], a[28:31], v[144:147], v[84:87]\n\t"
      "buffer_load_dwordx4 v212, s[16:19], s51 offen offset:1024 lds\n\t"
      "v_mfma_f32_16x16x32_bf16 v[88:91], a[32:35], v[144:147], v[88:91]\n\t"
      "buffer_load_dwordx4 v213, s[16:19], s51 offen offset:2048 lds\n\t"
      "s_waitcnt vmcnt(15)\n\t"
      "ds_read_b64_tr_b16 a[50:51], v214 offset:24576\n\t"
      "v_mfma_f32_16x16x32_bf16 v[92:95], a[36:39], v[144:147], v[92:95]\n\t"
      "ds_read_b64_tr_b16 a[54:55], v214 offset:24584\n\t"
      "ds_read_b64_tr_b16 a[58:59], v214 offset:24640\n\t"
      "v_mfma_f32_16x16x32_bf16 v[96:99], a[40:43], v[144:147], v[96:99]\n\t"
      "ds_read_b64_tr_b16 a[62:63], v214 offset:24648\n\t"
      "ds_read_b64_tr_b16 a[66:67], v214 offset:24704\n\t"
      "v_mfma_f32_16x16x32_bf16 v[100:103], a[44:47], v[144:147], v[100:103]\n\t"
      "ds_read_b64_tr_b16 a[70:71], v214 offset:24712\n\t"
      "ds_read_b128 v[160:163], v198 offset:12288\n\t"
      "s_waitcnt lgkmcnt(8)\n\t"
      "v_mfma_f32_16x16x32_bf16 v[104:107], a[24:27], v[156:159], v[104:107]\n\t"
      "s_add_u32 m0, s41, 0x15000\n\t"
      "s_mov_b32 s51, 0x1ec00\n\t"
      "buffer_load_dwordx4 v211, s[16:19], s51 offen lds\n\t"
      "v_mfma_f32_16x16x32_bf16 v[108:111], a[28:31], v[156:159], v[108:111]\n\t"
      "buffer_load_dwordx4 v212, s[16:19], s51 offen offset:1024 lds\n\t"
      "v_mfma_f32_16x16x32_bf16 v[112:115], a[32:35], v[156:159], v[112:115]\n\t"
      "buffer_load_dwordx4 v213, s[16:19], s51 offen offset:2048 lds\n\t"
      "s_waitcnt vmcnt(15)\n\t"
      "ds_read_b64_tr_b16 a[72:73], v214 offset:36864\n\t"
      "v_mfma_f32_16x16x32_bf16 v[116:119], a[36:39], v[156:159], v[116:119]\n\t"
      "ds_read_b64_tr_b16 a[76:77], v214 offset:36872\n\t"
      "ds_read_b64_tr_b16 a[80:81], v214 offset:36928\n\t"
      "v_mfma_f32_16x16x32_bf16 v[120:123], a[40:43], v[156:159], v[120:123]\n\t"
      "ds_read_b64_tr_b16 a[84:85], v214 offset:36936\n\t"
      "ds_read_b64_tr_b16 a[88:89], v214 offset:36992\n\t"
      "v_mfma_f32_16x16x32_bf16 v[132:135], a[44:47], v[156:159], v[132:135]\n\t"
      "ds_read_b64_tr_b16 a[92:93], v214 offset:37000\n\t"
      "ds_read_b128 v[140:143], v199 offset:0\n\t"
      "s_waitcnt lgkmcnt(8)\n\t"
      "v_mfma_f32_16x16x32_bf16 v[80:83], a[48:51], v[148:151], v[80:83]\n\t"
      "s_add_u32 m0, s41, 0x18000\n\t"
      "s_mov_b32 s51, 0x24000\n\t"
      "buffer_load_dwordx4 v211, s[16:19], s51 offen lds\n\t"
      "v_mfma_f32_16x16x32_bf16 v[84:87], a[52:55], v[148:151], v[84:87]\n\t"
      "buffer_load_dwordx4 v212, s[16:19], s51 offen offset:1024 lds\n\t"
      "v_mfma_f32_16x16x32_bf16 v[88:91], a[56:59], v[148:151], v[88:91]\n\t"
      "buffer_load_dwordx4 v213, s[16:19], s51 offen offset:2048 lds\n\t"
      "s_waitcnt vmcnt(15)\n\t"
      "ds_read_b64_tr_b16 a[74:75], v214 offset:49152\n\t"
      "v_mfma_f32_16x16x32_bf16 v[92:95], a[60:63], v[148:151], v[92:95]\n\t"
      "ds_read_b64_tr_b16 a[78:79], v214 offset:49160\n\t"
      "ds_read_b64_tr_b16 a[82:83], v214 offset:49216\n\t"
      "v_mfma_f32_16x16x32_bf16 v[96:99], a[64:67], v[148:151], v[96:99]\n\t"
      "ds_read_b64_tr_b16 a[86:87], v214 offset:49224\n\t"
      "ds_read_b64_tr_b16 a[90:91], v214 offset:49280\n\t"
      "v_mfma_f32_16x16x32_bf16 v[100:103], a[68:71], v[148:151], v[100:103]\n\t"
      "ds_read_b64_tr_b16 a[94:95], v214 offset:49288\n\t"
      "ds_read_b128 v[152:155], v199 offset:12288\n\t"
      "s_waitcnt lgkmcnt(8)\n\t"
      "v_mfma_f32_16x16x32_bf16 v[104:107], a[48:51], v[160:163], v[104:107]\n\t"
      "s_add_u32 m0, s41, 0x1b000\n\t"
      "s_mov_b32 s51, 0x24c00\n\t"
      "buffer_load_dwordx4 v211, s[16:19], s51 offen lds\n\t"
      "v_mfma_f32_16x16x32_bf16 v[108:111], a[52:55], v[160:163], v[108:111]\n\t"
      "buffer_load_dwordx4 v212, s[16:19], s51 offen offset:1024 lds\n\t"
      "v_mfma_f32_16x16x32_bf16 v[112:115], a[56:59], v[160:163], v[112:115]\n\t"
      "buffer_load_dwordx4 v213, s[16:19], s51 offen offset:2048 lds\n\t"
      "s_waitcnt vmcnt(15)\n\t"
      "ds_read_b64_tr_b16 a[96:97], v221 offset:0\n\t"
      "v_mfma_f32_16x16x32_bf16 v[116:119], a[60:63], v[160:163], v[116:119]\n\t"
      "ds_read_b64_tr_b16 a[100:101], v221 offset:8\n\t"
      "ds_read_b64_tr_b16 a[104:105], v221 offset:64\n\t"
      "v_mfma_f32_16x16x32_bf16 v[120:123], a[64:67], v[160:163], v[120:123]\n\t"
      "ds_read_b64_tr_b16 a[108:109], v221 offset:72\n\t"
      "ds_read_b64_tr_b16 a[112:113], v221 offset:128\n\t"
      "v_mfma_f32_16x16x32_bf16 v[132:135], a[68:71], v[160:163], v[132:135]\n\t"
      "ds_read_b64_tr_b16 a[116:117], v221 offset:136\n\t"
      "ds_read_b128 v[144:147], v197 offset:256\n\t"
      "s_waitcnt lgkmcnt(8)\n\t"
      "v_mfma_f32_16x16x32_bf16 v[80:83], a[72:75], v[140:143], v[80:83]\n\t"
      "s_add_u32 m0, s41, 0x1e000\n\t"
      "s_mov_b32 s51, 0x2a000\n\t"
      "buffer_load_dwordx4 v211, s[16:19], s51 offen lds\n\t"
      "v_mfma_f32_16x16x32_bf16 v[84:87], a[76:79], v[140:143], v[84:87]\n\t"
      "buffer_load_dwordx4 v212, s[16:19], s51 offen offset:1024 lds\n\t"
      "v_mfma_f32_16x16x32_bf16 v[88:91], a[80:83], v[140:143], v[88:91]\n\t"
      "buffer_load_dwordx4 v213, s[16:19], s51 offen offset:2048 lds\n\t"
      "s_waitcnt vmcnt(15)\n\t"
      "ds_read_b64_tr_b16 a[98:99], v221 offset:12288\n\t"
      "v_mfma_f32_16x16x32_bf16 v[92:95], a[84:87], v[140:143], v[92:95]\n\t"
      "ds_read_b64_tr_b16 a[102:103], v221 offset:12296\n\t"
      "ds_read_b64_tr_b16 a[106:107], v221 offset:12352\n\t"
      "v_mfma_f32_16x16x32_bf16 v[96:99], a[88:91], v[140:143], v[96:99]\n\t"
      "ds_read_b64_tr_b16 a[110:111], v221 offset:12360\n\t"
      "ds_read_b64_tr_b16 a[114:115], v221 offset:12416\n\t"
      "v_mfma_f32_16x16x32_bf16 v[100:103], a[92:95], v[140:143], v[100:103]\n\t"
      "ds_read_b64_tr_b16 a[118:119], v221 offset:12424\n\t"
      "ds_read_b128 v[156:159], v197 offset:12544\n\t"
      "s_waitcnt lgkmcnt(8)\n\t"
      "v_mfma_f32_16x16x32_bf16 v[104:107], a[72:75], v[152:155], v[104:107]\n\t"
      "s_add_u32 m0, s41, 0x21000\n\t"
      "s_mov_b32 s51, 0x2ac00\n\t"
      "buffer_load_dwordx4 v211, s[16:19], s51 offen lds\n\t"
      "v_mfma_f32_16x16x32_bf16 v[108:111], a[76:79], v[152:155], v[108:111]\n\t"
      "buffer_load_dwordx4 v212, s[16:19], s51 offen offset:1024 lds\n\t"
      "v_mfma_f32_16x16x32_bf16 v[112:115], a[80:83], v[152:155], v[112:115]\n\t"
      "buffer_load_dwordx4 v213, s[16:19], s51 offen offset:2048 lds\n\t"
      "s_waitcnt vmcnt(15)\n\t"
      "ds_read_b64_tr_b16 a[120:121], v214 offset:0\n\t"
      "v_mfma_f32_16x16x32_bf16 v[116:119], a[84:87], v[152:155], v[116:119]\n\t"
      "ds_read_b64_tr_b16 a[124:125], v214 offset:8\n\t"
      "ds_read_b64_tr_b16 a[128:129], v214 offset:64\n\t"
      "v_mfma_f32_16x16x32_bf16 v[120:123], a[88:91], v[152:155], v[120:123]\n\t"
      "ds_read_b64_tr_b16 a[132:133], v214 offset:72\n\t"
      "ds_read_b64_tr_b16 a[136:137], v214 offset:128\n\t"
      "v_mfma_f32_16x16x32_bf16 v[132:135], a[92:95], v[152:155], v[132:135]\n\t"
      "ds_read_b64_tr_b16 a[140:141], v214 offset:136\n\t"
      "ds_read_b128 v[148:151], v198 offset:256\n\t"
      "s_waitcnt lgkmcnt(8)\n\t"
      "v_mfma_f32_16x16x32_bf16 v[80:83], a[96:99], v[144:147], v[80:83]\n\t"
      "s_add_u32 m0, s41, 0x24000\n\t"
      "s_mov_b32 s51, 0x30000\n\t"
      "buffer_load_dwordx4 v211, s[16:19], s51 offen lds\n\t"
      "v_mfma_f32_16x16x32_bf16 v[84:87], a[100:103], v[144:147], v[84:87]\n\t"
      "buffer_load_dwordx4 v212, s[16:19], s51 offen offset:1024 lds\n\t"
      "v_mfma_f32_16x16x32_bf16 v[88:91], a[104:107], v[144:147], v[88:91]\n\t"
      "buffer_load_dwordx4 v213, s[16:19], s51 offen offset:2048 lds\n\t"
      "s_waitcnt vmcnt(15)\n\t"
      "ds_read_b64_tr_b16 a[122:123], v214 offset:12288\n\t"
      "v_mfma_f32_16x16x32_bf16 v[92:95], a[108:111], v[144:147], v[92:95]\n\t"
      "ds_read_b64_tr_b16 a[126:127], v214 offset:12296\n\t"
      "ds_read_b64_tr_b16 a[130:131], v214 offset:12352\n\t"
      "v_mfma_f32_16x16x32_bf16 v[96:99], a[112:115], v[144:147], v[96:99]\n\t"
      "ds_read_b64_tr_b16 a[134:135], v214 offset:12360\n\t"
      "ds_read_b64_tr_b16 a[138:139], v214 offset:12416\n\t"
      "v_mfma_f32_16x16x32_bf16 v[100:103], a[116:119], v[144:147], v[100:103]\n\t"
      "ds_read_b64_tr_b16 a[142:143], v214 offset:12424\n\t"
      "ds_read_b128 v[160:163], v198 offset:12544\n\t"
      "s_waitcnt lgkmcnt(8)\n\t"
      "v_mfma_f32_16x16x32_bf16 v[104:107], a[96:99], v[156:159], v[104:107]\n\t"
      "s_add_u32 m0, s41, 0x12000\n\t"
      "s_mov_b32 s51, 0x30c00\n\t"
      "buffer_load_dwordx4 v211, s[16:19], s51 offen lds\n\t"
      "v_mfma_f32_16x16x32_bf16 v[108:111], a[100:103], v[156:159], v[108:111]\n\t"
      "buffer_load_dwordx4 v212, s[16:19], s51 offen offset:1024 lds\n\t"
      "v_mfma_f32_16x16x32_bf16 v[112:115], a[104:107], v[156:159], v[112:115]\n\t"
      "buffer_load_dwordx4 v213, s[16:19], s51 offen offset:2048 lds\n\t"
      "s_waitcnt vmcnt(15)\n\t"
      "ds_read_b64_tr_b16 a[144:145], v214 offset:24576\n\t"
      "v_mfma_f32_16x16x32_bf16 v[116:119], a[108:111], v[156:159], v[116:119]\n\t"
      "ds_read_b64_tr_b16 a[148:149], v214 offset:24584\n\t"
      "ds_read_b64_tr_b16 a[152:153], v214 offset:24640\n\t"
      "v_mfma_f32_16x16x32_bf16 v[120:123], a[112:115], v[156:159], v[120:123]\n\t"
      "ds_read_b64_tr_b16 a[156:157], v214 offset:24648\n\t"
      "ds_read_b64_tr_b16 a[160:161], v214 offset:24704\n\t"
      "v_mfma_f32_16x16x32_bf16 v[132:135], a[116:119], v[156:159], v[132:135]\n\t"
      "ds_read_b64_tr_b16 a[164:165], v214 offset:24712\n\t"
      "ds_read_b128 v[140:143], v199 offset:256\n\t"
      "s_waitcnt lgkmcnt(8)\n\t"
      "v_mfma_f32_16x16x32_bf16 v[80:83], a[120:123], v[148:151], v[80:83]\n\t"
      "s_add_u32 m0, s41, 0x15000\n\t"
      "s_mov_b32 s51, 0x36000\n\t"
      "buffer_load_dwordx4 v211, s[16:19], s51 offen lds\n\t"
      "v_mfma_f32_16x16x32_bf16 v[84:87], a[124:127], v[148:151], v[84:87]\n\t"
      "buffer_load_dwordx4 v212, s[16:19], s51 offen offset:1024 lds\n\t"
      "v_mfma_f32_16x16x32_bf16 v[88:91], a[128:131], v[148:151], v[88:91]\n\t"
      "buffer_load_dwordx4 v213, s[16:19], s51 offen offset:2048 lds\n\t"
      "s_waitcnt vmcnt(15)\n\t"
      "ds_read_b64_tr_b16 a[146:147], v214 offset:36864\n\t"
      "v_mfma_f32_16x16x32_bf16 v[92:95], a[132:135], v[148:151], v[92:95]\n\t"
      "ds_read_b64_tr_b16 a[150:151], v214 offset:36872\n\t"
      "ds_read_b64_tr_b16 a[154:155], v214 offset:36928\n\t"
      "v_mfma_f32_16x16x32_bf16 v[96:99], a[136:139], v[148:151], v[96:99]\n\t"
      "ds_read_b64_tr_b16 a[158:159], v214 offset:36936\n\t"
      "ds_read_b64_tr_b16 a[162:163], v214 offset:36992\n\t"
      "v_mfma_f32_16x16x32_bf16 v[100:103], a[140:143], v[148:151], v[100:103]\n\t"
      "ds_read_b64_tr_b16 a[166:167], v214 offset:37000\n\t"
      "ds_read_b128 v[152:155], v199 offset:12544\n\t"
      "s_waitcnt lgkmcnt(8)\n\t"
      "v_mfma_f32_16x16x32_bf16 v[104:107], a[120:123], v[160:163], v[104:107]\n\t"
      "s_add_u32 m0, s41, 0x18000\n\t"
      "s_mov_b32 s51, 0x36c00\n\t"
      "buffer_load_dwordx4 v211, s[16:19], s51 offen lds\n\t"
      "v_mfma_f32_16x16x32_bf16 v[108:111], a[124:127], v[160:163], v[108:111]\n\t"
      "buffer_load_dwordx4 v212, s[16:19], s51 offen offset:1024 lds\n\t"
      "v_mfma_f32_16x16x32_bf16 v[112:115], a[128:131], v[160:163], v[112:115]\n\t"
      "buffer_load_dwordx4 v213, s[16:19], s51 offen offset:2048 lds\n\t"
      "s_waitcnt vmcnt(15)\n\t"
      "ds_read_b64_tr_b16 a[168:169], v214 offset:49152\n\t"
      "v_mfma_f32_16x16x32_bf16 v[116:119], a[132:135], v[160:163], v[116:119]\n\t"
      "ds_read_b64_tr_b16 a[172:173], v214 offset:49160\n\t"
      "ds_read_b64_tr_b16 a[176:177], v214 offset:49216\n\t"
      "v_mfma_f32_16x16x32_bf16 v[120:123], a[136:139], v[160:163], v[120:123]\n\t"
      "ds_read_b64_tr_b16 a[180:181], v214 offset:49224\n\t"
      "ds_read_b64_tr_b16 a[184:185], v214 offset:49280\n\t"
      "v_mfma_f32_16x16x32_bf16 v[132:135], a[140:143], v[160:163], v[132:135]\n\t"
      "ds_read_b64_tr_b16 a[188:189], v214 offset:49288\n\t"
      "ds_read_b128 v[144:147], v196 offset:256\n\t"
      "s_waitcnt lgkmcnt(8)\n\t"
      "v_mfma_f32_16x16x32_bf16 v[80:83], a[144:147], v[140:143], v[80:83]\n\t"
      "s_add_u32 m0, s41, 0x1b000\n\t"
      "s_mov_b32 s51, 0x3c000\n\t"
      "buffer_load_dwordx4 v211, s[16:19], s51 offen lds\n\t"
      "v_mfma_f32_16x16x32_bf16 v[84:87], a[148:151], v[140:143], v[84:87]\n\t"
      "buffer_load_dwordx4 v212, s[16:19], s51 offen offset:1024 lds\n\t"
      "v_mfma_f32_16x16x32_bf16 v[88:91], a[152:155], v[140:143], v[88:91]\n\t"
      "buffer_load_dwordx4 v213, s[16:19], s51 offen offset:2048 lds\n\t"
      "s_waitcnt vmcnt(15)\n\t"
      "ds_read_b64_tr_b16 a[170:171], v221 offset:0\n\t"
      "v_mfma_f32_16x16x32_bf16 v[92:95], a[156:159], v[140:143], v[92:95]\n\t"
      "ds_read_b64_tr_b16 a[174:175], v221 offset:8\n\t"
      "ds_read_b64_tr_b16 a[178:179], v221 offset:64\n\t"
      "v_mfma_f32_16x16x32_bf16 v[96:99], a[160:163], v[140:143], v[96:99]\n\t"
      "ds_read_b64_tr_b16 a[182:183], v221 offset:72\n\t"
      "ds_read_b64_tr_b16 a[186:187], v221 offset:128\n\t"
      "v_mfma_f32_16x16x32_bf16 v[100:103], a[164:167], v[140:143], v[100:103]\n\t"
      "ds_read_b64_tr_b16 a[190:191], v221 offset:136\n\t"
      "ds_read_b128 v[156:159], v196 offset:12544\n\t"
      "s_waitcnt lgkmcnt(8)\n\t"
      "v_mfma_f32_16x16x32_bf16 v[104:107], a[144:147], v[152:155], v[104:107]\n\t"
      "s_add_u32 m0, s41, 0x1e000\n\t"
      "s_mov_b32 s51, 0x3cc00\n\t"
      "buffer_load_dwordx4 v211, s[16:19], s51 offen lds\n\t"
      "v_mfma_f32_16x16x32_bf16 v[108:111], a[148:151], v[152:155], v[108:111]\n\t"
      "buffer_load_dwordx4 v212, s[16:19], s51 offen offset:1024 lds\n\t"
      "v_mfma_f32_16x16x32_bf16 v[112:115], a[152:155], v[152:155], v[112:115]\n\t"
      "buffer_load_dwordx4 v213, s[16:19], s51 offen offset:2048 lds\n\t"
      "s_waitcnt vmcnt(15)\n\t"
      "ds_read_b64_tr_b16 a[192:193], v221 offset:12288\n\t"
      "v_mfma_f32_16x16x32_bf16 v[116:119], a[156:159], v[152:155], v[116:119]\n\t"
      "ds_read_b64_tr_b16 a[196:197], v221 offset:12296\n\t"
      "ds_read_b64_tr_b16 a[200:201], v221 offset:12352\n\t"
      "v_mfma_f32_16x16x32_bf16 v[120:123], a[160:163], v[152:155], v[120:123]\n\t"
      "ds_read_b64_tr_b16 a[204:205], v221 offset:12360\n\t"
      "ds_read_b64_tr_b16 a[208:209], v221 offset:12416\n\t"
      "v_mfma_f32_16x16x32_bf16 v[132:135], a[164:167], v[152:155], v[132:135]\n\t"
      "ds_read_b64_tr_b16 a[212:213], v221 offset:12424\n\t"
      "ds_read_b128 v[148:151], v198 offset:512\n\t"
      "s_waitcnt lgkmcnt(8)\n\t"
      "v_mfma_f32_16x16x32_bf16 v[80:83], a[168:171], v[144:147], v[80:83]\n\t"
      "s_add_u32 m0, s41, 0x21000\n\t"
      "s_mov_b32 s51, 0x42000\n\t"
      "buffer_load_dwordx4 v211, s[16:19], s51 offen lds\n\t"
      "v_mfma_f32_16x16x32_bf16 v[84:87], a[172:175], v[144:147], v[84:87]\n\t"
      "buffer_load_dwordx4 v212, s[16:19], s51 offen offset:1024 lds\n\t"
      "v_mfma_f32_16x16x32_bf16 v[88:91], a[176:179], v[144:147], v[88:91]\n\t"
      "buffer_load_dwordx4 v213, s[16:19], s51 offen offset:2048 lds\n\t"
      "s_waitcnt vmcnt(15)\n\t"
      "ds_read_b64_tr_b16 a[194:195], v214 offset:0\n\t"
      "v_mfma_f32_16x16x32_bf16 v[92:95], a[180:183], v[144:147], v[92:95]\n\t"
      "ds_read_b64_tr_b16 a[198:199], v214 offset:8\n\t"
      "ds_read_b64_tr_b16 a[202:203], v214 offset:64\n\t"
      "v_mfma_f32_16x16x32_bf16 v[96:99], a[184:187], v[144:147], v[96:99]\n\t"
      "ds_read_b64_tr_b16 a[206:207], v214 offset:72\n\t"
      "ds_read_b64_tr_b16 a[210:211], v214 offset:128\n\t"
      "v_mfma_f32_16x16x32_bf16 v[100:103], a[188:191], v[144:147], v[100:103]\n\t"
      "ds_read_b64_tr_b16 a[214:215], v214 offset:136\n\t"
      "ds_read_b128 v[160:163], v198 offset:12800\n\t"
      "s_waitcnt lgkmcnt(8)\n\t"
      "v_mfma_f32_16x16x32_bf16 v[104:107], a[168:171], v[156:159], v[104:107]\n\t"
      "s_add_u32 m0, s41, 0x24000\n\t"
      "s_mov_b32 s51, 0x42c00\n\t"
      "buffer_load_dwordx4 v211, s[16:19], s51 offen lds\n\t"
      "v_mfma_f32_16x16x32_bf16 v[108:111], a[172:175], v[156:159], v[108:111]\n\t"
      "buffer_load_dwordx4 v212, s[16:19], s51 offen offset:1024 lds\n\t"
      "v_mfma_f32_16x16x32_bf16 v[112:115], a[176:179], v[156:159], v[112:115]\n\t"
      "buffer_load_dwordx4 v213, s[16:19], s51 offen offset:2048 lds\n\t"
      "s_waitcnt vmcnt(15)\n\t"
      "ds_read_b64_tr_b16 a[216:217], v214 offset:12288\n\t"
      "v_mfma_f32_16x16x32_bf16 v[116:119], a[180:183], v[156:159], v[116:119]\n\t"
      "ds_read_b64_tr_b16 a[220:221], v214 offset:12296\n\t"
      "ds_read_b64_tr_b16 a[224:225], v214 offset:12352\n\t"
      "v_mfma_f32_16x16x32_bf16 v[120:123], a[184:187], v[156:159], v[120:123]\n\t"
      "ds_read_b64_tr_b16 a[228:229], v214 offset:12360\n\t"
      "ds_read_b64_tr_b16 a[232:233], v214 offset:12416\n\t"
      "v_mfma_f32_16x16x32_bf16 v[132:135], a[188:191], v[156:159], v[132:135]\n\t"
      "ds_read_b64_tr_b16 a[236:237], v214 offset:12424\n\t"
      "ds_read_b128 v[140:143], v199 offset:512\n\t"
      "s_waitcnt lgkmcnt(8)\n\t"
      "v_mfma_f32_16x16x32_bf16 v[80:83], a[192:195], v[148:151], v[80:83]\n\t"
      "s_add_u32 m0, s41, 0x6000\n\t"
      "s_add_u32 s51, s40, 0x600000\n\t"
      "buffer_load_dwordx4 v208, s[20:23], s51 offen lds\n\t"
      "v_mfma_f32_16x16x32_bf16 v[84:87], a[196:199], v[148:151], v[84:87]\n\t"
      "buffer_load_dwordx4 v208, s[20:23], s51 offen offset:1024 lds\n\t"
      "v_mfma_f32_16x16x32_bf16 v[88:91], a[200:203], v[148:151], v[88:91]\n\t"
      "buffer_load_dwordx4 v208, s[20:23], s51 offen offset:2048 lds\n\t"
      "s_waitcnt vmcnt(15)\n\t"
      "ds_read_b64_tr_b16 a[218:219], v214 offset:24576\n\t"
      "v_mfma_f32_16x16x32_bf16 v[92:95], a[204:207], v[148:151], v[92:95]\n\t"
      "ds_read_b64_tr_b16 a[222:223], v214 offset:24584\n\t"
      "ds_read_b64_tr_b16 a[226:227], v214 offset:24640\n\t"
      "v_mfma_f32_16x16x32_bf16 v[96:99], a[208:211], v[148:151], v[96:99]\n\t"
      "ds_read_b64_tr_b16 a[230:231], v214 offset:24648\n\t"
      "ds_read_b64_tr_b16 a[234:235], v214 offset:24704\n\t"
      "v_mfma_f32_16x16x32_bf16 v[100:103], a[212:215], v[148:151], v[100:103]\n\t"
      "ds_read_b64_tr_b16 a[238:239], v214 offset:24712\n\t"
      "ds_read_b128 v[152:155], v199 offset:12800\n\t"
      "s_waitcnt lgkmcnt(14)\n\t"
      "v_mfma_f32_16x16x32_bf16 v[104:107], a[192:195], v[160:163], v[104:107]\n\t"
      "s_add_u32 m0, s41, 0x9000\n\t"
      "s_add_u32 s51, s40, 0x600000\n\t"
      "buffer_load_dwordx4 v209, s[20:23], s51 offen lds\n\t"
      "v_mfma_f32_16x16x32_bf16 v[108:111], a[196:199], v[160:163], v[108:111]\n\t"
      "buffer_load_dwordx4 v209, s[20:23], s51 offen offset:1024 lds\n\t"
      "v_mfma_f32_16x16x32_bf16 v[112:115], a[200:203], v[160:163], v[112:115]\n\t"
      "buffer_load_dwordx4 v209, s[20:23], s51 offen offset:2048 lds\n\t"
      "s_waitcnt vmcnt(15)\n\t"
      "ds_read_b64_tr_b16 a[240:241], v214 offset:36864\n\t"
      "v_mfma_f32_16x16x32_bf16 v[116:119], a[204:207], v[160:163], v[116:119]\n\t"
      "ds_read_b64_tr_b16 a[244:245], v214 offset:36872\n\t"
      "ds_read_b64_tr_b16 a[248:249], v214 offset:36928\n\t"
      "v_mfma_f32_16x16x32_bf16 v[120:123], a[208:211], v[160:163], v[120:123]\n\t"
      "ds_read_b64_tr_b16 a[252:253], v214 offset:36936\n\t"
      "ds_read_b64_tr_b16 v[164:165], v214 offset:36992\n\t"
      "v_mfma_f32_16x16x32_bf16 v[132:135], a[212:215], v[160:163], v[132:135]\n\t"
      "ds_read_b64_tr_b16 v[168:169], v214 offset:37000\n\t"
      "ds_read_b128 v[144:147], v196 offset:512\n\t"
      "s_waitcnt lgkmcnt(8)\n\t"
      "v_mfma_f32_16x16x32_bf16 v[80:83], a[216:219], v[140:143], v[80:83]\n\t"
      "s_add_u32 m0, s41, 0xc000\n\t"
      "s_add_u32 s51, s40, 0xc00000\n\t"
      "buffer_load_dwordx4 v208, s[20:23], s51 offen lds\n\t"
      "v_mfma_f32_16x16x32_bf16 v[84:87], a[220:223], v[140:143], v[84:87]\n\t"
      "buffer_load_dwordx4 v208, s[20:23], s51 offen offset:1024 lds\n\t"
      "v_mfma_f32_16x16x32_bf16 v[88:91], a[224:227], v[140:143], v[88:91]\n\t"
      "buffer_load_dwordx4 v208, s[20:23], s51 offen offset:2048 lds\n\t"
      "s_waitcnt vmcnt(15)\n\t"
      "ds_read_b64_tr_b16 a[242:243], v214 offset:49152\n\t"
      "v_mfma_f32_16x16x32_bf16 v[92:95], a[228:231], v[140:143], v[92:95]\n\t"
      "ds_read_b64_tr_b16 a[246:247], v214 offset:49160\n\t"
      "ds_read_b64_tr_b16 a[250:251], v214 offset:49216\n\t"
      "v_mfma_f32_16x16x32_bf16 v[96:99], a[232:235], v[140:143], v[96:99]\n\t"
      "ds_read_b64_tr_b16 a[254:255], v214 offset:49224\n\t"
      "ds_read_b64_tr_b16 v[166:167], v214 offset:49280\n\t"
      "v_mfma_f32_16x16x32_bf16 v[100:103], a[236:239], v[140:143], v[100:103]\n\t"
      "ds_read_b64_tr_b16 v[170:171], v214 offset:49288\n\t"
      "ds_read_b128 v[156:159], v196 offset:12800\n\t"
      "s_waitcnt lgkmcnt(14)\n\t"
      "v_mfma_f32_16x16x32_bf16 v[104:107], a[216:219], v[152:155], v[104:107]\n\t"
      "s_add_u32 m0, s41, 0xf000\n\t"
      "s_add_u32 s51, s40, 0xc00000\n\t"
      "buffer_load_dwordx4 v209, s[20:23], s51 offen lds\n\t"
      "v_mfma_f32_16x16x32_bf16 v[108:111], a[220:223], v[152:155], v[108:111]\n\t"
      "buffer_load_dwordx4 v209, s[20:23], s51 offen offset:1024 lds\n\t"
      "v_mfma_f32_16x16x32_bf16 v[112:115], a[224:227], v[152:155], v[112:115]\n\t"
      "buffer_load_dwordx4 v209, s[20:23], s51 offen offset:2048 lds\n\t"
      "s_waitcnt vmcnt(15)\n\t"
      "ds_read_b64_tr_b16 v[172:173], v221 offset:0\n\t"
      "v_mfma_f32_16x16x32_bf16 v[116:119], a[228:231], v[152:155], v[116:119]\n\t"
      "ds_read_b64_tr_b16 v[176:177], v221 offset:8\n\t"
      "ds_read_b64_tr_b16 v[180:181], v221 offset:64\n\t"
      "ds_read_b64_tr_b16 v[184:185], v221 offset:72\n\t"
      "v_mfma_f32_16x16x32_bf16 v[120:123], a[232:235], v[152:155], v[120:123]\n\t"
      "ds_read_b64_tr_b16 v[188:189], v221 offset:128\n\t"
      "ds_read_b64_tr_b16 v[192:193], v221 offset:136\n\t"
      "v_mfma_f32_16x16x32_bf16 v[132:135], a[236:239], v[152:155], v[132:135]\n\t"
      "ds_read_b128 v[148:151], v197 offset:512\n\t"
      "ds_read_b128 v[160:163], v197 offset:12800\n\t"
      "s_waitcnt lgkmcnt(9)\n\t"
      "v_mfma_f32_16x16x32_bf16 v[80:83], a[240:243], v[144:147], v[80:83]\n\t"
      "buffer_load_dwordx2 v[240:241], v245, s[24:27], 0 offen sc1\n\t"
      "v_mfma_f32_16x16x32_bf16 v[84:87], a[244:247], v[144:147], v[84:87]\n\t"
      "s_cmp_eq_u32 s41, s43\n\t"
      "s_cbranch_scc0 .Lc3_%=\n\t"
      "buffer_load_dword v254, v253, s[24:27], 0 offen sc1\n\t"
      ".Lc3_%=:\n\t"
      "v_mfma_f32_16x16x32_bf16 v[88:91], a[248:251], v[144:147], v[88:91]\n\t"
      "s_waitcnt vmcnt(13)\n\t"
      "ds_read_b64_tr_b16 v[174:175], v221 offset:12288\n\t"
      "v_mfma_f32_16x16x32_bf16 v[92:95], a[252:255], v[144:147], v[92:95]\n\t"
      "ds_read_b64_tr_b16 v[178:179], v221 offset:12296\n\t"
      "ds_read_b64_tr_b16 v[182:183], v221 offset:12352\n\t"
      "v_mfma_f32_16x16x32_bf16 v[96:99], v[164:167], v[144:147], v[96:99]\n\t"
      "ds_read_b64_tr_b16 v[186:187], v221 offset:12360\n\t"
      "ds_read_b64_tr_b16 v[190:191], v221 offset:12416\n\t"
      "v_mfma_f32_16x16x32_bf16 v[100:103], v[168:171], v[144:147], v[100:103]\n\t"
      "ds_read_b64_tr_b16 v[194:195], v221 offset:12424\n\t"
      "s_memrealtime s[80:81]\n\t"
      "v_add_u32_e32 v196, s46, v200\n\t"
      "v_add_u32_e32 v197, s46, v201\n\t"
      "v_add_u32_e32 v198, s46, v202\n\t"
      "v_add_u32_e32 v199, s46, v203\n\t"
      "s_waitcnt vmcnt(10)\n\t"
      "s_waitcnt lgkmcnt(6)\n\t"
      "s_barrier\n\t"
      "v_mfma_f32_16x16x32_bf16 v[104:107], a[240:243], v[156:159], v[104:107]\n\t"
      "ds_read_b128 v[32:35], v196 offset:0\n\t"
      "v_mfma_f32_16x16x32_bf16 v[108:111], a[244:247], v[156:159], v[108:111]\n\t"
      "ds_read_b128 v[36:39], v197 offset:0\n\t"
      "v_mfma_f32_16x16x32_bf16 v[112:115], a[248:251], v[156:159], v[112:115]\n\t"
      "ds_read_b128 v[40:43], v198 offset:0\n\t"
      "v_mfma_f32_16x16x32_bf16 v[116:119], a[252:255], v[156:159], v[116:119]\n\t"
      "ds_read_b128 v[44:47], v199 offset:0\n\t"
      "v_mfma_f32_16x16x32_bf16 v[120:123], v[164:167], v[156:159], v[120:123]\n\t"
      "ds_read_b128 v[48:51], v197 offset:256\n\t"
      "v_mfma_f32_16x16x32_bf16 v[132:135], v[168:171], v[156:159], v[132:135]\n\t"
      "s_waitcnt lgkmcnt(5)\n\t"
      "s_mov_b32 s86, 77\n\t"
      "s_mov_b32 s84, 100\n\t"
      "v_mfma_f32_16x16x32_bf16 v[80:83], v[172:175], v[148:151], v[80:83]\n\t"
      "ds_read_b128 v[52:55], v198 offset:256\n\t"
      "v_mfma_f32_16x16x32_bf16 v[84:87], v[176:179], v[148:151], v[84:87]\n\t"
      "ds_read_b128 v[56:59], v199 offset:256\n\t"
      "v_mfma_f32_16x16x32_bf16 v[88:91], v[180:183], v[148:151], v[88:91]\n\t"
      "ds_read_b128 v[60:63], v196 offset:256\n\t"
      "v_mfma_f32_16x16x32_bf16 v[92:95], v[184:187], v[148:151], v[92:95]\n\t"
      "ds_read_b128 v[64:67], v198 offset:512\n\t"
      "v_mfma_f32_16x16x32_bf16 v[96:99], v[188:191], v[148:151], v[96:99]\n\t"
      "ds_read_b128 v[68:71], v199 offset:512\n\t"
      "v_mfma_f32_16x16x32_bf16 v[100:103], v[192:195], v[148:151], v[100:103]\n\t"
      "ds_read_b128 v[72:75], v196 offset:512\n\t"
      "v_mfma_f32_16x16x32_bf16 v[104:107], v[172:175], v[160:163], v[104:107]\n\t"
      "ds_read_b128 v[76:79], v197 offset:512\n\t"
      "v_mfma_f32_16x16x32_bf16 v[108:111], v[176:179], v[160:163], v[108:111]\n\t"
      "v_mfma_f32_16x16x32_bf16 v[112:115], v[180:183], v[160:163], v[112:115]\n\t"
      "v_mfma_f32_16x16x32_bf16 v[116:119], v[184:187], v[160:163], v[116:119]\n\t"
      "v_mfma_f32_16x16x32_bf16 v[120:123], v[188:191], v[160:163], v[120:123]\n\t"
      "v_mfma_f32_16x16x32_bf16 v[132:135], v[192:195], v[160:163], v[132:135]\n\t"
      ".Lp2_%=:\n\t"
      "s_cbranch_vccnz .Lwsb_%=\n\t"
      "s_waitcnt lgkmcnt(8)\n\t"
      "v_mfma_f32_16x16x32_bf16 v[128:131], a[0:3], v[32:35], v[8:11]\n\t"
      "v_cvt_pk_bf16_f32 v136, v120, v121\n\t"
      "v_mfma_f32_16x16x32_bf16 v[128:131], a[24:27], v[36:39], v[128:131]\n\t"
      "v_cvt_pk_bf16_f32 v137, v122, v123\n\t"
      "v_mfma_f32_16x16x32_bf16 v[128:131], a[48:51], v[40:43], v[128:131]\n\t"
      "v_cvt_pk_bf16_f32 v138, v132, v133\n\t"
      "v_mfma_f32_16x16x32_bf16 v[128:131], a[72:75], v[44:47], v[128:131]\n\t"
      "v_cvt_pk_bf16_f32 v139, v134, v135\n\t"
      "s_waitcnt lgkmcnt(4)\n\t"
      "v_mfma_f32_16x16x32_bf16 v[128:131], a[96:99], v[48:51], v[128:131]\n\t"
      "ds_write_b128 v219, v[136:139] offset:12288\n\t"
      "v_mfma_f32_16x16x32_bf16 v[128:131], a[120:123], v[52:55], v[128:131]\n\t"
      "v_cvt_pk_bf16_f32 v80, v80, v81\n\t"
      "v_mfma_f32_16x16x32_bf16 v[128:131], a[144:147], v[56:59], v[128:131]\n\t"
      "v_cvt_pk_bf16_f32 v81, v82, v83\n\t"
      "v_mfma_f32_16x16x32_bf16 v[128:131], a[168:171], v[60:63], v[128:131]\n\t"
      "v_cvt_pk_bf16_f32 v82, v84, v85\n\t"
      "s_waitcnt lgkmcnt(1)\n\t"
      "v_mfma_f32_16x16x32_bf16 v[128:131], a[192:195], v[64:67], v[128:131]\n\t"
      "v_cvt_pk_bf16_f32 v83, v86, v87\n\t"
      "v_mfma_f32_16x16x32_bf16 v[128:131], a[216:219], v[68:71], v[128:131]\n\t"
      "ds_write_b128 v225, v[80:83] offset:0\n\t"
      "v_mfma_f32_16x16x32_bf16 v[128:131], a[240:243], v[72:75], v[128:131]\n\t"
      "v_cvt_pk_bf16_f32 v88, v88, v89\n\t"
      "v_mfma_f32_16x16x32_bf16 v[128:131], v[172:175], v[76:79], v[128:131]\n\t"
      "v_mfma_f32_16x16x32_bf16 v[132:135], a[4:7], v[32:35], v[12:15]\n\t"
      "v_cvt_pk_bf16_f32 v89, v90, v91\n\t"
      "v_mfma_f32_16x16x32_bf16 v[132:135], a[28:31], v[36:39], v[132:135]\n\t"
      "v_cvt_pk_bf16_f32 v90, v92, v93\n\t"
      "v_mfma_f32_16x16x32_bf16 v[132:135], a[52:55], v[40:43], v[132:135]\n\t"
      "v_cvt_pk_bf16_f32 v136, v128, v129\n\t"
      "v_cvt_pk_bf16_f32 v91, v94, v95\n\t"
      "v_mfma_f32_16x16x32_bf16 v[132:135], a[76:79], v[44:47], v[132:135]\n\t"
      "v_cvt_pk_bf16_f32 v137, v130, v131\n\t"
      "ds_write_b128 v239, v[88:91] offset:0\n\t"
      "v_mfma_f32_16x16x32_bf16 v[132:135], a[100:103], v[48:51], v[132:135]\n\t"
      "v_cvt_pk_bf16_f32 v96, v96, v97\n\t"
      "s_add_u32 s48, s47, 0x6000\n\t"
      "v_mfma_f32_16x16x32_bf16 v[132:135], a[124:127], v[52:55], v[132:135]\n\t"
      "s_cmp_eq_u32 s48, s50\n\t"
      "s_cselect_b32 s48, s41, s48\n\t"
      "v_mfma_f32_16x16x32_bf16 v[132:135], a[148:151], v[56:59], v[132:135]\n\t"
      "v_cvt_pk_bf16_f32 v97, v98, v99\n\t"
      "v_mfma_f32_16x16x32_bf16 v[132:135], a[172:175], v[60:63], v[132:135]\n\t"
      "v_cvt_pk_bf16_f32 v98, v100, v101\n\t"
      "v_add_u32_e32 v217, s46, v204\n\t"
      "v_mfma_f32_16x16x32_bf16 v[132:135], a[196:199], v[64:67], v[132:135]\n\t"
      "v_cvt_pk_bf16_f32 v99, v102, v103\n\t"
      "v_add_u32_e32 v218, s46, v205\n\t"
      "v_mfma_f32_16x16x32_bf16 v[132:135], a[220:223], v[68:71], v[132:135]\n\t"
      "v_readlane_b32 s73, v248, s85\n\t"
      "v_add_u32_e32 v219, s46, v206\n\t"
      "v_mfma_f32_16x16x32_bf16 v[132:135], a[244:247], v[72:75], v[132:135]\n\t"
      "s_mov_b32 m0, s45\n\t"
      "v_mfma_f32_16x16x32_bf16 v[132:135], v[176:179], v[76:79], v[132:135]\n\t"
      "s_waitcnt lgkmcnt(0)\n\t"
      "s_barrier\n\t"
      "v_mfma_f32_16x16x32_bf16 v[128:131], a[8:11], v[32:35], v[16:19]\n\t"
      "ds_write_b128 v210, v[96:99] offset:0\n\t"
      "s_mov_b32 s82, s80\n\t"
      "v_mfma_f32_16x16x32_bf16 v[128:131], a[32:35], v[36:39], v[128:131]\n\t"
      "v_cvt_pk_bf16_f32 v104, v104, v105\n\t"
      "s_memrealtime s[80:81]\n\t"
      "v_mfma_f32_16x16x32_bf16 v[128:131], a[56:59], v[40:43], v[128:131]\n\t"
      "v_cvt_pk_bf16_f32 v138, v132, v133\n\t"
      "v_cvt_pk_bf16_f32 v105, v106, v107\n\t"
      "v_mfma_f32_16x16x32_bf16 v[128:131], a[80:83], v[44:47], v[128:131]\n\t"
      "v_cvt_pk_bf16_f32 v139, v134, v135\n\t"
      "v_cvt_pk_bf16_f32 v106, v108, v109\n\t"
      "v_mfma_f32_16x16x32_bf16 v[128:131], a[104:107], v[48:51], v[128:131]\n\t"
      "ds_write_b128 v217, v[136:139] offset:0\n\t"
      "v_mfma_f32_16x16x32_bf16 v[128:131], a[128:131], v[52:55], v[128:131]\n\t"
      "buffer_load_dwordx4 v208, s[20:23], s73 offen lds\n\t"
      "v_mfma_f32_16x16x32_bf16 v[128:131], a[152:155], v[56:59], v[128:131]\n\t"
      "v_cvt_pk_bf16_f32 v107, v110, v111\n\t"
      "v_mfma_f32_16x16x32_bf16 v[128:131], a[176:179], v[60:63], v[128:131]\n\t"
      "ds_write_b128 v225, v[104:107] offset:12288\n\t"
      "v_mfma_f32_16x16x32_bf16 v[128:131], a[200:203], v[64:67], v[128:131]\n\t"
      "v_cvt_pk_bf16_f32 v112, v112, v113\n\t"
      "v_mfma_f32_16x16x32_bf16 v[128:131], a[224:227], v[68:71], v[128:131]\n\t"
      "v_cvt_pk_bf16_f32 v113, v114, v115\n\t"
      "v_mfma_f32_16x16x32_bf16 v[128:131], a[248:251], v[72:75], v[128:131]\n\t"
      "v_cvt_pk_bf16_f32 v114, v116, v117\n\t"
      "v_mfma_f32_16x16x32_bf16 v[128:131], v[180:183], v[76:79], v[128:131]\n\t"
      "v_mfma_f32_16x16x32_bf16 v[132:135], a[12:15], v[32:35], v[20:23]\n\t"
      "v_cvt_pk_bf16_f32 v115, v118, v119\n\t"
      "v_mfma_f32_16x16x32_bf16 v[132:135], a[36:39], v[36:39], v[132:135]\n\t"
      "buffer_load_dwordx4 v208, s[20:23], s73 offen offset:1024 lds\n\t"
      "v_mfma_f32_16x16x32_bf16 v[132:135], a[60:63], v[40:43], v[132:135]\n\t"
      "v_cvt_pk_bf16_f32 v136, v128, v129\n\t"
      "ds_write_b128 v239, v[112:115] offset:12288\n\t"
      "v_mfma_f32_16x16x32_bf16 v[132:135], a[84:87], v[44:47], v[132:135]\n\t"
      "v_cvt_pk_bf16_f32 v137, v130, v131\n\t"
      "v_mfma_f32_16x16x32_bf16 v[132:135], a[108:111], v[48:51], v[132:135]\n\t"
      "v_mfma_f32_16x16x32_bf16 v[132:135], a[132:135], v[52:55], v[132:135]\n\t"
      "v_mfma_f32_16x16x32_bf16 v[132:135], a[156:159], v[56:59], v[132:135]\n\t"
      "v_mfma_f32_16x16x32_bf16 v[132:135], a[180:183], v[60:63], v[132:135]\n\t"
      "v_mfma_f32_16x16x32_bf16 v[132:135], a[204:207], v[64:67], v[132:135]\n\t"
      "v_mfma_f32_16x16x32_bf16 v[132:135], a[228:231], v[68:71], v[132:135]\n\t"
      "buffer_load_dwordx4 v208, s[20:23], s73 offen offset:2048 lds\n\t"
      "v_mfma_f32_16x16x32_bf16 v[132:135], a[252:255], v[72:75], v[132:135]\n\t"
      "v_readlane_b32 s73, v249, s85\n\t"
      "s_add_u32 m0, s45, 0x3000\n\t"
      "v_mfma_f32_16x16x32_bf16 v[132:135], v[184:187], v[76:79], v[132:135]\n\t"
      "s_waitcnt vmcnt(10)\n\t"
      "s_barrier\n\t"
      "v_mfma_f32_16x16x32_bf16 v[128:131], a[16:19], v[32:35], v[24:27]\n\t"
      "ds_read_b128 v[80:83], v196 offset:12288\n\t"
      "v_mfma_f32_16x16x32_bf16 v[128:131], a[40:43], v[36:39], v[128:131]\n\t"
      "v_mfma_f32_16x16x32_bf16 v[128:131], a[64:67], v[40:43], v[128:131]\n\t"
      "ds_read_b128 v[84:87], v197 offset:12288\n\t"
      "v_cvt_pk_bf16_f32 v138, v132, v133\n\t"
      "v_mfma_f32_16x16x32_bf16 v[128:131], a[88:91], v[44:47], v[128:131]\n\t"
      "ds_read_b128 v[88:91], v198 offset:12288\n\t"
      "v_cvt_pk_bf16_f32 v139, v134, v135\n\t"
      "v_mfma_f32_16x16x32_bf16 v[128:131], a[112:115], v[48:51], v[128:131]\n\t"
      "ds_write_b128 v218, v[136:139] offset:0\n\t"
      "v_mfma_f32_16x16x32_bf16 v[128:131], a[136:139], v[52:55], v[128:131]\n\t"
      "buffer_load_dwordx4 v209, s[20:23], s73 offen lds\n\t"
      "s_waitcnt vmcnt(10)\n\t"
      "s_cmp_eq_u32 s94, 0\n\t"
      "s_cbranch_scc0 .Lwgvw_0_%=\n\t"
      "s_waitcnt vmcnt(4)\n\t"
      ".Lwgvw_0_%=:\n\t"
      "v_subrev_u32_e32 v242, s63, v240\n\t"
      "v_xor_b32_e32 v241, s53, v241\n\t"
      "v_mfma_f32_16x16x32_bf16 v[128:131], a[160:163], v[56:59], v[128:131]\n\t"
      "ds_read_b128 v[92:95], v199 offset:12288\n\t"
      "v_lshrrev_b32_e32 v240, 13, v242\n\t"
      "v_or_b32_e32 v240, v240, v241\n\t"
      "v_mfma_f32_16x16x32_bf16 v[128:131], a[184:187], v[60:63], v[128:131]\n\t"
      "ds_read_b128 v[96:99], v197 offset:12544\n\t"
      "v_mfma_f32_16x16x32_bf16 v[128:131], a[208:211], v[64:67], v[128:131]\n\t"
      "v_cmp_ne_u32_e32 vcc, 0, v240\n\t"
      "v_and_b32_e32 v242, 15, v242\n\t"
      "v_mfma_f32_16x16x32_bf16 v[128:131], a[232:235], v[68:71], v[128:131]\n\t"
      "v_mfma_f32_16x16x32_bf16 v[128:131], v[164:167], v[72:75], v[128:131]\n\t"
      "ds_read_b128 v[100:103], v198 offset:12544\n\t"
      "s_cbranch_vccnz .Lgs_%=\n\t"
      ".Lgr_%=:\n\t"
      "ds_write_b32 v246, v242\n\t"
      "v_mfma_f32_16x16x32_bf16 v[128:131], v[188:191], v[76:79], v[128:131]\n\t"
      "ds_read_b128 v[104:107], v199 offset:12544\n\t"
      "v_mfma_f32_16x16x32_bf16 v[132:135], a[20:23], v[32:35], v[28:31]\n\t"
      "ds_read_b128 v[108:111], v196 offset:12544\n\t"
      "v_mfma_f32_16x16x32_bf16 v[132:135], a[44:47], v[36:39], v[132:135]\n\t"
      "buffer_load_dwordx4 v209, s[20:23], s73 offen offset:1024 lds\n\t"
      "v_mfma_f32_16x16x32_bf16 v[132:135], a[68:71], v[40:43], v[132:135]\n\t"
      "ds_read_b128 v[112:115], v198 offset:12800\n\t"
      "v_cvt_pk_bf16_f32 v136, v128, v129\n\t"
      "v_add_u32_e32 v220, s46, v207\n\t"
      "v_mfma_f32_16x16x32_bf16 v[132:135], a[92:95], v[44:47], v[132:135]\n\t"
      "ds_read_b128 v[116:119], v199 offset:12800\n\t"
      "v_cvt_pk_bf16_f32 v137, v130, v131\n\t"
      "s_sub_u32 s84, s84, 4\n\t"
      "s_max_i32 s84, s84, s86\n\t"
      "v_mfma_f32_16x16x32_bf16 v[132:135], a[116:119], v[48:51], v[132:135]\n\t"
      "s_sub_u32 s83, s82, s80\n\t"
      "v_mfma_f32_16x16x32_bf16 v[132:135], a[140:143], v[52:55], v[132:135]\n\t"
      "s_max_i32 s83, s83, 0\n\t"
      "s_add_u32 s82, s80, s83\n\t"
      "v_mfma_f32_16x16x32_bf16 v[132:135], a[164:167], v[56:59], v[132:135]\n\t"
      "ds_read_b128 v[120:123], v196 offset:12800\n\t"
      "v_mfma_f32_16x16x32_bf16 v[132:135], a[188:191], v[60:63], v[132:135]\n\t"
      "ds_read_b128 v[124:127], v197 offset:12800\n\t"
      "v_mfma_f32_16x16x32_bf16 v[132:135], a[212:215], v[64:67], v[132:135]\n\t"
      "v_mfma_f32_16x16x32_bf16 v[132:135], a[236:239], v[68:71], v[132:135]\n\t"
      "s_add_u32 s82, s82, s84\n\t"
      "v_mfma_f32_16x16x32_bf16 v[132:135], v[168:171], v[72:75], v[132:135]\n\t"
      "s_add_u32 s44, s44, 1\n\t"
      "s_add_u32 s85, s85, 1\n\t"
      "v_mfma_f32_16x16x32_bf16 v[132:135], v[192:195], v[76:79], v[132:135]\n\t"
      "s_waitcnt lgkmcnt(9)\n\t"
      "v_mfma_f32_16x16x32_bf16 v[128:131], a[0:3], v[80:83], v[8:11]\n\t"
      "v_mfma_f32_16x16x32_bf16 v[128:131], a[24:27], v[84:87], v[128:131]\n\t"
      "v_readlane_b32 s72, v248, s44\n\t"
      "v_mfma_f32_16x16x32_bf16 v[128:131], a[48:51], v[88:91], v[128:131]\n\t"
      "v_cvt_pk_bf16_f32 v138, v132, v133\n\t"
      "v_mfma_f32_16x16x32_bf16 v[128:131], a[72:75], v[92:95], v[128:131]\n\t"
      "v_cvt_pk_bf16_f32 v139, v134, v135\n\t"
      "s_waitcnt lgkmcnt(4)\n\t"
      "v_mfma_f32_16x16x32_bf16 v[128:131], a[96:99], v[96:99], v[128:131]\n\t"
      "ds_write_b128 v219, v[136:139] offset:0\n\t"
      "v_mfma_f32_16x16x32_bf16 v[128:131], a[120:123], v[100:103], v[128:131]\n\t"
      "v_mfma_f32_16x16x32_bf16 v[128:131], a[144:147], v[104:107], v[128:131]\n\t"
      "buffer_load_dwordx4 v209, s[20:23], s73 offen offset:2048 lds\n\t"
      "v_mfma_f32_16x16x32_bf16 v[128:131], a[168:171], v[108:111], v[128:131]\n\t"
      "v_add_u32_e32 v196, s47, v200\n\t"
      "s_waitcnt lgkmcnt(1)\n\t"
      "v_mfma_f32_16x16x32_bf16 v[128:131], a[192:195], v[112:115], v[128:131]\n\t"
      "v_add_u32_e32 v197, s47, v201\n\t"
      "v_mfma_f32_16x16x32_bf16 v[128:131], a[216:219], v[116:119], v[128:131]\n\t"
      "v_add_u32_e32 v198, s47, v202\n\t"
      "v_mfma_f32_16x16x32_bf16 v[128:131], a[240:243], v[120:123], v[128:131]\n\t"
      "v_add_u32_e32 v199, s47, v203\n\t"
      "v_mfma_f32_16x16x32_bf16 v[128:131], v[172:175], v[124:127], v[128:131]\n\t"
      "s_cmp_gt_i32 s83, 1\n\t"
      "s_cbranch_scc1 .LslpP3_%=\n\t"
      ".LretP3_%=:\n\t"
      "s_cmp_eq_u32 s94, 1\n\t"
      "s_cbranch_scc0 .LwP3barv_1_%=\n\t"
      "s_waitcnt vmcnt(9)\n\t"
      ".LwP3barv_1_%=:\n\t"
      "s_waitcnt lgkmcnt(0)\n\t"
      "s_barrier\n\t"
      "v_mfma_f32_16x16x32_bf16 v[132:135], a[4:7], v[80:83], v[12:15]\n\t"
      "ds_read_b128 v[140:143], v220 offset:0\n\t"
      "v_mfma_f32_16x16x32_bf16 v[132:135], a[28:31], v[84:87], v[132:135]\n\t"
      "ds_read_b128 v[240:243], v247 offset:0\n\t"
      "v_mfma_f32_16x16x32_bf16 v[132:135], a[52:55], v[88:91], v[132:135]\n\t"
      "v_cvt_pk_bf16_f32 v136, v128, v129\n\t"
      "v_mfma_f32_16x16x32_bf16 v[132:135], a[76:79], v[92:95], v[132:135]\n\t"
      "ds_read_b128 v[32:35], v196 offset:0\n\t"
      "v_cvt_pk_bf16_f32 v137, v130, v131\n\t"
      "v_mfma_f32_16x16x32_bf16 v[132:135], a[100:103], v[96:99], v[132:135]\n\t"
      "v_readlane_b32 s68, v248, s94\n\t"
      "v_mfma_f32_16x16x32_bf16 v[132:135], a[124:127], v[100:103], v[132:135]\n\t"
      "ds_read_b128 v[152:155], v238 offset:0\n\t"
      "v_mfma_f32_16x16x32_bf16 v[132:135], a[148:151], v[104:107], v[132:135]\n\t"
      "ds_read_b128 v[36:39], v197 offset:0\n\t"
      "v_mfma_f32_16x16x32_bf16 v[132:135], a[172:175], v[108:111], v[132:135]\n\t"
      "v_mfma_f32_16x16x32_bf16 v[132:135], a[196:199], v[112:115], v[132:135]\n\t"
      "s_waitcnt lgkmcnt(3)\n\t"
      "v_add3_u32 v240, v240, v241, v242\n\t"
      "v_mfma_f32_16x16x32_bf16 v[132:135], a[220:223], v[116:119], v[132:135]\n\t"
      "ds_read_b128 v[40:43], v198 offset:0\n\t"
      "v_mfma_f32_16x16x32_bf16 v[132:135], a[244:247], v[120:123], v[132:135]\n\t"
      "v_add_u32_e32 v240, v240, v243\n\t"
      "v_mfma_f32_16x16x32_bf16 v[132:135], v[176:179], v[124:127], v[132:135]\n\t"
      "buffer_store_dwordx4 v[140:143], v208, s[28:31], s72 offen sc1\n\t"
      "v_mfma_f32_16x16x32_bf16 v[128:131], a[8:11], v[80:83], v[16:19]\n\t"
      "ds_read_b128 v[44:47], v199 offset:0\n\t"
      "v_mfma_f32_16x16x32_bf16 v[128:131], a[32:35], v[84:87], v[128:131]\n\t"
      "v_add_u32_dpp v241, v240, v240 quad_perm:[1,0,3,2] row_mask:0xf bank_mask:0xf\n\t"
      "s_memrealtime s[80:81]\n\t"
      "v_mfma_f32_16x16x32_bf16 v[128:131], a[56:59], v[88:91], v[128:131]\n\t"
      "v_cvt_pk_bf16_f32 v138, v132, v133\n\t"
      "v_mfma_f32_16x16x32_bf16 v[128:131], a[80:83], v[92:95], v[128:131]\n\t"
      "v_cvt_pk_bf16_f32 v139, v134, v135\n\t"
      "v_mfma_f32_16x16x32_bf16 v[128:131], a[104:107], v[96:99], v[128:131]\n\t"
      "ds_write_b128 v217, v[136:139] offset:12288\n\t"
      "v_add_u32_dpp v240, v241, v241 quad_perm:[2,3,0,1] row_mask:0xf bank_mask:0xf\n\t"
      "v_mfma_f32_16x16x32_bf16 v[128:131], a[128:131], v[100:103], v[128:131]\n\t"
      "s_mov_b32 m0, s46\n\t"
      "v_mfma_f32_16x16x32_bf16 v[128:131], a[152:155], v[104:107], v[128:131]\n\t"
      "s_waitcnt lgkmcnt(4)\n\t"
      "buffer_store_dwordx4 v[152:155], v208, s[28:31], s68 offen sc1\n\t"
      "v_cmp_le_u32_e64 s[76:77], 17, v240\n\t"
      "v_cmp_ge_u32_e64 s[78:79], 15, v240\n\t"
      "v_mfma_f32_16x16x32_bf16 v[128:131], a[176:179], v[108:111], v[128:131]\n\t"
      "ds_read_b128 v[48:51], v197 offset:256\n\t"
      "v_mfma_f32_16x16x32_bf16 v[128:131], a[200:203], v[112:115], v[128:131]\n\t"
      "v_cmp_lt_u32_e32 vcc, 63, v240\n\t"
      "s_and_b32 s65, s76, s77\n\t"
      "v_mfma_f32_16x16x32_bf16 v[128:131], a[224:227], v[116:119], v[128:131]\n\t"
      "ds_read_b128 v[52:55], v198 offset:256\n\t"
      "v_mfma_f32_16x16x32_bf16 v[128:131], a[248:251], v[120:123], v[128:131]\n\t"
      "ds_read_b128 v[156:159], v238 offset:1024\n\t"
      "s_and_b32 s78, s78, s79\n\t"
      "s_and_b32 s67, s76, 0x11111111\n\t"
      "v_mfma_f32_16x16x32_bf16 v[128:131], v[180:183], v[124:127], v[128:131]\n\t"
      "v_mfma_f32_16x16x32_bf16 v[132:135], a[12:15], v[80:83], v[20:23]\n\t"
      "ds_read_b128 v[56:59], v199 offset:256\n\t"
      "s_or_b32 s65, s65, s78\n\t"
      "s_cmp_lg_u64 vcc, 0\n\t"
      "s_cselect_b32 s65, 0, s65\n\t"
      "s_cselect_b32 s67, 0, s67\n\t"
      "v_mfma_f32_16x16x32_bf16 v[132:135], a[36:39], v[84:87], v[132:135]\n\t"
      "v_and_b32_e32 v240, s67, v252\n\t"
      "v_and_b32_e32 v241, s65, v252\n\t"
      "v_mfma_f32_16x16x32_bf16 v[132:135], a[60:63], v[88:91], v[132:135]\n\t"
      "v_cvt_pk_bf16_f32 v136, v128, v129\n\t"
      "v_mfma_f32_16x16x32_bf16 v[132:135], a[84:87], v[92:95], v[132:135]\n\t"
      "v_cvt_pk_bf16_f32 v137, v130, v131\n\t"
      "ds_read_b128 v[144:147], v220 offset:1024\n\t"
      "v_mfma_f32_16x16x32_bf16 v[132:135], a[108:111], v[96:99], v[132:135]\n\t"
      "v_cmp_ne_u32_e64 s[76:77], 0, v240\n\t"
      "v_cmp_ne_u32_e64 s[78:79], 0, v241\n\t"
      "v_mfma_f32_16x16x32_bf16 v[132:135], a[132:135], v[100:103], v[132:135]\n\t"
      "s_waitcnt lgkmcnt(2)\n\t"
      "buffer_store_dwordx4 v[156:159], v208, s[28:31], s68 offen offset:1024 sc1\n\t"
      "v_mfma_f32_16x16x32_bf16 v[132:135], a[156:159], v[104:107], v[132:135]\n\t"
      "ds_read_b128 v[60:63], v196 offset:256\n\t"
      "s_bcnt1_i32_b32 s51, s78\n\t"
      "s_cmp_lt_u32 s51, 4\n\t"
      "s_cselect_b32 s93, s92, s93\n\t"
      "s_cselect_b32 s92, 16, 0\n\t"
      "s_mov_b32 s51, 0\n\t"
      "v_mfma_f32_16x16x32_bf16 v[132:135], a[180:183], v[108:111], v[132:135]\n\t"
      "s_xor_b32 s75, s75, s76\n\t"
      "s_xor_b32 s74, s74, s76\n\t"
      "v_mfma_f32_16x16x32_bf16 v[132:135], a[204:207], v[112:115], v[132:135]\n\t"
      "s_and_b32 s76, s75, s93\n\t"
      "v_cndmask_b32_e64 v249, v248, v251, s[76:77]\n\t"
      "v_mfma_f32_16x16x32_bf16 v[132:135], a[228:231], v[116:119], v[132:135]\n\t"
      "ds_read_b128 v[64:67], v198 offset:512\n\t"
      "v_mfma_f32_16x16x32_bf16 v[132:135], a[252:255], v[120:123], v[132:135]\n\t"
      "s_and_b32 s76, s74, s93\n\t"
      "v_cndmask_b32_e64 v248, v248, v251, s[76:77]\n\t"
      "v_mfma_f32_16x16x32_bf16 v[132:135], v[184:187], v[124:127], v[132:135]\n\t"
      "s_waitcnt lgkmcnt(2)\n\t"
      "buffer_store_dwordx4 v[144:147], v208, s[28:31], s72 offen offset:1024 sc1\n\t"
      "v_mfma_f32_16x16x32_bf16 v[128:131], a[16:19], v[80:83], v[24:27]\n\t"
      "ds_read_b128 v[68:71], v199 offset:512\n\t"
      "s_sub_u32 s51, s51, s92\n\t"
      "v_mfma_f32_16x16x32_bf16 v[128:131], a[40:43], v[84:87], v[128:131]\n\t"
      "s_ashr_i32 s51, s51, 4\n\t"
      "s_and_b32 s64, s65, 0x22222222\n\t"
      "v_mfma_f32_16x16x32_bf16 v[128:131], a[64:67], v[88:91], v[128:131]\n\t"
      "v_cvt_pk_bf16_f32 v138, v132, v133\n\t"
      "v_readlane_b32 s73, v248, s85\n\t"
      "v_mfma_f32_16x16x32_bf16 v[128:131], a[88:91], v[92:95], v[128:131]\n\t"
      "v_cvt_pk_bf16_f32 v139, v134, v135\n\t"
      "ds_read_b128 v[160:163], v238 offset:2048\n\t"
      "v_mfma_f32_16x16x32_bf16 v[128:131], a[112:115], v[96:99], v[128:131]\n\t"
      "ds_write_b128 v218, v[136:139] offset:12288\n\t"
      "v_mfma_f32_16x16x32_bf16 v[128:131], a[136:139], v[100:103], v[128:131]\n\t"
      "v_subrev_u32_e32 v250, s51, v250\n\t"
      "v_mfma_f32_16x16x32_bf16 v[128:131], a[160:163], v[104:107], v[128:131]\n\t"
      "v_mfma_f32_16x16x32_bf16 v[128:131], a[184:187], v[108:111], v[128:131]\n\t"
      "ds_read_b128 v[148:151], v220 offset:2048\n\t"
      "v_mfma_f32_16x16x32_bf16 v[128:131], a[208:211], v[112:115], v[128:131]\n\t"
      "buffer_load_dwordx4 v208, s[20:23], s73 offen lds\n\t"
      "v_mfma_f32_16x16x32_bf16 v[128:131], a[232:235], v[116:119], v[128:131]\n\t"
      "ds_read_b128 v[72:75], v196 offset:512\n\t"
      "s_or_b32 s64, s64, s67\n\t"
      "v_mfma_f32_16x16x32_bf16 v[128:131], v[164:167], v[120:123], v[128:131]\n\t"
      "v_readlane_b32 s86, v250, s44\n\t"
      "v_mfma_f32_16x16x32_bf16 v[128:131], v[188:191], v[124:127], v[128:131]\n\t"
      "v_mfma_f32_16x16x32_bf16 v[132:135], a[20:23], v[80:83], v[28:31]\n\t"
      "ds_read_b128 v[76:79], v197 offset:512\n\t"
      "v_mfma_f32_16x16x32_bf16 v[132:135], a[44:47], v[84:87], v[132:135]\n\t"
      "buffer_load_dwordx4 v208, s[20:23], s73 offen offset:1024 lds\n\t"
      "v_mfma_f32_16x16x32_bf16 v[132:135], a[68:71], v[88:91], v[132:135]\n\t"
      "v_cvt_pk_bf16_f32 v136, v128, v129\n\t"
      "v_mfma_f32_16x16x32_bf16 v[132:135], a[92:95], v[92:95], v[132:135]\n\t"
      "v_cvt_pk_bf16_f32 v137, v130, v131\n\t"
      "s_sub_u32 s84, s84, 4\n\t"
      "s_max_i32 s84, s84, s86\n\t"
      "v_mfma_f32_16x16x32_bf16 v[132:135], a[116:119], v[96:99], v[132:135]\n\t"
      "s_sub_u32 s83, s82, s80\n\t"
      "v_mfma_f32_16x16x32_bf16 v[132:135], a[140:143], v[100:103], v[132:135]\n\t"
      "s_waitcnt lgkmcnt(4)\n\t"
      "buffer_store_dwordx4 v[160:163], v208, s[28:31], s68 offen offset:2048 sc1\n\t"
      "s_max_i32 s83, s83, 0\n\t"
      "s_add_u32 s82, s80, s83\n\t"
      "v_mfma_f32_16x16x32_bf16 v[132:135], a[164:167], v[104:107], v[132:135]\n\t"
      "s_mov_b32 s45, s46\n\t"
      "v_mfma_f32_16x16x32_bf16 v[132:135], a[188:191], v[108:111], v[132:135]\n\t"
      "s_mov_b32 s46, s47\n\t"
      "v_mfma_f32_16x16x32_bf16 v[132:135], a[212:215], v[112:115], v[132:135]\n\t"
      "s_mov_b32 s47, s48\n\t"
      "v_mfma_f32_16x16x32_bf16 v[132:135], a[236:239], v[116:119], v[132:135]\n\t"
      "s_add_u32 s82, s82, s84\n\t"
      "v_mfma_f32_16x16x32_bf16 v[132:135], v[168:171], v[120:123], v[132:135]\n\t"
      "s_cmp_lg_u32 s44, s49\n\t"
      "v_mfma_f32_16x16x32_bf16 v[132:135], v[192:195], v[124:127], v[132:135]\n\t"
      "s_waitcnt lgkmcnt(2)\n\t"
      "buffer_store_dwordx4 v[148:151], v208, s[28:31], s72 offen offset:2048 sc1\n\t"
      "v_mfma_f32_16x16x32_bf16 v[128:131], a[0:3], v[32:35], v[8:11]\n\t"
      "v_mfma_f32_16x16x32_bf16 v[128:131], a[24:27], v[36:39], v[128:131]\n\t"
      "v_readlane_b32 s72, v249, s44\n\t"
      "v_mfma_f32_16x16x32_bf16 v[128:131], a[48:51], v[40:43], v[128:131]\n\t"
      "v_cvt_pk_bf16_f32 v138, v132, v133\n\t"
      "v_mfma_f32_16x16x32_bf16 v[128:131], a[72:75], v[44:47], v[128:131]\n\t"
      "v_cvt_pk_bf16_f32 v139, v134, v135\n\t"
      "v_mfma_f32_16x16x32_bf16 v[128:131], a[96:99], v[48:51], v[128:131]\n\t"
      "ds_write_b128 v219, v[136:139] offset:12288\n\t"
      "v_mfma_f32_16x16x32_bf16 v[128:131], a[120:123], v[52:55], v[128:131]\n\t"
      "v_mfma_f32_16x16x32_bf16 v[128:131], a[144:147], v[56:59], v[128:131]\n\t"
      "buffer_load_dwordx4 v208, s[20:23], s73 offen offset:2048 lds\n\t"
      "v_mfma_f32_16x16x32_bf16 v[128:131], a[168:171], v[60:63], v[128:131]\n\t"
      "s_waitcnt lgkmcnt(1)\n\t"
      "v_mfma_f32_16x16x32_bf16 v[128:131], a[192:195], v[64:67], v[128:131]\n\t"
      "v_add_u32_e32 v217, s46, v204\n\t"
      "v_mfma_f32_16x16x32_bf16 v[128:131], a[216:219], v[68:71], v[128:131]\n\t"
      "v_add_u32_e32 v218, s46, v205\n\t"
      "v_mfma_f32_16x16x32_bf16 v[128:131], a[240:243], v[72:75], v[128:131]\n\t"
      "v_add_u32_e32 v219, s46, v206\n\t"
      "v_mfma_f32_16x16x32_bf16 v[128:131], v[172:175], v[76:79], v[128:131]\n\t"
      "s_cmp_gt_i32 s83, 1\n\t"
      "s_cbranch_scc1 .LslpP4_%=\n\t"
      ".LretP4_%=:\n\t"
      "s_waitcnt vmcnt(15) lgkmcnt(0)\n\t"
      "s_barrier\n\t"
      "v_mfma_f32_16x16x32_bf16 v[132:135], a[4:7], v[32:35], v[12:15]\n\t"
      "ds_read_b128 v[140:143], v220 offset:12288\n\t"
      "v_mfma_f32_16x16x32_bf16 v[132:135], a[28:31], v[36:39], v[132:135]\n\t"
      "v_mfma_f32_16x16x32_bf16 v[132:135], a[52:55], v[40:43], v[132:135]\n\t"
      "v_cvt_pk_bf16_f32 v136, v128, v129\n\t"
      "v_mfma_f32_16x16x32_bf16 v[132:135], a[76:79], v[44:47], v[132:135]\n\t"
      "ds_read_b128 v[80:83], v196 offset:12288\n\t"
      "v_cvt_pk_bf16_f32 v137, v130, v131\n\t"
      "v_mfma_f32_16x16x32_bf16 v[132:135], a[100:103], v[48:51], v[132:135]\n\t"
      "v_readlane_b32 s68, v248, s94\n\t"
      "s_add_u32 s48, s47, 0x6000\n\t"
      "v_mfma_f32_16x16x32_bf16 v[132:135], a[124:127], v[52:55], v[132:135]\n\t"
      "ds_read_b128 v[152:155], v238 offset:12288\n\t"
      "s_cmp_eq_u32 s48, s50\n\t"
      "s_cselect_b32 s48, s41, s48\n\t"
      "v_mfma_f32_16x16x32_bf16 v[132:135], a[148:151], v[56:59], v[132:135]\n\t"
      "ds_read_b128 v[84:87], v197 offset:12288\n\t"
      "v_mfma_f32_16x16x32_bf16 v[132:135], a[172:175], v[60:63], v[132:135]\n\t"
      "v_readlane_b32 s73, v249, s85\n\t"
      "v_mfma_f32_16x16x32_bf16 v[132:135], a[196:199], v[64:67], v[132:135]\n\t"
      "v_mfma_f32_16x16x32_bf16 v[132:135], a[220:223], v[68:71], v[132:135]\n\t"
      "ds_read_b128 v[88:91], v198 offset:12288\n\t"
      "v_mfma_f32_16x16x32_bf16 v[132:135], a[244:247], v[72:75], v[132:135]\n\t"
      "v_mfma_f32_16x16x32_bf16 v[132:135], v[176:179], v[76:79], v[132:135]\n\t"
      "s_waitcnt lgkmcnt(4)\n\t"
      "buffer_store_dwordx4 v[140:143], v209, s[28:31], s72 offen sc1\n\t"
      "v_mfma_f32_16x16x32_bf16 v[128:131], a[8:11], v[32:35], v[16:19]\n\t"
      "ds_read_b128 v[92:95], v199 offset:12288\n\t"
      "v_mfma_f32_16x16x32_bf16 v[128:131], a[32:35], v[36:39], v[128:131]\n\t"
      "s_memrealtime s[80:81]\n\t"
      "v_mfma_f32_16x16x32_bf16 v[128:131], a[56:59], v[40:43], v[128:131]\n\t"
      "v_cvt_pk_bf16_f32 v138, v132, v133\n\t"
      "v_mfma_f32_16x16x32_bf16 v[128:131], a[80:83], v[44:47], v[128:131]\n\t"
      "v_cvt_pk_bf16_f32 v139, v134, v135\n\t"
      "v_mfma_f32_16x16x32_bf16 v[128:131], a[104:107], v[48:51], v[128:131]\n\t"
      "ds_write_b128 v217, v[136:139] offset:0\n\t"
      "v_mfma_f32_16x16x32_bf16 v[128:131], a[128:131], v[52:55], v[128:131]\n\t"
      "s_add_u32 m0, s45, 0x3000\n\t"
      "v_mfma_f32_16x16x32_bf16 v[128:131], a[152:155], v[56:59], v[128:131]\n\t"
      "s_waitcnt lgkmcnt(4)\n\t"
      "buffer_store_dwordx4 v[152:155], v209, s[28:31], s68 offen sc1\n\t"
      "v_mfma_f32_16x16x32_bf16 v[128:131], a[176:179], v[60:63], v[128:131]\n\t"
      "ds_read_b128 v[96:99], v197 offset:12544\n\t"
      "v_mfma_f32_16x16x32_bf16 v[128:131], a[200:203], v[64:67], v[128:131]\n\t"
      "v_mfma_f32_16x16x32_bf16 v[128:131], a[224:227], v[68:71], v[128:131]\n\t"
      "ds_read_b128 v[100:103], v198 offset:12544\n\t"
      "v_mfma_f32_16x16x32_bf16 v[128:131], a[248:251], v[72:75], v[128:131]\n\t"
      "ds_read_b128 v[156:159], v238 offset:13312\n\t"
      "v_mfma_f32_16x16x32_bf16 v[128:131], v[180:183], v[76:79], v[128:131]\n\t"
      "buffer_load_dwordx4 v209, s[20:23], s73 offen lds\n\t"
      "v_mfma_f32_16x16x32_bf16 v[132:135], a[12:15], v[32:35], v[20:23]\n\t"
      "ds_read_b128 v[104:107], v199 offset:12544\n\t"
      "v_mfma_f32_16x16x32_bf16 v[132:135], a[36:39], v[36:39], v[132:135]\n\t"
      "v_mfma_f32_16x16x32_bf16 v[132:135], a[60:63], v[40:43], v[132:135]\n\t"
      "v_cvt_pk_bf16_f32 v136, v128, v129\n\t"
      "v_mfma_f32_16x16x32_bf16 v[132:135], a[84:87], v[44:47], v[132:135]\n\t"
      "v_cvt_pk_bf16_f32 v137, v130, v131\n\t"
      "ds_read_b128 v[144:147], v220 offset:13312\n\t"
      "v_mfma_f32_16x16x32_bf16 v[132:135], a[108:111], v[48:51], v[132:135]\n\t"
      "v_mfma_f32_16x16x32_bf16 v[132:135], a[132:135], v[52:55], v[132:135]\n\t"
      "s_waitcnt lgkmcnt(2)\n\t"
      "buffer_store_dwordx4 v[156:159], v209, s[28:31], s68 offen offset:1024 sc1\n\t"
      "v_mfma_f32_16x16x32_bf16 v[132:135], a[156:159], v[56:59], v[132:135]\n\t"
      "ds_read_b128 v[108:111], v196 offset:12544\n\t"
      "v_mfma_f32_16x16x32_bf16 v[132:135], a[180:183], v[60:63], v[132:135]\n\t"
      "v_mfma_f32_16x16x32_bf16 v[132:135], a[204:207], v[64:67], v[132:135]\n\t"
      "v_mfma_f32_16x16x32_bf16 v[132:135], a[228:231], v[68:71], v[132:135]\n\t"
      "ds_read_b128 v[112:115], v198 offset:12800\n\t"
      "v_mfma_f32_16x16x32_bf16 v[132:135], a[252:255], v[72:75], v[132:135]\n\t"
      "v_mfma_f32_16x16x32_bf16 v[132:135], v[184:187], v[76:79], v[132:135]\n\t"
      "s_waitcnt lgkmcnt(2)\n\t"
      "buffer_store_dwordx4 v[144:147], v209, s[28:31], s72 offen offset:1024 sc1\n\t"
      "v_mfma_f32_16x16x32_bf16 v[128:131], a[16:19], v[32:35], v[24:27]\n\t"
      "ds_read_b128 v[116:119], v199 offset:12800\n\t"
      "v_mfma_f32_16x16x32_bf16 v[128:131], a[40:43], v[36:39], v[128:131]\n\t"
      "v_mfma_f32_16x16x32_bf16 v[128:131], a[64:67], v[40:43], v[128:131]\n\t"
      "v_cvt_pk_bf16_f32 v138, v132, v133\n\t"
      "v_mfma_f32_16x16x32_bf16 v[128:131], a[88:91], v[44:47], v[128:131]\n\t"
      "v_cvt_pk_bf16_f32 v139, v134, v135\n\t"
      "ds_read_b128 v[160:163], v238 offset:14336\n\t"
      "v_mfma_f32_16x16x32_bf16 v[128:131], a[112:115], v[48:51], v[128:131]\n\t"
      "ds_write_b128 v218, v[136:139] offset:0\n\t"
      "v_mfma_f32_16x16x32_bf16 v[128:131], a[136:139], v[52:55], v[128:131]\n\t"
      "v_mfma_f32_16x16x32_bf16 v[128:131], a[160:163], v[56:59], v[128:131]\n\t"
      "v_mfma_f32_16x16x32_bf16 v[128:131], a[184:187], v[60:63], v[128:131]\n\t"
      "ds_read_b128 v[148:151], v220 offset:14336\n\t"
      "v_mfma_f32_16x16x32_bf16 v[128:131], a[208:211], v[64:67], v[128:131]\n\t"
      "v_mfma_f32_16x16x32_bf16 v[128:131], a[232:235], v[68:71], v[128:131]\n\t"
      "ds_read_b128 v[120:123], v196 offset:12800\n\t"
      "v_mfma_f32_16x16x32_bf16 v[128:131], v[164:167], v[72:75], v[128:131]\n\t"
      "v_mfma_f32_16x16x32_bf16 v[128:131], v[188:191], v[76:79], v[128:131]\n\t"
      "buffer_load_dwordx4 v209, s[20:23], s73 offen offset:1024 lds\n\t"
      "v_mfma_f32_16x16x32_bf16 v[132:135], a[20:23], v[32:35], v[28:31]\n\t"
      "ds_read_b128 v[124:127], v197 offset:12800\n\t"
      "v_mfma_f32_16x16x32_bf16 v[132:135], a[44:47], v[36:39], v[132:135]\n\t"
      "v_mfma_f32_16x16x32_bf16 v[132:135], a[68:71], v[40:43], v[132:135]\n\t"
      "v_cvt_pk_bf16_f32 v136, v128, v129\n\t"
      "v_add_u32_e32 v220, s46, v207\n\t"
      "v_mfma_f32_16x16x32_bf16 v[132:135], a[92:95], v[44:47], v[132:135]\n\t"
      "v_cvt_pk_bf16_f32 v137, v130, v131\n\t"
      "s_sub_u32 s84, s84, 4\n\t"
      "s_max_i32 s84, s84, s86\n\t"
      "v_mfma_f32_16x16x32_bf16 v[132:135], a[116:119], v[48:51], v[132:135]\n\t"
      "s_sub_u32 s83, s82, s80\n\t"
      "v_mfma_f32_16x16x32_bf16 v[132:135], a[140:143], v[52:55], v[132:135]\n\t"
      "s_waitcnt lgkmcnt(4)\n\t"
      "buffer_store_dwordx4 v[160:163], v209, s[28:31], s68 offen offset:2048 sc1\n\t"
      "s_max_i32 s83, s83, 0\n\t"
      "s_add_u32 s82, s80, s83\n\t"
      "v_mfma_f32_16x16x32_bf16 v[132:135], a[164:167], v[56:59], v[132:135]\n\t"
      "v_mfma_f32_16x16x32_bf16 v[132:135], a[188:191], v[60:63], v[132:135]\n\t"
      "v_mfma_f32_16x16x32_bf16 v[132:135], a[212:215], v[64:67], v[132:135]\n\t"
      "s_add_u32 s44, s44, 1\n\t"
      "v_mfma_f32_16x16x32_bf16 v[132:135], a[236:239], v[68:71], v[132:135]\n\t"
      "s_add_u32 s82, s82, s84\n\t"
      "v_mfma_f32_16x16x32_bf16 v[132:135], v[168:171], v[72:75], v[132:135]\n\t"
      "s_add_u32 s85, s85, 1\n\t"
      "v_mfma_f32_16x16x32_bf16 v[132:135], v[192:195], v[76:79], v[132:135]\n\t"
      "s_waitcnt lgkmcnt(2)\n\t"
      "buffer_store_dwordx4 v[148:151], v209, s[28:31], s72 offen offset:2048 sc1\n\t"
      "v_mfma_f32_16x16x32_bf16 v[128:131], a[0:3], v[80:83], v[8:11]\n\t"
      "v_mfma_f32_16x16x32_bf16 v[128:131], a[24:27], v[84:87], v[128:131]\n\t"
      "v_readlane_b32 s72, v248, s44\n\t"
      "v_mfma_f32_16x16x32_bf16 v[128:131], a[48:51], v[88:91], v[128:131]\n\t"
      "v_cvt_pk_bf16_f32 v138, v132, v133\n\t"
      "v_mfma_f32_16x16x32_bf16 v[128:131], a[72:75], v[92:95], v[128:131]\n\t"
      "v_cvt_pk_bf16_f32 v139, v134, v135\n\t"
      "v_mfma_f32_16x16x32_bf16 v[128:131], a[96:99], v[96:99], v[128:131]\n\t"
      "ds_write_b128 v219, v[136:139] offset:0\n\t"
      "v_mfma_f32_16x16x32_bf16 v[128:131], a[120:123], v[100:103], v[128:131]\n\t"
      "v_mfma_f32_16x16x32_bf16 v[128:131], a[144:147], v[104:107], v[128:131]\n\t"
      "buffer_load_dwordx4 v209, s[20:23], s73 offen offset:2048 lds\n\t"
      "v_mfma_f32_16x16x32_bf16 v[128:131], a[168:171], v[108:111], v[128:131]\n\t"
      "v_add_u32_e32 v196, s47, v200\n\t"
      "s_waitcnt lgkmcnt(1)\n\t"
      "v_mfma_f32_16x16x32_bf16 v[128:131], a[192:195], v[112:115], v[128:131]\n\t"
      "v_add_u32_e32 v197, s47, v201\n\t"
      "v_mfma_f32_16x16x32_bf16 v[128:131], a[216:219], v[116:119], v[128:131]\n\t"
      "v_add_u32_e32 v198, s47, v202\n\t"
      "v_mfma_f32_16x16x32_bf16 v[128:131], a[240:243], v[120:123], v[128:131]\n\t"
      "v_add_u32_e32 v199, s47, v203\n\t"
      "v_mfma_f32_16x16x32_bf16 v[128:131], v[172:175], v[124:127], v[128:131]\n\t"
      "s_cmp_gt_i32 s83, 1\n\t"
      "s_cbranch_scc1 .LslpP5_%=\n\t"
      ".LretP5_%=:\n\t"
      "s_waitcnt vmcnt(21) lgkmcnt(0)\n\t"
      "s_barrier\n\t"
      "v_mfma_f32_16x16x32_bf16 v[132:135], a[4:7], v[80:83], v[12:15]\n\t"
      "ds_read_b128 v[140:143], v220 offset:0\n\t"
      "v_mfma_f32_16x16x32_bf16 v[132:135], a[28:31], v[84:87], v[132:135]\n\t"
      "v_mfma_f32_16x16x32_bf16 v[132:135], a[52:55], v[88:91], v[132:135]\n\t"
      "v_cvt_pk_bf16_f32 v136, v128, v129\n\t"
      "v_mfma_f32_16x16x32_bf16 v[132:135], a[76:79], v[92:95], v[132:135]\n\t"
      "ds_read_b128 v[32:35], v196 offset:0\n\t"
      "v_cvt_pk_bf16_f32 v137, v130, v131\n\t"
      "v_mfma_f32_16x16x32_bf16 v[132:135], a[100:103], v[96:99], v[132:135]\n\t"
      "v_mfma_f32_16x16x32_bf16 v[132:135], a[124:127], v[100:103], v[132:135]\n\t"
      "v_mfma_f32_16x16x32_bf16 v[132:135], a[148:151], v[104:107], v[132:135]\n\t"
      "ds_read_b128 v[36:39], v197 offset:0\n\t"
      "v_mfma_f32_16x16x32_bf16 v[132:135], a[172:175], v[108:111], v[132:135]\n\t"
      "v_readlane_b32 s73, v248, s85\n\t"
      "v_mfma_f32_16x16x32_bf16 v[132:135], a[196:199], v[112:115], v[132:135]\n\t"
      "v_mfma_f32_16x16x32_bf16 v[132:135], a[220:223], v[116:119], v[132:135]\n\t"
      "ds_read_b128 v[40:43], v198 offset:0\n\t"
      "v_mfma_f32_16x16x32_bf16 v[132:135], a[244:247], v[120:123], v[132:135]\n\t"
      "v_mfma_f32_16x16x32_bf16 v[132:135], v[176:179], v[124:127], v[132:135]\n\t"
      "s_waitcnt lgkmcnt(3)\n\t"
      "buffer_store_dwordx4 v[140:143], v208, s[28:31], s72 offen sc1\n\t"
      "v_mfma_f32_16x16x32_bf16 v[128:131], a[8:11], v[80:83], v[16:19]\n\t"
      "ds_read_b128 v[44:47], v199 offset:0\n\t"
      "v_mfma_f32_16x16x32_bf16 v[128:131], a[32:35], v[84:87], v[128:131]\n\t"
      "s_memrealtime s[80:81]\n\t"
      "v_mfma_f32_16x16x32_bf16 v[128:131], a[56:59], v[88:91], v[128:131]\n\t"
      "v_cvt_pk_bf16_f32 v138, v132, v133\n\t"
      "v_mfma_f32_16x16x32_bf16 v[128:131], a[80:83], v[92:95], v[128:131]\n\t"
      "v_cvt_pk_bf16_f32 v139, v134, v135\n\t"
      "v_mfma_f32_16x16x32_bf16 v[128:131], a[104:107], v[96:99], v[128:131]\n\t"
      "ds_write_b128 v217, v[136:139] offset:12288\n\t"
      "v_mfma_f32_16x16x32_bf16 v[128:131], a[128:131], v[100:103], v[128:131]\n\t"
      "s_mov_b32 m0, s46\n\t"
      "v_mfma_f32_16x16x32_bf16 v[128:131], a[152:155], v[104:107], v[128:131]\n\t"
      "v_mfma_f32_16x16x32_bf16 v[128:131], a[176:179], v[108:111], v[128:131]\n\t"
      "ds_read_b128 v[48:51], v197 offset:256\n\t"
      "v_mfma_f32_16x16x32_bf16 v[128:131], a[200:203], v[112:115], v[128:131]\n\t"
      "v_mfma_f32_16x16x32_bf16 v[128:131], a[224:227], v[116:119], v[128:131]\n\t"
      "ds_read_b128 v[52:55], v198 offset:256\n\t"
      "v_mfma_f32_16x16x32_bf16 v[128:131], a[248:251], v[120:123], v[128:131]\n\t"
      "v_mfma_f32_16x16x32_bf16 v[128:131], v[180:183], v[124:127], v[128:131]\n\t"
      "buffer_load_dwordx4 v208, s[20:23], s73 offen lds\n\t"
      "v_mfma_f32_16x16x32_bf16 v[132:135], a[12:15], v[80:83], v[20:23]\n\t"
      "ds_read_b128 v[56:59], v199 offset:256\n\t"
      "v_mfma_f32_16x16x32_bf16 v[132:135], a[36:39], v[84:87], v[132:135]\n\t"
      "v_mfma_f32_16x16x32_bf16 v[132:135], a[60:63], v[88:91], v[132:135]\n\t"
      "v_cvt_pk_bf16_f32 v136, v128, v129\n\t"
      "v_mfma_f32_16x16x32_bf16 v[132:135], a[84:87], v[92:95], v[132:135]\n\t"
      "v_cvt_pk_bf16_f32 v137, v130, v131\n\t"
      "ds_read_b128 v[144:147], v220 offset:1024\n\t"
      "v_mfma_f32_16x16x32_bf16 v[132:135], a[108:111], v[96:99], v[132:135]\n\t"
      "v_mfma_f32_16x16x32_bf16 v[132:135], a[132:135], v[100:103], v[132:135]\n\t"
      "v_mfma_f32_16x16x32_bf16 v[132:135], a[156:159], v[104:107], v[132:135]\n\t"
      "ds_read_b128 v[60:63], v196 offset:256\n\t"
      "v_mfma_f32_16x16x32_bf16 v[132:135], a[180:183], v[108:111], v[132:135]\n\t"
      "v_mfma_f32_16x16x32_bf16 v[132:135], a[204:207], v[112:115], v[132:135]\n\t"
      "v_mfma_f32_16x16x32_bf16 v[132:135], a[228:231], v[116:119], v[132:135]\n\t"
      "ds_read_b128 v[64:67], v198 offset:512\n\t"
      "v_mfma_f32_16x16x32_bf16 v[132:135], a[252:255], v[120:123], v[132:135]\n\t"
      "v_mfma_f32_16x16x32_bf16 v[132:135], v[184:187], v[124:127], v[132:135]\n\t"
      "s_waitcnt lgkmcnt(2)\n\t"
      "buffer_store_dwordx4 v[144:147], v208, s[28:31], s72 offen offset:1024 sc1\n\t"
      "v_mfma_f32_16x16x32_bf16 v[128:131], a[16:19], v[80:83], v[24:27]\n\t"
      "ds_read_b128 v[68:71], v199 offset:512\n\t"
      "v_mfma_f32_16x16x32_bf16 v[128:131], a[40:43], v[84:87], v[128:131]\n\t"
      "v_readlane_b32 s86, v250, s44\n\t"
      "v_mfma_f32_16x16x32_bf16 v[128:131], a[64:67], v[88:91], v[128:131]\n\t"
      "v_cvt_pk_bf16_f32 v138, v132, v133\n\t"
      "v_mfma_f32_16x16x32_bf16 v[128:131], a[88:91], v[92:95], v[128:131]\n\t"
      "v_cvt_pk_bf16_f32 v139, v134, v135\n\t"
      "v_mfma_f32_16x16x32_bf16 v[128:131], a[112:115], v[96:99], v[128:131]\n\t"
      "ds_write_b128 v218, v[136:139] offset:12288\n\t"
      "v_mfma_f32_16x16x32_bf16 v[128:131], a[136:139], v[100:103], v[128:131]\n\t"
      "v_mfma_f32_16x16x32_bf16 v[128:131], a[160:163], v[104:107], v[128:131]\n\t"
      "v_mfma_f32_16x16x32_bf16 v[128:131], a[184:187], v[108:111], v[128:131]\n\t"
      "ds_read_b128 v[148:151], v220 offset:2048\n\t"
      "v_mfma_f32_16x16x32_bf16 v[128:131], a[208:211], v[112:115], v[128:131]\n\t"
      "v_mfma_f32_16x16x32_bf16 v[128:131], a[232:235], v[116:119], v[128:131]\n\t"
      "ds_read_b128 v[72:75], v196 offset:512\n\t"
      "v_mfma_f32_16x16x32_bf16 v[128:131], v[164:167], v[120:123], v[128:131]\n\t"
      "v_mfma_f32_16x16x32_bf16 v[128:131], v[188:191], v[124:127], v[128:131]\n\t"
      "buffer_load_dwordx4 v208, s[20:23], s73 offen offset:1024 lds\n\t"
      "v_mfma_f32_16x16x32_bf16 v[132:135], a[20:23], v[80:83], v[28:31]\n\t"
      "ds_read_b128 v[76:79], v197 offset:512\n\t"
      "v_mfma_f32_16x16x32_bf16 v[132:135], a[44:47], v[84:87], v[132:135]\n\t"
      "v_mfma_f32_16x16x32_bf16 v[132:135], a[68:71], v[88:91], v[132:135]\n\t"
      "v_cvt_pk_bf16_f32 v136, v128, v129\n\t"
      "v_mfma_f32_16x16x32_bf16 v[132:135], a[92:95], v[92:95], v[132:135]\n\t"
      "v_cvt_pk_bf16_f32 v137, v130, v131\n\t"
      "s_sub_u32 s84, s84, 4\n\t"
      "s_max_i32 s84, s84, s86\n\t"
      "v_mfma_f32_16x16x32_bf16 v[132:135], a[116:119], v[96:99], v[132:135]\n\t"
      "s_sub_u32 s83, s82, s80\n\t"
      "v_mfma_f32_16x16x32_bf16 v[132:135], a[140:143], v[100:103], v[132:135]\n\t"
      "s_max_i32 s83, s83, 0\n\t"
      "s_add_u32 s82, s80, s83\n\t"
      "v_mfma_f32_16x16x32_bf16 v[132:135], a[164:167], v[104:107], v[132:135]\n\t"
      "s_mov_b32 s45, s46\n\t"
      "v_mfma_f32_16x16x32_bf16 v[132:135], a[188:191], v[108:111], v[132:135]\n\t"
      "s_mov_b32 s46, s47\n\t"
      "v_mfma_f32_16x16x32_bf16 v[132:135], a[212:215], v[112:115], v[132:135]\n\t"
      "s_mov_b32 s47, s48\n\t"
      "v_mfma_f32_16x16x32_bf16 v[132:135], a[236:239], v[116:119], v[132:135]\n\t"
      "s_add_u32 s82, s82, s84\n\t"
      "v_mfma_f32_16x16x32_bf16 v[132:135], v[168:171], v[120:123], v[132:135]\n\t"
      "s_cmp_lg_u32 s44, s49\n\t"
      "v_mfma_f32_16x16x32_bf16 v[132:135], v[192:195], v[124:127], v[132:135]\n\t"
      "s_waitcnt lgkmcnt(2)\n\t"
      "buffer_store_dwordx4 v[148:151], v208, s[28:31], s72 offen offset:2048 sc1\n\t"
      ".p2align 6\n\t"
      ".Lloop_%=:\n\t"
      "s_waitcnt lgkmcnt(12)\n\t"
      "v_mfma_f32_16x16x32_bf16 v[128:131], a[0:3], v[32:35], v[8:11]\n\t"
      "v_mfma_f32_16x16x32_bf16 v[128:131], a[24:27], v[36:39], v[128:131]\n\t"
      "v_readlane_b32 s72, v249, s44\n\t"
      "v_mfma_f32_16x16x32_bf16 v[128:131], a[48:51], v[40:43], v[128:131]\n\t"
      "v_cvt_pk_bf16_f32 v138, v132, v133\n\t"
      "v_mfma_f32_16x16x32_bf16 v[128:131], a[72:75], v[44:47], v[128:131]\n\t"
      "v_cvt_pk_bf16_f32 v139, v134, v135\n\t"
      "s_waitcnt lgkmcnt(6)\n\t"
      "v_mfma_f32_16x16x32_bf16 v[128:131], a[96:99], v[48:51], v[128:131]\n\t"
      "ds_write_b128 v219, v[136:139] offset:12288\n\t"
      "v_mfma_f32_16x16x32_bf16 v[128:131], a[120:123], v[52:55], v[128:131]\n\t"
      "v_mfma_f32_16x16x32_bf16 v[128:131], a[144:147], v[56:59], v[128:131]\n\t"
      "buffer_load_dwordx4 v208, s[20:23], s73 offen offset:2048 lds\n\t"
      "v_mfma_f32_16x16x32_bf16 v[128:131], a[168:171], v[60:63], v[128:131]\n\t"
      "s_waitcnt lgkmcnt(1)\n\t"
      "v_mfma_f32_16x16x32_bf16 v[128:131], a[192:195], v[64:67], v[128:131]\n\t"
      "v_add_u32_e32 v217, s46, v204\n\t"
      "v_mfma_f32_16x16x32_bf16 v[128:131], a[216:219], v[68:71], v[128:131]\n\t"
      "v_add_u32_e32 v218, s46, v205\n\t"
      "v_mfma_f32_16x16x32_bf16 v[128:131], a[240:243], v[72:75], v[128:131]\n\t"
      "v_add_u32_e32 v219, s46, v206\n\t"
      "v_mfma_f32_16x16x32_bf16 v[128:131], v[172:175], v[76:79], v[128:131]\n\t"
      "s_cmp_gt_i32 s83, 1\n\t"
      "s_cbranch_scc1 .LslpA_%=\n\t"
      ".LretA_%=:\n\t"
      "s_waitcnt vmcnt(18) lgkmcnt(0)\n\t"
      "s_barrier\n\t"
      "v_mfma_f32_16x16x32_bf16 v[132:135], a[4:7], v[32:35], v[12:15]\n\t"
      "ds_read_b128 v[140:143], v220 offset:12288\n\t"
      "v_mfma_f32_16x16x32_bf16 v[132:135], a[28:31], v[36:39], v[132:135]\n\t"
      "v_mfma_f32_16x16x32_bf16 v[132:135], a[52:55], v[40:43], v[132:135]\n\t"
      "v_cvt_pk_bf16_f32 v136, v128, v129\n\t"
      "v_mfma_f32_16x16x32_bf16 v[132:135], a[76:79], v[44:47], v[132:135]\n\t"
      "ds_read_b128 v[80:83], v196 offset:12288\n\t"
      "v_cvt_pk_bf16_f32 v137, v130, v131\n\t"
      "v_mfma_f32_16x16x32_bf16 v[132:135], a[100:103], v[48:51], v[132:135]\n\t"
      "s_add_u32 s48, s47, 0x6000\n\t"
      "v_mfma_f32_16x16x32_bf16 v[132:135], a[124:127], v[52:55], v[132:135]\n\t"
      "s_cmp_eq_u32 s48, s50\n\t"
      "s_cselect_b32 s48, s41, s48\n\t"
      "v_mfma_f32_16x16x32_bf16 v[132:135], a[148:151], v[56:59], v[132:135]\n\t"
      "ds_read_b128 v[84:87], v197 offset:12288\n\t"
      "v_mfma_f32_16x16x32_bf16 v[132:135], a[172:175], v[60:63], v[132:135]\n\t"
      "v_readlane_b32 s73, v249, s85\n\t"
      "v_mfma_f32_16x16x32_bf16 v[132:135], a[196:199], v[64:67], v[132:135]\n\t"
      "v_mfma_f32_16x16x32_bf16 v[132:135], a[220:223], v[68:71], v[132:135]\n\t"
      "ds_read_b128 v[88:91], v198 offset:12288\n\t"
      "v_mfma_f32_16x16x32_bf16 v[132:135], a[244:247], v[72:75], v[132:135]\n\t"
      "v_mfma_f32_16x16x32_bf16 v[132:135], v[176:179], v[76:79], v[132:135]\n\t"
      "s_waitcnt lgkmcnt(3)\n\t"
      "buffer_store_dwordx4 v[140:143], v209, s[28:31], s72 offen sc1\n\t"
      "v_mfma_f32_16x16x32_bf16 v[128:131], a[8:11], v[32:35], v[16:19]\n\t"
      "ds_read_b128 v[92:95], v199 offset:12288\n\t"
      "v_mfma_f32_16x16x32_bf16 v[128:131], a[32:35], v[36:39], v[128:131]\n\t"
      "s_memrealtime s[80:81]\n\t"
      "v_mfma_f32_16x16x32_bf16 v[128:131], a[56:59], v[40:43], v[128:131]\n\t"
      "v_cvt_pk_bf16_f32 v138, v132, v133\n\t"
      "v_mfma_f32_16x16x32_bf16 v[128:131], a[80:83], v[44:47], v[128:131]\n\t"
      "v_cvt_pk_bf16_f32 v139, v134, v135\n\t"
      "v_mfma_f32_16x16x32_bf16 v[128:131], a[104:107], v[48:51], v[128:131]\n\t"
      "ds_write_b128 v217, v[136:139] offset:0\n\t"
      "v_mfma_f32_16x16x32_bf16 v[128:131], a[128:131], v[52:55], v[128:131]\n\t"
      "s_add_u32 m0, s45, 0x3000\n\t"
      "v_mfma_f32_16x16x32_bf16 v[128:131], a[152:155], v[56:59], v[128:131]\n\t"
      "v_mfma_f32_16x16x32_bf16 v[128:131], a[176:179], v[60:63], v[128:131]\n\t"
      "ds_read_b128 v[96:99], v197 offset:12544\n\t"
      "v_mfma_f32_16x16x32_bf16 v[128:131], a[200:203], v[64:67], v[128:131]\n\t"
      "v_mfma_f32_16x16x32_bf16 v[128:131], a[224:227], v[68:71], v[128:131]\n\t"
      "ds_read_b128 v[100:103], v198 offset:12544\n\t"
      "v_mfma_f32_16x16x32_bf16 v[128:131], a[248:251], v[72:75], v[128:131]\n\t"
      "v_mfma_f32_16x16x32_bf16 v[128:131], v[180:183], v[76:79], v[128:131]\n\t"
      "buffer_load_dwordx4 v209, s[20:23], s73 offen lds\n\t"
      "v_mfma_f32_16x16x32_bf16 v[132:135], a[12:15], v[32:35], v[20:23]\n\t"
      "ds_read_b128 v[104:107], v199 offset:12544\n\t"
      "v_mfma_f32_16x16x32_bf16 v[132:135], a[36:39], v[36:39], v[132:135]\n\t"
      "v_mfma_f32_16x16x32_bf16 v[132:135], a[60:63], v[40:43], v[132:135]\n\t"
      "v_cvt_pk_bf16_f32 v136, v128, v129\n\t"
      "v_mfma_f32_16x16x32_bf16 v[132:135], a[84:87], v[44:47], v[132:135]\n\t"
      "v_cvt_pk_bf16_f32 v137, v130, v131\n\t"
      "ds_read_b128 v[144:147], v220 offset:13312\n\t"
      "v_mfma_f32_16x16x32_bf16 v[132:135], a[108:111], v[48:51], v[132:135]\n\t"
      "v_mfma_f32_16x16x32_bf16 v[132:135], a[132:135], v[52:55], v[132:135]\n\t"
      "v_mfma_f32_16x16x32_bf16 v[132:135], a[156:159], v[56:59], v[132:135]\n\t"
      "ds_read_b128 v[108:111], v196 offset:12544\n\t"
      "v_mfma_f32_16x16x32_bf16 v[132:135], a[180:183], v[60:63], v[132:135]\n\t"
      "v_mfma_f32_16x16x32_bf16 v[132:135], a[204:207], v[64:67], v[132:135]\n\t"
      "v_mfma_f32_16x16x32_bf16 v[132:135], a[228:231], v[68:71], v[132:135]\n\t"
      "ds_read_b128 v[112:115], v198 offset:12800\n\t"
      "v_mfma_f32_16x16x32_bf16 v[132:135], a[252:255], v[72:75], v[132:135]\n\t"
      "v_mfma_f32_16x16x32_bf16 v[132:135], v[184:187], v[76:79], v[132:135]\n\t"
      "s_waitcnt lgkmcnt(2)\n\t"
      "buffer_store_dwordx4 v[144:147], v209, s[28:31], s72 offen offset:1024 sc1\n\t"
      "v_mfma_f32_16x16x32_bf16 v[128:131], a[16:19], v[32:35], v[24:27]\n\t"
      "ds_read_b128 v[116:119], v199 offset:12800\n\t"
      "v_mfma_f32_16x16x32_bf16 v[128:131], a[40:43], v[36:39], v[128:131]\n\t"
      "v_mfma_f32_16x16x32_bf16 v[128:131], a[64:67], v[40:43], v[128:131]\n\t"
      "v_cvt_pk_bf16_f32 v138, v132, v133\n\t"
      "v_mfma_f32_16x16x32_bf16 v[128:131], a[88:91], v[44:47], v[128:131]\n\t"
      "v_cvt_pk_bf16_f32 v139, v134, v135\n\t"
      "v_mfma_f32_16x16x32_bf16 v[128:131], a[112:115], v[48:51], v[128:131]\n\t"
      "ds_write_b128 v218, v[136:139] offset:0\n\t"
      "v_mfma_f32_16x16x32_bf16 v[128:131], a[136:139], v[52:55], v[128:131]\n\t"
      "v_mfma_f32_16x16x32_bf16 v[128:131], a[160:163], v[56:59], v[128:131]\n\t"
      "v_mfma_f32_16x16x32_bf16 v[128:131], a[184:187], v[60:63], v[128:131]\n\t"
      "ds_read_b128 v[148:151], v220 offset:14336\n\t"
      "v_mfma_f32_16x16x32_bf16 v[128:131], a[208:211], v[64:67], v[128:131]\n\t"
      "v_mfma_f32_16x16x32_bf16 v[128:131], a[232:235], v[68:71], v[128:131]\n\t"
      "ds_read_b128 v[120:123], v196 offset:12800\n\t"
      "v_mfma_f32_16x16x32_bf16 v[128:131], v[164:167], v[72:75], v[128:131]\n\t"
      "v_mfma_f32_16x16x32_bf16 v[128:131], v[188:191], v[76:79], v[128:131]\n\t"
      "buffer_load_dwordx4 v209, s[20:23], s73 offen offset:1024 lds\n\t"
      "v_mfma_f32_16x16x32_bf16 v[132:135], a[20:23], v[32:35], v[28:31]\n\t"
      "ds_read_b128 v[124:127], v197 offset:12800\n\t"
      "v_mfma_f32_16x16x32_bf16 v[132:135], a[44:47], v[36:39], v[132:135]\n\t"
      "v_mfma_f32_16x16x32_bf16 v[132:135], a[68:71], v[40:43], v[132:135]\n\t"
      "v_cvt_pk_bf16_f32 v136, v128, v129\n\t"
      "v_add_u32_e32 v220, s46, v207\n\t"
      "v_mfma_f32_16x16x32_bf16 v[132:135], a[92:95], v[44:47], v[132:135]\n\t"
      "v_cvt_pk_bf16_f32 v137, v130, v131\n\t"
      "s_sub_u32 s84, s84, 4\n\t"
      "s_max_i32 s84, s84, s86\n\t"
      "v_mfma_f32_16x16x32_bf16 v[132:135], a[116:119], v[48:51], v[132:135]\n\t"
      "s_sub_u32 s83, s82, s80\n\t"
      "v_mfma_f32_16x16x32_bf16 v[132:135], a[140:143], v[52:55], v[132:135]\n\t"
      "s_max_i32 s83, s83, 0\n\t"
      "s_add_u32 s82, s80, s83\n\t"
      "v_mfma_f32_16x16x32_bf16 v[132:135], a[164:167], v[56:59], v[132:135]\n\t"
      "v_mfma_f32_16x16x32_bf16 v[132:135], a[188:191], v[60:63], v[132:135]\n\t"
      "v_mfma_f32_16x16x32_bf16 v[132:135], a[212:215], v[64:67], v[132:135]\n\t"
      "s_add_u32 s44, s44, 1\n\t"
      "v_mfma_f32_16x16x32_bf16 v[132:135], a[236:239], v[68:71], v[132:135]\n\t"
      "s_add_u32 s82, s82, s84\n\t"
      "v_mfma_f32_16x16x32_bf16 v[132:135], v[168:171], v[72:75], v[132:135]\n\t"
      "s_add_u32 s85, s85, 1\n\t"
      "v_mfma_f32_16x16x32_bf16 v[132:135], v[192:195], v[76:79], v[132:135]\n\t"
      "s_waitcnt lgkmcnt(2)\n\t"
      "buffer_store_dwordx4 v[148:151], v209, s[28:31], s72 offen offset:2048 sc1\n\t"
      "s_waitcnt lgkmcnt(12)\n\t"
      "v_mfma_f32_16x16x32_bf16 v[128:131], a[0:3], v[80:83], v[8:11]\n\t"
      "v_mfma_f32_16x16x32_bf16 v[128:131], a[24:27], v[84:87], v[128:131]\n\t"
      "v_readlane_b32 s72, v248, s44\n\t"
      "v_mfma_f32_16x16x32_bf16 v[128:131], a[48:51], v[88:91], v[128:131]\n\t"
      "v_cvt_pk_bf16_f32 v138, v132, v133\n\t"
      "v_mfma_f32_16x16x32_bf16 v[128:131], a[72:75], v[92:95], v[128:131]\n\t"
      "v_cvt_pk_bf16_f32 v139, v134, v135\n\t"
      "s_waitcnt lgkmcnt(6)\n\t"
      "v_mfma_f32_16x16x32_bf16 v[128:131], a[96:99], v[96:99], v[128:131]\n\t"
      "ds_write_b128 v219, v[136:139] offset:0\n\t"
      "v_mfma_f32_16x16x32_bf16 v[128:131], a[120:123], v[100:103], v[128:131]\n\t"
      "v_mfma_f32_16x16x32_bf16 v[128:131], a[144:147], v[104:107], v[128:131]\n\t"
      "buffer_load_dwordx4 v209, s[20:23], s73 offen offset:2048 lds\n\t"
      "v_mfma_f32_16x16x32_bf16 v[128:131], a[168:171], v[108:111], v[128:131]\n\t"
      "v_add_u32_e32 v196, s47, v200\n\t"
      "s_waitcnt lgkmcnt(1)\n\t"
      "v_mfma_f32_16x16x32_bf16 v[128:131], a[192:195], v[112:115], v[128:131]\n\t"
      "v_add_u32_e32 v197, s47, v201\n\t"
      "v_mfma_f32_16x16x32_bf16 v[128:131], a[216:219], v[116:119], v[128:131]\n\t"
      "v_add_u32_e32 v198, s47, v202\n\t"
      "v_mfma_f32_16x16x32_bf16 v[128:131], a[240:243], v[120:123], v[128:131]\n\t"
      "v_add_u32_e32 v199, s47, v203\n\t"
      "v_mfma_f32_16x16x32_bf16 v[128:131], v[172:175], v[124:127], v[128:131]\n\t"
      "s_cmp_gt_i32 s83, 1\n\t"
      "s_cbranch_scc1 .LslpB_%=\n\t"
      ".LretB_%=:\n\t"
      "s_waitcnt vmcnt(18) lgkmcnt(0)\n\t"
      "s_barrier\n\t"
      "v_mfma_f32_16x16x32_bf16 v[132:135], a[4:7], v[80:83], v[12:15]\n\t"
      "ds_read_b128 v[140:143], v220 offset:0\n\t"
      "v_mfma_f32_16x16x32_bf16 v[132:135], a[28:31], v[84:87], v[132:135]\n\t"
      "v_mfma_f32_16x16x32_bf16 v[132:135], a[52:55], v[88:91], v[132:135]\n\t"
      "v_cvt_pk_bf16_f32 v136, v128, v129\n\t"
      "v_mfma_f32_16x16x32_bf16 v[132:135], a[76:79], v[92:95], v[132:135]\n\t"
      "ds_read_b128 v[32:35], v196 offset:0\n\t"
      "v_cvt_pk_bf16_f32 v137, v130, v131\n\t"
      "v_mfma_f32_16x16x32_bf16 v[132:135], a[100:103], v[96:99], v[132:135]\n\t"
      "v_mfma_f32_16x16x32_bf16 v[132:135], a[124:127], v[100:103], v[132:135]\n\t"
      "v_mfma_f32_16x16x32_bf16 v[132:135], a[148:151], v[104:107], v[132:135]\n\t"
      "ds_read_b128 v[36:39], v197 offset:0\n\t"
      "v_mfma_f32_16x16x32_bf16 v[132:135], a[172:175], v[108:111], v[132:135]\n\t"
      "v_readlane_b32 s73, v248, s85\n\t"
      "v_mfma_f32_16x16x32_bf16 v[132:135], a[196:199], v[112:115], v[132:135]\n\t"
      "v_mfma_f32_16x16x32_bf16 v[132:135], a[220:223], v[116:119], v[132:135]\n\t"
      "ds_read_b128 v[40:43], v198 offset:0\n\t"
      "v_mfma_f32_16x16x32_bf16 v[132:135], a[244:247], v[120:123], v[132:135]\n\t"
      "v_mfma_f32_16x16x32_bf16 v[132:135], v[176:179], v[124:127], v[132:135]\n\t"
      "s_waitcnt lgkmcnt(3)\n\t"
      "buffer_store_dwordx4 v[140:143], v208, s[28:31], s72 offen sc1\n\t"
      "v_mfma_f32_16x16x32_bf16 v[128:131], a[8:11], v[80:83], v[16:19]\n\t"
      "ds_read_b128 v[44:47], v199 offset:0\n\t"
      "v_mfma_f32_16x16x32_bf16 v[128:131], a[32:35], v[84:87], v[128:131]\n\t"
      "s_memrealtime s[80:81]\n\t"
      "v_mfma_f32_16x16x32_bf16 v[128:131], a[56:59], v[88:91], v[128:131]\n\t"
      "v_cvt_pk_bf16_f32 v138, v132, v133\n\t"
      "v_mfma_f32_16x16x32_bf16 v[128:131], a[80:83], v[92:95], v[128:131]\n\t"
      "v_cvt_pk_bf16_f32 v139, v134, v135\n\t"
      "v_mfma_f32_16x16x32_bf16 v[128:131], a[104:107], v[96:99], v[128:131]\n\t"
      "ds_write_b128 v217, v[136:139] offset:12288\n\t"
      "v_mfma_f32_16x16x32_bf16 v[128:131], a[128:131], v[100:103], v[128:131]\n\t"
      "s_mov_b32 m0, s46\n\t"
      "v_mfma_f32_16x16x32_bf16 v[128:131], a[152:155], v[104:107], v[128:131]\n\t"
      "v_mfma_f32_16x16x32_bf16 v[128:131], a[176:179], v[108:111], v[128:131]\n\t"
      "ds_read_b128 v[48:51], v197 offset:256\n\t"
      "v_mfma_f32_16x16x32_bf16 v[128:131], a[200:203], v[112:115], v[128:131]\n\t"
      "v_mfma_f32_16x16x32_bf16 v[128:131], a[224:227], v[116:119], v[128:131]\n\t"
      "ds_read_b128 v[52:55], v198 offset:256\n\t"
      "v_mfma_f32_16x16x32_bf16 v[128:131], a[248:251], v[120:123], v[128:131]\n\t"
      "v_mfma_f32_16x16x32_bf16 v[128:131], v[180:183], v[124:127], v[128:131]\n\t"
      "buffer_load_dwordx4 v208, s[20:23], s73 offen lds\n\t"
      "v_mfma_f32_16x16x32_bf16 v[132:135], a[12:15], v[80:83], v[20:23]\n\t"
      "ds_read_b128 v[56:59], v199 offset:256\n\t"
      "v_mfma_f32_16x16x32_bf16 v[132:135], a[36:39], v[84:87], v[132:135]\n\t"
      "v_mfma_f32_16x16x32_bf16 v[132:135], a[60:63], v[88:91], v[132:135]\n\t"
      "v_cvt_pk_bf16_f32 v136, v128, v129\n\t"
      "v_mfma_f32_16x16x32_bf16 v[132:135], a[84:87], v[92:95], v[132:135]\n\t"
      "v_cvt_pk_bf16_f32 v137, v130, v131\n\t"
      "ds_read_b128 v[144:147], v220 offset:1024\n\t"
      "v_mfma_f32_16x16x32_bf16 v[132:135], a[108:111], v[96:99], v[132:135]\n\t"
      "v_mfma_f32_16x16x32_bf16 v[132:135], a[132:135], v[100:103], v[132:135]\n\t"
      "v_mfma_f32_16x16x32_bf16 v[132:135], a[156:159], v[104:107], v[132:135]\n\t"
      "ds_read_b128 v[60:63], v196 offset:256\n\t"
      "v_mfma_f32_16x16x32_bf16 v[132:135], a[180:183], v[108:111], v[132:135]\n\t"
      "v_mfma_f32_16x16x32_bf16 v[132:135], a[204:207], v[112:115], v[132:135]\n\t"
      "v_mfma_f32_16x16x32_bf16 v[132:135], a[228:231], v[116:119], v[132:135]\n\t"
      "ds_read_b128 v[64:67], v198 offset:512\n\t"
      "v_mfma_f32_16x16x32_bf16 v[132:135], a[252:255], v[120:123], v[132:135]\n\t"
      "v_mfma_f32_16x16x32_bf16 v[132:135], v[184:187], v[124:127], v[132:135]\n\t"
      "s_waitcnt lgkmcnt(2)\n\t"
      "buffer_store_dwordx4 v[144:147], v208, s[28:31], s72 offen offset:1024 sc1\n\t"
      "v_mfma_f32_16x16x32_bf16 v[128:131], a[16:19], v[80:83], v[24:27]\n\t"
      "ds_read_b128 v[68:71], v199 offset:512\n\t"
      "v_mfma_f32_16x16x32_bf16 v[128:131], a[40:43], v[84:87], v[128:131]\n\t"
      "v_readlane_b32 s86, v250, s44\n\t"
      "v_mfma_f32_16x16x32_bf16 v[128:131], a[64:67], v[88:91], v[128:131]\n\t"
      "v_cvt_pk_bf16_f32 v138, v132, v133\n\t"
      "v_mfma_f32_16x16x32_bf16 v[128:131], a[88:91], v[92:95], v[128:131]\n\t"
      "v_cvt_pk_bf16_f32 v139, v134, v135\n\t"
      "v_mfma_f32_16x16x32_bf16 v[128:131], a[112:115], v[96:99], v[128:131]\n\t"
      "ds_write_b128 v218, v[136:139] offset:12288\n\t"
      "v_mfma_f32_16x16x32_bf16 v[128:131], a[136:139], v[100:103], v[128:131]\n\t"
      "v_mfma_f32_16x16x32_bf16 v[128:131], a[160:163], v[104:107], v[128:131]\n\t"
      "v_mfma_f32_16x16x32_bf16 v[128:131], a[184:187], v[108:111], v[128:131]\n\t"
      "ds_read_b128 v[148:151], v220 offset:2048\n\t"
      "v_mfma_f32_16x16x32_bf16 v[128:131], a[208:211], v[112:115], v[128:131]\n\t"
      "v_mfma_f32_16x16x32_bf16 v[128:131], a[232:235], v[116:119], v[128:131]\n\t"
      "ds_read_b128 v[72:75], v196 offset:512\n\t"
      "v_mfma_f32_16x16x32_bf16 v[128:131], v[164:167], v[120:123], v[128:131]\n\t"
      "v_mfma_f32_16x16x32_bf16 v[128:131], v[188:191], v[124:127], v[128:131]\n\t"
      "buffer_load_dwordx4 v208, s[20:23], s73 offen offset:1024 lds\n\t"
      "v_mfma_f32_16x16x32_bf16 v[132:135], a[20:23], v[80:83], v[28:31]\n\t"
      "ds_read_b128 v[76:79], v197 offset:512\n\t"
      "v_mfma_f32_16x16x32_bf16 v[132:135], a[44:47], v[84:87], v[132:135]\n\t"
      "v_mfma_f32_16x16x32_bf16 v[132:135], a[68:71], v[88:91], v[132:135]\n\t"
      "v_cvt_pk_bf16_f32 v136, v128, v129\n\t"
      "v_mfma_f32_16x16x32_bf16 v[132:135], a[92:95], v[92:95], v[132:135]\n\t"
      "v_cvt_pk_bf16_f32 v137, v130, v131\n\t"
      "s_sub_u32 s84, s84, 4\n\t"
      "s_max_i32 s84, s84, s86\n\t"
      "v_mfma_f32_16x16x32_bf16 v[132:135], a[116:119], v[96:99], v[132:135]\n\t"
      "s_sub_u32 s83, s82, s80\n\t"
      "v_mfma_f32_16x16x32_bf16 v[132:135], a[140:143], v[100:103], v[132:135]\n\t"
      "s_max_i32 s83, s83, 0\n\t"
      "s_add_u32 s82, s80, s83\n\t"
      "v_mfma_f32_16x16x32_bf16 v[132:135], a[164:167], v[104:107], v[132:135]\n\t"
      "s_mov_b32 s45, s46\n\t"
      "v_mfma_f32_16x16x32_bf16 v[132:135], a[188:191], v[108:111], v[132:135]\n\t"
      "s_mov_b32 s46, s47\n\t"
      "v_mfma_f32_16x16x32_bf16 v[132:135], a[212:215], v[112:115], v[132:135]\n\t"
      "s_mov_b32 s47, s48\n\t"
      "v_mfma_f32_16x16x32_bf16 v[132:135], a[236:239], v[116:119], v[132:135]\n\t"
      "s_add_u32 s82, s82, s84\n\t"
      "v_mfma_f32_16x16x32_bf16 v[132:135], v[168:171], v[120:123], v[132:135]\n\t"
      "s_cmp_lg_u32 s44, s49\n\t"
      "v_mfma_f32_16x16x32_bf16 v[132:135], v[192:195], v[124:127], v[132:135]\n\t"
      "s_waitcnt lgkmcnt(2)\n\t"
      "buffer_store_dwordx4 v[148:151], v208, s[28:31], s72 offen offset:2048 sc1\n\t"
      "s_cbranch_scc1 .Lloop_%=\n\t"
      "s_cmp_lg_u32 s44, 17\n\t"
      "s_cbranch_scc1 .Lstop_%=\n\t"
      "v_readlane_b32 s72, v249, s44\n\t"
      "ds_read_b32 v241, v247\n\t"
      "s_nop 2\n\t"
      "v_cvt_pk_bf16_f32 v138, v132, v133\n\t"
      "v_cvt_pk_bf16_f32 v139, v134, v135\n\t"
      "ds_write_b128 v219, v[136:139] offset:12288\n\t"
      "s_waitcnt lgkmcnt(0)\n\t"
      "s_barrier\n\t"
      "ds_read_b128 v[32:35], v220 offset:12288\n\t"
      "ds_read_b128 v[36:39], v220 offset:13312\n\t"
      "ds_read_b128 v[40:43], v220 offset:14336\n\t"
      "v_cmp_ne_u32_e32 vcc, 0, v241\n\t"
      "s_cmp_lg_u32 s62, 0\n\t"
      "s_waitcnt lgkmcnt(2)\n\t"
      "buffer_store_dwordx4 v[32:35], v209, s[28:31], s72 offen sc1\n\t"
      "s_waitcnt lgkmcnt(1)\n\t"
      "buffer_store_dwordx4 v[36:39], v209, s[28:31], s72 offen offset:1024 sc1\n\t"
      "s_waitcnt lgkmcnt(0)\n\t"
      "buffer_store_dwordx4 v[40:43], v209, s[28:31], s72 offen offset:2048 sc1\n\t"
      "s_cbranch_scc1 .Lfin_%=\n\t"
      "s_cbranch_vccnz .Lsec_%=\n\t"
      ".Lfin_%=:\n\t"
      "s_endpgm\n\t"
      ".Lstop_%=:\n\t"
      "s_and_b32 s49, s71, 31\n\t"
      "s_lshr_b32 s71, s71, 5\n\t"
      "s_sub_u32 s78, s44, s89\n\t"
      "s_cmp_eq_u32 s78, 10\n\t"
      "s_cbranch_scc1 .Lstop2_%=\n\t"
      "s_cmp_eq_u32 s78, 5\n\t"
      "s_cbranch_scc1 .Lstop1_%=\n\t"
      "s_waitcnt vmcnt(12)\n\t"
      "v_readlane_b32 s76, v254, 1\n\t"
      "v_readlane_b32 s78, v255, 1\n\t"
      "s_andn2_b32 s76, 4, s76\n\t"
      "s_or_b32 s76, s76, s64\n\t"
      "s_xor_b32 s76, s76, s78\n\t"
      "v_mov_b32_e32 v240, s76\n\t"
      "ds_write_b32 v247, v240\n\t"
      "s_branch .Lloop_%=\n\t"
      ".Lstop1_%=:\n\t"
      "s_cmp_eq_u32 s94, 0\n\t"
      "s_cbranch_scc1 .Llt0_%=\n\t"
      "ds_read_b128 v[226:229], v238 offset:24576\n\t"
      "ds_read_b128 v[230:233], v238 offset:25600\n\t"
      "ds_read_b128 v[234:237], v238 offset:26624\n\t"
      "v_readlane_b32 s68, v248, 0\n\t"
      "s_nop 4\n\t"
      "s_waitcnt lgkmcnt(2)\n\t"
      "buffer_store_dwordx4 v[226:229], v208, s[28:31], s68 offen sc1\n\t"
      "s_waitcnt lgkmcnt(1)\n\t"
      "buffer_store_dwordx4 v[230:233], v208, s[28:31], s68 offen offset:1024 sc1\n\t"
      "s_waitcnt lgkmcnt(0)\n\t"
      "buffer_store_dwordx4 v[234:237], v208, s[28:31], s68 offen offset:2048 sc1\n\t"
      ".Llt0_%=:\n\t"
      "v_readlane_b32 s76, v254, 0\n\t"
      "s_cmp_lg_u32 s41, s43\n\t"
      "s_cbranch_scc1 .Lloop_%=\n\t"
      "s_andn2_b32 s76, 4, s76\n\t"
      "s_or_b32 s76, s76, s64\n\t"
      "v_mov_b32_e32 v240, s76\n\t"
      "s_mov_b64 exec, 1\n\t"
      "buffer_store_dword v240, v253, s[24:27], 0 offen sc1\n\t"
      "s_mov_b64 exec, -1\n\t"
      "s_branch .Lloop_%=\n\t"
      ".Lstop2_%=:\n\t"
      "s_cmp_eq_u32 s94, 0\n\t"
      "s_cbranch_scc1 .Llt1_%=\n\t"
      "ds_read_b128 v[226:229], v238 offset:36864\n\t"
      "ds_read_b128 v[230:233], v238 offset:37888\n\t"
      "ds_read_b128 v[234:237], v238 offset:38912\n\t"
      "v_readlane_b32 s68, v249, 0\n\t"
      "s_nop 4\n\t"
      "s_waitcnt lgkmcnt(2)\n\t"
      "buffer_store_dwordx4 v[226:229], v209, s[28:31], s68 offen sc1\n\t"
      "s_waitcnt lgkmcnt(1)\n\t"
      "buffer_store_dwordx4 v[230:233], v209, s[28:31], s68 offen offset:1024 sc1\n\t"
      "s_waitcnt lgkmcnt(0)\n\t"
      "buffer_store_dwordx4 v[234:237], v209, s[28:31], s68 offen offset:2048 sc1\n\t"
      ".Llt1_%=:\n\t"
      "s_cmp_lg_u32 s41, s43\n\t"
      "s_cbranch_scc1 .Lloop_%=\n\t"
      "buffer_load_dword v255, v253, s[24:27], 0 offen sc1\n\t"
      "s_branch .Lloop_%=\n\t"
      ".LslpP3_%=:\n\t"
      "s_sleep 1\n\t"
      "s_sub_u32 s83, s83, 3\n\t"
      "s_cmp_gt_i32 s83, 1\n\t"
      "s_cbranch_scc1 .LslpP3_%=\n\t"
      "s_branch .LretP3_%=\n\t"
      ".LslpP4_%=:\n\t"
      "s_sleep 1\n\t"
      "s_sub_u32 s83, s83, 3\n\t"
      "s_cmp_gt_i32 s83, 1\n\t"
      "s_cbranch_scc1 .LslpP4_%=\n\t"
      "s_branch .LretP4_%=\n\t"
      ".LslpP5_%=:\n\t"
      "s_sleep 1\n\t"
      "s_sub_u32 s83, s83, 3\n\t"
      "s_cmp_gt_i32 s83, 1\n\t"
      "s_cbranch_scc1 .LslpP5_%=\n\t"
      "s_branch .LretP5_%=\n\t"
      ".LslpA_%=:\n\t"
      "s_sleep 1\n\t"
      "s_sub_u32 s83, s83, 3\n\t"
      "s_cmp_gt_i32 s83, 1\n\t"
      "s_cbranch_scc1 .LslpA_%=\n\t"
      "s_branch .LretA_%=\n\t"
      ".LslpB_%=:\n\t"
      "s_sleep 1\n\t"
      "s_sub_u32 s83, s83, 3\n\t"
      "s_cmp_gt_i32 s83, 1\n\t"
      "s_cbranch_scc1 .LslpB_%=\n\t"
      "s_branch .LretB_%=\n\t"
      ".Lsec_%=:\n\t"
      "s_mov_b32 s62, 1\n\t"
      "v_lshl_add_u32 v240, v251, 1, s66\n\t"
      "v_sub_u32_e32 v248, v240, v248\n\t"
      "v_sub_u32_e32 v249, v240, v249\n\t"
      "v_cmp_eq_u32_e32 vcc, 0, v252\n\t"
      "v_mov_b32_e32 v240, 0x6c00000\n\t"
      "s_nop 1\n\t"
      "v_cndmask_b32_e32 v248, v248, v240, vcc\n\t"
      "v_cndmask_b32_e32 v249, v249, v240, vcc\n\t"
      "s_add_u32 s44, s94, 3\n\t"
      "s_add_u32 s85, s44, 1\n\t"
      "s_mov_b32 s83, 0\n\t"
      "s_waitcnt vmcnt(0) lgkmcnt(0)\n\t"
      "s_barrier\n\t"
      "v_readlane_b32 s73, v248, s85\n\t"
      "s_mov_b32 m0, s46\n\t"
      "s_nop 4\n\t"
      "buffer_load_dwordx4 v208, s[20:23], s73 offen lds\n\t"
      "buffer_load_dwordx4 v208, s[20:23], s73 offen offset:1024 lds\n\t"
      "buffer_load_dwordx4 v208, s[20:23], s73 offen offset:2048 lds\n\t"
      "v_readlane_b32 s73, v249, s85\n\t"
      "s_add_u32 m0, s46, 0x3000\n\t"
      "s_nop 4\n\t"
      "buffer_load_dwordx4 v209, s[20:23], s73 offen lds\n\t"
      "buffer_load_dwordx4 v209, s[20:23], s73 offen offset:1024 lds\n\t"
      "buffer_load_dwordx4 v209, s[20:23], s73 offen offset:2048 lds\n\t"
      "s_add_u32 s85, s85, 1\n\t"
      "s_nop 4\n\t"
      "v_readlane_b32 s73, v248, s85\n\t"
      "s_mov_b32 m0, s47\n\t"
      "s_nop 4\n\t"
      "buffer_load_dwordx4 v208, s[20:23], s73 offen lds\n\t"
      "buffer_load_dwordx4 v208, s[20:23], s73 offen offset:1024 lds\n\t"
      "buffer_load_dwordx4 v208, s[20:23], s73 offen offset:2048 lds\n\t"
      "v_readlane_b32 s73, v249, s85\n\t"
      "s_add_u32 m0, s47, 0x3000\n\t"
      "s_nop 4\n\t"
      "buffer_load_dwordx4 v209, s[20:23], s73 offen lds\n\t"
      "buffer_load_dwordx4 v209, s[20:23], s73 offen offset:1024 lds\n\t"
      "buffer_load_dwordx4 v209, s[20:23], s73 offen offset:2048 lds\n\t"
      "s_add_u32 s85, s85, 1\n\t"
      "s_nop 4\n\t"
      "v_readlane_b32 s73, v248, s85\n\t"
      "s_mov_b32 m0, s45\n\t"
      "s_nop 4\n\t"
      "buffer_load_dwordx4 v208, s[20:23], s73 offen lds\n\t"
      "buffer_load_dwordx4 v208, s[20:23], s73 offen offset:1024 lds\n\t"
      "buffer_load_dwordx4 v208, s[20:23], s73 offen offset:2048 lds\n\t"
      "s_waitcnt vmcnt(0)\n\t"
      "s_barrier\n\t"
      "ds_read_b128 v[32:35], v196 offset:0\n\t"
      "ds_read_b128 v[36:39], v197 offset:0\n\t"
      "ds_read_b128 v[40:43], v198 offset:0\n\t"
      "ds_read_b128 v[44:47], v199 offset:0\n\t"
      "ds_read_b128 v[48:51], v197 offset:256\n\t"
      "ds_read_b128 v[52:55], v198 offset:256\n\t"
      "ds_read_b128 v[56:59], v199 offset:256\n\t"
      "ds_read_b128 v[60:63], v196 offset:256\n\t"
      "ds_read_b128 v[64:67], v198 offset:512\n\t"
      "ds_read_b128 v[68:71], v199 offset:512\n\t"
      "ds_read_b128 v[72:75], v196 offset:512\n\t"
      "ds_read_b128 v[76:79], v197 offset:512\n\t"
      "s_waitcnt lgkmcnt(0)\n\t"
      "s_branch .Lloop_%=\n\t"
      ".Lgs_%=:\n\t"
      "s_sleep 8\n\t"
      "buffer_load_dwordx2 v[240:241], v245, s[24:27], 0 offen sc1\n\t"
      "s_memrealtime s[76:77]\n\t"
      "s_waitcnt vmcnt(0) lgkmcnt(0)\n\t"
      "v_subrev_u32_e32 v242, s63, v240\n\t"
      "v_xor_b32_e32 v241, s53, v241\n\t"
      "v_lshrrev_b32_e32 v240, 13, v242\n\t"
      "v_or_b32_e32 v240, v240, v241\n\t"
      "v_cmp_ne_u32_e32 vcc, 0, v240\n\t"
      "v_and_b32_e32 v242, 15, v242\n\t"
      "s_cbranch_vccz .Lgr_%=\n\t"
      "s_sub_u32 s76, s76, s52\n\t"
      "s_cmp_lt_u32 s76, 2000\n\t"
      "s_cbranch_scc1 .Lgs_%=\n\t"
      "v_mov_b32_e32 v242, 64\n\t"
      "s_branch .Lgr_%=\n\t"
      ".Lpb_%=:\n\t"
      "s_mov_b64 vcc, -1\n\t"
      "s_branch .Lp2_%=\n\t"
      ".Lwsb_%=:\n\t"
      "s_mov_b64 vcc, 0\n\t"
      "s_waitcnt vmcnt(12)\n\t"
      "v_lshlrev_b32_e32 v8, 16, v226\n\t"
      "v_and_b32_e32 v9, 0xffff0000, v226\n\t"
      "v_lshlrev_b32_e32 v10, 16, v227\n\t"
      "v_and_b32_e32 v11, 0xffff0000, v227\n\t"
      "v_lshlrev_b32_e32 v12, 16, v228\n\t"
      "v_and_b32_e32 v13, 0xffff0000, v228\n\t"
      "v_lshlrev_b32_e32 v14, 16, v229\n\t"
      "v_and_b32_e32 v15, 0xffff0000, v229\n\t"
      "v_lshlrev_b32_e32 v16, 16, v230\n\t"
      "v_and_b32_e32 v17, 0xffff0000, v230\n\t"
      "v_lshlrev_b32_e32 v18, 16, v231\n\t"
      "v_and_b32_e32 v19, 0xffff0000, v231\n\t"
      "v_lshlrev_b32_e32 v20, 16, v232\n\t"
      "v_and_b32_e32 v21, 0xffff0000, v232\n\t"
      "v_lshlrev_b32_e32 v22, 16, v233\n\t"
      "v_and_b32_e32 v23, 0xffff0000, v233\n\t"
      "v_lshlrev_b32_e32 v24, 16, v234\n\t"
      "v_and_b32_e32 v25, 0xffff0000, v234\n\t"
      "v_lshlrev_b32_e32 v26, 16, v235\n\t"
      "v_and_b32_e32 v27, 0xffff0000, v235\n\t"
      "v_lshlrev_b32_e32 v28, 16, v236\n\t"
      "v_and_b32_e32 v29, 0xffff0000, v236\n\t"
      "v_lshlrev_b32_e32 v30, 16, v237\n\t"
      "v_and_b32_e32 v31, 0xffff0000, v237\n\t"
      "s_add_u32 m0, s41, 0x1e000\n\t"
      "s_mov_b32 s51, 0x0\n\t"
      "buffer_load_dwordx4 v211, s[16:19], s51 offen lds\n\t"
      "buffer_load_dwordx4 v212, s[16:19], s51 offen offset:1024 lds\n\t"
      "buffer_load_dwordx4 v213, s[16:19], s51 offen offset:2048 lds\n\t"
      "s_add_u32 m0, s41, 0x21000\n\t"
      "s_mov_b32 s51, 0xc00\n\t"
      "buffer_load_dwordx4 v211, s[16:19], s51 offen lds\n\t"
      "buffer_load_dwordx4 v212, s[16:19], s51 offen offset:1024 lds\n\t"
      "buffer_load_dwordx4 v213, s[16:19], s51 offen offset:2048 lds\n\t"
      "s_add_u32 m0, s41, 0x24000\n\t"
      "s_mov_b32 s51, 0x6000\n\t"
      "buffer_load_dwordx4 v211, s[16:19], s51 offen lds\n\t"
      "buffer_load_dwordx4 v212, s[16:19], s51 offen offset:1024 lds\n\t"
      "buffer_load_dwordx4 v213, s[16:19], s51 offen offset:2048 lds\n\t"
      "s_add_u32 m0, s41, 0x12000\n\t"
      "s_mov_b32 s51, 0x6c00\n\t"
      "buffer_load_dwordx4 v211, s[16:19], s51 offen lds\n\t"
      "buffer_load_dwordx4 v212, s[16:19], s51 offen offset:1024 lds\n\t"
      "buffer_load_dwordx4 v213, s[16:19], s51 offen offset:2048 lds\n\t"
      "s_add_u32 m0, s41, 0x15000\n\t"
      "s_mov_b32 s51, 0xc000\n\t"
      "buffer_load_dwordx4 v211, s[16:19], s51 offen lds\n\t"
      "buffer_load_dwordx4 v212, s[16:19], s51 offen offset:1024 lds\n\t"
      "buffer_load_dwordx4 v213, s[16:19], s51 offen offset:2048 lds\n\t"
      "s_mov_b32 s44, 1\n\t"
      "s_mov_b32 s85, 4\n\t"
      "s_add_u32 s45, s41, 0x6000\n\t"
      "s_add_u32 s46, s41, 0xc000\n\t"
      "s_mov_b32 s47, s41\n\t"
      "s_waitcnt vmcnt(12)\n\t"
      "s_barrier\n\t"
      "ds_read_b64_tr_b16 a[0:1], v214 offset:49152\n\t"
      "ds_read_b64_tr_b16 a[4:5], v214 offset:49160\n\t"
      "ds_read_b64_tr_b16 a[8:9], v214 offset:49216\n\t"
      "ds_read_b64_tr_b16 a[12:13], v214 offset:49224\n\t"
      "ds_read_b64_tr_b16 a[16:17], v214 offset:49280\n\t"
      "ds_read_b64_tr_b16 a[20:21], v214 offset:49288\n\t"
      "ds_read_b128 v[156:159], v196 offset:0\n\t"
      "ds_read_b128 v[160:163], v196 offset:12288\n\t"
      "ds_read_b128 v[128:131], v196 offset:24576\n\t"
      "s_add_u32 m0, s41, 0x18000\n\t"
      "s_mov_b32 s51, 0xcc00\n\t"
      "buffer_load_dwordx4 v211, s[16:19], s51 offen lds\n\t"
      "buffer_load_dwordx4 v212, s[16:19], s51 offen offset:1024 lds\n\t"
      "buffer_load_dwordx4 v213, s[16:19], s51 offen offset:2048 lds\n\t"
      "s_waitcnt vmcnt(12)\n\t"
      "ds_read_b64_tr_b16 a[2:3], v221 offset:0\n\t"
      "ds_read_b64_tr_b16 a[6:7], v221 offset:8\n\t"
      "ds_read_b64_tr_b16 a[10:11], v221 offset:64\n\t"
      "ds_read_b64_tr_b16 a[14:15], v221 offset:72\n\t"
      "ds_read_b64_tr_b16 a[18:19], v221 offset:128\n\t"
      "ds_read_b64_tr_b16 a[22:23], v221 offset:136\n\t"
      "s_add_u32 m0, s41, 0x1b000\n\t"
      "s_mov_b32 s51, 0x12000\n\t"
      "buffer_load_dwordx4 v211, s[16:19], s51 offen lds\n\t"
      "buffer_load_dwordx4 v212, s[16:19], s51 offen offset:1024 lds\n\t"
      "buffer_load_dwordx4 v213, s[16:19], s51 offen offset:2048 lds\n\t"
      "s_waitcnt lgkmcnt(0)\n\t"
      "v_mfma_f32_16x16x32_bf16 v[32:35], a[0:3], v[156:159], v[8:11]\n\t"
      "v_mfma_f32_16x16x32_bf16 v[36:39], a[4:7], v[156:159], v[12:15]\n\t"
      "s_waitcnt vmcnt(12)\n\t"
      "ds_read_b64_tr_b16 a[24:25], v221 offset:12288\n\t"
      "v_mfma_f32_16x16x32_bf16 v[40:43], a[8:11], v[156:159], v[16:19]\n\t"
      "ds_read_b64_tr_b16 a[28:29], v221 offset:12296\n\t"
      "v_mfma_f32_16x16x32_bf16 v[44:47], a[12:15], v[156:159], v[20:23]\n\t"
      "ds_read_b64_tr_b16 a[32:33], v221 offset:12352\n\t"
      "v_mfma_f32_16x16x32_bf16 v[48:51], a[16:19], v[156:159], v[24:27]\n\t"
      "ds_read_b64_tr_b16 a[36:37], v221 offset:12360\n\t"
      "ds_read_b128 v[136:139], v196 offset:36864\n\t"
      "v_mfma_f32_16x16x32_bf16 v[52:55], a[20:23], v[156:159], v[28:31]\n\t"
      "ds_read_b64_tr_b16 a[40:41], v221 offset:12416\n\t"
      "v_mfma_f32_16x16x32_bf16 v[56:59], a[0:3], v[160:163], v[8:11]\n\t"
      "ds_read_b64_tr_b16 a[44:45], v221 offset:12424\n\t"
      "v_mfma_f32_16x16x32_bf16 v[60:63], a[4:7], v[160:163], v[12:15]\n\t"
      "v_mfma_f32_16x16x32_bf16 v[64:67], a[8:11], v[160:163], v[16:19]\n\t"
      "s_waitcnt vmcnt(9)\n\t"
      "ds_read_b64_tr_b16 a[26:27], v214 offset:0\n\t"
      "v_mfma_f32_16x16x32_bf16 v[68:71], a[12:15], v[160:163], v[20:23]\n\t"
      "ds_read_b64_tr_b16 a[30:31], v214 offset:8\n\t"
      "v_mfma_f32_16x16x32_bf16 v[72:75], a[16:19], v[160:163], v[24:27]\n\t"
      "ds_read_b64_tr_b16 a[34:35], v214 offset:64\n\t"
      "ds_read_b128 v[140:143], v197 offset:0\n\t"
      "v_mfma_f32_16x16x32_bf16 v[76:79], a[20:23], v[160:163], v[28:31]\n\t"
      "ds_read_b64_tr_b16 a[38:39], v214 offset:72\n\t"
      "v_mfma_f32_16x16x32_bf16 v[80:83], a[0:3], v[128:131], v[8:11]\n\t"
      "ds_read_b64_tr_b16 a[42:43], v214 offset:128\n\t"
      "s_add_u32 m0, s41, 0x1e000\n\t"
      "s_mov_b32 s51, 0x12c00\n\t"
      "buffer_load_dwordx4 v211, s[16:19], s51 offen lds\n\t"
      "v_mfma_f32_16x16x32_bf16 v[84:87], a[4:7], v[128:131], v[12:15]\n\t"
      "ds_read_b64_tr_b16 a[46:47], v214 offset:136\n\t"
      "v_mfma_f32_16x16x32_bf16 v[88:91], a[8:11], v[128:131], v[16:19]\n\t"
      "v_mfma_f32_16x16x32_bf16 v[92:95], a[12:15], v[128:131], v[20:23]\n\t"
      "buffer_load_dwordx4 v212, s[16:19], s51 offen offset:1024 lds\n\t"
      "v_mfma_f32_16x16x32_bf16 v[96:99], a[16:19], v[128:131], v[24:27]\n\t"
      "ds_read_b128 v[144:147], v197 offset:12288\n\t"
      "v_mfma_f32_16x16x32_bf16 v[100:103], a[20:23], v[128:131], v[28:31]\n\t"
      "s_waitcnt lgkmcnt(10)\n\t"
      "v_mfma_f32_16x16x32_bf16 v[104:107], a[0:3], v[136:139], v[8:11]\n\t"
      "buffer_load_dwordx4 v213, s[16:19], s51 offen offset:2048 lds\n\t"
      "v_mfma_f32_16x16x32_bf16 v[108:111], a[4:7], v[136:139], v[12:15]\n\t"
      "v_mfma_f32_16x16x32_bf16 v[112:115], a[8:11], v[136:139], v[16:19]\n\t"
      "s_waitcnt vmcnt(9)\n\t"
      "ds_read_b64_tr_b16 a[48:49], v214 offset:12288\n\t"
      "v_mfma_f32_16x16x32_bf16 v[116:119], a[12:15], v[136:139], v[20:23]\n\t"
      "ds_read_b64_tr_b16 a[52:53], v214 offset:12296\n\t"
      "s_add_u32 m0, s41, 0x21000\n\t"
      "s_mov_b32 s51, 0x18000\n\t"
      "buffer_load_dwordx4 v211, s[16:19], s51 offen lds\n\t"
      "v_mfma_f32_16x16x32_bf16 v[120:123], a[16:19], v[136:139], v[24:27]\n\t"
      "ds_read_b64_tr_b16 a[56:57], v214 offset:12352\n\t"
      "ds_read_b128 v[148:151], v197 offset:24576\n\t"
      "v_mfma_f32_16x16x32_bf16 v[132:135], a[20:23], v[136:139], v[28:31]\n\t"
      "ds_read_b64_tr_b16 a[60:61], v214 offset:12360\n\t"
      "s_waitcnt lgkmcnt(6)\n\t"
      "v_mfma_f32_16x16x32_bf16 v[32:35], a[24:27], v[140:143], v[32:35]\n\t"
      "ds_read_b64_tr_b16 a[64:65], v214 offset:12416\n\t"
      "buffer_load_dwordx4 v212, s[16:19], s51 offen offset:1024 lds\n\t"
      "v_mfma_f32_16x16x32_bf16 v[36:39], a[28:31], v[140:143], v[36:39]\n\t"
      "ds_read_b64_tr_b16 a[68:69], v214 offset:12424\n\t"
      "v_mfma_f32_16x16x32_bf16 v[40:43], a[32:35], v[140:143], v[40:43]\n\t"
      "v_mfma_f32_16x16x32_bf16 v[44:47], a[36:39], v[140:143], v[44:47]\n\t"
      "buffer_load_dwordx4 v213, s[16:19], s51 offen offset:2048 lds\n\t"
      "v_mfma_f32_16x16x32_bf16 v[48:51], a[40:43], v[140:143], v[48:51]\n\t"
      "ds_read_b128 v[152:155], v197 offset:36864\n\t"
      "v_mfma_f32_16x16x32_bf16 v[52:55], a[44:47], v[140:143], v[52:55]\n\t"
      "s_waitcnt lgkmcnt(8)\n\t"
      "v_mfma_f32_16x16x32_bf16 v[56:59], a[24:27], v[144:147], v[56:59]\n\t"
      "s_add_u32 m0, s41, 0x24000\n\t"
      "s_mov_b32 s51, 0x18c00\n\t"
      "buffer_load_dwordx4 v211, s[16:19], s51 offen lds\n\t"
      "v_mfma_f32_16x16x32_bf16 v[60:63], a[28:31], v[144:147], v[60:63]\n\t"
      "v_mfma_f32_16x16x32_bf16 v[64:67], a[32:35], v[144:147], v[64:67]\n\t"
      "s_waitcnt vmcnt(10)\n\t"
      "ds_read_b64_tr_b16 a[50:51], v214 offset:24576\n\t"
      "v_mfma_f32_16x16x32_bf16 v[68:71], a[36:39], v[144:147], v[68:71]\n\t"
      "ds_read_b64_tr_b16 a[54:55], v214 offset:24584\n\t"
      "buffer_load_dwordx4 v212, s[16:19], s51 offen offset:1024 lds\n\t"
      "v_mfma_f32_16x16x32_bf16 v[72:75], a[40:43], v[144:147], v[72:75]\n\t"
      "ds_read_b64_tr_b16 a[58:59], v214 offset:24640\n\t"
      "ds_read_b128 v[156:159], v198 offset:0\n\t"
      "v_mfma_f32_16x16x32_bf16 v[76:79], a[44:47], v[144:147], v[76:79]\n\t"
      "ds_read_b64_tr_b16 a[62:63], v214 offset:24648\n\t"
      "s_waitcnt lgkmcnt(9)\n\t"
      "v_mfma_f32_16x16x32_bf16 v[80:83], a[24:27], v[148:151], v[80:83]\n\t"
      "ds_read_b64_tr_b16 a[66:67], v214 offset:24704\n\t"
      "buffer_load_dwordx4 v213, s[16:19], s51 offen offset:2048 lds\n\t"
      "v_mfma_f32_16x16x32_bf16 v[84:87], a[28:31], v[148:151], v[84:87]\n\t"
      "ds_read_b64_tr_b16 a[70:71], v214 offset:24712\n\t"
      "v_mfma_f32_16x16x32_bf16 v[88:91], a[32:35], v[148:151], v[88:91]\n\t"
      "v_mfma_f32_16x16x32_bf16 v[92:95], a[36:39], v[148:151], v[92:95]\n\t"
      "s_add_u32 m0, s41, 0x12000\n\t"
      "s_mov_b32 s51, 0x1e000\n\t"
      "buffer_load_dwordx4 v211, s[16:19], s51 offen lds\n\t"
      "v_mfma_f32_16x16x32_bf16 v[96:99], a[40:43], v[148:151], v[96:99]\n\t"
      "ds_read_b128 v[160:163], v198 offset:12288\n\t"
      "v_mfma_f32_16x16x32_bf16 v[100:103], a[44:47], v[148:151], v[100:103]\n\t"
      "s_waitcnt lgkmcnt(8)\n\t"
      "v_mfma_f32_16x16x32_bf16 v[104:107], a[24:27], v[152:155], v[104:107]\n\t"
      "buffer_load_dwordx4 v212, s[16:19], s51 offen offset:1024 lds\n\t"
      "v_mfma_f32_16x16x32_bf16 v[108:111], a[28:31], v[152:155], v[108:111]\n\t"
      "v_mfma_f32_16x16x32_bf16 v[112:115], a[32:35], v[152:155], v[112:115]\n\t"
      "s_waitcnt vmcnt(11)\n\t"
      "ds_read_b64_tr_b16 a[72:73], v214 offset:36864\n\t"
      "v_mfma_f32_16x16x32_bf16 v[116:119], a[36:39], v[152:155], v[116:119]\n\t"
      "ds_read_b64_tr_b16 a[76:77], v214 offset:36872\n\t"
      "buffer_load_dwordx4 v213, s[16:19], s51 offen offset:2048 lds\n\t"
      "v_mfma_f32_16x16x32_bf16 v[120:123], a[40:43], v[152:155], v[120:123]\n\t"
      "ds_read_b64_tr_b16 a[80:81], v214 offset:36928\n\t"
      "ds_read_b128 v[128:131], v198 offset:24576\n\t"
      "v_mfma_f32_16x16x32_bf16 v[132:135], a[44:47], v[152:155], v[132:135]\n\t"
      "ds_read_b64_tr_b16 a[84:85], v214 offset:36936\n\t"
      "s_waitcnt lgkmcnt(6)\n\t"
      "v_mfma_f32_16x16x32_bf16 v[32:35], a[48:51], v[156:159], v[32:35]\n\t"
      "ds_read_b64_tr_b16 a[88:89], v214 offset:36992\n\t"
      "s_add_u32 m0, s41, 0x15000\n\t"
      "s_mov_b32 s51, 0x1ec00\n\t"
      "buffer_load_dwordx4 v211, s[16:19], s51 offen lds\n\t"
      "v_mfma_f32_16x16x32_bf16 v[36:39], a[52:55], v[156:159], v[36:39]\n\t"
      "ds_read_b64_tr_b16 a[92:93], v214 offset:37000\n\t"
      "v_mfma_f32_16x16x32_bf16 v[40:43], a[56:59], v[156:159], v[40:43]\n\t"
      "v_mfma_f32_16x16x32_bf16 v[44:47], a[60:63], v[156:159], v[44:47]\n\t"
      "buffer_load_dwordx4 v212, s[16:19], s51 offen offset:1024 lds\n\t"
      "v_mfma_f32_16x16x32_bf16 v[48:51], a[64:67], v[156:159], v[48:51]\n\t"
      "ds_read_b128 v[136:139], v198 offset:36864\n\t"
      "v_mfma_f32_16x16x32_bf16 v[52:55], a[68:71], v[156:159], v[52:55]\n\t"
      "s_waitcnt lgkmcnt(8)\n\t"
      "v_mfma_f32_16x16x32_bf16 v[56:59], a[48:51], v[160:163], v[56:59]\n\t"
      "buffer_load_dwordx4 v213, s[16:19], s51 offen offset:2048 lds\n\t"
      "v_mfma_f32_16x16x32_bf16 v[60:63], a[52:55], v[160:163], v[60:63]\n\t"
      "v_mfma_f32_16x16x32_bf16 v[64:67], a[56:59], v[160:163], v[64:67]\n\t"
      "s_waitcnt vmcnt(12)\n\t"
      "ds_read_b64_tr_b16 a[74:75], v214 offset:49152\n\t"
      "v_mfma_f32_16x16x32_bf16 v[68:71], a[60:63], v[160:163], v[68:71]\n\t"
      "ds_read_b64_tr_b16 a[78:79], v214 offset:49160\n\t"
      "s_add_u32 m0, s41, 0x18000\n\t"
      "s_mov_b32 s51, 0x24000\n\t"
      "buffer_load_dwordx4 v211, s[16:19], s51 offen lds\n\t"
      "v_mfma_f32_16x16x32_bf16 v[72:75], a[64:67], v[160:163], v[72:75]\n\t"
      "ds_read_b64_tr_b16 a[82:83], v214 offset:49216\n\t"
      "ds_read_b128 v[140:143], v199 offset:0\n\t"
      "v_mfma_f32_16x16x32_bf16 v[76:79], a[68:71], v[160:163], v[76:79]\n\t"
      "ds_read_b64_tr_b16 a[86:87], v214 offset:49224\n\t"
      "s_waitcnt lgkmcnt(9)\n\t"
      "v_mfma_f32_16x16x32_bf16 v[80:83], a[48:51], v[128:131], v[80:83]\n\t"
      "ds_read_b64_tr_b16 a[90:91], v214 offset:49280\n\t"
      "buffer_load_dwordx4 v212, s[16:19], s51 offen offset:1024 lds\n\t"
      "v_mfma_f32_16x16x32_bf16 v[84:87], a[52:55], v[128:131], v[84:87]\n\t"
      "ds_read_b64_tr_b16 a[94:95], v214 offset:49288\n\t"
      "v_mfma_f32_16x16x32_bf16 v[88:91], a[56:59], v[128:131], v[88:91]\n\t"
      "v_mfma_f32_16x16x32_bf16 v[92:95], a[60:63], v[128:131], v[92:95]\n\t"
      "buffer_load_dwordx4 v213, s[16:19], s51 offen offset:2048 lds\n\t"
      "v_mfma_f32_16x16x32_bf16 v[96:99], a[64:67], v[128:131], v[96:99]\n\t"
      "ds_read_b128 v[144:147], v199 offset:12288\n\t"
      "v_mfma_f32_16x16x32_bf16 v[100:103], a[68:71], v[128:131], v[100:103]\n\t"
      "s_waitcnt lgkmcnt(8)\n\t"
      "v_mfma_f32_16x16x32_bf16 v[104:107], a[48:51], v[136:139], v[104:107]\n\t"
      "s_add_u32 m0, s41, 0x1b000\n\t"
      "s_mov_b32 s51, 0x24c00\n\t"
      "buffer_load_dwordx4 v211, s[16:19], s51 offen lds\n\t"
      "v_mfma_f32_16x16x32_bf16 v[108:111], a[52:55], v[136:139], v[108:111]\n\t"
      "v_mfma_f32_16x16x32_bf16 v[112:115], a[56:59], v[136:139], v[112:115]\n\t"
      "s_waitcnt vmcnt(13)\n\t"
      "ds_read_b64_tr_b16 a[96:97], v221 offset:0\n\t"
      "v_mfma_f32_16x16x32_bf16 v[116:119], a[60:63], v[136:139], v[116:119]\n\t"
      "ds_read_b64_tr_b16 a[100:101], v221 offset:8\n\t"
      "buffer_load_dwordx4 v212, s[16:19], s51 offen offset:1024 lds\n\t"
      "v_mfma_f32_16x16x32_bf16 v[120:123], a[64:67], v[136:139], v[120:123]\n\t"
      "ds_read_b64_tr_b16 a[104:105], v221 offset:64\n\t"
      "ds_read_b128 v[148:151], v199 offset:24576\n\t"
      "v_mfma_f32_16x16x32_bf16 v[132:135], a[68:71], v[136:139], v[132:135]\n\t"
      "ds_read_b64_tr_b16 a[108:109], v221 offset:72\n\t"
      "s_waitcnt lgkmcnt(6)\n\t"
      "v_mfma_f32_16x16x32_bf16 v[32:35], a[72:75], v[140:143], v[32:35]\n\t"
      "ds_read_b64_tr_b16 a[112:113], v221 offset:128\n\t"
      "buffer_load_dwordx4 v213, s[16:19], s51 offen offset:2048 lds\n\t"
      "v_mfma_f32_16x16x32_bf16 v[36:39], a[76:79], v[140:143], v[36:39]\n\t"
      "ds_read_b64_tr_b16 a[116:117], v221 offset:136\n\t"
      "v_mfma_f32_16x16x32_bf16 v[40:43], a[80:83], v[140:143], v[40:43]\n\t"
      "v_mfma_f32_16x16x32_bf16 v[44:47], a[84:87], v[140:143], v[44:47]\n\t"
      "s_add_u32 m0, s41, 0x1e000\n\t"
      "s_mov_b32 s51, 0x2a000\n\t"
      "buffer_load_dwordx4 v211, s[16:19], s51 offen lds\n\t"
      "v_mfma_f32_16x16x32_bf16 v[48:51], a[88:91], v[140:143], v[48:51]\n\t"
      "ds_read_b128 v[152:155], v199 offset:36864\n\t"
      "v_mfma_f32_16x16x32_bf16 v[52:55], a[92:95], v[140:143], v[52:55]\n\t"
      "s_waitcnt lgkmcnt(8)\n\t"
      "v_mfma_f32_16x16x32_bf16 v[56:59], a[72:75], v[144:147], v[56:59]\n\t"
      "buffer_load_dwordx4 v212, s[16:19], s51 offen offset:1024 lds\n\t"
      "v_mfma_f32_16x16x32_bf16 v[60:63], a[76:79], v[144:147], v[60:63]\n\t"
      "v_mfma_f32_16x16x32_bf16 v[64:67], a[80:83], v[144:147], v[64:67]\n\t"
      "s_waitcnt vmcnt(14)\n\t"
      "ds_read_b64_tr_b16 a[98:99], v221 offset:12288\n\t"
      "v_mfma_f32_16x16x32_bf16 v[68:71], a[84:87], v[144:147], v[68:71]\n\t"
      "ds_read_b64_tr_b16 a[102:103], v221 offset:12296\n\t"
      "buffer_load_dwordx4 v213, s[16:19], s51 offen offset:2048 lds\n\t"
      "v_mfma_f32_16x16x32_bf16 v[72:75], a[88:91], v[144:147], v[72:75]\n\t"
      "ds_read_b64_tr_b16 a[106:107], v221 offset:12352\n\t"
      "ds_read_b128 v[156:159], v197 offset:256\n\t"
      "v_mfma_f32_16x16x32_bf16 v[76:79], a[92:95], v[144:147], v[76:79]\n\t"
      "ds_read_b64_tr_b16 a[110:111], v221 offset:12360\n\t"
      "s_waitcnt lgkmcnt(9)\n\t"
      "v_mfma_f32_16x16x32_bf16 v[80:83], a[72:75], v[148:151], v[80:83]\n\t"
      "ds_read_b64_tr_b16 a[114:115], v221 offset:12416\n\t"
      "s_waitcnt lgkmcnt(7)\n\t"
      "s_add_u32 m0, s41, 0x21000\n\t"
      "s_mov_b32 s51, 0x2ac00\n\t"
      "buffer_load_dwordx4 v211, s[16:19], s51 offen lds\n\t"
      "v_mfma_f32_16x16x32_bf16 v[84:87], a[76:79], v[148:151], v[84:87]\n\t"
      "ds_read_b64_tr_b16 a[118:119], v221 offset:12424\n\t"
      "v_mfma_f32_16x16x32_bf16 v[88:91], a[80:83], v[148:151], v[88:91]\n\t"
      "v_mfma_f32_16x16x32_bf16 v[92:95], a[84:87], v[148:151], v[92:95]\n\t"
      "buffer_load_dwordx4 v212, s[16:19], s51 offen offset:1024 lds\n\t"
      "v_mfma_f32_16x16x32_bf16 v[96:99], a[88:91], v[148:151], v[96:99]\n\t"
      "ds_read_b128 v[160:163], v197 offset:12544\n\t"
      "v_mfma_f32_16x16x32_bf16 v[100:103], a[92:95], v[148:151], v[100:103]\n\t"
      "s_waitcnt lgkmcnt(8)\n\t"
      "v_mfma_f32_16x16x32_bf16 v[104:107], a[72:75], v[152:155], v[104:107]\n\t"
      "buffer_load_dwordx4 v213, s[16:19], s51 offen offset:2048 lds\n\t"
      "v_mfma_f32_16x16x32_bf16 v[108:111], a[76:79], v[152:155], v[108:111]\n\t"
      "v_mfma_f32_16x16x32_bf16 v[112:115], a[80:83], v[152:155], v[112:115]\n\t"
      "s_waitcnt vmcnt(15)\n\t"
      "ds_read_b64_tr_b16 a[120:121], v214 offset:0\n\t"
      "v_mfma_f32_16x16x32_bf16 v[116:119], a[84:87], v[152:155], v[116:119]\n\t"
      "ds_read_b64_tr_b16 a[124:125], v214 offset:8\n\t"
      "s_waitcnt lgkmcnt(3)\n\t"
      "s_add_u32 m0, s41, 0x24000\n\t"
      "s_mov_b32 s51, 0x30000\n\t"
      "buffer_load_dwordx4 v211, s[16:19], s51 offen lds\n\t"
      "v_mfma_f32_16x16x32_bf16 v[120:123], a[88:91], v[152:155], v[120:123]\n\t"
      "ds_read_b64_tr_b16 a[128:129], v214 offset:64\n\t"
      "ds_read_b128 v[128:131], v197 offset:24832\n\t"
      "v_mfma_f32_16x16x32_bf16 v[132:135], a[92:95], v[152:155], v[132:135]\n\t"
      "ds_read_b64_tr_b16 a[132:133], v214 offset:72\n\t"
      "v_mfma_f32_16x16x32_bf16 v[32:35], a[96:99], v[156:159], v[32:35]\n\t"
      "ds_read_b64_tr_b16 a[136:137], v214 offset:128\n\t"
      "buffer_load_dwordx4 v212, s[16:19], s51 offen offset:1024 lds\n\t"
      "v_mfma_f32_16x16x32_bf16 v[36:39], a[100:103], v[156:159], v[36:39]\n\t"
      "ds_read_b64_tr_b16 a[140:141], v214 offset:136\n\t"
      "v_mfma_f32_16x16x32_bf16 v[40:43], a[104:107], v[156:159], v[40:43]\n\t"
      "v_mfma_f32_16x16x32_bf16 v[44:47], a[108:111], v[156:159], v[44:47]\n\t"
      "buffer_load_dwordx4 v213, s[16:19], s51 offen offset:2048 lds\n\t"
      "v_mfma_f32_16x16x32_bf16 v[48:51], a[112:115], v[156:159], v[48:51]\n\t"
      "ds_read_b128 v[136:139], v197 offset:37120\n\t"
      "v_mfma_f32_16x16x32_bf16 v[52:55], a[116:119], v[156:159], v[52:55]\n\t"
      "s_waitcnt lgkmcnt(8)\n\t"
      "v_mfma_f32_16x16x32_bf16 v[56:59], a[96:99], v[160:163], v[56:59]\n\t"
      "v_mfma_f32_16x16x32_bf16 v[60:63], a[100:103], v[160:163], v[60:63]\n\t"
      "v_mfma_f32_16x16x32_bf16 v[64:67], a[104:107], v[160:163], v[64:67]\n\t"
      "s_waitcnt vmcnt(15)\n\t"
      "ds_read_b64_tr_b16 a[122:123], v214 offset:12288\n\t"
      "s_waitcnt lgkmcnt(2)\n\t"
      "s_add_u32 m0, s41, 0x12000\n\t"
      "s_mov_b32 s51, 0x30c00\n\t"
      "buffer_load_dwordx4 v211, s[16:19], s51 offen lds\n\t"
      "v_mfma_f32_16x16x32_bf16 v[68:71], a[108:111], v[160:163], v[68:71]\n\t"
      "ds_read_b64_tr_b16 a[126:127], v214 offset:12296\n\t"
      "v_mfma_f32_16x16x32_bf16 v[72:75], a[112:115], v[160:163], v[72:75]\n\t"
      "ds_read_b64_tr_b16 a[130:131], v214 offset:12352\n\t"
      "ds_read_b128 v[140:143], v198 offset:256\n\t"
      "v_mfma_f32_16x16x32_bf16 v[76:79], a[116:119], v[160:163], v[76:79]\n\t"
      "ds_read_b64_tr_b16 a[134:135], v214 offset:12360\n\t"
      "buffer_load_dwordx4 v212, s[16:19], s51 offen offset:1024 lds\n\t"
      "v_mfma_f32_16x16x32_bf16 v[80:83], a[96:99], v[128:131], v[80:83]\n\t"
      "ds_read_b64_tr_b16 a[138:139], v214 offset:12416\n\t"
      "v_mfma_f32_16x16x32_bf16 v[84:87], a[100:103], v[128:131], v[84:87]\n\t"
      "ds_read_b64_tr_b16 a[142:143], v214 offset:12424\n\t"
      "v_mfma_f32_16x16x32_bf16 v[88:91], a[104:107], v[128:131], v[88:91]\n\t"
      "buffer_load_dwordx4 v213, s[16:19], s51 offen offset:2048 lds\n\t"
      "v_mfma_f32_16x16x32_bf16 v[92:95], a[108:111], v[128:131], v[92:95]\n\t"
      "v_mfma_f32_16x16x32_bf16 v[96:99], a[112:115], v[128:131], v[96:99]\n\t"
      "ds_read_b128 v[144:147], v198 offset:12544\n\t"
      "v_mfma_f32_16x16x32_bf16 v[100:103], a[116:119], v[128:131], v[100:103]\n\t"
      "s_waitcnt lgkmcnt(8)\n\t"
      "v_mfma_f32_16x16x32_bf16 v[104:107], a[96:99], v[136:139], v[104:107]\n\t"
      "v_mfma_f32_16x16x32_bf16 v[108:111], a[100:103], v[136:139], v[108:111]\n\t"
      "v_mfma_f32_16x16x32_bf16 v[112:115], a[104:107], v[136:139], v[112:115]\n\t"
      "s_waitcnt vmcnt(15)\n\t"
      "ds_read_b64_tr_b16 a[144:145], v214 offset:24576\n\t"
      "s_waitcnt lgkmcnt(2)\n\t"
      "s_add_u32 m0, s41, 0x15000\n\t"
      "s_mov_b32 s51, 0x36000\n\t"
      "buffer_load_dwordx4 v211, s[16:19], s51 offen lds\n\t"
      "v_mfma_f32_16x16x32_bf16 v[116:119], a[108:111], v[136:139], v[116:119]\n\t"
      "ds_read_b64_tr_b16 a[148:149], v214 offset:24584\n\t"
      "v_mfma_f32_16x16x32_bf16 v[120:123], a[112:115], v[136:139], v[120:123]\n\t"
      "ds_read_b64_tr_b16 a[152:153], v214 offset:24640\n\t"
      "ds_read_b128 v[148:151], v198 offset:24832\n\t"
      "v_mfma_f32_16x16x32_bf16 v[132:135], a[116:119], v[136:139], v[132:135]\n\t"
      "ds_read_b64_tr_b16 a[156:157], v214 offset:24648\n\t"
      "buffer_load_dwordx4 v212, s[16:19], s51 offen offset:1024 lds\n\t"
      "v_mfma_f32_16x16x32_bf16 v[32:35], a[120:123], v[140:143], v[32:35]\n\t"
      "ds_read_b64_tr_b16 a[160:161], v214 offset:24704\n\t"
      "v_mfma_f32_16x16x32_bf16 v[36:39], a[124:127], v[140:143], v[36:39]\n\t"
      "ds_read_b64_tr_b16 a[164:165], v214 offset:24712\n\t"
      "v_mfma_f32_16x16x32_bf16 v[40:43], a[128:131], v[140:143], v[40:43]\n\t"
      "buffer_load_dwordx4 v213, s[16:19], s51 offen offset:2048 lds\n\t"
      "v_mfma_f32_16x16x32_bf16 v[44:47], a[132:135], v[140:143], v[44:47]\n\t"
      "v_mfma_f32_16x16x32_bf16 v[48:51], a[136:139], v[140:143], v[48:51]\n\t"
      "ds_read_b128 v[152:155], v198 offset:37120\n\t"
      "v_mfma_f32_16x16x32_bf16 v[52:55], a[140:143], v[140:143], v[52:55]\n\t"
      "s_waitcnt lgkmcnt(8)\n\t"
      "v_mfma_f32_16x16x32_bf16 v[56:59], a[120:123], v[144:147], v[56:59]\n\t"
      "v_mfma_f32_16x16x32_bf16 v[60:63], a[124:127], v[144:147], v[60:63]\n\t"
      "v_mfma_f32_16x16x32_bf16 v[64:67], a[128:131], v[144:147], v[64:67]\n\t"
      "s_waitcnt vmcnt(15)\n\t"
      "ds_read_b64_tr_b16 a[146:147], v214 offset:36864\n\t"
      "s_waitcnt lgkmcnt(2)\n\t"
      "s_add_u32 m0, s41, 0x18000\n\t"
      "s_mov_b32 s51, 0x36c00\n\t"
      "buffer_load_dwordx4 v211, s[16:19], s51 offen lds\n\t"
      "v_mfma_f32_16x16x32_bf16 v[68:71], a[132:135], v[144:147], v[68:71]\n\t"
      "ds_read_b64_tr_b16 a[150:151], v214 offset:36872\n\t"
      "v_mfma_f32_16x16x32_bf16 v[72:75], a[136:139], v[144:147], v[72:75]\n\t"
      "ds_read_b64_tr_b16 a[154:155], v214 offset:36928\n\t"
      "ds_read_b128 v[156:159], v199 offset:256\n\t"
      "v_mfma_f32_16x16x32_bf16 v[76:79], a[140:143], v[144:147], v[76:79]\n\t"
      "ds_read_b64_tr_b16 a[158:159], v214 offset:36936\n\t"
      "buffer_load_dwordx4 v212, s[16:19], s51 offen offset:1024 lds\n\t"
      "v_mfma_f32_16x16x32_bf16 v[80:83], a[120:123], v[148:151], v[80:83]\n\t"
      "ds_read_b64_tr_b16 a[162:163], v214 offset:36992\n\t"
      "v_mfma_f32_16x16x32_bf16 v[84:87], a[124:127], v[148:151], v[84:87]\n\t"
      "ds_read_b64_tr_b16 a[166:167], v214 offset:37000\n\t"
      "v_mfma_f32_16x16x32_bf16 v[88:91], a[128:131], v[148:151], v[88:91]\n\t"
      "buffer_load_dwordx4 v213, s[16:19], s51 offen offset:2048 lds\n\t"
      "v_mfma_f32_16x16x32_bf16 v[92:95], a[132:135], v[148:151], v[92:95]\n\t"
      "v_mfma_f32_16x16x32_bf16 v[96:99], a[136:139], v[148:151], v[96:99]\n\t"
      "ds_read_b128 v[160:163], v199 offset:12544\n\t"
      "v_mfma_f32_16x16x32_bf16 v[100:103], a[140:143], v[148:151], v[100:103]\n\t"
      "s_waitcnt lgkmcnt(8)\n\t"
      "v_mfma_f32_16x16x32_bf16 v[104:107], a[120:123], v[152:155], v[104:107]\n\t"
      "v_mfma_f32_16x16x32_bf16 v[108:111], a[124:127], v[152:155], v[108:111]\n\t"
      "v_mfma_f32_16x16x32_bf16 v[112:115], a[128:131], v[152:155], v[112:115]\n\t"
      "s_waitcnt vmcnt(15)\n\t"
      "ds_read_b64_tr_b16 a[168:169], v214 offset:49152\n\t"
      "s_waitcnt lgkmcnt(2)\n\t"
      "s_add_u32 m0, s41, 0x1b000\n\t"
      "s_mov_b32 s51, 0x3c000\n\t"
      "buffer_load_dwordx4 v211, s[16:19], s51 offen lds\n\t"
      "v_mfma_f32_16x16x32_bf16 v[116:119], a[132:135], v[152:155], v[116:119]\n\t"
      "ds_read_b64_tr_b16 a[172:173], v214 offset:49160\n\t"
      "v_mfma_f32_16x16x32_bf16 v[120:123], a[136:139], v[152:155], v[120:123]\n\t"
      "ds_read_b64_tr_b16 a[176:177], v214 offset:49216\n\t"
      "ds_read_b128 v[128:131], v199 offset:24832\n\t"
      "v_mfma_f32_16x16x32_bf16 v[132:135], a[140:143], v[152:155], v[132:135]\n\t"
      "ds_read_b64_tr_b16 a[180:181], v214 offset:49224\n\t"
      "buffer_load_dwordx4 v212, s[16:19], s51 offen offset:1024 lds\n\t"
      "v_mfma_f32_16x16x32_bf16 v[32:35], a[144:147], v[156:159], v[32:35]\n\t"
      "ds_read_b64_tr_b16 a[184:185], v214 offset:49280\n\t"
      "v_mfma_f32_16x16x32_bf16 v[36:39], a[148:151], v[156:159], v[36:39]\n\t"
      "ds_read_b64_tr_b16 a[188:189], v214 offset:49288\n\t"
      "v_mfma_f32_16x16x32_bf16 v[40:43], a[152:155], v[156:159], v[40:43]\n\t"
      "buffer_load_dwordx4 v213, s[16:19], s51 offen offset:2048 lds\n\t"
      "v_mfma_f32_16x16x32_bf16 v[44:47], a[156:159], v[156:159], v[44:47]\n\t"
      "v_mfma_f32_16x16x32_bf16 v[48:51], a[160:163], v[156:159], v[48:51]\n\t"
      "ds_read_b128 v[136:139], v199 offset:37120\n\t"
      "v_mfma_f32_16x16x32_bf16 v[52:55], a[164:167], v[156:159], v[52:55]\n\t"
      "s_waitcnt lgkmcnt(8)\n\t"
      "v_mfma_f32_16x16x32_bf16 v[56:59], a[144:147], v[160:163], v[56:59]\n\t"
      "v_mfma_f32_16x16x32_bf16 v[60:63], a[148:151], v[160:163], v[60:63]\n\t"
      "v_mfma_f32_16x16x32_bf16 v[64:67], a[152:155], v[160:163], v[64:67]\n\t"
      "s_waitcnt vmcnt(15)\n\t"
      "ds_read_b64_tr_b16 a[170:171], v221 offset:0\n\t"
      "s_waitcnt lgkmcnt(2)\n\t"
      "s_add_u32 m0, s41, 0x1e000\n\t"
      "s_mov_b32 s51, 0x3cc00\n\t"
      "buffer_load_dwordx4 v211, s[16:19], s51 offen lds\n\t"
      "v_mfma_f32_16x16x32_bf16 v[68:71], a[156:159], v[160:163], v[68:71]\n\t"
      "ds_read_b64_tr_b16 a[174:175], v221 offset:8\n\t"
      "v_mfma_f32_16x16x32_bf16 v[72:75], a[160:163], v[160:163], v[72:75]\n\t"
      "ds_read_b64_tr_b16 a[178:179], v221 offset:64\n\t"
      "ds_read_b128 v[140:143], v196 offset:256\n\t"
      "v_mfma_f32_16x16x32_bf16 v[76:79], a[164:167], v[160:163], v[76:79]\n\t"
      "ds_read_b64_tr_b16 a[182:183], v221 offset:72\n\t"
      "buffer_load_dwordx4 v212, s[16:19], s51 offen offset:1024 lds\n\t"
      "v_mfma_f32_16x16x32_bf16 v[80:83], a[144:147], v[128:131], v[80:83]\n\t"
      "ds_read_b64_tr_b16 a[186:187], v221 offset:128\n\t"
      "v_mfma_f32_16x16x32_bf16 v[84:87], a[148:151], v[128:131], v[84:87]\n\t"
      "ds_read_b64_tr_b16 a[190:191], v221 offset:136\n\t"
      "v_mfma_f32_16x16x32_bf16 v[88:91], a[152:155], v[128:131], v[88:91]\n\t"
      "buffer_load_dwordx4 v213, s[16:19], s51 offen offset:2048 lds\n\t"
      "v_mfma_f32_16x16x32_bf16 v[92:95], a[156:159], v[128:131], v[92:95]\n\t"
      "v_mfma_f32_16x16x32_bf16 v[96:99], a[160:163], v[128:131], v[96:99]\n\t"
      "ds_read_b128 v[144:147], v196 offset:12544\n\t"
      "v_mfma_f32_16x16x32_bf16 v[100:103], a[164:167], v[128:131], v[100:103]\n\t"
      "s_waitcnt lgkmcnt(8)\n\t"
      "v_mfma_f32_16x16x32_bf16 v[104:107], a[144:147], v[136:139], v[104:107]\n\t"
      "v_mfma_f32_16x16x32_bf16 v[108:111], a[148:151], v[136:139], v[108:111]\n\t"
      "v_mfma_f32_16x16x32_bf16 v[112:115], a[152:155], v[136:139], v[112:115]\n\t"
      "s_waitcnt vmcnt(15)\n\t"
      "ds_read_b64_tr_b16 a[192:193], v221 offset:12288\n\t"
      "s_waitcnt lgkmcnt(2)\n\t"
      "s_add_u32 m0, s41, 0x21000\n\t"
      "s_mov_b32 s51, 0x42000\n\t"
      "buffer_load_dwordx4 v211, s[16:19], s51 offen lds\n\t"
      "v_mfma_f32_16x16x32_bf16 v[116:119], a[156:159], v[136:139], v[116:119]\n\t"
      "ds_read_b64_tr_b16 a[196:197], v221 offset:12296\n\t"
      "v_mfma_f32_16x16x32_bf16 v[120:123], a[160:163], v[136:139], v[120:123]\n\t"
      "ds_read_b64_tr_b16 a[200:201], v221 offset:12352\n\t"
      "ds_read_b128 v[148:151], v196 offset:24832\n\t"
      "v_mfma_f32_16x16x32_bf16 v[132:135], a[164:167], v[136:139], v[132:135]\n\t"
      "ds_read_b64_tr_b16 a[204:205], v221 offset:12360\n\t"
      "buffer_load_dwordx4 v212, s[16:19], s51 offen offset:1024 lds\n\t"
      "v_mfma_f32_16x16x32_bf16 v[32:35], a[168:171], v[140:143], v[32:35]\n\t"
      "ds_read_b64_tr_b16 a[208:209], v221 offset:12416\n\t"
      "v_mfma_f32_16x16x32_bf16 v[36:39], a[172:175], v[140:143], v[36:39]\n\t"
      "ds_read_b64_tr_b16 a[212:213], v221 offset:12424\n\t"
      "v_mfma_f32_16x16x32_bf16 v[40:43], a[176:179], v[140:143], v[40:43]\n\t"
      "buffer_load_dwordx4 v213, s[16:19], s51 offen offset:2048 lds\n\t"
      "v_mfma_f32_16x16x32_bf16 v[44:47], a[180:183], v[140:143], v[44:47]\n\t"
      "v_mfma_f32_16x16x32_bf16 v[48:51], a[184:187], v[140:143], v[48:51]\n\t"
      "ds_read_b128 v[152:155], v196 offset:37120\n\t"
      "v_mfma_f32_16x16x32_bf16 v[52:55], a[188:191], v[140:143], v[52:55]\n\t"
      "s_waitcnt lgkmcnt(8)\n\t"
      "v_mfma_f32_16x16x32_bf16 v[56:59], a[168:171], v[144:147], v[56:59]\n\t"
      "v_mfma_f32_16x16x32_bf16 v[60:63], a[172:175], v[144:147], v[60:63]\n\t"
      "v_mfma_f32_16x16x32_bf16 v[64:67], a[176:179], v[144:147], v[64:67]\n\t"
      "s_waitcnt vmcnt(15)\n\t"
      "ds_read_b64_tr_b16 a[194:195], v214 offset:0\n\t"
      "s_waitcnt lgkmcnt(2)\n\t"
      "s_add_u32 m0, s41, 0x24000\n\t"
      "s_mov_b32 s51, 0x42c00\n\t"
      "buffer_load_dwordx4 v211, s[16:19], s51 offen lds\n\t"
      "v_mfma_f32_16x16x32_bf16 v[68:71], a[180:183], v[144:147], v[68:71]\n\t"
      "ds_read_b64_tr_b16 a[198:199], v214 offset:8\n\t"
      "v_mfma_f32_16x16x32_bf16 v[72:75], a[184:187], v[144:147], v[72:75]\n\t"
      "ds_read_b64_tr_b16 a[202:203], v214 offset:64\n\t"
      "ds_read_b128 v[156:159], v198 offset:512\n\t"
      "v_mfma_f32_16x16x32_bf16 v[76:79], a[188:191], v[144:147], v[76:79]\n\t"
      "ds_read_b64_tr_b16 a[206:207], v214 offset:72\n\t"
      "buffer_load_dwordx4 v212, s[16:19], s51 offen offset:1024 lds\n\t"
      "v_mfma_f32_16x16x32_bf16 v[80:83], a[168:171], v[148:151], v[80:83]\n\t"
      "ds_read_b64_tr_b16 a[210:211], v214 offset:128\n\t"
      "v_mfma_f32_16x16x32_bf16 v[84:87], a[172:175], v[148:151], v[84:87]\n\t"
      "ds_read_b64_tr_b16 a[214:215], v214 offset:136\n\t"
      "v_mfma_f32_16x16x32_bf16 v[88:91], a[176:179], v[148:151], v[88:91]\n\t"
      "buffer_load_dwordx4 v213, s[16:19], s51 offen offset:2048 lds\n\t"
      "v_mfma_f32_16x16x32_bf16 v[92:95], a[180:183], v[148:151], v[92:95]\n\t"
      "v_mfma_f32_16x16x32_bf16 v[96:99], a[184:187], v[148:151], v[96:99]\n\t"
      "ds_read_b128 v[160:163], v198 offset:12800\n\t"
      "v_mfma_f32_16x16x32_bf16 v[100:103], a[188:191], v[148:151], v[100:103]\n\t"
      "s_waitcnt lgkmcnt(8)\n\t"
      "v_mfma_f32_16x16x32_bf16 v[104:107], a[168:171], v[152:155], v[104:107]\n\t"
      "s_add_u32 m0, s41, 0xc000\n\t"
      "s_add_u32 s51, s40, 0xc00000\n\t"
      "buffer_load_dwordx4 v208, s[20:23], s51 offen lds\n\t"
      "v_mfma_f32_16x16x32_bf16 v[108:111], a[172:175], v[152:155], v[108:111]\n\t"
      "v_mfma_f32_16x16x32_bf16 v[112:115], a[176:179], v[152:155], v[112:115]\n\t"
      "s_waitcnt vmcnt(16)\n\t"
      "ds_read_b64_tr_b16 a[216:217], v214 offset:12288\n\t"
      "buffer_load_dwordx4 v208, s[20:23], s51 offen offset:1024 lds\n\t"
      "v_mfma_f32_16x16x32_bf16 v[116:119], a[180:183], v[152:155], v[116:119]\n\t"
      "ds_read_b64_tr_b16 a[220:221], v214 offset:12296\n\t"
      "v_mfma_f32_16x16x32_bf16 v[120:123], a[184:187], v[152:155], v[120:123]\n\t"
      "ds_read_b64_tr_b16 a[224:225], v214 offset:12352\n\t"
      "ds_read_b128 v[128:131], v198 offset:25088\n\t"
      "buffer_load_dwordx4 v208, s[20:23], s51 offen offset:2048 lds\n\t"
      "v_mfma_f32_16x16x32_bf16 v[132:135], a[188:191], v[152:155], v[132:135]\n\t"
      "ds_read_b64_tr_b16 a[228:229], v214 offset:12360\n\t"
      "s_waitcnt lgkmcnt(6)\n\t"
      "v_mfma_f32_16x16x32_bf16 v[32:35], a[192:195], v[156:159], v[32:35]\n\t"
      "ds_read_b64_tr_b16 a[232:233], v214 offset:12416\n\t"
      "s_add_u32 m0, s41, 0xf000\n\t"
      "s_add_u32 s51, s40, 0xc00000\n\t"
      "buffer_load_dwordx4 v209, s[20:23], s51 offen lds\n\t"
      "v_mfma_f32_16x16x32_bf16 v[36:39], a[196:199], v[156:159], v[36:39]\n\t"
      "ds_read_b64_tr_b16 a[236:237], v214 offset:12424\n\t"
      "v_mfma_f32_16x16x32_bf16 v[40:43], a[200:203], v[156:159], v[40:43]\n\t"
      "buffer_load_dwordx4 v209, s[20:23], s51 offen offset:1024 lds\n\t"
      "v_mfma_f32_16x16x32_bf16 v[44:47], a[204:207], v[156:159], v[44:47]\n\t"
      "v_mfma_f32_16x16x32_bf16 v[48:51], a[208:211], v[156:159], v[48:51]\n\t"
      "ds_read_b128 v[136:139], v198 offset:37376\n\t"
      "buffer_load_dwordx4 v209, s[20:23], s51 offen offset:2048 lds\n\t"
      "v_mfma_f32_16x16x32_bf16 v[52:55], a[212:215], v[156:159], v[52:55]\n\t"
      "s_waitcnt lgkmcnt(8)\n\t"
      "v_mfma_f32_16x16x32_bf16 v[56:59], a[192:195], v[160:163], v[56:59]\n\t"
      "v_mfma_f32_16x16x32_bf16 v[60:63], a[196:199], v[160:163], v[60:63]\n\t"
      "v_mfma_f32_16x16x32_bf16 v[64:67], a[200:203], v[160:163], v[64:67]\n\t"
      "s_waitcnt vmcnt(18)\n\t"
      "ds_read_b64_tr_b16 a[218:219], v214 offset:24576\n\t"
      "v_mfma_f32_16x16x32_bf16 v[68:71], a[204:207], v[160:163], v[68:71]\n\t"
      "ds_read_b64_tr_b16 a[222:223], v214 offset:24584\n\t"
      "v_mfma_f32_16x16x32_bf16 v[72:75], a[208:211], v[160:163], v[72:75]\n\t"
      "ds_read_b64_tr_b16 a[226:227], v214 offset:24640\n\t"
      "ds_read_b128 v[140:143], v199 offset:512\n\t"
      "v_mfma_f32_16x16x32_bf16 v[76:79], a[212:215], v[160:163], v[76:79]\n\t"
      "ds_read_b64_tr_b16 a[230:231], v214 offset:24648\n\t"
      "s_waitcnt lgkmcnt(9)\n\t"
      "v_mfma_f32_16x16x32_bf16 v[80:83], a[192:195], v[128:131], v[80:83]\n\t"
      "ds_read_b64_tr_b16 a[234:235], v214 offset:24704\n\t"
      "v_mfma_f32_16x16x32_bf16 v[84:87], a[196:199], v[128:131], v[84:87]\n\t"
      "ds_read_b64_tr_b16 a[238:239], v214 offset:24712\n\t"
      "v_mfma_f32_16x16x32_bf16 v[88:91], a[200:203], v[128:131], v[88:91]\n\t"
      "v_mfma_f32_16x16x32_bf16 v[92:95], a[204:207], v[128:131], v[92:95]\n\t"
      "v_mfma_f32_16x16x32_bf16 v[96:99], a[208:211], v[128:131], v[96:99]\n\t"
      "ds_read_b128 v[144:147], v199 offset:12800\n\t"
      "v_mfma_f32_16x16x32_bf16 v[100:103], a[212:215], v[128:131], v[100:103]\n\t"
      "s_waitcnt lgkmcnt(8)\n\t"
      "v_mfma_f32_16x16x32_bf16 v[104:107], a[192:195], v[136:139], v[104:107]\n\t"
      "v_mfma_f32_16x16x32_bf16 v[108:111], a[196:199], v[136:139], v[108:111]\n\t"
      "v_mfma_f32_16x16x32_bf16 v[112:115], a[200:203], v[136:139], v[112:115]\n\t"
      "s_waitcnt vmcnt(15)\n\t"
      "ds_read_b64_tr_b16 a[240:241], v214 offset:36864\n\t"
      "v_mfma_f32_16x16x32_bf16 v[116:119], a[204:207], v[136:139], v[116:119]\n\t"
      "ds_read_b64_tr_b16 a[244:245], v214 offset:36872\n\t"
      "v_mfma_f32_16x16x32_bf16 v[120:123], a[208:211], v[136:139], v[120:123]\n\t"
      "ds_read_b64_tr_b16 a[248:249], v214 offset:36928\n\t"
      "ds_read_b128 v[148:151], v199 offset:25088\n\t"
      "v_mfma_f32_16x16x32_bf16 v[132:135], a[212:215], v[136:139], v[132:135]\n\t"
      "ds_read_b64_tr_b16 a[252:253], v214 offset:36936\n\t"
      "s_waitcnt lgkmcnt(6)\n\t"
      "v_mfma_f32_16x16x32_bf16 v[32:35], a[216:219], v[140:143], v[32:35]\n\t"
      "ds_read_b64_tr_b16 v[164:165], v214 offset:36992\n\t"
      "v_mfma_f32_16x16x32_bf16 v[36:39], a[220:223], v[140:143], v[36:39]\n\t"
      "ds_read_b64_tr_b16 v[168:169], v214 offset:37000\n\t"
      "v_mfma_f32_16x16x32_bf16 v[40:43], a[224:227], v[140:143], v[40:43]\n\t"
      "s_waitcnt vmcnt(12)\n\t"
      "ds_read_b64_tr_b16 a[242:243], v214 offset:49152\n\t"
      "v_mfma_f32_16x16x32_bf16 v[44:47], a[228:231], v[140:143], v[44:47]\n\t"
      "ds_read_b64_tr_b16 a[246:247], v214 offset:49160\n\t"
      "v_mfma_f32_16x16x32_bf16 v[48:51], a[232:235], v[140:143], v[48:51]\n\t"
      "ds_read_b64_tr_b16 a[250:251], v214 offset:49216\n\t"
      "ds_read_b128 v[152:155], v199 offset:37376\n\t"
      "v_mfma_f32_16x16x32_bf16 v[52:55], a[236:239], v[140:143], v[52:55]\n\t"
      "ds_read_b64_tr_b16 a[254:255], v214 offset:49224\n\t"
      "s_waitcnt lgkmcnt(12)\n\t"
      "v_mfma_f32_16x16x32_bf16 v[56:59], a[216:219], v[144:147], v[56:59]\n\t"
      "ds_read_b64_tr_b16 v[166:167], v214 offset:49280\n\t"
      "v_mfma_f32_16x16x32_bf16 v[60:63], a[220:223], v[144:147], v[60:63]\n\t"
      "ds_read_b64_tr_b16 v[170:171], v214 offset:49288\n\t"
      "v_mfma_f32_16x16x32_bf16 v[64:67], a[224:227], v[144:147], v[64:67]\n\t"
      "s_waitcnt vmcnt(9)\n\t"
      "ds_read_b64_tr_b16 v[172:173], v221 offset:0\n\t"
      "v_mfma_f32_16x16x32_bf16 v[68:71], a[228:231], v[144:147], v[68:71]\n\t"
      "ds_read_b64_tr_b16 v[176:177], v221 offset:8\n\t"
      "v_mfma_f32_16x16x32_bf16 v[72:75], a[232:235], v[144:147], v[72:75]\n\t"
      "ds_read_b64_tr_b16 v[180:181], v221 offset:64\n\t"
      "ds_read_b128 v[156:159], v196 offset:512\n\t"
      "v_mfma_f32_16x16x32_bf16 v[76:79], a[236:239], v[144:147], v[76:79]\n\t"
      "ds_read_b64_tr_b16 v[184:185], v221 offset:72\n\t"
      "s_waitcnt lgkmcnt(14)\n\t"
      "v_mfma_f32_16x16x32_bf16 v[80:83], a[216:219], v[148:151], v[80:83]\n\t"
      "ds_read_b64_tr_b16 v[188:189], v221 offset:128\n\t"
      "v_mfma_f32_16x16x32_bf16 v[84:87], a[220:223], v[148:151], v[84:87]\n\t"
      "ds_read_b64_tr_b16 v[192:193], v221 offset:136\n\t"
      "v_mfma_f32_16x16x32_bf16 v[88:91], a[224:227], v[148:151], v[88:91]\n\t"
      "v_mfma_f32_16x16x32_bf16 v[92:95], a[228:231], v[148:151], v[92:95]\n\t"
      "v_mfma_f32_16x16x32_bf16 v[96:99], a[232:235], v[148:151], v[96:99]\n\t"
      "ds_read_b128 v[160:163], v196 offset:12800\n\t"
      "v_mfma_f32_16x16x32_bf16 v[100:103], a[236:239], v[148:151], v[100:103]\n\t"
      "s_waitcnt lgkmcnt(11)\n\t"
      "v_mfma_f32_16x16x32_bf16 v[104:107], a[216:219], v[152:155], v[104:107]\n\t"
      "v_mfma_f32_16x16x32_bf16 v[108:111], a[220:223], v[152:155], v[108:111]\n\t"
      "v_mfma_f32_16x16x32_bf16 v[112:115], a[224:227], v[152:155], v[112:115]\n\t"
      "s_waitcnt vmcnt(6)\n\t"
      "ds_read_b64_tr_b16 v[174:175], v221 offset:12288\n\t"
      "buffer_load_dwordx2 v[240:241], v245, s[24:27], 0 offen sc1\n\t"
      "v_mfma_f32_16x16x32_bf16 v[116:119], a[228:231], v[152:155], v[116:119]\n\t"
      "ds_read_b64_tr_b16 v[178:179], v221 offset:12296\n\t"
      "s_cmp_eq_u32 s41, s43\n\t"
      "s_cbranch_scc0 .Lc4_%=\n\t"
      "buffer_load_dword v254, v253, s[24:27], 0 offen sc1\n\t"
      ".Lc4_%=:\n\t"
      "v_mfma_f32_16x16x32_bf16 v[120:123], a[232:235], v[152:155], v[120:123]\n\t"
      "ds_read_b64_tr_b16 v[182:183], v221 offset:12352\n\t"
      "ds_read_b128 v[140:143], v197 offset:512\n\t"
      "v_mfma_f32_16x16x32_bf16 v[132:135], a[236:239], v[152:155], v[132:135]\n\t"
      "ds_read_b64_tr_b16 v[186:187], v221 offset:12360\n\t"
      "s_waitcnt lgkmcnt(9)\n\t"
      "v_mfma_f32_16x16x32_bf16 v[32:35], a[240:243], v[156:159], v[32:35]\n\t"
      "ds_read_b64_tr_b16 v[190:191], v221 offset:12416\n\t"
      "v_mfma_f32_16x16x32_bf16 v[36:39], a[244:247], v[156:159], v[36:39]\n\t"
      "ds_read_b64_tr_b16 v[194:195], v221 offset:12424\n\t"
      "v_mfma_f32_16x16x32_bf16 v[40:43], a[248:251], v[156:159], v[40:43]\n\t"
      "v_mfma_f32_16x16x32_bf16 v[44:47], a[252:255], v[156:159], v[44:47]\n\t"
      "v_mfma_f32_16x16x32_bf16 v[48:51], v[164:167], v[156:159], v[48:51]\n\t"
      "ds_read_b128 v[144:147], v197 offset:12800\n\t"
      "v_mfma_f32_16x16x32_bf16 v[52:55], v[168:171], v[156:159], v[52:55]\n\t"
      "s_waitcnt lgkmcnt(8)\n\t"
      "v_mfma_f32_16x16x32_bf16 v[56:59], a[240:243], v[160:163], v[56:59]\n\t"
      "v_mfma_f32_16x16x32_bf16 v[60:63], a[244:247], v[160:163], v[60:63]\n\t"
      "v_mfma_f32_16x16x32_bf16 v[64:67], a[248:251], v[160:163], v[64:67]\n\t"
      "v_mfma_f32_16x16x32_bf16 v[68:71], a[252:255], v[160:163], v[68:71]\n\t"
      "v_mfma_f32_16x16x32_bf16 v[72:75], v[164:167], v[160:163], v[72:75]\n\t"
      "ds_read_b128 v[128:131], v196 offset:25088\n\t"
      "v_mfma_f32_16x16x32_bf16 v[76:79], v[168:171], v[160:163], v[76:79]\n\t"
      "s_waitcnt lgkmcnt(2)\n\t"
      "v_mfma_f32_16x16x32_bf16 v[32:35], v[172:175], v[140:143], v[32:35]\n\t"
      "v_mfma_f32_16x16x32_bf16 v[36:39], v[176:179], v[140:143], v[36:39]\n\t"
      "v_mfma_f32_16x16x32_bf16 v[40:43], v[180:183], v[140:143], v[40:43]\n\t"
      "v_mfma_f32_16x16x32_bf16 v[44:47], v[184:187], v[140:143], v[44:47]\n\t"
      "v_mfma_f32_16x16x32_bf16 v[48:51], v[188:191], v[140:143], v[48:51]\n\t"
      "ds_read_b128 v[136:139], v196 offset:37376\n\t"
      "v_mfma_f32_16x16x32_bf16 v[52:55], v[192:195], v[140:143], v[52:55]\n\t"
      "s_waitcnt lgkmcnt(2)\n\t"
      "v_mfma_f32_16x16x32_bf16 v[56:59], v[172:175], v[144:147], v[56:59]\n\t"
      "v_mfma_f32_16x16x32_bf16 v[60:63], v[176:179], v[144:147], v[60:63]\n\t"
      "s_waitcnt vmcnt(4) lgkmcnt(2)\n\t"
      "s_barrier\n\t"
      "v_mfma_f32_16x16x32_bf16 v[64:67], v[180:183], v[144:147], v[64:67]\n\t"
      "v_cvt_pk_bf16_f32 v32, v32, v33\n\t"
      "v_cvt_pk_bf16_f32 v33, v34, v35\n\t"
      "v_mfma_f32_16x16x32_bf16 v[68:71], v[184:187], v[144:147], v[68:71]\n\t"
      "v_cvt_pk_bf16_f32 v34, v36, v37\n\t"
      "v_cvt_pk_bf16_f32 v35, v38, v39\n\t"
      "v_mfma_f32_16x16x32_bf16 v[72:75], v[188:191], v[144:147], v[72:75]\n\t"
      "ds_read_b128 v[148:151], v197 offset:25088\n\t"
      "ds_write_b128 v217, v[32:35] offset:24576\n\t"
      "v_cvt_pk_bf16_f32 v40, v40, v41\n\t"
      "v_mfma_f32_16x16x32_bf16 v[76:79], v[192:195], v[144:147], v[76:79]\n\t"
      "v_cvt_pk_bf16_f32 v41, v42, v43\n\t"
      "v_cvt_pk_bf16_f32 v42, v44, v45\n\t"
      "s_mov_b32 m0, s41\n\t"
      "s_add_u32 s51, s40, 0x1200000\n\t"
      "buffer_load_dwordx4 v208, s[20:23], s51 offen lds\n\t"
      "s_waitcnt lgkmcnt(3)\n\t"
      "v_mfma_f32_16x16x32_bf16 v[80:83], a[240:243], v[128:131], v[80:83]\n\t"
      "v_cvt_pk_bf16_f32 v43, v46, v47\n\t"
      "ds_write_b128 v218, v[40:43] offset:24576\n\t"
      "ds_read_b128 v[32:35], v196 offset:49152\n\t"
      "v_mfma_f32_16x16x32_bf16 v[84:87], a[244:247], v[128:131], v[84:87]\n\t"
      "v_cvt_pk_bf16_f32 v48, v48, v49\n\t"
      "v_cvt_pk_bf16_f32 v49, v50, v51\n\t"
      "v_mfma_f32_16x16x32_bf16 v[88:91], a[248:251], v[128:131], v[88:91]\n\t"
      "v_cvt_pk_bf16_f32 v50, v52, v53\n\t"
      "v_cvt_pk_bf16_f32 v51, v54, v55\n\t"
      "buffer_load_dwordx4 v208, s[20:23], s51 offen offset:1024 lds\n\t"
      "v_mfma_f32_16x16x32_bf16 v[92:95], a[252:255], v[128:131], v[92:95]\n\t"
      "ds_write_b128 v219, v[48:51] offset:24576\n\t"
      "ds_read_b128 v[36:39], v197 offset:49152\n\t"
      "v_cvt_pk_bf16_f32 v56, v56, v57\n\t"
      "v_mfma_f32_16x16x32_bf16 v[96:99], v[164:167], v[128:131], v[96:99]\n\t"
      "ds_read_b128 v[152:155], v197 offset:37376\n\t"
      "v_cvt_pk_bf16_f32 v57, v58, v59\n\t"
      "v_cvt_pk_bf16_f32 v58, v60, v61\n\t"
      "v_mfma_f32_16x16x32_bf16 v[100:103], v[168:171], v[128:131], v[100:103]\n\t"
      "v_cvt_pk_bf16_f32 v59, v62, v63\n\t"
      "ds_write_b128 v217, v[56:59] offset:36864\n\t"
      "ds_read_b128 v[40:43], v198 offset:49152\n\t"
      "s_waitcnt lgkmcnt(9)\n\t"
      "v_mfma_f32_16x16x32_bf16 v[104:107], a[240:243], v[136:139], v[104:107]\n\t"
      "v_cvt_pk_bf16_f32 v64, v64, v65\n\t"
      "v_cvt_pk_bf16_f32 v65, v66, v67\n\t"
      "buffer_load_dwordx4 v208, s[20:23], s51 offen offset:2048 lds\n\t"
      "v_mfma_f32_16x16x32_bf16 v[108:111], a[244:247], v[136:139], v[108:111]\n\t"
      "v_cvt_pk_bf16_f32 v66, v68, v69\n\t"
      "v_cvt_pk_bf16_f32 v67, v70, v71\n\t"
      "v_mfma_f32_16x16x32_bf16 v[112:115], a[248:251], v[136:139], v[112:115]\n\t"
      "ds_write_b128 v218, v[64:67] offset:36864\n\t"
      "ds_read_b128 v[44:47], v199 offset:49152\n\t"
      "v_cvt_pk_bf16_f32 v72, v72, v73\n\t"
      "v_mfma_f32_16x16x32_bf16 v[116:119], a[252:255], v[136:139], v[116:119]\n\t"
      "v_cvt_pk_bf16_f32 v73, v74, v75\n\t"
      "v_cvt_pk_bf16_f32 v74, v76, v77\n\t"
      "v_mfma_f32_16x16x32_bf16 v[120:123], v[164:167], v[136:139], v[120:123]\n\t"
      "v_cvt_pk_bf16_f32 v75, v78, v79\n\t"
      "ds_write_b128 v219, v[72:75] offset:36864\n\t"
      "ds_read_b128 v[48:51], v197 offset:49408\n\t"
      "v_mfma_f32_16x16x32_bf16 v[132:135], v[168:171], v[136:139], v[132:135]\n\t"
      "s_add_u32 m0, s41, 0x3000\n\t"
      "s_add_u32 s51, s40, 0x1200000\n\t"
      "buffer_load_dwordx4 v209, s[20:23], s51 offen lds\n\t"
      "ds_read_b128 v[52:55], v198 offset:49408\n\t"
      "s_waitcnt lgkmcnt(13)\n\t"
      "v_mfma_f32_16x16x32_bf16 v[80:83], v[172:175], v[148:151], v[80:83]\n\t"
      "ds_read_b128 v[56:59], v199 offset:49408\n\t"
      "ds_read_b128 v[60:63], v196 offset:49408\n\t"
      "v_mfma_f32_16x16x32_bf16 v[84:87], v[176:179], v[148:151], v[84:87]\n\t"
      "ds_read_b128 v[64:67], v198 offset:49664\n\t"
      "s_memrealtime s[80:81]\n\t"
      "v_mfma_f32_16x16x32_bf16 v[88:91], v[180:183], v[148:151], v[88:91]\n\t"
      "buffer_load_dwordx4 v209, s[20:23], s51 offen offset:1024 lds\n\t"
      "ds_read_b128 v[68:71], v199 offset:49664\n\t"
      "v_mfma_f32_16x16x32_bf16 v[92:95], v[184:187], v[148:151], v[92:95]\n\t"
      "ds_read_b128 v[72:75], v196 offset:49664\n\t"
      "ds_read_b128 v[76:79], v197 offset:49664\n\t"
      "v_mfma_f32_16x16x32_bf16 v[96:99], v[188:191], v[148:151], v[96:99]\n\t"
      "v_mfma_f32_16x16x32_bf16 v[100:103], v[192:195], v[148:151], v[100:103]\n\t"
      "buffer_load_dwordx4 v209, s[20:23], s51 offen offset:2048 lds\n\t"
      "s_waitcnt lgkmcnt(13)\n\t"
      "v_mfma_f32_16x16x32_bf16 v[104:107], v[172:175], v[152:155], v[104:107]\n\t"
      "v_mfma_f32_16x16x32_bf16 v[108:111], v[176:179], v[152:155], v[108:111]\n\t"
      "v_mfma_f32_16x16x32_bf16 v[112:115], v[180:183], v[152:155], v[112:115]\n\t"
      "v_mfma_f32_16x16x32_bf16 v[116:119], v[184:187], v[152:155], v[116:119]\n\t"
      "v_mfma_f32_16x16x32_bf16 v[120:123], v[188:191], v[152:155], v[120:123]\n\t"
      "v_mfma_f32_16x16x32_bf16 v[132:135], v[192:195], v[152:155], v[132:135]\n\t"
      "v_add_u32_e32 v196, s46, v200\n\t"
      "v_add_u32_e32 v197, s46, v201\n\t"
      "v_add_u32_e32 v198, s46, v202\n\t"
      "v_add_u32_e32 v199, s46, v203\n\t"
      "s_mov_b32 s86, 77\n\t"
      "s_mov_b32 s84, 96\n\t"
      "s_branch .Lp2_%=\n\t"
      ".Lend_%=:\n\t"
      ".Lconv_end:\n\t"
      :
      : "{s[16:31]}"(d16), "{s[36:39]}"(db), "{s[52:53]}"(rs), "{s40}"(t0), "{s41}"(p0), "{s43}"(lds0), "{s94}"(ty), "{s95}"(tres), "{s[60:61]}"(pres),
      "{s69}"(pre1), "{s74}"(plm), "{s75}"(plb), "{s66}"(sst ? 24576u : 0u - 24576u), "{s89}"((blk >> 4) % 5u),
      "{s71}"(wv == 0u ? 568645u + ((blk >> 4) % 5u) * 1057u : ty ? 17733u + ((blk >> 4) % 5u) * 33u : 17u), "{s93}"((0x3fff0u << ty) & 0x3ffffu),
      "{s92}"((0x3ffffu << (5u + ((((blk >> 4) * 5u + (blk & 3u) * 4u) & 15u) * 11u >> 4))) & 0x3ffffu), "{v208}"(ga), "{v209}"(gb), "{v211}"(woff[0]),
      "{v212}"(woff[1]), "{v213}"(woff[2]), "{v248}"(ta), "{v249}"(ta), "{v250}"(tp), "{v251}"(t1), "{v252}"(mk), "{v245}"(vgv), "{v246}"(vxw),
      "{v247}"(vxr), "{v253}"(vcell), "{v200}"(cfa[0]), "{v201}"(cfa[1]), "{v202}"(cfa[2]), "{v203}"(cfa[3]), "{v204}"(csw[0]), "{v205}"(csw[1]),
      "{v206}"(csw[2]), "{v207}"(csr), "{v214}"(tbase + 73728u), "{v221}"(tbase + 135168u), "{v[226:229]}"(br0), "{v[230:233]}"(br1),
      "{v[234:237]}"(br2), "{v6}"(cpo)
      : "memory", "scc", "vcc",
      "s42", "s44", "s45", "s46", "s47", "s48", "s49", "s50", "s51", "s54", "s55", "s56", "s57", "s58", "s59", "s62", "s63", "s64", "s65", "s67",
      "s68", "s70", "s72", "s73", "s76", "s77", "s78", "s79", "s80", "s81", "s82", "s83", "s84", "s85", "s86", "s87", "s88", "s90", "s91", "s96",
      "s97", "s98", "s99",
      "v1", "v2", "v3", "v4", "v5", "v7", "v8", "v9", "v10", "v11", "v12", "v13", "v14", "v15", "v16", "v17", "v18", "v19", "v20", "v21", "v22", "v23",
      "v24", "v25", "v26", "v27", "v28", "v29", "v30", "v31", "v32", "v33", "v34", "v35", "v36", "v37", "v38", "v39", "v40", "v41", "v42", "v43",
      "v44", "v45", "v46", "v47", "v48", "v49", "v50", "v51", "v52", "v53", "v54", "v55", "v56", "v57", "v58", "v59", "v60", "v61", "v62", "v63",
      "v64", "v65", "v66", "v67", "v68", "v69", "v70", "v71", "v72", "v73", "v74", "v75", "v76", "v77", "v78", "v79", "v80", "v81", "v82", "v83",
      "v84", "v85", "v86", "v87", "v88", "v89", "v90", "v91", "v92", "v93", "v94", "v95", "v96", "v97", "v98", "v99", "v100", "v101", "v102", "v103",
      "v104", "v105", "v106", "v107", "v108", "v109", "v110", "v111", "v112", "v113", "v114", "v115", "v116", "v117", "v118", "v119", "v120", "v121",
      "v122", "v123", "v124", "v125", "v126", "v127", "v128", "v129", "v130", "v131", "v132", "v133", "v134", "v135", "v136", "v137", "v138", "v139",
      "v140", "v141", "v142", "v143", "v144", "v145", "v146", "v147", "v148", "v149", "v150", "v151", "v152", "v153", "v154", "v155", "v156", "v157",
      "v158", "v159", "v160", "v161", "v162", "v163", "v164", "v165", "v166", "v167", "v168", "v169", "v170", "v171", "v172", "v173", "v174", "v175",
      "v176", "v177", "v178", "v179", "v180", "v181", "v182", "v183", "v184", "v185", "v186", "v187", "v188", "v189", "v190", "v191", "v192", "v193",
      "v194", "v195", "v196", "v197", "v198", "v199", "v210", "v215", "v216", "v217", "v218", "v219", "v220", "v222", "v223", "v224", "v225", "v238",
      "v239", "v240", "v241", "v242", "v243", "v244", "v254", "v255",
      "a0", "a1", "a2", "a3", "a4", "a5", "a6", "a7", "a8", "a9", "a10", "a11", "a12", "a13", "a14", "a15", "a16", "a17", "a18", "a19", "a20", "a21",
      "a22", "a23", "a24", "a25", "a26", "a27", "a28", "a29", "a30", "a31", "a32", "a33", "a34", "a35", "a36", "a37", "a38", "a39", "a40", "a41",
      "a42", "a43", "a44", "a45", "a46", "a47", "a48", "a49", "a50", "a51", "a52", "a53", "a54", "a55", "a56", "a57", "a58", "a59", "a60", "a61",
      "a62", "a63", "a64", "a65", "a66", "a67", "a68", "a69", "a70", "a71", "a72", "a73", "a74", "a75", "a76", "a77", "a78", "a79", "a80", "a81",
      "a82", "a83", "a84", "a85", "a86", "a87", "a88", "a89", "a90", "a91", "a92", "a93", "a94", "a95", "a96", "a97", "a98", "a99", "a100", "a101",
      "a102", "a103", "a104", "a105", "a106", "a107", "a108", "a109", "a110", "a111", "a112", "a113", "a114", "a115", "a116", "a117", "a118", "a119",
      "a120", "a121", "a122", "a123", "a124", "a125", "a126", "a127", "a128", "a129", "a130", "a131", "a132", "a133", "a134", "a135", "a136", "a137",
      "a138", "a139", "a140", "a141", "a142", "a143", "a144", "a145", "a146", "a147", "a148", "a149", "a150", "a151", "a152", "a153", "a154", "a155",
      "a156", "a157", "a158", "a159", "a160", "a161", "a162", "a163", "a164", "a165", "a166", "a167", "a168", "a169", "a170", "a171", "a172", "a173",
      "a174", "a175", "a176", "a177", "a178", "a179", "a180", "a181", "a182", "a183", "a184", "a185", "a186", "a187", "a188", "a189", "a190", "a191",
      "a192", "a193", "a194", "a195", "a196", "a197", "a198", "a199", "a200", "a201", "a202", "a203", "a204", "a205", "a206", "a207", "a208", "a209",
      "a210", "a211", "a212", "a213", "a214", "a215", "a216", "a217", "a218", "a219", "a220", "a221", "a222", "a223", "a224", "a225", "a226", "a227",
      "a228", "a229", "a230", "a231", "a232", "a233", "a234", "a235", "a236", "a237", "a238", "a239", "a240", "a241", "a242", "a243", "a244", "a245",
      "a246", "a247", "a248", "a249", "a250", "a251", "a252", "a253", "a254", "a255");
}

extern "C" void kernel_launch(void* const* d_in, const int* in_sizes, int n_in,
                              void* d_out, int out_size, void* d_ws, size_t ws_size,
                              hipStream_t stream) {
  conv1x1_kernel<<<256, 256, 0, stream>>>(d_in[1], 0x0002000000048000ull, d_in[0], 0x0002000006c00000ull, d_ws, 0x0002000000000c00ull, d_out, d_in[2]);
}
```

```cpp
#include <hip/hip_runtime.h>
#include <stdint.h>

#define LAS __attribute__((address_space(3)))
typedef __attribute__((ext_vector_type(16))) unsigned u32x16;
typedef __attribute__((ext_vector_type(4))) unsigned u32x4;
typedef unsigned long long u64;

__global__ __launch_bounds__(256, 1) void conv1x1_kernel(const void* w, u64 cw, const void* x, u64 cx, void* ws, u64 cs, void* out, const void* bias) {
  __shared__ __attribute__((aligned(1024))) unsigned char lds[160768];

  u32x16 d16;
  u32x4 db;
  u64 rs;
  asm volatile(
      "s_load_dwordx16 s[16:31], %3, 0x0\n\t"
      "s_memrealtime s[52:53]\n\t"
      "s_movk_i32 s38, 0x300\n\t"
      "s_mov_b32 s39, 0x20000\n\t"
      : "={s[16:31]}"(d16), "={s[36:39]}"(db), "={s[52:53]}"(rs)
      : "s"(__builtin_amdgcn_kernarg_segment_ptr())
      : "memory");
  unsigned cpo = ((blockIdx.x >> 3) * 256u + threadIdx.x) * 4u;
  asm volatile(
      "s_getpc_b64 s[92:93]\n\t"
      ".Lconv_cp:\n\t"
      "v_min_u32_e32 v6, .Lconv_end-.Lconv_cp-4, v6\n\t"
      "global_load_dword v6, v6, s[92:93]\n\t"
      : "+{v6}"(cpo)
      :
      : "memory", "s92", "s93");
  __builtin_amdgcn_sched_barrier(0);
  unsigned tid  = threadIdx.x;
  unsigned lane = tid & 63;
  unsigned wv   = __builtin_amdgcn_readfirstlane(tid >> 6);
  const unsigned lds0 = (unsigned)(size_t)(LAS unsigned char*)lds;
  unsigned blk  = blockIdx.x;
  const unsigned p0   = lds0 + 3072u * wv;
  const unsigned lq = lane >> 4, ph = (lane >> 2) & 3, pl = lane & 3;
  const unsigned gs = 256u * lq + 64u * ((ph - lq) & 3) + 16u * (pl ^ ((4u - wv) & 3));
  const unsigned ga = gs + 3072u * (wv < 3u ? wv : 5u);
  const unsigned gb = gs + 3072u * (wv < 2u ? wv + 6u : wv + 1u);
  const unsigned boff = 192u * wv + 16u * lq;
  const unsigned role = (blk >> 2) & 1u;
  unsigned pk = (blk & 3u) * 32u + (blk >> 3);
  const unsigned sst  = (role ^ (blk >> 4)) & 1u;
  const unsigned t0   = pk * 49152u + sst * 24576u;
  const unsigned pv = blk >> 7, ps = ((blk & 7u) | ((blk >> 1) & 56u)) ^ (4u * pv), pgr = (ps >> 2) & 7u, pi = (ps & 3u) * 2u + pv;
  const unsigned ppg = min(pgr * 8u, 52u) + (pi & (0x13777777u >> (4u * pgr)) & 7u);
  const unsigned pop = (ppg << 21) + ((ps & 3u) * 4u + pv) * 0x1c080u + 0x24080u;
  unsigned pfire = ((role ^ ppg) & 1u) * 2u, psel = ps >> 5;
  const unsigned vpub = lane == 0u ? 0u + (pv * 64u + ps) * 8u : 0x40000000u;
  const unsigned pfo = (blk >> 4) * 18432u;
  const unsigned pfv = tid * 16u;
  const unsigned ty = (blk >> 3) & 1u;
  const unsigned tidx = (blk >> 3) * 4u + wv, tsel = tidx >> 6;
  const unsigned toff = (tidx & 63u) < 54u ? (tidx & 63u) << 21 : 113246204u;
  unsigned tres, pre1;
  u64 pres;
  u32x4 br0, br1, br2;
  asm volatile(
      "s_add_u32 m0, s43, 0x27000\n\t"
      "s_waitcnt lgkmcnt(0)\n\t"
      "s_mov_b64 s[36:37], s[30:31]\n\t"
      "s_mov_b64 s[30:31], s[22:23]\n\t"
      "s_cmp_eq_u32 s94, 1\n\t"
      "s_cbranch_scc0 .Lc1_%=\n\t"
      "s_cmp_eq_u32 s41, s43\n\t"
      "s_cbranch_scc0 .Lc0_%=\n\t"
      "s_cmp_eq_u32 s78, 0\n\t"
      "s_cselect_b64 s[96:97], s[20:21], s[28:29]\n\t"
      "s_load_dword s69, s[96:97], s76 offset:0x0\n\t"
      "s_load_dwordx2 s[60:61], s[96:97], s76 offset:0x3300\n\t"
      ".Lc0_%=:\n\t"
      "buffer_load_dwordx4 v216, s[16:19], s42 offen lds\n\t"
      "s_add_u32 s51, s42, 0x1000\n\t"
      "buffer_load_dwordx4 v216, s[16:19], s51 offen lds\n\t"
      "s_add_u32 s51, s42, 0x2000\n\t"
      "buffer_load_dwordx4 v216, s[16:19], s51 offen lds\n\t"
      "s_add_u32 s51, s42, 0x3000\n\t"
      "buffer_load_dwordx4 v216, s[16:19], s51 offen lds\n\t"
      "s_add_u32 s51, s42, 0x4000\n\t"
      "buffer_load_dwordx4 v216, s[16:19], s51 offen lds\n\t"
      ".Lc1_%=:\n\t"
      "buffer_load_dwordx4 v[226:229], v215, s[36:39], 0 offen offset:0\n\t"
      "buffer_load_dwordx4 v[230:233], v215, s[36:39], 0 offen offset:64\n\t"
      "buffer_load_dwordx4 v[234:237], v215, s[36:39], 0 offen offset:128\n\t"
      "s_mov_b32 m0, s41\n\t"
      "s_nop 0\n\t"
      "buffer_load_dwordx4 v208, s[20:23], s40 offen lds\n\t"
      "buffer_load_dwordx4 v208, s[20:23], s40 offen offset:1024 lds\n\t"
      "buffer_load_dwordx4 v208, s[20:23], s40 offen offset:2048 lds\n\t"
      "s_cmp_eq_u32 s94, 0\n\t"
      "s_cbranch_scc0 .Lvb0_%=\n\t"
      "s_add_u32 m0, s41, 0x3000\n\t"
      "s_nop 0\n\t"
      "buffer_load_dwordx4 v209, s[20:23], s40 offen lds\n\t"
      "buffer_load_dwordx4 v209, s[20:23], s40 offen offset:1024 lds\n\t"
      "buffer_load_dwordx4 v209, s[20:23], s40 offen offset:2048 lds\n\t"
      "s_cmp_eq_u32 s99, 0\n\t"
      "s_cselect_b64 s[96:97], s[20:21], s[28:29]\n\t"
      "s_load_dword s95, s[96:97], s98\n\t"
      "s_branch .Lvb1_%=\n\t"
      ".Lvb0_%=:\n\t"
      "s_cmp_eq_u32 s41, s43\n\t"
      "s_cbranch_scc1 .Lev0_%=\n\t"
      "s_cmp_eq_u32 s99, 0\n\t"
      "s_cselect_b64 s[96:97], s[20:21], s[28:29]\n\t"
      "s_load_dword s95, s[96:97], s98\n\t"
      "s_cmp_eq_u32 s94, 0\n\t"
      "s_cbranch_scc1 .Lev1_%=\n\t"
      "s_sleep 8\n\t"
      "s_branch .Lev1_%=\n\t"
      ".Lev0_%=:\n\t"
      "s_andn2_b32 s79, s52, 15\n\t"
      "v_mov_b32_e32 v241, s53\n\t"
      "s_waitcnt lgkmcnt(2)\n\t"
      "s_nop 7\n\t"
      "s_getreg_b32 s78, hwreg(HW_REG_IB_STS, 8, 4)\n\t"
      "s_cmp_eq_u32 s78, 1\n\t"
      "s_cselect_b32 s51, 2, 0\n\t"
      "s_xor_b32 s77, s77, s51\n\t"
      "s_cmp_lg_u32 s78, 0\n\t"
      "s_cselect_b32 s77, s77, 1\n\t"
      "s_or_b32 s79, s79, s77\n\t"
      "v_mov_b32_e32 v240, s79\n\t"
      "buffer_store_dwordx2 v[240:241], v244, s[24:27], 0 offen sc1\n\t"
      "s_cmp_eq_u32 s99, 0\n\t"
      "s_cselect_b64 s[96:97], s[20:21], s[28:29]\n\t"
      "s_load_dword s95, s[96:97], s98\n\t"
      ".Lev1_%=:\n\t"
      "s_add_u32 m0, s41, 0x3000\n\t"
      "s_nop 0\n\t"
      "buffer_load_dwordx4 v209, s[20:23], s40 offen lds\n\t"
      "buffer_load_dwordx4 v209, s[20:23], s40 offen offset:1024 lds\n\t"
      "buffer_load_dwordx4 v209, s[20:23], s40 offen offset:2048 lds\n\t"
      "s_add_u32 m0, s41, 0x6000\n\t"
      "s_add_u32 s51, s40, 0x600000\n\t"
      "buffer_load_dwordx4 v208, s[20:23], s51 offen lds\n\t"
      "buffer_load_dwordx4 v208, s[20:23], s51 offen offset:1024 lds\n\t"
      "buffer_load_dwordx4 v208, s[20:23], s51 offen offset:2048 lds\n\t"
      "s_add_u32 m0, s41, 0x9000\n\t"
      "s_add_u32 s51, s40, 0x600000\n\t"
      "buffer_load_dwordx4 v209, s[20:23], s51 offen lds\n\t"
      "buffer_load_dwordx4 v209, s[20:23], s51 offen offset:1024 lds\n\t"
      "buffer_load_dwordx4 v209, s[20:23], s51 offen offset:2048 lds\n\t"
      ".Lvb1_%=:\n\t"
      : "={s95}"(tres), "={s[60:61]}"(pres), "={s69}"(pre1), "={v[226:229]}"(br0), "={v[230:233]}"(br1), "={v[234:237]}"(br2),
        "+{s[16:31]}"(d16), "+{s[36:39]}"(db), "+v"(lane), "+{s77}"(pfire), "+{s78}"(psel), "+{v6}"(cpo)
      : "{s40}"(t0), "{s41}"(p0), "{s43}"(lds0), "{s94}"(ty),
        "{v208}"(ga), "{v209}"(gb), "{v215}"(boff), "{v216}"(pfv),
        "{s42}"(pfo), "{s98}"(toff), "{s99}"(tsel), "{s76}"(pop), "{s[52:53]}"(rs), "{v244}"(vpub)
      : "memory", "scc", "s51", "s92", "s93", "s96", "s97", "s79", "v240", "v241");
  asm volatile("" : "+s"(wv), "+s"(pk));
  __builtin_amdgcn_sched_barrier(0);
  const unsigned vcell = lane < 2u ? 2048u + pk * 8u + 4u * (role ^ lane) : 0x40000000u;
  const unsigned ob = (unsigned)(((lane < 12u ? 0x434ec32ca418820ull >> (5u * lane) : 0x23053d2eull >> (5u * ((lane - 12u) & 7u)))) & 31u) * 6291456u + pk * 49152u;
  const unsigned pg = (ob + 24576u) >> 21;
  const unsigned ta = lane < 18u ? ob + 24576u * sst : 113246208u, t1 = ob + 24576u * (sst ^ 1u);
  const unsigned tq = (lane >= 9u ? 80u : 77u) - (blk & 1u) * 2u + (lane < 4u ? 3u : 0u), tp = lane >= 14u ? 0u : tq;
  const unsigned mk = lane >= 4u + ty && lane < 18u ? 1u << (pg < 52u ? (pg >> 3) * 4u : 28u) : 0u;
  const unsigned plm = (unsigned)__builtin_amdgcn_ballot_w64(((pg ^ role ^ sst) & 1u) != 0u), plb = wv < 2u ? plm : ~plm;
  const unsigned vgv = 0u + (wv & 1u) * 512u + lane * 8u;
  const unsigned vxr = lds0 + 159744u + lane * 16u, vxw = vxr + wv * 4u;
  const unsigned li = lane & 15, lg = lane >> 4, rl = li & 3, hi = li >> 2;
  const unsigned rb = 768u * li + 16u * (lg ^ ((4u - hi) & 3)) - 3072u * wv;
  unsigned cfa[4], csw[3];
#pragma unroll
  for (unsigned u = 0; u < 4; ++u) cfa[u] = rb + 64u * ((u - rl) & 3);
#pragma unroll
  for (unsigned j = 0; j < 3; ++j) {
    const unsigned e = 3u * wv + j;
    csw[j] = rb + 256u * (e >> 2) + 64u * (((e & 3) + (e >> 2) - rl) & 3);
  }
  const unsigned csr = 16u * lane;
  const unsigned tbase = p0 + (4u * lg + (li >> 2)) * 192u + 16u * (li & 3);
  unsigned woff[3];
#pragma unroll
  for (unsigned j = 0; j < 3; ++j) {
    const unsigned rr = (__umul24(lane, 171u) + 10944u * j) >> 11;
    woff[j] = __umul24(rr, 576u) + __umul24(rr & 12u, 768u) + (16u * lane + 192u * wv);
  }
  asm volatile(
      "s_cmp_eq_u32 s94, 0\n\t"
      "s_cbranch_scc0 .Lc2_%=\n\t"
      "s_add_u32 m0, s41, 0x1e000\n\t"
      "s_mov_b32 s51, 0x0\n\t"
      "buffer_load_dwordx4 v211, s[16:19], s51 offen lds\n\t"
      "buffer_load_dwordx4 v212, s[16:19], s51 offen offset:1024 lds\n\t"
      "buffer_load_dwordx4 v213, s[16:19], s51 offen offset:2048 lds\n\t"
      "s_add_u32 m0, s41, 0x21000\n\t"
      "s_mov_b32 s51, 0xc00\n\t"
      "buffer_load_dwordx4 v211, s[16:19], s51 offen lds\n\t"
      "buffer_load_dwordx4 v212, s[16:19], s51 offen offset:1024 lds\n\t"
      "buffer_load_dwordx4 v213, s[16:19], s51 offen offset:2048 lds\n\t"
      "s_add_u32 m0, s41, 0x24000\n\t"
      "s_mov_b32 s51, 0x6000\n\t"
      "buffer_load_dwordx4 v211, s[16:19], s51 offen lds\n\t"
      "buffer_load_dwordx4 v212, s[16:19], s51 offen offset:1024 lds\n\t"
      "buffer_load_dwordx4 v213, s[16:19], s51 offen offset:2048 lds\n\t"
      "s_add_u32 m0, s41, 0x12000\n\t"
      "s_mov_b32 s51, 0x6c00\n\t"
      "buffer_load_dwordx4 v211, s[16:19], s51 offen lds\n\t"
      "buffer_load_dwordx4 v212, s[16:19], s51 offen offset:1024 lds\n\t"
      "buffer_load_dwordx4 v213, s[16:19], s51 offen offset:2048 lds\n\t"
      ".Lc2_%=:\n\t"
      "s_mov_b32 s62, 0\n\t"
      "s_add_u32 s50, s41, 0x12000\n\t"
      "s_add_u32 s51, s41, 0x12000\n\t"
      "s_sub_u32 s63, s52, 800\n\t"
      "s_andn2_b32 s63, s63, 15\n\t"
      "v_add_u32_e32 v196, s41, v200\n\t"
      "v_add_u32_e32 v197, s41, v201\n\t"
      "v_add_u32_e32 v198, s41, v202\n\t"
      "v_add_u32_e32 v199, s41, v203\n\t"
      "v_add_u32_e32 v217, s51, v204\n\t"
      "v_add_u32_e32 v218, s51, v205\n\t"
      "v_add_u32_e32 v219, s51, v206\n\t"
      "v_add_u32_e32 v220, s51, v207\n\t"
      "v_add_u32_e32 v225, s51, v204\n\t"
      "v_add_u32_e32 v7, s51, v205\n\t"
      "v_add_u32_e32 v210, s51, v206\n\t"
      "v_add_u32_e32 v238, s51, v207\n\t"
      "s_and_b32 s49, s71, 31\n\t"
      "s_lshr_b32 s71, s71, 5\n\t"
      "s_lshl_b32 s88, 2, s94\n\t"
      "v_mov_b32_e32 v239, v208\n\t"
      "s_cmp_eq_u32 s94, 0\n\t"
      "s_cbranch_scc0 .Lpb_%=\n\t"
      "s_add_u32 m0, s41, 0x15000\n\t"
      "s_mov_b32 s51, 0xc000\n\t"
      "buffer_load_dwordx4 v211, s[16:19], s51 offen lds\n\t"
      "buffer_load_dwordx4 v212, s[16:19], s51 offen offset:1024 lds\n\t"
      "buffer_load_dwordx4 v213, s[16:19], s51 offen offset:2048 lds\n\t"
      "s_waitcnt vmcnt(21)\n\t"
      "v_lshlrev_b32_e32 v8, 16, v226\n\t"
      "v_and_b32_e32 v9, 0xffff0000, v226\n\t"
      "v_lshlrev_b32_e32 v10, 16, v227\n\t"
      "v_and_b32_e32 v11, 0xffff0000, v227\n\t"
      "v_lshlrev_b32_e32 v12, 16, v228\n\t"
      "v_and_b32_e32 v13, 0xffff0000, v228\n\t"
      "v_lshlrev_b32_e32 v14, 16, v229\n\t"
      "v_and_b32_e32 v15, 0xffff0000, v229\n\t"
      "v_lshlrev_b32_e32 v16, 16, v230\n\t"
      "v_and_b32_e32 v17, 0xffff0000, v230\n\t"
      "v_lshlrev_b32_e32 v18, 16, v231\n\t"
      "v_and_b32_e32 v19, 0xffff0000, v231\n\t"
      "v_lshlrev_b32_e32 v20, 16, v232\n\t"
      "v_and_b32_e32 v21, 0xffff0000, v232\n\t"
      "v_lshlrev_b32_e32 v22, 16, v233\n\t"
      "v_and_b32_e32 v23, 0xffff0000, v233\n\t"
      "v_lshlrev_b32_e32 v24, 16, v234\n\t"
      "v_and_b32_e32 v25, 0xffff0000, v234\n\t"
      "v_lshlrev_b32_e32 v26, 16, v235\n\t"
      "v_and_b32_e32 v27, 0xffff0000, v235\n\t"
      "v_lshlrev_b32_e32 v28, 16, v236\n\t"
      "v_and_b32_e32 v29, 0xffff0000, v236\n\t"
      "v_lshlrev_b32_e32 v30, 16, v237\n\t"
      "v_and_b32_e32 v31, 0xffff0000, v237\n\t"
      "s_add_u32 m0, s41, 0x18000\n\t"
      "s_mov_b32 s51, 0xcc00\n\t"
      "buffer_load_dwordx4 v211, s[16:19], s51 offen lds\n\t"
      "buffer_load_dwordx4 v212, s[16:19], s51 offen offset:1024 lds\n\t"
      "buffer_load_dwordx4 v213, s[16:19], s51 offen offset:2048 lds\n\t"
      "s_add_u32 m0, s41, 0x1b000\n\t"
      "s_mov_b32 s51, 0x12000\n\t"
      "buffer_load_dwordx4 v211, s[16:19], s51 offen lds\n\t"
      "buffer_load_dwordx4 v212, s[16:19], s51 offen offset:1024 lds\n\t"
      "buffer_load_dwordx4 v213, s[16:19], s51 offen offset:2048 lds\n\t"
      "s_mov_b32 s44, 0\n\t"
      "s_mov_b32 s85, 3\n\t"
      "s_mov_b32 s45, s41\n\t"
      "s_add_u32 s46, s41, 0x6000\n\t"
      "s_add_u32 s47, s41, 0xc000\n\t"
      "s_mov_b64 vcc, 0\n\t"
      "s_waitcnt vmcnt(18)\n\t"
      "s_barrier\n\t"
      "ds_read_b64_tr_b16 a[0:1], v214 offset:49152\n\t"
      "ds_read_b64_tr_b16 a[4:5], v214 offset:49160\n\t"
      "ds_read_b64_tr_b16 a[8:9], v214 offset:49216\n\t"
      "ds_read_b64_tr_b16 a[12:13], v214 offset:49224\n\t"
      "ds_read_b64_tr_b16 a[16:17], v214 offset:49280\n\t"
      "ds_read_b64_tr_b16 a[20:21], v214 offset:49288\n\t"
      "ds_read_b128 v[140:143], v196 offset:0\n\t"
      "s_waitcnt vmcnt(15)\n\t"
      "ds_read_b64_tr_b16 a[2:3], v221 offset:0\n\t"
      "ds_read_b64_tr_b16 a[6:7], v221 offset:8\n\t"
      "ds_read_b64_tr_b16 a[10:11], v221 offset:64\n\t"
      "ds_read_b64_tr_b16 a[14:15], v221 offset:72\n\t"
      "ds_read_b64_tr_b16 a[18:19], v221 offset:128\n\t"
      "ds_read_b64_tr_b16 a[22:23], v221 offset:136\n\t"
      "ds_read_b128 v[152:155], v196 offset:12288\n\t"
      "s_waitcnt lgkmcnt(8)\n\t"
      "s_add_u32 m0, s41, 0x1e000\n\t"
      "s_mov_b32 s51, 0x12c00\n\t"
      "buffer_load_dwordx4 v211, s[16:19], s51 offen lds\n\t"
      "buffer_load_dwordx4 v212, s[16:19], s51 offen offset:1024 lds\n\t"
      "buffer_load_dwordx4 v213, s[16:19], s51 offen offset:2048 lds\n\t"
      "s_waitcnt vmcnt(15)\n\t"
      "ds_read_b64_tr_b16 a[24:25], v221 offset:12288\n\t"
      "ds_read_b64_tr_b16 a[28:29], v221 offset:12296\n\t"
      "ds_read_b64_tr_b16 a[32:33], v221 offset:12352\n\t"
      "ds_read_b64_tr_b16 a[36:37], v221 offset:12360\n\t"
      "ds_read_b64_tr_b16 a[40:41], v221 offset:12416\n\t"
      "ds_read_b64_tr_b16 a[44:45], v221 offset:12424\n\t"
      "ds_read_b128 v[144:147], v197 offset:0\n\t"
      "s_waitcnt lgkmcnt(8)\n\t"
      "v_mfma_f32_16x16x32_bf16 v[80:83], a[0:3], v[140:143], v[8:11]\n\t"
      "s_add_u32 m0, s41, 0x21000\n\t"
      "s_mov_b32 s51, 0x18000\n\t"
      "buffer_load_dwordx4 v211, s[16:19], s51 offen lds\n\t"
      "v_mfma_f32_16x16x32_bf16 v[84:87], a[4:7], v[140:143], v[12:15]\n\t"
      "buffer_load_dwordx4 v212, s[16:19], s51 offen offset:1024 lds\n\t"
      "v_mfma_f32_16x16x32_bf16 v[88:91], a[8:11], v[140:143], v[16:19]\n\t"
      "buffer_load_dwordx4 v213, s[16:19], s51 offen offset:2048 lds\n\t"
      "s_waitcnt vmcnt(15)\n\t"
      "ds_read_b64_tr_b16 a[26:27], v214 offset:0\n\t"
      "v_mfma_f32_16x16x32_bf16 v[92:95], a[12:15], v[140:143], v[20:23]\n\t"
      "ds_read_b64_tr_b16 a[30:31], v214 offset:8\n\t"
      "ds_read_b64_tr_b16 a[34:35], v214 offset:64\n\t"
      "v_mfma_f32_16x16x32_bf16 v[96:99], a[16:19], v[140:143], v[24:27]\n\t"
      "ds_read_b64_tr_b16 a[38:39], v214 offset:72\n\t"
      "ds_read_b64_tr_b16 a[42:43], v214 offset:128\n\t"
      "v_mfma_f32_16x16x32_bf16 v[100:103], a[20:23], v[140:143], v[28:31]\n\t"
      "ds_read_b64_tr_b16 a[46:47], v214 offset:136\n\t"
      "ds_read_b128 v[156:159], v197 offset:12288\n\t"
      "s_waitcnt lgkmcnt(8)\n\t"
      "v_mfma_f32_16x16x32_bf16 v[104:107], a[0:3], v[152:155], v[8:11]\n\t"
      "s_add_u32 m0, s41, 0x24000\n\t"
      "s_mov_b32 s51, 0x18c00\n\t"
      "buffer_load_dwordx4 v211, s[16:19], s51 offen lds\n\t"
      "v_mfma_f32_16x16x32_bf16 v[108:111], a[4:7], v[152:155], v[12:15]\n\t"
      "buffer_load_dwordx4 v212, s[16:19], s51 offen offset:1024 lds\n\t"
      "v_mfma_f32_16x16x32_bf16 v[112:115], a[8:11], v[152:155], v[16:19]\n\t"
      "buffer_load_dwordx4 v213, s[16:19], s51 offen offset:2048 lds\n\t"
      "s_waitcnt vmcnt(15)\n\t"
      "ds_read_b64_tr_b16 a[48:49], v214 offset:12288\n\t"
      "v_mfma_f32_16x16x32_bf16 v[116:119], a[12:15], v[152:155], v[20:23]\n\t"
      "ds_read_b64_tr_b16 a[52:53], v214 offset:12296\n\t"
      "ds_read_b64_tr_b16 a[56:57], v214 offset:12352\n\t"
      "v_mfma_f32_16x16x32_bf16 v[120:123], a[16:19], v[152:155], v[24:27]\n\t"
      "ds_read_b64_tr_b16 a[60:61], v214 offset:12360\n\t"
      "ds_read_b64_tr_b16 a[64:65], v214 offset:12416\n\t"
      "v_mfma_f32_16x16x32_bf16 v[132:135], a[20:23], v[152:155], v[28:31]\n\t"
      "ds_read_b64_tr_b16 a[68:69], v214 offset:12424\n\t"
      "ds_read_b128 v[148:151], v198 offset:0\n\t"
      "s_waitcnt lgkmcnt(8)\n\t"
      "v_mfma_f32_16x16x32_bf16 v[80:83], a[24:27], v[144:147], v[80:83]\n\t"
      "s_add_u32 m0, s41, 0x12000\n\t"
      "s_mov_b32 s51, 0x1e000\n\t"
      "buffer_load_dwordx4 v211, s[16:19], s51 offen lds\n\t"
      "v_mfma_f32_16x16x32_bf16 v[84:87], a[28:31], v[144:147], v[84:87]\n\t"
      "buffer_load_dwordx4 v212, s[16:19], s51 offen offset:1024 lds\n\t"
      "v_mfma_f32_16x16x32_bf16 v[88:91], a[32:35], v[144:147], v[88:91]\n\t"
      "buffer_load_dwordx4 v213, s[16:19], s51 offen offset:2048 lds\n\t"
      "s_waitcnt vmcnt(15)\n\t"
      "ds_read_b64_tr_b16 a[50:51], v214 offset:24576\n\t"
      "v_mfma_f32_16x16x32_bf16 v[92:95], a[36:39], v[144:147], v[92:95]\n\t"
      "ds_read_b64_tr_b16 a[54:55], v214 offset:24584\n\t"
      "ds_read_b64_tr_b16 a[58:59], v214 offset:24640\n\t"
      "v_mfma_f32_16x16x32_bf16 v[96:99], a[40:43], v[144:147], v[96:99]\n\t"
      "ds_read_b64_tr_b16 a[62:63], v214 offset:24648\n\t"
      "ds_read_b64_tr_b16 a[66:67], v214 offset:24704\n\t"
      "v_mfma_f32_16x16x32_bf16 v[100:103], a[44:47], v[144:147], v[100:103]\n\t"
      "ds_read_b64_tr_b16 a[70:71], v214 offset:24712\n\t"
      "ds_read_b128 v[160:163], v198 offset:12288\n\t"
      "s_waitcnt lgkmcnt(8)\n\t"
      "v_mfma_f32_16x16x32_bf16 v[104:107], a[24:27], v[156:159], v[104:107]\n\t"
      "s_add_u32 m0, s41, 0x15000\n\t"
      "s_mov_b32 s51, 0x1ec00\n\t"
      "buffer_load_dwordx4 v211, s[16:19], s51 offen lds\n\t"
      "v_mfma_f32_16x16x32_bf16 v[108:111], a[28:31], v[156:159], v[108:111]\n\t"
      "buffer_load_dwordx4 v212, s[16:19], s51 offen offset:1024 lds\n\t"
      "v_mfma_f32_16x16x32_bf16 v[112:115], a[32:35], v[156:159], v[112:115]\n\t"
      "buffer_load_dwordx4 v213, s[16:19], s51 offen offset:2048 lds\n\t"
      "s_waitcnt vmcnt(15)\n\t"
      "ds_read_b64_tr_b16 a[72:73], v214 offset:36864\n\t"
      "v_mfma_f32_16x16x32_bf16 v[116:119], a[36:39], v[156:159], v[116:119]\n\t"
      "ds_read_b64_tr_b16 a[76:77], v214 offset:36872\n\t"
      "ds_read_b64_tr_b16 a[80:81], v214 offset:36928\n\t"
      "v_mfma_f32_16x16x32_bf16 v[120:123], a[40:43], v[156:159], v[120:123]\n\t"
      "ds_read_b64_tr_b16 a[84:85], v214 offset:36936\n\t"
      "ds_read_b64_tr_b16 a[88:89], v214 offset:36992\n\t"
      "v_mfma_f32_16x16x32_bf16 v[132:135], a[44:47], v[156:159], v[132:135]\n\t"
      "ds_read_b64_tr_b16 a[92:93], v214 offset:37000\n\t"
      "ds_read_b128 v[140:143], v199 offset:0\n\t"
      "s_waitcnt lgkmcnt(8)\n\t"
      "v_mfma_f32_16x16x32_bf16 v[80:83], a[48:51], v[148:151], v[80:83]\n\t"
      "s_add_u32 m0, s41, 0x18000\n\t"
      "s_mov_b32 s51, 0x24000\n\t"
      "buffer_load_dwordx4 v211, s[16:19], s51 offen lds\n\t"
      "v_mfma_f32_16x16x32_bf16 v[84:87], a[52:55], v[148:151], v[84:87]\n\t"
      "buffer_load_dwordx4 v212, s[16:19], s51 offen offset:1024 lds\n\t"
      "v_mfma_f32_16x16x32_bf16 v[88:91], a[56:59], v[148:151], v[88:91]\n\t"
      "buffer_load_dwordx4 v213, s[16:19], s51 offen offset:2048 lds\n\t"
      "s_waitcnt vmcnt(15)\n\t"
      "ds_read_b64_tr_b16 a[74:75], v214 offset:49152\n\t"
      "v_mfma_f32_16x16x32_bf16 v[92:95], a[60:63], v[148:151], v[92:95]\n\t"
      "ds_read_b64_tr_b16 a[78:79], v214 offset:49160\n\t"
      "ds_read_b64_tr_b16 a[82:83], v214 offset:49216\n\t"
      "v_mfma_f32_16x16x32_bf16 v[96:99], a[64:67], v[148:151], v[96:99]\n\t"
      "ds_read_b64_tr_b16 a[86:87], v214 offset:49224\n\t"
      "ds_read_b64_tr_b16 a[90:91], v214 offset:49280\n\t"
      "v_mfma_f32_16x16x32_bf16 v[100:103], a[68:71], v[148:151], v[100:103]\n\t"
      "ds_read_b64_tr_b16 a[94:95], v214 offset:49288\n\t"
      "ds_read_b128 v[152:155], v199 offset:12288\n\t"
      "s_waitcnt lgkmcnt(8)\n\t"
      "v_mfma_f32_16x16x32_bf16 v[104:107], a[48:51], v[160:163], v[104:107]\n\t"
      "s_add_u32 m0, s41, 0x1b000\n\t"
      "s_mov_b32 s51, 0x24c00\n\t"
      "buffer_load_dwordx4 v211, s[16:19], s51 offen lds\n\t"
      "v_mfma_f32_16x16x32_bf16 v[108:111], a[52:55], v[160:163], v[108:111]\n\t"
      "buffer_load_dwordx4 v212, s[16:19], s51 offen offset:1024 lds\n\t"
      "v_mfma_f32_16x16x32_bf16 v[112:115], a[56:59], v[160:163], v[112:115]\n\t"
      "buffer_load_dwordx4 v213, s[16:19], s51 offen offset:2048 lds\n\t"
      "s_waitcnt vmcnt(15)\n\t"
      "ds_read_b64_tr_b16 a[96:97], v221 offset:0\n\t"
      "v_mfma_f32_16x16x32_bf16 v[116:119], a[60:63], v[160:163], v[116:119]\n\t"
      "ds_read_b64_tr_b16 a[100:101], v221 offset:8\n\t"
      "ds_read_b64_tr_b16 a[104:105], v221 offset:64\n\t"
      "v_mfma_f32_16x16x32_bf16 v[120:123], a[64:67], v[160:163], v[120:123]\n\t"
      "ds_read_b64_tr_b16 a[108:109], v221 offset:72\n\t"
      "ds_read_b64_tr_b16 a[112:113], v221 offset:128\n\t"
      "v_mfma_f32_16x16x32_bf16 v[132:135], a[68:71], v[160:163], v[132:135]\n\t"
      "ds_read_b64_tr_b16 a[116:117], v221 offset:136\n\t"
      "ds_read_b128 v[144:147], v197 offset:256\n\t"
      "s_waitcnt lgkmcnt(8)\n\t"
      "v_mfma_f32_16x16x32_bf16 v[80:83], a[72:75], v[140:143], v[80:83]\n\t"
      "s_add_u32 m0, s41, 0x1e000\n\t"
      "s_mov_b32 s51, 0x2a000\n\t"
      "buffer_load_dwordx4 v211, s[16:19], s51 offen lds\n\t"
      "v_mfma_f32_16x16x32_bf16 v[84:87], a[76:79], v[140:143], v[84:87]\n\t"
      "buffer_load_dwordx4 v212, s[16:19], s51 offen offset:1024 lds\n\t"
      "v_mfma_f32_16x16x32_bf16 v[88:91], a[80:83], v[140:143], v[88:91]\n\t"
      "buffer_load_dwordx4 v213, s[16:19], s51 offen offset:2048 lds\n\t"
      "s_waitcnt vmcnt(15)\n\t"
      "ds_read_b64_tr_b16 a[98:99], v221 offset:12288\n\t"
      "v_mfma_f32_16x16x32_bf16 v[92:95], a[84:87], v[140:143], v[92:95]\n\t"
      "ds_read_b64_tr_b16 a[102:103], v221 offset:12296\n\t"
      "ds_read_b64_tr_b16 a[106:107], v221 offset:12352\n\t"
      "v_mfma_f32_16x16x32_bf16 v[96:99], a[88:91], v[140:143], v[96:99]\n\t"
      "ds_read_b64_tr_b16 a[110:111], v221 offset:12360\n\t"
      "ds_read_b64_tr_b16 a[114:115], v221 offset:12416\n\t"
      "v_mfma_f32_16x16x32_bf16 v[100:103], a[92:95], v[140:143], v[100:103]\n\t"
      "ds_read_b64_tr_b16 a[118:119], v221 offset:12424\n\t"
      "ds_read_b128 v[156:159], v197 offset:12544\n\t"
      "s_waitcnt lgkmcnt(8)\n\t"
      "v_mfma_f32_16x16x32_bf16 v[104:107], a[72:75], v[152:155], v[104:107]\n\t"
      "s_add_u32 m0, s41, 0x21000\n\t"
      "s_mov_b32 s51, 0x2ac00\n\t"
      "buffer_load_dwordx4 v211, s[16:19], s51 offen lds\n\t"
      "v_mfma_f32_16x16x32_bf16 v[108:111], a[76:79], v[152:155], v[108:111]\n\t"
      "buffer_load_dwordx4 v212, s[16:19], s51 offen offset:1024 lds\n\t"
      "v_mfma_f32_16x16x32_bf16 v[112:115], a[80:83], v[152:155], v[112:115]\n\t"
      "buffer_load_dwordx4 v213, s[16:19], s51 offen offset:2048 lds\n\t"
      "s_waitcnt vmcnt(15)\n\t"
      "ds_read_b64_tr_b16 a[120:121], v214 offset:0\n\t"
      "v_mfma_f32_16x16x32_bf16 v[116:119], a[84:87], v[152:155], v[116:119]\n\t"
      "ds_read_b64_tr_b16 a[124:125], v214 offset:8\n\t"
      "ds_read_b64_tr_b16 a[128:129], v214 offset:64\n\t"
      "v_mfma_f32_16x16x32_bf16 v[120:123], a[88:91], v[152:155], v[120:123]\n\t"
      "ds_read_b64_tr_b16 a[132:133], v214 offset:72\n\t"
      "ds_read_b64_tr_b16 a[136:137], v214 offset:128\n\t"
      "v_mfma_f32_16x16x32_bf16 v[132:135], a[92:95], v[152:155], v[132:135]\n\t"
      "ds_read_b64_tr_b16 a[140:141], v214 offset:136\n\t"
      "ds_read_b128 v[148:151], v198 offset:256\n\t"
      "s_waitcnt lgkmcnt(8)\n\t"
      "v_mfma_f32_16x16x32_bf16 v[80:83], a[96:99], v[144:147], v[80:83]\n\t"
      "s_add_u32 m0, s41, 0x24000\n\t"
      "s_mov_b32 s51, 0x30000\n\t"
      "buffer_load_dwordx4 v211, s[16:19], s51 offen lds\n\t"
      "v_mfma_f32_16x16x32_bf16 v[84:87], a[100:103], v[144:147], v[84:87]\n\t"
      "buffer_load_dwordx4 v212, s[16:19], s51 offen offset:1024 lds\n\t"
      "v_mfma_f32_16x16x32_bf16 v[88:91], a[104:107], v[144:147], v[88:91]\n\t"
      "buffer_load_dwordx4 v213, s[16:19], s51 offen offset:2048 lds\n\t"
      "s_waitcnt vmcnt(15)\n\t"
      "ds_read_b64_tr_b16 a[122:123], v214 offset:12288\n\t"
      "v_mfma_f32_16x16x32_bf16 v[92:95], a[108:111], v[144:147], v[92:95]\n\t"
      "ds_read_b64_tr_b16 a[126:127], v214 offset:12296\n\t"
      "ds_read_b64_tr_b16 a[130:131], v214 offset:12352\n\t"
      "v_mfma_f32_16x16x32_bf16 v[96:99], a[112:115], v[144:147], v[96:99]\n\t"
      "ds_read_b64_tr_b16 a[134:135], v214 offset:12360\n\t"
      "ds_read_b64_tr_b16 a[138:139], v214 offset:12416\n\t"
      "v_mfma_f32_16x16x32_bf16 v[100:103], a[116:119], v[144:147], v[100:103]\n\t"
      "ds_read_b64_tr_b16 a[142:143], v214 offset:12424\n\t"
      "ds_read_b128 v[160:163], v198 offset:12544\n\t"
      "s_waitcnt lgkmcnt(8)\n\t"
      "v_mfma_f32_16x16x32_bf16 v[104:107], a[96:99], v[156:159], v[104:107]\n\t"
      "s_add_u32 m0, s41, 0x12000\n\t"
      "s_mov_b32 s51, 0x30c00\n\t"
      "buffer_load_dwordx4 v211, s[16:19], s51 offen lds\n\t"
      "v_mfma_f32_16x16x32_bf16 v[108:111], a[100:103], v[156:159], v[108:111]\n\t"
      "buffer_load_dwordx4 v212, s[16:19], s51 offen offset:1024 lds\n\t"
      "v_mfma_f32_16x16x32_bf16 v[112:115], a[104:107], v[156:159], v[112:115]\n\t"
      "buffer_load_dwordx4 v213, s[16:19], s51 offen offset:2048 lds\n\t"
      "s_waitcnt vmcnt(15)\n\t"
      "ds_read_b64_tr_b16 a[144:145], v214 offset:24576\n\t"
      "v_mfma_f32_16x16x32_bf16 v[116:119], a[108:111], v[156:159], v[116:119]\n\t"
      "ds_read_b64_tr_b16 a[148:149], v214 offset:24584\n\t"
      "ds_read_b64_tr_b16 a[152:153], v214 offset:24640\n\t"
      "v_mfma_f32_16x16x32_bf16 v[120:123], a[112:115], v[156:159], v[120:123]\n\t"
      "ds_read_b64_tr_b16 a[156:157], v214 offset:24648\n\t"
      "ds_read_b64_tr_b16 a[160:161], v214 offset:24704\n\t"
      "v_mfma_f32_16x16x32_bf16 v[132:135], a[116:119], v[156:159], v[132:135]\n\t"
      "ds_read_b64_tr_b16 a[164:165], v214 offset:24712\n\t"
      "ds_read_b128 v[140:143], v199 offset:256\n\t"
      "s_waitcnt lgkmcnt(8)\n\t"
      "v_mfma_f32_16x16x32_bf16 v[80:83], a[120:123], v[148:151], v[80:83]\n\t"
      "s_add_u32 m0, s41, 0x15000\n\t"
      "s_mov_b32 s51, 0x36000\n\t"
      "buffer_load_dwordx4 v211, s[16:19], s51 offen lds\n\t"
      "v_mfma_f32_16x16x32_bf16 v[84:87], a[124:127], v[148:151], v[84:87]\n\t"
      "buffer_load_dwordx4 v212, s[16:19], s51 offen offset:1024 lds\n\t"
      "v_mfma_f32_16x16x32_bf16 v[88:91], a[128:131], v[148:151], v[88:91]\n\t"
      "buffer_load_dwordx4 v213, s[16:19], s51 offen offset:2048 lds\n\t"
      "s_waitcnt vmcnt(15)\n\t"
      "ds_read_b64_tr_b16 a[146:147], v214 offset:36864\n\t"
      "v_mfma_f32_16x16x32_bf16 v[92:95], a[132:135], v[148:151], v[92:95]\n\t"
      "ds_read_b64_tr_b16 a[150:151], v214 offset:36872\n\t"
      "ds_read_b64_tr_b16 a[154:155], v214 offset:36928\n\t"
      "v_mfma_f32_16x16x32_bf16 v[96:99], a[136:139], v[148:151], v[96:99]\n\t"
      "ds_read_b64_tr_b16 a[158:159], v214 offset:36936\n\t"
      "ds_read_b64_tr_b16 a[162:163], v214 offset:36992\n\t"
      "v_mfma_f32_16x16x32_bf16 v[100:103], a[140:143], v[148:151], v[100:103]\n\t"
      "ds_read_b64_tr_b16 a[166:167], v214 offset:37000\n\t"
      "ds_read_b128 v[152:155], v199 offset:12544\n\t"
      "s_waitcnt lgkmcnt(8)\n\t"
      "v_mfma_f32_16x16x32_bf16 v[104:107], a[120:123], v[160:163], v[104:107]\n\t"
      "s_add_u32 m0, s41, 0x18000\n\t"
      "s_mov_b32 s51, 0x36c00\n\t"
      "buffer_load_dwordx4 v211, s[16:19], s51 offen lds\n\t"
      "v_mfma_f32_16x16x32_bf16 v[108:111], a[124:127], v[160:163], v[108:111]\n\t"
      "buffer_load_dwordx4 v212, s[16:19], s51 offen offset:1024 lds\n\t"
      "v_mfma_f32_16x16x32_bf16 v[112:115], a[128:131], v[160:163], v[112:115]\n\t"
      "buffer_load_dwordx4 v213, s[16:19], s51 offen offset:2048 lds\n\t"
      "s_waitcnt vmcnt(15)\n\t"
      "ds_read_b64_tr_b16 a[168:169], v214 offset:49152\n\t"
      "v_mfma_f32_16x16x32_bf16 v[116:119], a[132:135], v[160:163], v[116:119]\n\t"
      "ds_read_b64_tr_b16 a[172:173], v214 offset:49160\n\t"
      "ds_read_b64_tr_b16 a[176:177], v214 offset:49216\n\t"
      "v_mfma_f32_16x16x32_bf16 v[120:123], a[136:139], v[160:163], v[120:123]\n\t"
      "ds_read_b64_tr_b16 a[180:181], v214 offset:49224\n\t"
      "ds_read_b64_tr_b16 a[184:185], v214 offset:49280\n\t"
      "v_mfma_f32_16x16x32_bf16 v[132:135], a[140:143], v[160:163], v[132:135]\n\t"
      "ds_read_b64_tr_b16 a[188:189], v214 offset:49288\n\t"
      "ds_read_b128 v[144:147], v196 offset:256\n\t"
      "s_waitcnt lgkmcnt(8)\n\t"
      "v_mfma_f32_16x16x32_bf16 v[80:83], a[144:147], v[140:143], v[80:83]\n\t"
      "s_add_u32 m0, s41, 0x1b000\n\t"
      "s_mov_b32 s51, 0x3c000\n\t"
      "buffer_load_dwordx4 v211, s[16:19], s51 offen lds\n\t"
      "v_mfma_f32_16x16x32_bf16 v[84:87], a[148:151], v[140:143], v[84:87]\n\t"
      "buffer_load_dwordx4 v212, s[16:19], s51 offen offset:1024 lds\n\t"
      "v_mfma_f32_16x16x32_bf16 v[88:91], a[152:155], v[140:143], v[88:91]\n\t"
      "buffer_load_dwordx4 v213, s[16:19], s51 offen offset:2048 lds\n\t"
      "s_waitcnt vmcnt(15)\n\t"
      "ds_read_b64_tr_b16 a[170:171], v221 offset:0\n\t"
      "v_mfma_f32_16x16x32_bf16 v[92:95], a[156:159], v[140:143], v[92:95]\n\t"
      "ds_read_b64_tr_b16 a[174:175], v221 offset:8\n\t"
      "ds_read_b64_tr_b16 a[178:179], v221 offset:64\n\t"
      "v_mfma_f32_16x16x32_bf16 v[96:99], a[160:163], v[140:143], v[96:99]\n\t"
      "ds_read_b64_tr_b16 a[182:183], v221 offset:72\n\t"
      "ds_read_b64_tr_b16 a[186:187], v221 offset:128\n\t"
      "v_mfma_f32_16x16x32_bf16 v[100:103], a[164:167], v[140:143], v[100:103]\n\t"
      "ds_read_b64_tr_b16 a[190:191], v221 offset:136\n\t"
      "ds_read_b128 v[156:159], v196 offset:12544\n\t"
      "s_waitcnt lgkmcnt(8)\n\t"
      "v_mfma_f32_16x16x32_bf16 v[104:107], a[144:147], v[152:155], v[104:107]\n\t"
      "s_add_u32 m0, s41, 0x1e000\n\t"
      "s_mov_b32 s51, 0x3cc00\n\t"
      "buffer_load_dwordx4 v211, s[16:19], s51 offen lds\n\t"
      "v_mfma_f32_16x16x32_bf16 v[108:111], a[148:151], v[152:155], v[108:111]\n\t"
      "buffer_load_dwordx4 v212, s[16:19], s51 offen offset:1024 lds\n\t"
      "v_mfma_f32_16x16x32_bf16 v[112:115], a[152:155], v[152:155], v[112:115]\n\t"
      "buffer_load_dwordx4 v213, s[16:19], s51 offen offset:2048 lds\n\t"
      "s_waitcnt vmcnt(15)\n\t"
      "ds_read_b64_tr_b16 a[192:193], v221 offset:12288\n\t"
      "v_mfma_f32_16x16x32_bf16 v[116:119], a[156:159], v[152:155], v[116:119]\n\t"
      "ds_read_b64_tr_b16 a[196:197], v221 offset:12296\n\t"
      "ds_read_b64_tr_b16 a[200:201], v221 offset:12352\n\t"
      "v_mfma_f32_16x16x32_bf16 v[120:123], a[160:163], v[152:155], v[120:123]\n\t"
      "ds_read_b64_tr_b16 a[204:205], v221 offset:12360\n\t"
      "ds_read_b64_tr_b16 a[208:209], v221 offset:12416\n\t"
      "v_mfma_f32_16x16x32_bf16 v[132:135], a[164:167], v[152:155], v[132:135]\n\t"
      "ds_read_b64_tr_b16 a[212:213], v221 offset:12424\n\t"
      "ds_read_b128 v[148:151], v198 offset:512\n\t"
      "s_waitcnt lgkmcnt(8)\n\t"
      "v_mfma_f32_16x16x32_bf16 v[80:83], a[168:171], v[144:147], v[80:83]\n\t"
      "s_add_u32 m0, s41, 0x21000\n\t"
      "s_mov_b32 s51, 0x42000\n\t"
      "buffer_load_dwordx4 v211, s[16:19], s51 offen lds\n\t"
      "v_mfma_f32_16x16x32_bf16 v[84:87], a[172:175], v[144:147], v[84:87]\n\t"
      "buffer_load_dwordx4 v212, s[16:19], s51 offen offset:1024 lds\n\t"
      "v_mfma_f32_16x16x32_bf16 v[88:91], a[176:179], v[144:147], v[88:91]\n\t"
      "buffer_load_dwordx4 v213, s[16:19], s51 offen offset:2048 lds\n\t"
      "s_waitcnt vmcnt(15)\n\t"
      "ds_read_b64_tr_b16 a[194:195], v214 offset:0\n\t"
      "v_mfma_f32_16x16x32_bf16 v[92:95], a[180:183], v[144:147], v[92:95]\n\t"
      "ds_read_b64_tr_b16 a[198:199], v214 offset:8\n\t"
      "ds_read_b64_tr_b16 a[202:203], v214 offset:64\n\t"
      "v_mfma_f32_16x16x32_bf16 v[96:99], a[184:187], v[144:147], v[96:99]\n\t"
      "ds_read_b64_tr_b16 a[206:207], v214 offset:72\n\t"
      "ds_read_b64_tr_b16 a[210:211], v214 offset:128\n\t"
      "v_mfma_f32_16x16x32_bf16 v[100:103], a[188:191], v[144:147], v[100:103]\n\t"
      "ds_read_b64_tr_b16 a[214:215], v214 offset:136\n\t"
      "ds_read_b128 v[160:163], v198 offset:12800\n\t"
      "s_waitcnt lgkmcnt(8)\n\t"
      "v_mfma_f32_16x16x32_bf16 v[104:107], a[168:171], v[156:159], v[104:107]\n\t"
      "s_add_u32 m0, s41, 0x24000\n\t"
      "s_mov_b32 s51, 0x42c00\n\t"
      "buffer_load_dwordx4 v211, s[16:19], s51 offen lds\n\t"
      "v_mfma_f32_16x16x32_bf16 v[108:111], a[172:175], v[156:159], v[108:111]\n\t"
      "buffer_load_dwordx4 v212, s[16:19], s51 offen offset:1024 lds\n\t"
      "v_mfma_f32_16x16x32_bf16 v[112:115], a[176:179], v[156:159], v[112:115]\n\t"
      "buffer_load_dwordx4 v213, s[16:19], s51 offen offset:2048 lds\n\t"
      "s_waitcnt vmcnt(15)\n\t"
      "ds_read_b64_tr_b16 a[216:217], v214 offset:12288\n\t"
      "v_mfma_f32_16x16x32_bf16 v[116:119], a[180:183], v[156:159], v[116:119]\n\t"
      "ds_read_b64_tr_b16 a[220:221], v214 offset:12296\n\t"
      "ds_read_b64_tr_b16 a[224:225], v214 offset:12352\n\t"
      "v_mfma_f32_16x16x32_bf16 v[120:123], a[184:187], v[156:159], v[120:123]\n\t"
      "ds_read_b64_tr_b16 a[228:229], v214 offset:12360\n\t"
      "ds_read_b64_tr_b16 a[232:233], v214 offset:12416\n\t"
      "v_mfma_f32_16x16x32_bf16 v[132:135], a[188:191], v[156:159], v[132:135]\n\t"
      "ds_read_b64_tr_b16 a[236:237], v214 offset:12424\n\t"
      "ds_read_b128 v[140:143], v199 offset:512\n\t"
      "s_waitcnt lgkmcnt(8)\n\t"
      "v_mfma_f32_16x16x32_bf16 v[80:83], a[192:195], v[148:151], v[80:83]\n\t"
      "s_add_u32 m0, s41, 0x6000\n\t"
      "s_add_u32 s51, s40, 0x600000\n\t"
      "buffer_load_dwordx4 v208, s[20:23], s51 offen lds\n\t"
      "v_mfma_f32_16x16x32_bf16 v[84:87], a[196:199], v[148:151], v[84:87]\n\t"
      "buffer_load_dwordx4 v208, s[20:23], s51 offen offset:1024 lds\n\t"
      "v_mfma_f32_16x16x32_bf16 v[88:91], a[200:203], v[148:151], v[88:91]\n\t"
      "buffer_load_dwordx4 v208, s[20:23], s51 offen offset:2048 lds\n\t"
      "s_waitcnt vmcnt(15)\n\t"
      "ds_read_b64_tr_b16 a[218:219], v214 offset:24576\n\t"
      "v_mfma_f32_16x16x32_bf16 v[92:95], a[204:207], v[148:151], v[92:95]\n\t"
      "ds_read_b64_tr_b16 a[222:223], v214 offset:24584\n\t"
      "ds_read_b64_tr_b16 a[226:227], v214 offset:24640\n\t"
      "v_mfma_f32_16x16x32_bf16 v[96:99], a[208:211], v[148:151], v[96:99]\n\t"
      "ds_read_b64_tr_b16 a[230:231], v214 offset:24648\n\t"
      "ds_read_b64_tr_b16 a[234:235], v214 offset:24704\n\t"
      "v_mfma_f32_16x16x32_bf16 v[100:103], a[212:215], v[148:151], v[100:103]\n\t"
      "ds_read_b64_tr_b16 a[238:239], v214 offset:24712\n\t"
      "ds_read_b128 v[152:155], v199 offset:12800\n\t"
      "s_waitcnt lgkmcnt(14)\n\t"
      "v_mfma_f32_16x16x32_bf16 v[104:107], a[192:195], v[160:163], v[104:107]\n\t"
      "s_add_u32 m0, s41, 0x9000\n\t"
      "s_add_u32 s51, s40, 0x600000\n\t"
      "buffer_load_dwordx4 v209, s[20:23], s51 offen lds\n\t"
      "v_mfma_f32_16x16x32_bf16 v[108:111], a[196:199], v[160:163], v[108:111]\n\t"
      "buffer_load_dwordx4 v209, s[20:23], s51 offen offset:1024 lds\n\t"
      "v_mfma_f32_16x16x32_bf16 v[112:115], a[200:203], v[160:163], v[112:115]\n\t"
      "buffer_load_dwordx4 v209, s[20:23], s51 offen offset:2048 lds\n\t"
      "s_waitcnt vmcnt(15)\n\t"
      "ds_read_b64_tr_b16 a[240:241], v214 offset:36864\n\t"
      "v_mfma_f32_16x16x32_bf16 v[116:119], a[204:207], v[160:163], v[116:119]\n\t"
      "ds_read_b64_tr_b16 a[244:245], v214 offset:36872\n\t"
      "ds_read_b64_tr_b16 a[248:249], v214 offset:36928\n\t"
      "v_mfma_f32_16x16x32_bf16 v[120:123], a[208:211], v[160:163], v[120:123]\n\t"
      "ds_read_b64_tr_b16 a[252:253], v214 offset:36936\n\t"
      "ds_read_b64_tr_b16 v[164:165], v214 offset:36992\n\t"
      "v_mfma_f32_16x16x32_bf16 v[132:135], a[212:215], v[160:163], v[132:135]\n\t"
      "ds_read_b64_tr_b16 v[168:169], v214 offset:37000\n\t"
      "ds_read_b128 v[144:147], v196 offset:512\n\t"
      "s_waitcnt lgkmcnt(8)\n\t"
      "v_mfma_f32_16x16x32_bf16 v[80:83], a[216:219], v[140:143], v[80:83]\n\t"
      "s_add_u32 m0, s41, 0xc000\n\t"
      "s_add_u32 s51, s40, 0xc00000\n\t"
      "buffer_load_dwordx4 v208, s[20:23], s51 offen lds\n\t"
      "v_mfma_f32_16x16x32_bf16 v[84:87], a[220:223], v[140:143], v[84:87]\n\t"
      "buffer_load_dwordx4 v208, s[20:23], s51 offen offset:1024 lds\n\t"
      "v_mfma_f32_16x16x32_bf16 v[88:91], a[224:227], v[140:143], v[88:91]\n\t"
      "buffer_load_dwordx4 v208, s[20:23], s51 offen offset:2048 lds\n\t"
      "s_waitcnt vmcnt(15)\n\t"
      "ds_read_b64_tr_b16 a[242:243], v214 offset:49152\n\t"
      "v_mfma_f32_16x16x32_bf16 v[92:95], a[228:231], v[140:143], v[92:95]\n\t"
      "ds_read_b64_tr_b16 a[246:247], v214 offset:49160\n\t"
      "ds_read_b64_tr_b16 a[250:251], v214 offset:49216\n\t"
      "v_mfma_f32_16x16x32_bf16 v[96:99], a[232:235], v[140:143], v[96:99]\n\t"
      "ds_read_b64_tr_b16 a[254:255], v214 offset:49224\n\t"
      "ds_read_b64_tr_b16 v[166:167], v214 offset:49280\n\t"
      "v_mfma_f32_16x16x32_bf16 v[100:103], a[236:239], v[140:143], v[100:103]\n\t"
      "ds_read_b64_tr_b16 v[170:171], v214 offset:49288\n\t"
      "ds_read_b128 v[156:159], v196 offset:12800\n\t"
      "s_waitcnt lgkmcnt(14)\n\t"
      "v_mfma_f32_16x16x32_bf16 v[104:107], a[216:219], v[152:155], v[104:107]\n\t"
      "s_add_u32 m0, s41, 0xf000\n\t"
      "s_add_u32 s51, s40, 0xc00000\n\t"
      "buffer_load_dwordx4 v209, s[20:23], s51 offen lds\n\t"
      "v_mfma_f32_16x16x32_bf16 v[108:111], a[220:223], v[152:155], v[108:111]\n\t"
      "buffer_load_dwordx4 v209, s[20:23], s51 offen offset:1024 lds\n\t"
      "v_mfma_f32_16x16x32_bf16 v[112:115], a[224:227], v[152:155], v[112:115]\n\t"
      "buffer_load_dwordx4 v209, s[20:23], s51 offen offset:2048 lds\n\t"
      "s_waitcnt vmcnt(15)\n\t"
      "ds_read_b64_tr_b16 v[172:173], v221 offset:0\n\t"
      "v_mfma_f32_16x16x32_bf16 v[116:119], a[228:231], v[152:155], v[116:119]\n\t"
      "ds_read_b64_tr_b16 v[176:177], v221 offset:8\n\t"
      "ds_read_b64_tr_b16 v[180:181], v221 offset:64\n\t"
      "ds_read_b64_tr_b16 v[184:185], v221 offset:72\n\t"
      "v_mfma_f32_16x16x32_bf16 v[120:123], a[232:235], v[152:155], v[120:123]\n\t"
      "ds_read_b64_tr_b16 v[188:189], v221 offset:128\n\t"
      "ds_read_b64_tr_b16 v[192:193], v221 offset:136\n\t"
      "v_mfma_f32_16x16x32_bf16 v[132:135], a[236:239], v[152:155], v[132:135]\n\t"
      "ds_read_b128 v[148:151], v197 offset:512\n\t"
      "ds_read_b128 v[160:163], v197 offset:12800\n\t"
      "s_waitcnt lgkmcnt(9)\n\t"
      "v_mfma_f32_16x16x32_bf16 v[80:83], a[240:243], v[144:147], v[80:83]\n\t"
      "buffer_load_dwordx2 v[240:241], v245, s[24:27], 0 offen sc1\n\t"
      "v_mfma_f32_16x16x32_bf16 v[84:87], a[244:247], v[144:147], v[84:87]\n\t"
      "s_cmp_eq_u32 s41, s43\n\t"
      "s_cbranch_scc0 .Lc3_%=\n\t"
      "buffer_load_dword v254, v253, s[24:27], 0 offen sc1\n\t"
      ".Lc3_%=:\n\t"
      "v_mfma_f32_16x16x32_bf16 v[88:91], a[248:251], v[144:147], v[88:91]\n\t"
      "s_waitcnt vmcnt(13)\n\t"
      "ds_read_b64_tr_b16 v[174:175], v221 offset:12288\n\t"
      "v_mfma_f32_16x16x32_bf16 v[92:95], a[252:255], v[144:147], v[92:95]\n\t"
      "ds_read_b64_tr_b16 v[178:179], v221 offset:12296\n\t"
      "ds_read_b64_tr_b16 v[182:183], v221 offset:12352\n\t"
      "v_mfma_f32_16x16x32_bf16 v[96:99], v[164:167], v[144:147], v[96:99]\n\t"
      "ds_read_b64_tr_b16 v[186:187], v221 offset:12360\n\t"
      "ds_read_b64_tr_b16 v[190:191], v221 offset:12416\n\t"
      "v_mfma_f32_16x16x32_bf16 v[100:103], v[168:171], v[144:147], v[100:103]\n\t"
      "ds_read_b64_tr_b16 v[194:195], v221 offset:12424\n\t"
      "s_memrealtime s[80:81]\n\t"
      "v_add_u32_e32 v196, s46, v200\n\t"
      "v_add_u32_e32 v197, s46, v201\n\t"
      "v_add_u32_e32 v198, s46, v202\n\t"
      "v_add_u32_e32 v199, s46, v203\n\t"
      "s_waitcnt vmcnt(10)\n\t"
      "s_waitcnt lgkmcnt(6)\n\t"
      "s_barrier\n\t"
      "v_mfma_f32_16x16x32_bf16 v[104:107], a[240:243], v[156:159], v[104:107]\n\t"
      "ds_read_b128 v[32:35], v196 offset:0\n\t"
      "v_mfma_f32_16x16x32_bf16 v[108:111], a[244:247], v[156:159], v[108:111]\n\t"
      "ds_read_b128 v[36:39], v197 offset:0\n\t"
      "v_mfma_f32_16x16x32_bf16 v[112:115], a[248:251], v[156:159], v[112:115]\n\t"
      "ds_read_b128 v[40:43], v198 offset:0\n\t"
      "v_mfma_f32_16x16x32_bf16 v[116:119], a[252:255], v[156:159], v[116:119]\n\t"
      "ds_read_b128 v[44:47], v199 offset:0\n\t"
      "v_mfma_f32_16x16x32_bf16 v[120:123], v[164:167], v[156:159], v[120:123]\n\t"
      "ds_read_b128 v[48:51], v197 offset:256\n\t"
      "v_mfma_f32_16x16x32_bf16 v[132:135], v[168:171], v[156:159], v[132:135]\n\t"
      "s_waitcnt lgkmcnt(5)\n\t"
      "s_mov_b32 s86, 77\n\t"
      "s_mov_b32 s84, 98\n\t"
      "v_mfma_f32_16x16x32_bf16 v[80:83], v[172:175], v[148:151], v[80:83]\n\t"
      "ds_read_b128 v[52:55], v198 offset:256\n\t"
      "v_mfma_f32_16x16x32_bf16 v[84:87], v[176:179], v[148:151], v[84:87]\n\t"
      "ds_read_b128 v[56:59], v199 offset:256\n\t"
      "v_mfma_f32_16x16x32_bf16 v[88:91], v[180:183], v[148:151], v[88:91]\n\t"
      "ds_read_b128 v[60:63], v196 offset:256\n\t"
      "v_mfma_f32_16x16x32_bf16 v[92:95], v[184:187], v[148:151], v[92:95]\n\t"
      "ds_read_b128 v[64:67], v198 offset:512\n\t"
      "v_mfma_f32_16x16x32_bf16 v[96:99], v[188:191], v[148:151], v[96:99]\n\t"
      "ds_read_b128 v[68:71], v199 offset:512\n\t"
      "v_mfma_f32_16x16x32_bf16 v[100:103], v[192:195], v[148:151], v[100:103]\n\t"
      "ds_read_b128 v[72:75], v196 offset:512\n\t"
      "v_mfma_f32_16x16x32_bf16 v[104:107], v[172:175], v[160:163], v[104:107]\n\t"
      "ds_read_b128 v[76:79], v197 offset:512\n\t"
      "v_mfma_f32_16x16x32_bf16 v[108:111], v[176:179], v[160:163], v[108:111]\n\t"
      "v_mfma_f32_16x16x32_bf16 v[112:115], v[180:183], v[160:163], v[112:115]\n\t"
      "v_mfma_f32_16x16x32_bf16 v[116:119], v[184:187], v[160:163], v[116:119]\n\t"
      "v_mfma_f32_16x16x32_bf16 v[120:123], v[188:191], v[160:163], v[120:123]\n\t"
      "v_mfma_f32_16x16x32_bf16 v[132:135], v[192:195], v[160:163], v[132:135]\n\t"
      ".Lp2_%=:\n\t"
      "s_cbranch_vccnz .Lwsb_%=\n\t"
      "s_waitcnt lgkmcnt(8)\n\t"
      "v_mfma_f32_16x16x32_bf16 v[128:131], a[0:3], v[32:35], v[8:11]\n\t"
      "v_cvt_pk_bf16_f32 v136, v120, v121\n\t"
      "v_mfma_f32_16x16x32_bf16 v[128:131], a[24:27], v[36:39], v[128:131]\n\t"
      "v_cvt_pk_bf16_f32 v137, v122, v123\n\t"
      "v_mfma_f32_16x16x32_bf16 v[128:131], a[48:51], v[40:43], v[128:131]\n\t"
      "v_cvt_pk_bf16_f32 v138, v132, v133\n\t"
      "v_mfma_f32_16x16x32_bf16 v[128:131], a[72:75], v[44:47], v[128:131]\n\t"
      "v_cvt_pk_bf16_f32 v139, v134, v135\n\t"
      "s_waitcnt lgkmcnt(4)\n\t"
      "v_mfma_f32_16x16x32_bf16 v[128:131], a[96:99], v[48:51], v[128:131]\n\t"
      "ds_write_b128 v219, v[136:139] offset:12288\n\t"
      "v_mfma_f32_16x16x32_bf16 v[128:131], a[120:123], v[52:55], v[128:131]\n\t"
      "v_cvt_pk_bf16_f32 v80, v80, v81\n\t"
      "v_mfma_f32_16x16x32_bf16 v[128:131], a[144:147], v[56:59], v[128:131]\n\t"
      "v_cvt_pk_bf16_f32 v81, v82, v83\n\t"
      "v_mfma_f32_16x16x32_bf16 v[128:131], a[168:171], v[60:63], v[128:131]\n\t"
      "v_cvt_pk_bf16_f32 v82, v84, v85\n\t"
      "s_waitcnt lgkmcnt(1)\n\t"
      "v_mfma_f32_16x16x32_bf16 v[128:131], a[192:195], v[64:67], v[128:131]\n\t"
      "v_cvt_pk_bf16_f32 v83, v86, v87\n\t"
      "v_mfma_f32_16x16x32_bf16 v[128:131], a[216:219], v[68:71], v[128:131]\n\t"
      "ds_write_b128 v225, v[80:83] offset:0\n\t"
      "v_mfma_f32_16x16x32_bf16 v[128:131], a[240:243], v[72:75], v[128:131]\n\t"
      "v_cvt_pk_bf16_f32 v88, v88, v89\n\t"
      "v_mfma_f32_16x16x32_bf16 v[128:131], v[172:175], v[76:79], v[128:131]\n\t"
      "v_mfma_f32_16x16x32_bf16 v[132:135], a[4:7], v[32:35], v[12:15]\n\t"
      "v_cvt_pk_bf16_f32 v89, v90, v91\n\t"
      "v_mfma_f32_16x16x32_bf16 v[132:135], a[28:31], v[36:39], v[132:135]\n\t"
      "v_cvt_pk_bf16_f32 v90, v92, v93\n\t"
      "v_mfma_f32_16x16x32_bf16 v[132:135], a[52:55], v[40:43], v[132:135]\n\t"
      "v_cvt_pk_bf16_f32 v136, v128, v129\n\t"
      "v_cvt_pk_bf16_f32 v91, v94, v95\n\t"
      "v_mfma_f32_16x16x32_bf16 v[132:135], a[76:79], v[44:47], v[132:135]\n\t"
      "v_cvt_pk_bf16_f32 v137, v130, v131\n\t"
      "ds_write_b128 v7, v[88:91] offset:0\n\t"
      "v_mfma_f32_16x16x32_bf16 v[132:135], a[100:103], v[48:51], v[132:135]\n\t"
      "v_cvt_pk_bf16_f32 v96, v96, v97\n\t"
      "s_add_u32 s48, s47, 0x6000\n\t"
      "v_mfma_f32_16x16x32_bf16 v[132:135], a[124:127], v[52:55], v[132:135]\n\t"
      "s_cmp_eq_u32 s48, s50\n\t"
      "s_cselect_b32 s48, s41, s48\n\t"
      "v_mfma_f32_16x16x32_bf16 v[132:135], a[148:151], v[56:59], v[132:135]\n\t"
      "v_cvt_pk_bf16_f32 v97, v98, v99\n\t"
      "v_mfma_f32_16x16x32_bf16 v[132:135], a[172:175], v[60:63], v[132:135]\n\t"
      "v_cvt_pk_bf16_f32 v98, v100, v101\n\t"
      "v_add_u32_e32 v217, s46, v204\n\t"
      "v_mfma_f32_16x16x32_bf16 v[132:135], a[196:199], v[64:67], v[132:135]\n\t"
      "v_cvt_pk_bf16_f32 v99, v102, v103\n\t"
      "v_add_u32_e32 v218, s46, v205\n\t"
      "v_mfma_f32_16x16x32_bf16 v[132:135], a[220:223], v[68:71], v[132:135]\n\t"
      "v_readlane_b32 s73, v248, s85\n\t"
      "v_add_u32_e32 v219, s46, v206\n\t"
      "v_mfma_f32_16x16x32_bf16 v[132:135], a[244:247], v[72:75], v[132:135]\n\t"
      "s_mov_b32 m0, s45\n\t"
      "v_mfma_f32_16x16x32_bf16 v[132:135], v[176:179], v[76:79], v[132:135]\n\t"
      "s_waitcnt lgkmcnt(0)\n\t"
      "s_barrier\n\t"
      "v_mfma_f32_16x16x32_bf16 v[128:131], a[8:11], v[32:35], v[16:19]\n\t"
      "ds_write_b128 v210, v[96:99] offset:0\n\t"
      "s_mov_b32 s82, s80\n\t"
      "v_mfma_f32_16x16x32_bf16 v[128:131], a[32:35], v[36:39], v[128:131]\n\t"
      "v_cvt_pk_bf16_f32 v104, v104, v105\n\t"
      "s_memrealtime s[80:81]\n\t"
      "v_mfma_f32_16x16x32_bf16 v[128:131], a[56:59], v[40:43], v[128:131]\n\t"
      "v_cvt_pk_bf16_f32 v138, v132, v133\n\t"
      "v_cvt_pk_bf16_f32 v105, v106, v107\n\t"
      "v_mfma_f32_16x16x32_bf16 v[128:131], a[80:83], v[44:47], v[128:131]\n\t"
      "v_cvt_pk_bf16_f32 v139, v134, v135\n\t"
      "v_cvt_pk_bf16_f32 v106, v108, v109\n\t"
      "v_mfma_f32_16x16x32_bf16 v[128:131], a[104:107], v[48:51], v[128:131]\n\t"
      "ds_write_b128 v217, v[136:139] offset:0\n\t"
      "v_mfma_f32_16x16x32_bf16 v[128:131], a[128:131], v[52:55], v[128:131]\n\t"
      "buffer_load_dwordx4 v208, s[20:23], s73 offen lds\n\t"
      "v_mfma_f32_16x16x32_bf16 v[128:131], a[152:155], v[56:59], v[128:131]\n\t"
      "v_cvt_pk_bf16_f32 v107, v110, v111\n\t"
      "v_mfma_f32_16x16x32_bf16 v[128:131], a[176:179], v[60:63], v[128:131]\n\t"
      "ds_write_b128 v225, v[104:107] offset:12288\n\t"
      "v_mfma_f32_16x16x32_bf16 v[128:131], a[200:203], v[64:67], v[128:131]\n\t"
      "v_cvt_pk_bf16_f32 v112, v112, v113\n\t"
      "v_mfma_f32_16x16x32_bf16 v[128:131], a[224:227], v[68:71], v[128:131]\n\t"
      "v_cvt_pk_bf16_f32 v113, v114, v115\n\t"
      "v_mfma_f32_16x16x32_bf16 v[128:131], a[248:251], v[72:75], v[128:131]\n\t"
      "v_cvt_pk_bf16_f32 v114, v116, v117\n\t"
      "v_mfma_f32_16x16x32_bf16 v[128:131], v[180:183], v[76:79], v[128:131]\n\t"
      "v_mfma_f32_16x16x32_bf16 v[132:135], a[12:15], v[32:35], v[20:23]\n\t"
      "v_cvt_pk_bf16_f32 v115, v118, v119\n\t"
      "v_mfma_f32_16x16x32_bf16 v[132:135], a[36:39], v[36:39], v[132:135]\n\t"
      "buffer_load_dwordx4 v208, s[20:23], s73 offen offset:1024 lds\n\t"
      "v_mfma_f32_16x16x32_bf16 v[132:135], a[60:63], v[40:43], v[132:135]\n\t"
      "v_cvt_pk_bf16_f32 v136, v128, v129\n\t"
      "ds_write_b128 v7, v[112:115] offset:12288\n\t"
      "v_mfma_f32_16x16x32_bf16 v[132:135], a[84:87], v[44:47], v[132:135]\n\t"
      "v_cvt_pk_bf16_f32 v137, v130, v131\n\t"
      "v_mfma_f32_16x16x32_bf16 v[132:135], a[108:111], v[48:51], v[132:135]\n\t"
      "v_mfma_f32_16x16x32_bf16 v[132:135], a[132:135], v[52:55], v[132:135]\n\t"
      "v_mfma_f32_16x16x32_bf16 v[132:135], a[156:159], v[56:59], v[132:135]\n\t"
      "v_mfma_f32_16x16x32_bf16 v[132:135], a[180:183], v[60:63], v[132:135]\n\t"
      "v_mfma_f32_16x16x32_bf16 v[132:135], a[204:207], v[64:67], v[132:135]\n\t"
      "v_mfma_f32_16x16x32_bf16 v[132:135], a[228:231], v[68:71], v[132:135]\n\t"
      "buffer_load_dwordx4 v208, s[20:23], s73 offen offset:2048 lds\n\t"
      "v_mfma_f32_16x16x32_bf16 v[132:135], a[252:255], v[72:75], v[132:135]\n\t"
      "v_readlane_b32 s73, v249, s85\n\t"
      "s_add_u32 m0, s45, 0x3000\n\t"
      "v_mfma_f32_16x16x32_bf16 v[132:135], v[184:187], v[76:79], v[132:135]\n\t"
      "s_waitcnt vmcnt(10)\n\t"
      "s_barrier\n\t"
      "v_mfma_f32_16x16x32_bf16 v[128:131], a[16:19], v[32:35], v[24:27]\n\t"
      "ds_read_b128 v[80:83], v196 offset:12288\n\t"
      "v_mfma_f32_16x16x32_bf16 v[128:131], a[40:43], v[36:39], v[128:131]\n\t"
      "v_mfma_f32_16x16x32_bf16 v[128:131], a[64:67], v[40:43], v[128:131]\n\t"
      "ds_read_b128 v[84:87], v197 offset:12288\n\t"
      "v_cvt_pk_bf16_f32 v138, v132, v133\n\t"
      "v_mfma_f32_16x16x32_bf16 v[128:131], a[88:91], v[44:47], v[128:131]\n\t"
      "ds_read_b128 v[88:91], v198 offset:12288\n\t"
      "v_cvt_pk_bf16_f32 v139, v134, v135\n\t"
      "v_mfma_f32_16x16x32_bf16 v[128:131], a[112:115], v[48:51], v[128:131]\n\t"
      "ds_write_b128 v218, v[136:139] offset:0\n\t"
      "v_mfma_f32_16x16x32_bf16 v[128:131], a[136:139], v[52:55], v[128:131]\n\t"
      "buffer_load_dwordx4 v209, s[20:23], s73 offen lds\n\t"
      "s_waitcnt vmcnt(10)\n\t"
      "s_cmp_eq_u32 s94, 0\n\t"
      "s_cbranch_scc0 .Lwgvw_0_%=\n\t"
      "s_waitcnt vmcnt(4)\n\t"
      ".Lwgvw_0_%=:\n\t"
      "v_subrev_u32_e32 v242, s63, v240\n\t"
      "v_xor_b32_e32 v241, s53, v241\n\t"
      "v_mfma_f32_16x16x32_bf16 v[128:131], a[160:163], v[56:59], v[128:131]\n\t"
      "ds_read_b128 v[92:95], v199 offset:12288\n\t"
      "v_lshrrev_b32_e32 v240, 13, v242\n\t"
      "v_or_b32_e32 v240, v240, v241\n\t"
      "v_mfma_f32_16x16x32_bf16 v[128:131], a[184:187], v[60:63], v[128:131]\n\t"
      "ds_read_b128 v[96:99], v197 offset:12544\n\t"
      "v_mfma_f32_16x16x32_bf16 v[128:131], a[208:211], v[64:67], v[128:131]\n\t"
      "v_cmp_ne_u32_e32 vcc, 0, v240\n\t"
      "v_and_b32_e32 v242, 15, v242\n\t"
      "v_mfma_f32_16x16x32_bf16 v[128:131], a[232:235], v[68:71], v[128:131]\n\t"
      "v_mfma_f32_16x16x32_bf16 v[128:131], v[164:167], v[72:75], v[128:131]\n\t"
      "ds_read_b128 v[100:103], v198 offset:12544\n\t"
      "s_cbranch_vccnz .Lgs_%=\n\t"
      ".Lgr_%=:\n\t"
      "ds_write_b32 v246, v242\n\t"
      "v_mfma_f32_16x16x32_bf16 v[128:131], v[188:191], v[76:79], v[128:131]\n\t"
      "ds_read_b128 v[104:107], v199 offset:12544\n\t"
      "v_mfma_f32_16x16x32_bf16 v[132:135], a[20:23], v[32:35], v[28:31]\n\t"
      "ds_read_b128 v[108:111], v196 offset:12544\n\t"
      "v_mfma_f32_16x16x32_bf16 v[132:135], a[44:47], v[36:39], v[132:135]\n\t"
      "buffer_load_dwordx4 v209, s[20:23], s73 offen offset:1024 lds\n\t"
      "v_mfma_f32_16x16x32_bf16 v[132:135], a[68:71], v[40:43], v[132:135]\n\t"
      "ds_read_b128 v[112:115], v198 offset:12800\n\t"
      "v_cvt_pk_bf16_f32 v136, v128, v129\n\t"
      "v_add_u32_e32 v220, s46, v207\n\t"
      "v_mfma_f32_16x16x32_bf16 v[132:135], a[92:95], v[44:47], v[132:135]\n\t"
      "ds_read_b128 v[116:119], v199 offset:12800\n\t"
      "v_cvt_pk_bf16_f32 v137, v130, v131\n\t"
      "s_sub_u32 s84, s84, 6\n\t"
      "s_max_i32 s84, s84, s86\n\t"
      "v_mfma_f32_16x16x32_bf16 v[132:135], a[116:119], v[48:51], v[132:135]\n\t"
      "s_sub_u32 s83, s82, s80\n\t"
      "v_mfma_f32_16x16x32_bf16 v[132:135], a[140:143], v[52:55], v[132:135]\n\t"
      "s_max_i32 s83, s83, 0\n\t"
      "s_add_u32 s82, s80, s83\n\t"
      "v_mfma_f32_16x16x32_bf16 v[132:135], a[164:167], v[56:59], v[132:135]\n\t"
      "ds_read_b128 v[120:123], v196 offset:12800\n\t"
      "v_mfma_f32_16x16x32_bf16 v[132:135], a[188:191], v[60:63], v[132:135]\n\t"
      "ds_read_b128 v[124:127], v197 offset:12800\n\t"
      "v_mfma_f32_16x16x32_bf16 v[132:135], a[212:215], v[64:67], v[132:135]\n\t"
      "v_mfma_f32_16x16x32_bf16 v[132:135], a[236:239], v[68:71], v[132:135]\n\t"
      "s_add_u32 s82, s82, s84\n\t"
      "v_mfma_f32_16x16x32_bf16 v[132:135], v[168:171], v[72:75], v[132:135]\n\t"
      "s_add_u32 s44, s44, 1\n\t"
      "s_add_u32 s85, s85, 1\n\t"
      "v_mfma_f32_16x16x32_bf16 v[132:135], v[192:195], v[76:79], v[132:135]\n\t"
      "s_waitcnt lgkmcnt(9)\n\t"
      "v_mfma_f32_16x16x32_bf16 v[128:131], a[0:3], v[80:83], v[8:11]\n\t"
      "v_mfma_f32_16x16x32_bf16 v[128:131], a[24:27], v[84:87], v[128:131]\n\t"
      "v_readlane_b32 s72, v248, s44\n\t"
      "v_mfma_f32_16x16x32_bf16 v[128:131], a[48:51], v[88:91], v[128:131]\n\t"
      "v_cvt_pk_bf16_f32 v138, v132, v133\n\t"
      "v_mfma_f32_16x16x32_bf16 v[128:131], a[72:75], v[92:95], v[128:131]\n\t"
      "v_cvt_pk_bf16_f32 v139, v134, v135\n\t"
      "s_waitcnt lgkmcnt(4)\n\t"
      "v_mfma_f32_16x16x32_bf16 v[128:131], a[96:99], v[96:99], v[128:131]\n\t"
      "ds_write_b128 v219, v[136:139] offset:0\n\t"
      "v_mfma_f32_16x16x32_bf16 v[128:131], a[120:123], v[100:103], v[128:131]\n\t"
      "v_mfma_f32_16x16x32_bf16 v[128:131], a[144:147], v[104:107], v[128:131]\n\t"
      "buffer_load_dwordx4 v209, s[20:23], s73 offen offset:2048 lds\n\t"
      "v_mfma_f32_16x16x32_bf16 v[128:131], a[168:171], v[108:111], v[128:131]\n\t"
      "v_add_u32_e32 v196, s47, v200\n\t"
      "s_waitcnt lgkmcnt(1)\n\t"
      "v_mfma_f32_16x16x32_bf16 v[128:131], a[192:195], v[112:115], v[128:131]\n\t"
      "v_add_u32_e32 v197, s47, v201\n\t"
      "v_mfma_f32_16x16x32_bf16 v[128:131], a[216:219], v[116:119], v[128:131]\n\t"
      "v_add_u32_e32 v198, s47, v202\n\t"
      "v_mfma_f32_16x16x32_bf16 v[128:131], a[240:243], v[120:123], v[128:131]\n\t"
      "v_add_u32_e32 v199, s47, v203\n\t"
      "v_mfma_f32_16x16x32_bf16 v[128:131], v[172:175], v[124:127], v[128:131]\n\t"
      "s_cmp_gt_i32 s83, 1\n\t"
      "s_cbranch_scc1 .LslpP3_%=\n\t"
      ".LretP3_%=:\n\t"
      "s_cmp_eq_u32 s94, 1\n\t"
      "s_cbranch_scc0 .LwP3barv_1_%=\n\t"
      "s_waitcnt vmcnt(9)\n\t"
      ".LwP3barv_1_%=:\n\t"
      "s_waitcnt lgkmcnt(0)\n\t"
      "s_barrier\n\t"
      "v_mfma_f32_16x16x32_bf16 v[132:135], a[4:7], v[80:83], v[12:15]\n\t"
      "ds_read_b128 v[140:143], v220 offset:0\n\t"
      "v_mfma_f32_16x16x32_bf16 v[132:135], a[28:31], v[84:87], v[132:135]\n\t"
      "ds_read_b128 v[240:243], v247 offset:0\n\t"
      "v_mfma_f32_16x16x32_bf16 v[132:135], a[52:55], v[88:91], v[132:135]\n\t"
      "v_cvt_pk_bf16_f32 v136, v128, v129\n\t"
      "v_mfma_f32_16x16x32_bf16 v[132:135], a[76:79], v[92:95], v[132:135]\n\t"
      "ds_read_b128 v[32:35], v196 offset:0\n\t"
      "v_cvt_pk_bf16_f32 v137, v130, v131\n\t"
      "v_mfma_f32_16x16x32_bf16 v[132:135], a[100:103], v[96:99], v[132:135]\n\t"
      "v_mfma_f32_16x16x32_bf16 v[132:135], a[124:127], v[100:103], v[132:135]\n\t"
      "ds_read_b128 v[226:229], v238 offset:0\n\t"
      "v_mfma_f32_16x16x32_bf16 v[132:135], a[148:151], v[104:107], v[132:135]\n\t"
      "ds_read_b128 v[36:39], v197 offset:0\n\t"
      "v_mfma_f32_16x16x32_bf16 v[132:135], a[172:175], v[108:111], v[132:135]\n\t"
      "v_mfma_f32_16x16x32_bf16 v[132:135], a[196:199], v[112:115], v[132:135]\n\t"
      "s_waitcnt lgkmcnt(3)\n\t"
      "v_add3_u32 v240, v240, v241, v242\n\t"
      "v_mfma_f32_16x16x32_bf16 v[132:135], a[220:223], v[116:119], v[132:135]\n\t"
      "ds_read_b128 v[40:43], v198 offset:0\n\t"
      "v_mfma_f32_16x16x32_bf16 v[132:135], a[244:247], v[120:123], v[132:135]\n\t"
      "v_add_u32_e32 v240, v240, v243\n\t"
      "v_mfma_f32_16x16x32_bf16 v[132:135], v[176:179], v[124:127], v[132:135]\n\t"
      "buffer_store_dwordx4 v[140:143], v208, s[28:31], s72 offen sc1\n\t"
      "v_mfma_f32_16x16x32_bf16 v[128:131], a[8:11], v[80:83], v[16:19]\n\t"
      "ds_read_b128 v[44:47], v199 offset:0\n\t"
      "v_mfma_f32_16x16x32_bf16 v[128:131], a[32:35], v[84:87], v[128:131]\n\t"
      "v_add_u32_dpp v241, v240, v240 quad_perm:[1,0,3,2] row_mask:0xf bank_mask:0xf\n\t"
      "s_memrealtime s[80:81]\n\t"
      "v_mfma_f32_16x16x32_bf16 v[128:131], a[56:59], v[88:91], v[128:131]\n\t"
      "v_cvt_pk_bf16_f32 v138, v132, v133\n\t"
      "v_mfma_f32_16x16x32_bf16 v[128:131], a[80:83], v[92:95], v[128:131]\n\t"
      "v_cvt_pk_bf16_f32 v139, v134, v135\n\t"
      "v_mfma_f32_16x16x32_bf16 v[128:131], a[104:107], v[96:99], v[128:131]\n\t"
      "ds_write_b128 v217, v[136:139] offset:12288\n\t"
      "v_add_u32_dpp v240, v241, v241 quad_perm:[2,3,0,1] row_mask:0xf bank_mask:0xf\n\t"
      "v_mfma_f32_16x16x32_bf16 v[128:131], a[128:131], v[100:103], v[128:131]\n\t"
      "s_mov_b32 m0, s46\n\t"
      "v_mfma_f32_16x16x32_bf16 v[128:131], a[152:155], v[104:107], v[128:131]\n\t"
      "v_cmp_le_u32_e64 s[76:77], 17, v240\n\t"
      "v_cmp_ge_u32_e64 s[78:79], 15, v240\n\t"
      "v_mfma_f32_16x16x32_bf16 v[128:131], a[176:179], v[108:111], v[128:131]\n\t"
      "ds_read_b128 v[48:51], v197 offset:256\n\t"
      "v_mfma_f32_16x16x32_bf16 v[128:131], a[200:203], v[112:115], v[128:131]\n\t"
      "v_cmp_lt_u32_e32 vcc, 63, v240\n\t"
      "s_and_b32 s65, s76, s77\n\t"
      "v_mfma_f32_16x16x32_bf16 v[128:131], a[224:227], v[116:119], v[128:131]\n\t"
      "ds_read_b128 v[52:55], v198 offset:256\n\t"
      "v_mfma_f32_16x16x32_bf16 v[128:131], a[248:251], v[120:123], v[128:131]\n\t"
      "s_and_b32 s78, s78, s79\n\t"
      "s_and_b32 s67, s76, 0x11111111\n\t"
      "ds_read_b128 v[230:233], v238 offset:1024\n\t"
      "v_mfma_f32_16x16x32_bf16 v[128:131], v[180:183], v[124:127], v[128:131]\n\t"
      "v_mfma_f32_16x16x32_bf16 v[132:135], a[12:15], v[80:83], v[20:23]\n\t"
      "ds_read_b128 v[56:59], v199 offset:256\n\t"
      "s_or_b32 s65, s65, s78\n\t"
      "s_cmp_lg_u64 vcc, 0\n\t"
      "s_cselect_b32 s65, 0, s65\n\t"
      "s_cselect_b32 s67, 0, s67\n\t"
      "v_mfma_f32_16x16x32_bf16 v[132:135], a[36:39], v[84:87], v[132:135]\n\t"
      "v_and_b32_e32 v240, s67, v252\n\t"
      "v_and_b32_e32 v241, s65, v252\n\t"
      "v_mfma_f32_16x16x32_bf16 v[132:135], a[60:63], v[88:91], v[132:135]\n\t"
      "v_cvt_pk_bf16_f32 v136, v128, v129\n\t"
      "v_mfma_f32_16x16x32_bf16 v[132:135], a[84:87], v[92:95], v[132:135]\n\t"
      "v_cvt_pk_bf16_f32 v137, v130, v131\n\t"
      "ds_read_b128 v[144:147], v220 offset:1024\n\t"
      "v_mfma_f32_16x16x32_bf16 v[132:135], a[108:111], v[96:99], v[132:135]\n\t"
      "v_cmp_ne_u32_e64 s[76:77], 0, v240\n\t"
      "v_cmp_ne_u32_e64 s[78:79], 0, v241\n\t"
      "v_mfma_f32_16x16x32_bf16 v[132:135], a[132:135], v[100:103], v[132:135]\n\t"
      "v_mfma_f32_16x16x32_bf16 v[132:135], a[156:159], v[104:107], v[132:135]\n\t"
      "ds_read_b128 v[60:63], v196 offset:256\n\t"
      "s_bcnt1_i32_b32 s51, s78\n\t"
      "s_cmp_lt_u32 s51, 4\n\t"
      "s_cselect_b32 s93, s92, s93\n\t"
      "s_cselect_b32 s92, 16, 0\n\t"
      "s_mov_b32 s51, 0\n\t"
      "v_mfma_f32_16x16x32_bf16 v[132:135], a[180:183], v[108:111], v[132:135]\n\t"
      "s_xor_b32 s75, s75, s76\n\t"
      "s_xor_b32 s74, s74, s76\n\t"
      "v_mfma_f32_16x16x32_bf16 v[132:135], a[204:207], v[112:115], v[132:135]\n\t"
      "s_and_b32 s76, s75, s93\n\t"
      "v_cndmask_b32_e64 v249, v248, v251, s[76:77]\n\t"
      "v_mfma_f32_16x16x32_bf16 v[132:135], a[228:231], v[116:119], v[132:135]\n\t"
      "ds_read_b128 v[64:67], v198 offset:512\n\t"
      "v_mfma_f32_16x16x32_bf16 v[132:135], a[252:255], v[120:123], v[132:135]\n\t"
      "s_and_b32 s76, s74, s93\n\t"
      "v_cndmask_b32_e64 v248, v248, v251, s[76:77]\n\t"
      "v_mfma_f32_16x16x32_bf16 v[132:135], v[184:187], v[124:127], v[132:135]\n\t"
      "s_waitcnt lgkmcnt(2)\n\t"
      "buffer_store_dwordx4 v[144:147], v208, s[28:31], s72 offen offset:1024 sc1\n\t"
      "v_mfma_f32_16x16x32_bf16 v[128:131], a[16:19], v[80:83], v[24:27]\n\t"
      "ds_read_b128 v[68:71], v199 offset:512\n\t"
      "s_sub_u32 s51, s51, s92\n\t"
      "v_mfma_f32_16x16x32_bf16 v[128:131], a[40:43], v[84:87], v[128:131]\n\t"
      "s_ashr_i32 s51, s51, 4\n\t"
      "s_and_b32 s64, s65, 0x22222222\n\t"
      "v_mfma_f32_16x16x32_bf16 v[128:131], a[64:67], v[88:91], v[128:131]\n\t"
      "v_cvt_pk_bf16_f32 v138, v132, v133\n\t"
      "v_readlane_b32 s73, v248, s85\n\t"
      "v_mfma_f32_16x16x32_bf16 v[128:131], a[88:91], v[92:95], v[128:131]\n\t"
      "v_cvt_pk_bf16_f32 v139, v134, v135\n\t"
      "ds_read_b128 v[234:237], v238 offset:2048\n\t"
      "v_mfma_f32_16x16x32_bf16 v[128:131], a[112:115], v[96:99], v[128:131]\n\t"
      "ds_write_b128 v218, v[136:139] offset:12288\n\t"
      "v_mfma_f32_16x16x32_bf16 v[128:131], a[136:139], v[100:103], v[128:131]\n\t"
      "v_subrev_u32_e32 v250, s51, v250\n\t"
      "v_mfma_f32_16x16x32_bf16 v[128:131], a[160:163], v[104:107], v[128:131]\n\t"
      "v_mfma_f32_16x16x32_bf16 v[128:131], a[184:187], v[108:111], v[128:131]\n\t"
      "ds_read_b128 v[148:151], v220 offset:2048\n\t"
      "v_mfma_f32_16x16x32_bf16 v[128:131], a[208:211], v[112:115], v[128:131]\n\t"
      "buffer_load_dwordx4 v208, s[20:23], s73 offen lds\n\t"
      "v_mfma_f32_16x16x32_bf16 v[128:131], a[232:235], v[116:119], v[128:131]\n\t"
      "ds_read_b128 v[72:75], v196 offset:512\n\t"
      "s_or_b32 s64, s64, s67\n\t"
      "v_mfma_f32_16x16x32_bf16 v[128:131], v[164:167], v[120:123], v[128:131]\n\t"
      "v_readlane_b32 s86, v250, s44\n\t"
      "v_mfma_f32_16x16x32_bf16 v[128:131], v[188:191], v[124:127], v[128:131]\n\t"
      "v_mfma_f32_16x16x32_bf16 v[132:135], a[20:23], v[80:83], v[28:31]\n\t"
      "ds_read_b128 v[76:79], v197 offset:512\n\t"
      "v_mfma_f32_16x16x32_bf16 v[132:135], a[44:47], v[84:87], v[132:135]\n\t"
      "buffer_load_dwordx4 v208, s[20:23], s73 offen offset:1024 lds\n\t"
      "v_mfma_f32_16x16x32_bf16 v[132:135], a[68:71], v[88:91], v[132:135]\n\t"
      "v_cvt_pk_bf16_f32 v136, v128, v129\n\t"
      "v_mfma_f32_16x16x32_bf16 v[132:135], a[92:95], v[92:95], v[132:135]\n\t"
      "v_cvt_pk_bf16_f32 v137, v130, v131\n\t"
      "s_sub_u32 s84, s84, 6\n\t"
      "s_max_i32 s84, s84, s86\n\t"
      "v_mfma_f32_16x16x32_bf16 v[132:135], a[116:119], v[96:99], v[132:135]\n\t"
      "s_sub_u32 s83, s82, s80\n\t"
      "v_mfma_f32_16x16x32_bf16 v[132:135], a[140:143], v[100:103], v[132:135]\n\t"
      "s_max_i32 s83, s83, 0\n\t"
      "s_add_u32 s82, s80, s83\n\t"
      "v_mfma_f32_16x16x32_bf16 v[132:135], a[164:167], v[104:107], v[132:135]\n\t"
      "s_mov_b32 s45, s46\n\t"
      "v_mfma_f32_16x16x32_bf16 v[132:135], a[188:191], v[108:111], v[132:135]\n\t"
      "s_mov_b32 s46, s47\n\t"
      "v_mfma_f32_16x16x32_bf16 v[132:135], a[212:215], v[112:115], v[132:135]\n\t"
      "s_mov_b32 s47, s48\n\t"
      "v_mfma_f32_16x16x32_bf16 v[132:135], a[236:239], v[116:119], v[132:135]\n\t"
      "s_waitcnt vmcnt(7)\n\t"
      "s_add_u32 s82, s82, s84\n\t"
      "v_mfma_f32_16x16x32_bf16 v[132:135], v[168:171], v[120:123], v[132:135]\n\t"
      "s_cmp_lg_u32 s44, s49\n\t"
      "v_mfma_f32_16x16x32_bf16 v[132:135], v[192:195], v[124:127], v[132:135]\n\t"
      "s_waitcnt lgkmcnt(2)\n\t"
      "buffer_store_dwordx4 v[148:151], v208, s[28:31], s72 offen offset:2048 sc1\n\t"
      ".p2align 6\n\t"
      ".Lloop_%=:\n\t"
      "s_waitcnt lgkmcnt(12)\n\t"
      "v_mfma_f32_16x16x32_bf16 v[128:131], a[0:3], v[32:35], v[8:11]\n\t"
      "v_mfma_f32_16x16x32_bf16 v[128:131], a[24:27], v[36:39], v[128:131]\n\t"
      "v_readlane_b32 s72, v249, s44\n\t"
      "v_mfma_f32_16x16x32_bf16 v[128:131], a[48:51], v[40:43], v[128:131]\n\t"
      "v_cvt_pk_bf16_f32 v138, v132, v133\n\t"
      "v_mfma_f32_16x16x32_bf16 v[128:131], a[72:75], v[44:47], v[128:131]\n\t"
      "v_cvt_pk_bf16_f32 v139, v134, v135\n\t"
      "s_waitcnt lgkmcnt(6)\n\t"
      "v_mfma_f32_16x16x32_bf16 v[128:131], a[96:99], v[48:51], v[128:131]\n\t"
      "ds_write_b128 v219, v[136:139] offset:12288\n\t"
      "v_mfma_f32_16x16x32_bf16 v[128:131], a[120:123], v[52:55], v[128:131]\n\t"
      "v_mfma_f32_16x16x32_bf16 v[128:131], a[144:147], v[56:59], v[128:131]\n\t"
      "buffer_load_dwordx4 v208, s[20:23], s73 offen offset:2048 lds\n\t"
      "v_mfma_f32_16x16x32_bf16 v[128:131], a[168:171], v[60:63], v[128:131]\n\t"
      "s_waitcnt lgkmcnt(1)\n\t"
      "v_mfma_f32_16x16x32_bf16 v[128:131], a[192:195], v[64:67], v[128:131]\n\t"
      "v_add_u32_e32 v217, s46, v204\n\t"
      "v_mfma_f32_16x16x32_bf16 v[128:131], a[216:219], v[68:71], v[128:131]\n\t"
      "v_add_u32_e32 v218, s46, v205\n\t"
      "v_mfma_f32_16x16x32_bf16 v[128:131], a[240:243], v[72:75], v[128:131]\n\t"
      "v_add_u32_e32 v219, s46, v206\n\t"
      "v_mfma_f32_16x16x32_bf16 v[128:131], v[172:175], v[76:79], v[128:131]\n\t"
      "s_cmp_gt_i32 s83, 1\n\t"
      "s_cbranch_scc1 .LslpA_%=\n\t"
      ".LretA_%=:\n\t"
      "s_waitcnt vmcnt(18) lgkmcnt(0)\n\t"
      "s_barrier\n\t"
      "v_mfma_f32_16x16x32_bf16 v[132:135], a[4:7], v[32:35], v[12:15]\n\t"
      "ds_read_b128 v[140:143], v220 offset:12288\n\t"
      "v_mfma_f32_16x16x32_bf16 v[132:135], a[28:31], v[36:39], v[132:135]\n\t"
      "v_mfma_f32_16x16x32_bf16 v[132:135], a[52:55], v[40:43], v[132:135]\n\t"
      "v_cvt_pk_bf16_f32 v136, v128, v129\n\t"
      "v_mfma_f32_16x16x32_bf16 v[132:135], a[76:79], v[44:47], v[132:135]\n\t"
      "ds_read_b128 v[80:83], v196 offset:12288\n\t"
      "v_cvt_pk_bf16_f32 v137, v130, v131\n\t"
      "v_mfma_f32_16x16x32_bf16 v[132:135], a[100:103], v[48:51], v[132:135]\n\t"
      "s_add_u32 s48, s47, 0x6000\n\t"
      "v_mfma_f32_16x16x32_bf16 v[132:135], a[124:127], v[52:55], v[132:135]\n\t"
      "s_cmp_eq_u32 s48, s50\n\t"
      "s_cselect_b32 s48, s41, s48\n\t"
      "v_mfma_f32_16x16x32_bf16 v[132:135], a[148:151], v[56:59], v[132:135]\n\t"
      "ds_read_b128 v[84:87], v197 offset:12288\n\t"
      "v_mfma_f32_16x16x32_bf16 v[132:135], a[172:175], v[60:63], v[132:135]\n\t"
      "v_readlane_b32 s73, v249, s85\n\t"
      "v_mfma_f32_16x16x32_bf16 v[132:135], a[196:199], v[64:67], v[132:135]\n\t"
      "v_mfma_f32_16x16x32_bf16 v[132:135], a[220:223], v[68:71], v[132:135]\n\t"
      "ds_read_b128 v[88:91], v198 offset:12288\n\t"
      "v_mfma_f32_16x16x32_bf16 v[132:135], a[244:247], v[72:75], v[132:135]\n\t"
      "v_mfma_f32_16x16x32_bf16 v[132:135], v[176:179], v[76:79], v[132:135]\n\t"
      "s_waitcnt lgkmcnt(3)\n\t"
      "buffer_store_dwordx4 v[140:143], v209, s[28:31], s72 offen sc1\n\t"
      "v_mfma_f32_16x16x32_bf16 v[128:131], a[8:11], v[32:35], v[16:19]\n\t"
      "ds_read_b128 v[92:95], v199 offset:12288\n\t"
      "v_mfma_f32_16x16x32_bf16 v[128:131], a[32:35], v[36:39], v[128:131]\n\t"
      "s_memrealtime s[80:81]\n\t"
      "v_mfma_f32_16x16x32_bf16 v[128:131], a[56:59], v[40:43], v[128:131]\n\t"
      "v_cvt_pk_bf16_f32 v138, v132, v133\n\t"
      "v_mfma_f32_16x16x32_bf16 v[128:131], a[80:83], v[44:47], v[128:131]\n\t"
      "v_cvt_pk_bf16_f32 v139, v134, v135\n\t"
      "v_mfma_f32_16x16x32_bf16 v[128:131], a[104:107], v[48:51], v[128:131]\n\t"
      "ds_write_b128 v217, v[136:139] offset:0\n\t"
      "v_mfma_f32_16x16x32_bf16 v[128:131], a[128:131], v[52:55], v[128:131]\n\t"
      "s_add_u32 m0, s45, 0x3000\n\t"
      "v_mfma_f32_16x16x32_bf16 v[128:131], a[152:155], v[56:59], v[128:131]\n\t"
      "v_mfma_f32_16x16x32_bf16 v[128:131], a[176:179], v[60:63], v[128:131]\n\t"
      "ds_read_b128 v[96:99], v197 offset:12544\n\t"
      "v_mfma_f32_16x16x32_bf16 v[128:131], a[200:203], v[64:67], v[128:131]\n\t"
      "v_mfma_f32_16x16x32_bf16 v[128:131], a[224:227], v[68:71], v[128:131]\n\t"
      "ds_read_b128 v[100:103], v198 offset:12544\n\t"
      "v_mfma_f32_16x16x32_bf16 v[128:131], a[248:251], v[72:75], v[128:131]\n\t"
      "v_mfma_f32_16x16x32_bf16 v[128:131], v[180:183], v[76:79], v[128:131]\n\t"
      "buffer_load_dwordx4 v209, s[20:23], s73 offen lds\n\t"
      "v_mfma_f32_16x16x32_bf16 v[132:135], a[12:15], v[32:35], v[20:23]\n\t"
      "ds_read_b128 v[104:107], v199 offset:12544\n\t"
      "v_mfma_f32_16x16x32_bf16 v[132:135], a[36:39], v[36:39], v[132:135]\n\t"
      "v_mfma_f32_16x16x32_bf16 v[132:135], a[60:63], v[40:43], v[132:135]\n\t"
      "v_cvt_pk_bf16_f32 v136, v128, v129\n\t"
      "v_mfma_f32_16x16x32_bf16 v[132:135], a[84:87], v[44:47], v[132:135]\n\t"
      "v_cvt_pk_bf16_f32 v137, v130, v131\n\t"
      "ds_read_b128 v[144:147], v220 offset:13312\n\t"
      "v_mfma_f32_16x16x32_bf16 v[132:135], a[108:111], v[48:51], v[132:135]\n\t"
      "v_mfma_f32_16x16x32_bf16 v[132:135], a[132:135], v[52:55], v[132:135]\n\t"
      "v_mfma_f32_16x16x32_bf16 v[132:135], a[156:159], v[56:59], v[132:135]\n\t"
      "ds_read_b128 v[108:111], v196 offset:12544\n\t"
      "v_mfma_f32_16x16x32_bf16 v[132:135], a[180:183], v[60:63], v[132:135]\n\t"
      "v_mfma_f32_16x16x32_bf16 v[132:135], a[204:207], v[64:67], v[132:135]\n\t"
      "v_mfma_f32_16x16x32_bf16 v[132:135], a[228:231], v[68:71], v[132:135]\n\t"
      "ds_read_b128 v[112:115], v198 offset:12800\n\t"
      "v_mfma_f32_16x16x32_bf16 v[132:135], a[252:255], v[72:75], v[132:135]\n\t"
      "v_mfma_f32_16x16x32_bf16 v[132:135], v[184:187], v[76:79], v[132:135]\n\t"
      "s_waitcnt lgkmcnt(2)\n\t"
      "buffer_store_dwordx4 v[144:147], v209, s[28:31], s72 offen offset:1024 sc1\n\t"
      "v_mfma_f32_16x16x32_bf16 v[128:131], a[16:19], v[32:35], v[24:27]\n\t"
      "ds_read_b128 v[116:119], v199 offset:12800\n\t"
      "v_mfma_f32_16x16x32_bf16 v[128:131], a[40:43], v[36:39], v[128:131]\n\t"
      "v_mfma_f32_16x16x32_bf16 v[128:131], a[64:67], v[40:43], v[128:131]\n\t"
      "v_cvt_pk_bf16_f32 v138, v132, v133\n\t"
      "v_mfma_f32_16x16x32_bf16 v[128:131], a[88:91], v[44:47], v[128:131]\n\t"
      "v_cvt_pk_bf16_f32 v139, v134, v135\n\t"
      "v_mfma_f32_16x16x32_bf16 v[128:131], a[112:115], v[48:51], v[128:131]\n\t"
      "ds_write_b128 v218, v[136:139] offset:0\n\t"
      "v_mfma_f32_16x16x32_bf16 v[128:131], a[136:139], v[52:55], v[128:131]\n\t"
      "v_mfma_f32_16x16x32_bf16 v[128:131], a[160:163], v[56:59], v[128:131]\n\t"
      "v_mfma_f32_16x16x32_bf16 v[128:131], a[184:187], v[60:63], v[128:131]\n\t"
      "ds_read_b128 v[148:151], v220 offset:14336\n\t"
      "v_mfma_f32_16x16x32_bf16 v[128:131], a[208:211], v[64:67], v[128:131]\n\t"
      "v_mfma_f32_16x16x32_bf16 v[128:131], a[232:235], v[68:71], v[128:131]\n\t"
      "ds_read_b128 v[120:123], v196 offset:12800\n\t"
      "v_mfma_f32_16x16x32_bf16 v[128:131], v[164:167], v[72:75], v[128:131]\n\t"
      "v_mfma_f32_16x16x32_bf16 v[128:131], v[188:191], v[76:79], v[128:131]\n\t"
      "buffer_load_dwordx4 v209, s[20:23], s73 offen offset:1024 lds\n\t"
      "v_mfma_f32_16x16x32_bf16 v[132:135], a[20:23], v[32:35], v[28:31]\n\t"
      "ds_read_b128 v[124:127], v197 offset:12800\n\t"
      "v_mfma_f32_16x16x32_bf16 v[132:135], a[44:47], v[36:39], v[132:135]\n\t"
      "v_mfma_f32_16x16x32_bf16 v[132:135], a[68:71], v[40:43], v[132:135]\n\t"
      "v_cvt_pk_bf16_f32 v136, v128, v129\n\t"
      "v_add_u32_e32 v220, s46, v207\n\t"
      "v_mfma_f32_16x16x32_bf16 v[132:135], a[92:95], v[44:47], v[132:135]\n\t"
      "v_cvt_pk_bf16_f32 v137, v130, v131\n\t"
      "s_sub_u32 s84, s84, 6\n\t"
      "s_max_i32 s84, s84, s86\n\t"
      "v_mfma_f32_16x16x32_bf16 v[132:135], a[116:119], v[48:51], v[132:135]\n\t"
      "s_sub_u32 s83, s82, s80\n\t"
      "v_mfma_f32_16x16x32_bf16 v[132:135], a[140:143], v[52:55], v[132:135]\n\t"
      "s_max_i32 s83, s83, 0\n\t"
      "s_add_u32 s82, s80, s83\n\t"
      "v_mfma_f32_16x16x32_bf16 v[132:135], a[164:167], v[56:59], v[132:135]\n\t"
      "v_mfma_f32_16x16x32_bf16 v[132:135], a[188:191], v[60:63], v[132:135]\n\t"
      "v_mfma_f32_16x16x32_bf16 v[132:135], a[212:215], v[64:67], v[132:135]\n\t"
      "s_add_u32 s44, s44, 1\n\t"
      "v_mfma_f32_16x16x32_bf16 v[132:135], a[236:239], v[68:71], v[132:135]\n\t"
      "s_add_u32 s82, s82, s84\n\t"
      "v_mfma_f32_16x16x32_bf16 v[132:135], v[168:171], v[72:75], v[132:135]\n\t"
      "s_add_u32 s85, s85, 1\n\t"
      "v_mfma_f32_16x16x32_bf16 v[132:135], v[192:195], v[76:79], v[132:135]\n\t"
      "s_waitcnt lgkmcnt(2)\n\t"
      "buffer_store_dwordx4 v[148:151], v209, s[28:31], s72 offen offset:2048 sc1\n\t"
      "s_waitcnt lgkmcnt(12)\n\t"
      "v_mfma_f32_16x16x32_bf16 v[128:131], a[0:3], v[80:83], v[8:11]\n\t"
      "v_mfma_f32_16x16x32_bf16 v[128:131], a[24:27], v[84:87], v[128:131]\n\t"
      "v_readlane_b32 s72, v248, s44\n\t"
      "v_mfma_f32_16x16x32_bf16 v[128:131], a[48:51], v[88:91], v[128:131]\n\t"
      "v_cvt_pk_bf16_f32 v138, v132, v133\n\t"
      "v_mfma_f32_16x16x32_bf16 v[128:131], a[72:75], v[92:95], v[128:131]\n\t"
      "v_cvt_pk_bf16_f32 v139, v134, v135\n\t"
      "s_waitcnt lgkmcnt(6)\n\t"
      "v_mfma_f32_16x16x32_bf16 v[128:131], a[96:99], v[96:99], v[128:131]\n\t"
      "ds_write_b128 v219, v[136:139] offset:0\n\t"
      "v_mfma_f32_16x16x32_bf16 v[128:131], a[120:123], v[100:103], v[128:131]\n\t"
      "v_mfma_f32_16x16x32_bf16 v[128:131], a[144:147], v[104:107], v[128:131]\n\t"
      "buffer_load_dwordx4 v209, s[20:23], s73 offen offset:2048 lds\n\t"
      "v_mfma_f32_16x16x32_bf16 v[128:131], a[168:171], v[108:111], v[128:131]\n\t"
      "v_add_u32_e32 v196, s47, v200\n\t"
      "s_waitcnt lgkmcnt(1)\n\t"
      "v_mfma_f32_16x16x32_bf16 v[128:131], a[192:195], v[112:115], v[128:131]\n\t"
      "v_add_u32_e32 v197, s47, v201\n\t"
      "v_mfma_f32_16x16x32_bf16 v[128:131], a[216:219], v[116:119], v[128:131]\n\t"
      "v_add_u32_e32 v198, s47, v202\n\t"
      "v_mfma_f32_16x16x32_bf16 v[128:131], a[240:243], v[120:123], v[128:131]\n\t"
      "v_add_u32_e32 v199, s47, v203\n\t"
      "v_mfma_f32_16x16x32_bf16 v[128:131], v[172:175], v[124:127], v[128:131]\n\t"
      "s_cmp_gt_i32 s83, 1\n\t"
      "s_cbranch_scc1 .LslpB_%=\n\t"
      ".LretB_%=:\n\t"
      "s_waitcnt vmcnt(18) lgkmcnt(0)\n\t"
      "s_barrier\n\t"
      "v_mfma_f32_16x16x32_bf16 v[132:135], a[4:7], v[80:83], v[12:15]\n\t"
      "ds_read_b128 v[140:143], v220 offset:0\n\t"
      "v_mfma_f32_16x16x32_bf16 v[132:135], a[28:31], v[84:87], v[132:135]\n\t"
      "v_mfma_f32_16x16x32_bf16 v[132:135], a[52:55], v[88:91], v[132:135]\n\t"
      "v_cvt_pk_bf16_f32 v136, v128, v129\n\t"
      "v_mfma_f32_16x16x32_bf16 v[132:135], a[76:79], v[92:95], v[132:135]\n\t"
      "ds_read_b128 v[32:35], v196 offset:0\n\t"
      "v_cvt_pk_bf16_f32 v137, v130, v131\n\t"
      "v_mfma_f32_16x16x32_bf16 v[132:135], a[100:103], v[96:99], v[132:135]\n\t"
      "v_mfma_f32_16x16x32_bf16 v[132:135], a[124:127], v[100:103], v[132:135]\n\t"
      "v_mfma_f32_16x16x32_bf16 v[132:135], a[148:151], v[104:107], v[132:135]\n\t"
      "ds_read_b128 v[36:39], v197 offset:0\n\t"
      "v_mfma_f32_16x16x32_bf16 v[132:135], a[172:175], v[108:111], v[132:135]\n\t"
      "v_readlane_b32 s73, v248, s85\n\t"
      "v_mfma_f32_16x16x32_bf16 v[132:135], a[196:199], v[112:115], v[132:135]\n\t"
      "v_mfma_f32_16x16x32_bf16 v[132:135], a[220:223], v[116:119], v[132:135]\n\t"
      "ds_read_b128 v[40:43], v198 offset:0\n\t"
      "v_mfma_f32_16x16x32_bf16 v[132:135], a[244:247], v[120:123], v[132:135]\n\t"
      "v_mfma_f32_16x16x32_bf16 v[132:135], v[176:179], v[124:127], v[132:135]\n\t"
      "s_waitcnt lgkmcnt(3)\n\t"
      "buffer_store_dwordx4 v[140:143], v208, s[28:31], s72 offen sc1\n\t"
      "v_mfma_f32_16x16x32_bf16 v[128:131], a[8:11], v[80:83], v[16:19]\n\t"
      "ds_read_b128 v[44:47], v199 offset:0\n\t"
      "v_mfma_f32_16x16x32_bf16 v[128:131], a[32:35], v[84:87], v[128:131]\n\t"
      "s_memrealtime s[80:81]\n\t"
      "v_mfma_f32_16x16x32_bf16 v[128:131], a[56:59], v[88:91], v[128:131]\n\t"
      "v_cvt_pk_bf16_f32 v138, v132, v133\n\t"
      "v_mfma_f32_16x16x32_bf16 v[128:131], a[80:83], v[92:95], v[128:131]\n\t"
      "v_cvt_pk_bf16_f32 v139, v134, v135\n\t"
      "v_mfma_f32_16x16x32_bf16 v[128:131], a[104:107], v[96:99], v[128:131]\n\t"
      "ds_write_b128 v217, v[136:139] offset:12288\n\t"
      "v_mfma_f32_16x16x32_bf16 v[128:131], a[128:131], v[100:103], v[128:131]\n\t"
      "s_mov_b32 m0, s46\n\t"
      "v_mfma_f32_16x16x32_bf16 v[128:131], a[152:155], v[104:107], v[128:131]\n\t"
      "v_mfma_f32_16x16x32_bf16 v[128:131], a[176:179], v[108:111], v[128:131]\n\t"
      "ds_read_b128 v[48:51], v197 offset:256\n\t"
      "v_mfma_f32_16x16x32_bf16 v[128:131], a[200:203], v[112:115], v[128:131]\n\t"
      "v_mfma_f32_16x16x32_bf16 v[128:131], a[224:227], v[116:119], v[128:131]\n\t"
      "ds_read_b128 v[52:55], v198 offset:256\n\t"
      "v_mfma_f32_16x16x32_bf16 v[128:131], a[248:251], v[120:123], v[128:131]\n\t"
      "v_mfma_f32_16x16x32_bf16 v[128:131], v[180:183], v[124:127], v[128:131]\n\t"
      "buffer_load_dwordx4 v208, s[20:23], s73 offen lds\n\t"
      "v_mfma_f32_16x16x32_bf16 v[132:135], a[12:15], v[80:83], v[20:23]\n\t"
      "ds_read_b128 v[56:59], v199 offset:256\n\t"
      "v_mfma_f32_16x16x32_bf16 v[132:135], a[36:39], v[84:87], v[132:135]\n\t"
      "v_mfma_f32_16x16x32_bf16 v[132:135], a[60:63], v[88:91], v[132:135]\n\t"
      "v_cvt_pk_bf16_f32 v136, v128, v129\n\t"
      "v_mfma_f32_16x16x32_bf16 v[132:135], a[84:87], v[92:95], v[132:135]\n\t"
      "v_cvt_pk_bf16_f32 v137, v130, v131\n\t"
      "ds_read_b128 v[144:147], v220 offset:1024\n\t"
      "v_mfma_f32_16x16x32_bf16 v[132:135], a[108:111], v[96:99], v[132:135]\n\t"
      "v_mfma_f32_16x16x32_bf16 v[132:135], a[132:135], v[100:103], v[132:135]\n\t"
      "v_mfma_f32_16x16x32_bf16 v[132:135], a[156:159], v[104:107], v[132:135]\n\t"
      "ds_read_b128 v[60:63], v196 offset:256\n\t"
      "v_mfma_f32_16x16x32_bf16 v[132:135], a[180:183], v[108:111], v[132:135]\n\t"
      "v_mfma_f32_16x16x32_bf16 v[132:135], a[204:207], v[112:115], v[132:135]\n\t"
      "v_mfma_f32_16x16x32_bf16 v[132:135], a[228:231], v[116:119], v[132:135]\n\t"
      "ds_read_b128 v[64:67], v198 offset:512\n\t"
      "v_mfma_f32_16x16x32_bf16 v[132:135], a[252:255], v[120:123], v[132:135]\n\t"
      "v_mfma_f32_16x16x32_bf16 v[132:135], v[184:187], v[124:127], v[132:135]\n\t"
      "s_waitcnt lgkmcnt(2)\n\t"
      "buffer_store_dwordx4 v[144:147], v208, s[28:31], s72 offen offset:1024 sc1\n\t"
      "v_mfma_f32_16x16x32_bf16 v[128:131], a[16:19], v[80:83], v[24:27]\n\t"
      "ds_read_b128 v[68:71], v199 offset:512\n\t"
      "v_mfma_f32_16x16x32_bf16 v[128:131], a[40:43], v[84:87], v[128:131]\n\t"
      "v_readlane_b32 s86, v250, s44\n\t"
      "v_mfma_f32_16x16x32_bf16 v[128:131], a[64:67], v[88:91], v[128:131]\n\t"
      "v_cvt_pk_bf16_f32 v138, v132, v133\n\t"
      "v_mfma_f32_16x16x32_bf16 v[128:131], a[88:91], v[92:95], v[128:131]\n\t"
      "v_cvt_pk_bf16_f32 v139, v134, v135\n\t"
      "v_mfma_f32_16x16x32_bf16 v[128:131], a[112:115], v[96:99], v[128:131]\n\t"
      "ds_write_b128 v218, v[136:139] offset:12288\n\t"
      "v_mfma_f32_16x16x32_bf16 v[128:131], a[136:139], v[100:103], v[128:131]\n\t"
      "v_mfma_f32_16x16x32_bf16 v[128:131], a[160:163], v[104:107], v[128:131]\n\t"
      "v_mfma_f32_16x16x32_bf16 v[128:131], a[184:187], v[108:111], v[128:131]\n\t"
      "ds_read_b128 v[148:151], v220 offset:2048\n\t"
      "v_mfma_f32_16x16x32_bf16 v[128:131], a[208:211], v[112:115], v[128:131]\n\t"
      "v_mfma_f32_16x16x32_bf16 v[128:131], a[232:235], v[116:119], v[128:131]\n\t"
      "ds_read_b128 v[72:75], v196 offset:512\n\t"
      "v_mfma_f32_16x16x32_bf16 v[128:131], v[164:167], v[120:123], v[128:131]\n\t"
      "v_mfma_f32_16x16x32_bf16 v[128:131], v[188:191], v[124:127], v[128:131]\n\t"
      "buffer_load_dwordx4 v208, s[20:23], s73 offen offset:1024 lds\n\t"
      "v_mfma_f32_16x16x32_bf16 v[132:135], a[20:23], v[80:83], v[28:31]\n\t"
      "ds_read_b128 v[76:79], v197 offset:512\n\t"
      "v_mfma_f32_16x16x32_bf16 v[132:135], a[44:47], v[84:87], v[132:135]\n\t"
      "v_mfma_f32_16x16x32_bf16 v[132:135], a[68:71], v[88:91], v[132:135]\n\t"
      "v_cvt_pk_bf16_f32 v136, v128, v129\n\t"
      "v_mfma_f32_16x16x32_bf16 v[132:135], a[92:95], v[92:95], v[132:135]\n\t"
      "v_cvt_pk_bf16_f32 v137, v130, v131\n\t"
      "s_sub_u32 s84, s84, 6\n\t"
      "s_max_i32 s84, s84, s86\n\t"
      "v_mfma_f32_16x16x32_bf16 v[132:135], a[116:119], v[96:99], v[132:135]\n\t"
      "s_sub_u32 s83, s82, s80\n\t"
      "v_mfma_f32_16x16x32_bf16 v[132:135], a[140:143], v[100:103], v[132:135]\n\t"
      "s_max_i32 s83, s83, 0\n\t"
      "s_add_u32 s82, s80, s83\n\t"
      "v_mfma_f32_16x16x32_bf16 v[132:135], a[164:167], v[104:107], v[132:135]\n\t"
      "s_mov_b32 s45, s46\n\t"
      "v_mfma_f32_16x16x32_bf16 v[132:135], a[188:191], v[108:111], v[132:135]\n\t"
      "s_mov_b32 s46, s47\n\t"
      "v_mfma_f32_16x16x32_bf16 v[132:135], a[212:215], v[112:115], v[132:135]\n\t"
      "s_mov_b32 s47, s48\n\t"
      "v_mfma_f32_16x16x32_bf16 v[132:135], a[236:239], v[116:119], v[132:135]\n\t"
      "s_add_u32 s82, s82, s84\n\t"
      "v_mfma_f32_16x16x32_bf16 v[132:135], v[168:171], v[120:123], v[132:135]\n\t"
      "s_cmp_lg_u32 s44, s49\n\t"
      "v_mfma_f32_16x16x32_bf16 v[132:135], v[192:195], v[124:127], v[132:135]\n\t"
      "s_waitcnt lgkmcnt(2)\n\t"
      "buffer_store_dwordx4 v[148:151], v208, s[28:31], s72 offen offset:2048 sc1\n\t"
      "s_cbranch_scc1 .Lloop_%=\n\t"
      "s_cmp_lg_u32 s44, 17\n\t"
      "s_cbranch_scc1 .Lstop_%=\n\t"
      "v_readlane_b32 s72, v249, s44\n\t"
      "ds_read_b32 v241, v247\n\t"
      "s_nop 2\n\t"
      "v_cvt_pk_bf16_f32 v138, v132, v133\n\t"
      "v_cvt_pk_bf16_f32 v139, v134, v135\n\t"
      "ds_write_b128 v219, v[136:139] offset:12288\n\t"
      "s_waitcnt lgkmcnt(0)\n\t"
      "s_barrier\n\t"
      "ds_read_b128 v[32:35], v220 offset:12288\n\t"
      "ds_read_b128 v[36:39], v220 offset:13312\n\t"
      "ds_read_b128 v[40:43], v220 offset:14336\n\t"
      "v_cmp_ne_u32_e32 vcc, 0, v241\n\t"
      "s_cmp_lg_u32 s62, 0\n\t"
      "s_waitcnt lgkmcnt(2)\n\t"
      "buffer_store_dwordx4 v[32:35], v209, s[28:31], s72 offen sc1\n\t"
      "s_waitcnt lgkmcnt(1)\n\t"
      "buffer_store_dwordx4 v[36:39], v209, s[28:31], s72 offen offset:1024 sc1\n\t"
      "s_waitcnt lgkmcnt(0)\n\t"
      "buffer_store_dwordx4 v[40:43], v209, s[28:31], s72 offen offset:2048 sc1\n\t"
      "s_cbranch_scc1 .Lfin_%=\n\t"
      "s_cbranch_vccnz .Lsec_%=\n\t"
      ".Lfin_%=:\n\t"
      "s_endpgm\n\t"
      ".Lstop_%=:\n\t"
      "s_sub_u32 s88, s88, 1\n\t"
      "s_lshr_b32 s68, s88, 1\n\t"
      "s_and_b32 s49, s71, 31\n\t"
      "v_readlane_b32 s68, v248, s68\n\t"
      "s_lshr_b32 s71, s71, 5\n\t"
      "s_cmp_lt_i32 s88, 0\n\t"
      "s_cbranch_scc1 .Lst2_%=\n\t"
      "v_add_u32_e32 v238, 0x3000, v238\n\t"
      "s_sub_u32 s83, s83, 8\n\t"
      "buffer_store_dwordx4 v[226:229], v239, s[28:31], s68 offen sc1\n\t"
      "buffer_store_dwordx4 v[230:233], v239, s[28:31], s68 offen offset:1024 sc1\n\t"
      "buffer_store_dwordx4 v[234:237], v239, s[28:31], s68 offen offset:2048 sc1\n\t"
      "v_xor_b32_e32 v240, v208, v209\n\t"
      "v_xor_b32_e32 v239, v239, v240\n\t"
      "ds_read_b128 v[226:229], v238 offset:0\n\t"
      "ds_read_b128 v[230:233], v238 offset:1024\n\t"
      "ds_read_b128 v[234:237], v238 offset:2048\n\t"
      ".Lst2_%=:\n\t"
      "s_cmp_lg_u32 s41, s43\n\t"
      "s_cbranch_scc1 .Lloop_%=\n\t"
      "s_sub_u32 s78, s44, s89\n\t"
      "s_cmp_eq_u32 s78, 6\n\t"
      "s_cbranch_scc1 .Lstop1_%=\n\t"
      "s_cmp_lt_u32 s78, 12\n\t"
      "s_cbranch_scc1 .Lloop_%=\n\t"
      "s_cmp_eq_u32 s78, 12\n\t"
      "s_cbranch_scc1 .Lstop2_%=\n\t"
      "s_waitcnt vmcnt(12)\n\t"
      "v_readlane_b32 s76, v254, 1\n\t"
      "v_readlane_b32 s78, v255, 1\n\t"
      "s_andn2_b32 s76, 4, s76\n\t"
      "s_or_b32 s76, s76, s64\n\t"
      "s_xor_b32 s76, s76, s78\n\t"
      "v_mov_b32_e32 v240, s76\n\t"
      "ds_write_b32 v247, v240\n\t"
      "s_branch .Lloop_%=\n\t"
      ".Lstop1_%=:\n\t"
      "v_readlane_b32 s76, v254, 0\n\t"
      "s_andn2_b32 s76, 4, s76\n\t"
      "s_or_b32 s76, s76, s64\n\t"
      "v_mov_b32_e32 v240, s76\n\t"
      "s_mov_b64 exec, 1\n\t"
      "buffer_store_dword v240, v253, s[24:27], 0 offen sc1\n\t"
      "s_mov_b64 exec, -1\n\t"
      "s_branch .Lloop_%=\n\t"
      ".Lstop2_%=:\n\t"
      "buffer_load_dword v255, v253, s[24:27], 0 offen sc1\n\t"
      "s_branch .Lloop_%=\n\t"
      ".LslpP3_%=:\n\t"
      "s_sleep 1\n\t"
      "s_sub_u32 s83, s83, 3\n\t"
      "s_cmp_gt_i32 s83, 1\n\t"
      "s_cbranch_scc1 .LslpP3_%=\n\t"
      "s_branch .LretP3_%=\n\t"
      ".LslpA_%=:\n\t"
      "s_sleep 1\n\t"
      "s_sub_u32 s83, s83, 3\n\t"
      "s_cmp_gt_i32 s83, 1\n\t"
      "s_cbranch_scc1 .LslpA_%=\n\t"
      "s_branch .LretA_%=\n\t"
      ".LslpB_%=:\n\t"
      "s_sleep 1\n\t"
      "s_sub_u32 s83, s83, 3\n\t"
      "s_cmp_gt_i32 s83, 1\n\t"
      "s_cbranch_scc1 .LslpB_%=\n\t"
      "s_branch .LretB_%=\n\t"
      ".Lsec_%=:\n\t"
      "s_mov_b32 s62, 1\n\t"
      "v_lshl_add_u32 v240, v251, 1, s66\n\t"
      "v_sub_u32_e32 v248, v240, v248\n\t"
      "v_sub_u32_e32 v249, v240, v249\n\t"
      "v_cmp_eq_u32_e32 vcc, 0, v252\n\t"
      "v_mov_b32_e32 v240, 0x6c00000\n\t"
      "s_nop 1\n\t"
      "v_cndmask_b32_e32 v248, v248, v240, vcc\n\t"
      "v_cndmask_b32_e32 v249, v249, v240, vcc\n\t"
      "s_add_u32 s44, s94, 3\n\t"
      "s_add_u32 s85, s44, 1\n\t"
      "s_mov_b32 s83, 0\n\t"
      "s_waitcnt vmcnt(0) lgkmcnt(0)\n\t"
      "s_barrier\n\t"
      "v_readlane_b32 s73, v248, s85\n\t"
      "s_mov_b32 m0, s46\n\t"
      "s_nop 4\n\t"
      "buffer_load_dwordx4 v208, s[20:23], s73 offen lds\n\t"
      "buffer_load_dwordx4 v208, s[20:23], s73 offen offset:1024 lds\n\t"
      "buffer_load_dwordx4 v208, s[20:23], s73 offen offset:2048 lds\n\t"
      "v_readlane_b32 s73, v249, s85\n\t"
      "s_add_u32 m0, s46, 0x3000\n\t"
      "s_nop 4\n\t"
      "buffer_load_dwordx4 v209, s[20:23], s73 offen lds\n\t"
      "buffer_load_dwordx4 v209, s[20:23], s73 offen offset:1024 lds\n\t"
      "buffer_load_dwordx4 v209, s[20:23], s73 offen offset:2048 lds\n\t"
      "s_add_u32 s85, s85, 1\n\t"
      "s_nop 4\n\t"
      "v_readlane_b32 s73, v248, s85\n\t"
      "s_mov_b32 m0, s47\n\t"
      "s_nop 4\n\t"
      "buffer_load_dwordx4 v208, s[20:23], s73 offen lds\n\t"
      "buffer_load_dwordx4 v208, s[20:23], s73 offen offset:1024 lds\n\t"
      "buffer_load_dwordx4 v208, s[20:23], s73 offen offset:2048 lds\n\t"
      "v_readlane_b32 s73, v249, s85\n\t"
      "s_add_u32 m0, s47, 0x3000\n\t"
      "s_nop 4\n\t"
      "buffer_load_dwordx4 v209, s[20:23], s73 offen lds\n\t"
      "buffer_load_dwordx4 v209, s[20:23], s73 offen offset:1024 lds\n\t"
      "buffer_load_dwordx4 v209, s[20:23], s73 offen offset:2048 lds\n\t"
      "s_add_u32 s85, s85, 1\n\t"
      "s_nop 4\n\t"
      "v_readlane_b32 s73, v248, s85\n\t"
      "s_mov_b32 m0, s45\n\t"
      "s_nop 4\n\t"
      "buffer_load_dwordx4 v208, s[20:23], s73 offen lds\n\t"
      "buffer_load_dwordx4 v208, s[20:23], s73 offen offset:1024 lds\n\t"
      "buffer_load_dwordx4 v208, s[20:23], s73 offen offset:2048 lds\n\t"
      "s_waitcnt vmcnt(0)\n\t"
      "s_barrier\n\t"
      "ds_read_b128 v[32:35], v196 offset:0\n\t"
      "ds_read_b128 v[36:39], v197 offset:0\n\t"
      "ds_read_b128 v[40:43], v198 offset:0\n\t"
      "ds_read_b128 v[44:47], v199 offset:0\n\t"
      "ds_read_b128 v[48:51], v197 offset:256\n\t"
      "ds_read_b128 v[52:55], v198 offset:256\n\t"
      "ds_read_b128 v[56:59], v199 offset:256\n\t"
      "ds_read_b128 v[60:63], v196 offset:256\n\t"
      "ds_read_b128 v[64:67], v198 offset:512\n\t"
      "ds_read_b128 v[68:71], v199 offset:512\n\t"
      "ds_read_b128 v[72:75], v196 offset:512\n\t"
      "ds_read_b128 v[76:79], v197 offset:512\n\t"
      "s_waitcnt lgkmcnt(0)\n\t"
      "s_branch .Lloop_%=\n\t"
      ".Lgs_%=:\n\t"
      "s_sleep 8\n\t"
      "buffer_load_dwordx2 v[240:241], v245, s[24:27], 0 offen sc1\n\t"
      "s_memrealtime s[76:77]\n\t"
      "s_waitcnt vmcnt(0) lgkmcnt(0)\n\t"
      "v_subrev_u32_e32 v242, s63, v240\n\t"
      "v_xor_b32_e32 v241, s53, v241\n\t"
      "v_lshrrev_b32_e32 v240, 13, v242\n\t"
      "v_or_b32_e32 v240, v240, v241\n\t"
      "v_cmp_ne_u32_e32 vcc, 0, v240\n\t"
      "v_and_b32_e32 v242, 15, v242\n\t"
      "s_cbranch_vccz .Lgr_%=\n\t"
      "s_sub_u32 s76, s76, s52\n\t"
      "s_cmp_lt_u32 s76, 2000\n\t"
      "s_cbranch_scc1 .Lgs_%=\n\t"
      "v_mov_b32_e32 v242, 64\n\t"
      "s_branch .Lgr_%=\n\t"
      ".Lpb_%=:\n\t"
      "s_mov_b64 vcc, -1\n\t"
      "s_branch .Lp2_%=\n\t"
      ".Lwsb_%=:\n\t"
      "s_mov_b64 vcc, 0\n\t"
      "s_waitcnt vmcnt(12)\n\t"
      "v_lshlrev_b32_e32 v8, 16, v226\n\t"
      "v_and_b32_e32 v9, 0xffff0000, v226\n\t"
      "v_lshlrev_b32_e32 v10, 16, v227\n\t"
      "v_and_b32_e32 v11, 0xffff0000, v227\n\t"
      "v_lshlrev_b32_e32 v12, 16, v228\n\t"
      "v_and_b32_e32 v13, 0xffff0000, v228\n\t"
      "v_lshlrev_b32_e32 v14, 16, v229\n\t"
      "v_and_b32_e32 v15, 0xffff0000, v229\n\t"
      "v_lshlrev_b32_e32 v16, 16, v230\n\t"
      "v_and_b32_e32 v17, 0xffff0000, v230\n\t"
      "v_lshlrev_b32_e32 v18, 16, v231\n\t"
      "v_and_b32_e32 v19, 0xffff0000, v231\n\t"
      "v_lshlrev_b32_e32 v20, 16, v232\n\t"
      "v_and_b32_e32 v21, 0xffff0000, v232\n\t"
      "v_lshlrev_b32_e32 v22, 16, v233\n\t"
      "v_and_b32_e32 v23, 0xffff0000, v233\n\t"
      "v_lshlrev_b32_e32 v24, 16, v234\n\t"
      "v_and_b32_e32 v25, 0xffff0000, v234\n\t"
      "v_lshlrev_b32_e32 v26, 16, v235\n\t"
      "v_and_b32_e32 v27, 0xffff0000, v235\n\t"
      "v_lshlrev_b32_e32 v28, 16, v236\n\t"
      "v_and_b32_e32 v29, 0xffff0000, v236\n\t"
      "v_lshlrev_b32_e32 v30, 16, v237\n\t"
      "v_and_b32_e32 v31, 0xffff0000, v237\n\t"
      "s_add_u32 m0, s41, 0x1e000\n\t"
      "s_mov_b32 s51, 0x0\n\t"
      "buffer_load_dwordx4 v211, s[16:19], s51 offen lds\n\t"
      "buffer_load_dwordx4 v212, s[16:19], s51 offen offset:1024 lds\n\t"
      "buffer_load_dwordx4 v213, s[16:19], s51 offen offset:2048 lds\n\t"
      "s_add_u32 m0, s41, 0x21000\n\t"
      "s_mov_b32 s51, 0xc00\n\t"
      "buffer_load_dwordx4 v211, s[16:19], s51 offen lds\n\t"
      "buffer_load_dwordx4 v212, s[16:19], s51 offen offset:1024 lds\n\t"
      "buffer_load_dwordx4 v213, s[16:19], s51 offen offset:2048 lds\n\t"
      "s_add_u32 m0, s41, 0x24000\n\t"
      "s_mov_b32 s51, 0x6000\n\t"
      "buffer_load_dwordx4 v211, s[16:19], s51 offen lds\n\t"
      "buffer_load_dwordx4 v212, s[16:19], s51 offen offset:1024 lds\n\t"
      "buffer_load_dwordx4 v213, s[16:19], s51 offen offset:2048 lds\n\t"
      "s_add_u32 m0, s41, 0x12000\n\t"
      "s_mov_b32 s51, 0x6c00\n\t"
      "buffer_load_dwordx4 v211, s[16:19], s51 offen lds\n\t"
      "buffer_load_dwordx4 v212, s[16:19], s51 offen offset:1024 lds\n\t"
      "buffer_load_dwordx4 v213, s[16:19], s51 offen offset:2048 lds\n\t"
      "s_add_u32 m0, s41, 0x15000\n\t"
      "s_mov_b32 s51, 0xc000\n\t"
      "buffer_load_dwordx4 v211, s[16:19], s51 offen lds\n\t"
      "buffer_load_dwordx4 v212, s[16:19], s51 offen offset:1024 lds\n\t"
      "buffer_load_dwordx4 v213, s[16:19], s51 offen offset:2048 lds\n\t"
      "s_mov_b32 s44, 1\n\t"
      "s_mov_b32 s85, 4\n\t"
      "s_add_u32 s45, s41, 0x6000\n\t"
      "s_add_u32 s46, s41, 0xc000\n\t"
      "s_mov_b32 s47, s41\n\t"
      "s_waitcnt vmcnt(12)\n\t"
      "s_barrier\n\t"
      "ds_read_b64_tr_b16 a[0:1], v214 offset:49152\n\t"
      "ds_read_b64_tr_b16 a[4:5], v214 offset:49160\n\t"
      "ds_read_b64_tr_b16 a[8:9], v214 offset:49216\n\t"
      "ds_read_b64_tr_b16 a[12:13], v214 offset:49224\n\t"
      "ds_read_b64_tr_b16 a[16:17], v214 offset:49280\n\t"
      "ds_read_b64_tr_b16 a[20:21], v214 offset:49288\n\t"
      "ds_read_b128 v[156:159], v196 offset:0\n\t"
      "ds_read_b128 v[160:163], v196 offset:12288\n\t"
      "ds_read_b128 v[128:131], v196 offset:24576\n\t"
      "s_add_u32 m0, s41, 0x18000\n\t"
      "s_mov_b32 s51, 0xcc00\n\t"
      "buffer_load_dwordx4 v211, s[16:19], s51 offen lds\n\t"
      "buffer_load_dwordx4 v212, s[16:19], s51 offen offset:1024 lds\n\t"
      "buffer_load_dwordx4 v213, s[16:19], s51 offen offset:2048 lds\n\t"
      "s_waitcnt vmcnt(12)\n\t"
      "ds_read_b64_tr_b16 a[2:3], v221 offset:0\n\t"
      "ds_read_b64_tr_b16 a[6:7], v221 offset:8\n\t"
      "ds_read_b64_tr_b16 a[10:11], v221 offset:64\n\t"
      "ds_read_b64_tr_b16 a[14:15], v221 offset:72\n\t"
      "ds_read_b64_tr_b16 a[18:19], v221 offset:128\n\t"
      "ds_read_b64_tr_b16 a[22:23], v221 offset:136\n\t"
      "s_add_u32 m0, s41, 0x1b000\n\t"
      "s_mov_b32 s51, 0x12000\n\t"
      "buffer_load_dwordx4 v211, s[16:19], s51 offen lds\n\t"
      "buffer_load_dwordx4 v212, s[16:19], s51 offen offset:1024 lds\n\t"
      "buffer_load_dwordx4 v213, s[16:19], s51 offen offset:2048 lds\n\t"
      "s_waitcnt lgkmcnt(0)\n\t"
      "v_mfma_f32_16x16x32_bf16 v[32:35], a[0:3], v[156:159], v[8:11]\n\t"
      "v_mfma_f32_16x16x32_bf16 v[36:39], a[4:7], v[156:159], v[12:15]\n\t"
      "s_waitcnt vmcnt(12)\n\t"
      "ds_read_b64_tr_b16 a[24:25], v221 offset:12288\n\t"
      "v_mfma_f32_16x16x32_bf16 v[40:43], a[8:11], v[156:159], v[16:19]\n\t"
      "ds_read_b64_tr_b16 a[28:29], v221 offset:12296\n\t"
      "v_mfma_f32_16x16x32_bf16 v[44:47], a[12:15], v[156:159], v[20:23]\n\t"
      "ds_read_b64_tr_b16 a[32:33], v221 offset:12352\n\t"
      "v_mfma_f32_16x16x32_bf16 v[48:51], a[16:19], v[156:159], v[24:27]\n\t"
      "ds_read_b64_tr_b16 a[36:37], v221 offset:12360\n\t"
      "ds_read_b128 v[136:139], v196 offset:36864\n\t"
      "v_mfma_f32_16x16x32_bf16 v[52:55], a[20:23], v[156:159], v[28:31]\n\t"
      "ds_read_b64_tr_b16 a[40:41], v221 offset:12416\n\t"
      "v_mfma_f32_16x16x32_bf16 v[56:59], a[0:3], v[160:163], v[8:11]\n\t"
      "ds_read_b64_tr_b16 a[44:45], v221 offset:12424\n\t"
      "v_mfma_f32_16x16x32_bf16 v[60:63], a[4:7], v[160:163], v[12:15]\n\t"
      "v_mfma_f32_16x16x32_bf16 v[64:67], a[8:11], v[160:163], v[16:19]\n\t"
      "s_waitcnt vmcnt(9)\n\t"
      "ds_read_b64_tr_b16 a[26:27], v214 offset:0\n\t"
      "v_mfma_f32_16x16x32_bf16 v[68:71], a[12:15], v[160:163], v[20:23]\n\t"
      "ds_read_b64_tr_b16 a[30:31], v214 offset:8\n\t"
      "v_mfma_f32_16x16x32_bf16 v[72:75], a[16:19], v[160:163], v[24:27]\n\t"
      "ds_read_b64_tr_b16 a[34:35], v214 offset:64\n\t"
      "ds_read_b128 v[140:143], v197 offset:0\n\t"
      "v_mfma_f32_16x16x32_bf16 v[76:79], a[20:23], v[160:163], v[28:31]\n\t"
      "ds_read_b64_tr_b16 a[38:39], v214 offset:72\n\t"
      "v_mfma_f32_16x16x32_bf16 v[80:83], a[0:3], v[128:131], v[8:11]\n\t"
      "ds_read_b64_tr_b16 a[42:43], v214 offset:128\n\t"
      "s_add_u32 m0, s41, 0x1e000\n\t"
      "s_mov_b32 s51, 0x12c00\n\t"
      "buffer_load_dwordx4 v211, s[16:19], s51 offen lds\n\t"
      "v_mfma_f32_16x16x32_bf16 v[84:87], a[4:7], v[128:131], v[12:15]\n\t"
      "ds_read_b64_tr_b16 a[46:47], v214 offset:136\n\t"
      "v_mfma_f32_16x16x32_bf16 v[88:91], a[8:11], v[128:131], v[16:19]\n\t"
      "v_mfma_f32_16x16x32_bf16 v[92:95], a[12:15], v[128:131], v[20:23]\n\t"
      "buffer_load_dwordx4 v212, s[16:19], s51 offen offset:1024 lds\n\t"
      "v_mfma_f32_16x16x32_bf16 v[96:99], a[16:19], v[128:131], v[24:27]\n\t"
      "ds_read_b128 v[144:147], v197 offset:12288\n\t"
      "v_mfma_f32_16x16x32_bf16 v[100:103], a[20:23], v[128:131], v[28:31]\n\t"
      "s_waitcnt lgkmcnt(10)\n\t"
      "v_mfma_f32_16x16x32_bf16 v[104:107], a[0:3], v[136:139], v[8:11]\n\t"
      "buffer_load_dwordx4 v213, s[16:19], s51 offen offset:2048 lds\n\t"
      "v_mfma_f32_16x16x32_bf16 v[108:111], a[4:7], v[136:139], v[12:15]\n\t"
      "v_mfma_f32_16x16x32_bf16 v[112:115], a[8:11], v[136:139], v[16:19]\n\t"
      "s_waitcnt vmcnt(9)\n\t"
      "ds_read_b64_tr_b16 a[48:49], v214 offset:12288\n\t"
      "v_mfma_f32_16x16x32_bf16 v[116:119], a[12:15], v[136:139], v[20:23]\n\t"
      "ds_read_b64_tr_b16 a[52:53], v214 offset:12296\n\t"
      "s_add_u32 m0, s41, 0x21000\n\t"
      "s_mov_b32 s51, 0x18000\n\t"
      "buffer_load_dwordx4 v211, s[16:19], s51 offen lds\n\t"
      "v_mfma_f32_16x16x32_bf16 v[120:123], a[16:19], v[136:139], v[24:27]\n\t"
      "ds_read_b64_tr_b16 a[56:57], v214 offset:12352\n\t"
      "ds_read_b128 v[148:151], v197 offset:24576\n\t"
      "v_mfma_f32_16x16x32_bf16 v[132:135], a[20:23], v[136:139], v[28:31]\n\t"
      "ds_read_b64_tr_b16 a[60:61], v214 offset:12360\n\t"
      "s_waitcnt lgkmcnt(6)\n\t"
      "v_mfma_f32_16x16x32_bf16 v[32:35], a[24:27], v[140:143], v[32:35]\n\t"
      "ds_read_b64_tr_b16 a[64:65], v214 offset:12416\n\t"
      "buffer_load_dwordx4 v212, s[16:19], s51 offen offset:1024 lds\n\t"
      "v_mfma_f32_16x16x32_bf16 v[36:39], a[28:31], v[140:143], v[36:39]\n\t"
      "ds_read_b64_tr_b16 a[68:69], v214 offset:12424\n\t"
      "v_mfma_f32_16x16x32_bf16 v[40:43], a[32:35], v[140:143], v[40:43]\n\t"
      "v_mfma_f32_16x16x32_bf16 v[44:47], a[36:39], v[140:143], v[44:47]\n\t"
      "buffer_load_dwordx4 v213, s[16:19], s51 offen offset:2048 lds\n\t"
      "v_mfma_f32_16x16x32_bf16 v[48:51], a[40:43], v[140:143], v[48:51]\n\t"
      "ds_read_b128 v[152:155], v197 offset:36864\n\t"
      "v_mfma_f32_16x16x32_bf16 v[52:55], a[44:47], v[140:143], v[52:55]\n\t"
      "s_waitcnt lgkmcnt(8)\n\t"
      "v_mfma_f32_16x16x32_bf16 v[56:59], a[24:27], v[144:147], v[56:59]\n\t"
      "s_add_u32 m0, s41, 0x24000\n\t"
      "s_mov_b32 s51, 0x18c00\n\t"
      "buffer_load_dwordx4 v211, s[16:19], s51 offen lds\n\t"
      "v_mfma_f32_16x16x32_bf16 v[60:63], a[28:31], v[144:147], v[60:63]\n\t"
      "v_mfma_f32_16x16x32_bf16 v[64:67], a[32:35], v[144:147], v[64:67]\n\t"
      "s_waitcnt vmcnt(10)\n\t"
      "ds_read_b64_tr_b16 a[50:51], v214 offset:24576\n\t"
      "v_mfma_f32_16x16x32_bf16 v[68:71], a[36:39], v[144:147], v[68:71]\n\t"
      "ds_read_b64_tr_b16 a[54:55], v214 offset:24584\n\t"
      "buffer_load_dwordx4 v212, s[16:19], s51 offen offset:1024 lds\n\t"
      "v_mfma_f32_16x16x32_bf16 v[72:75], a[40:43], v[144:147], v[72:75]\n\t"
      "ds_read_b64_tr_b16 a[58:59], v214 offset:24640\n\t"
      "ds_read_b128 v[156:159], v198 offset:0\n\t"
      "v_mfma_f32_16x16x32_bf16 v[76:79], a[44:47], v[144:147], v[76:79]\n\t"
      "ds_read_b64_tr_b16 a[62:63], v214 offset:24648\n\t"
      "s_waitcnt lgkmcnt(9)\n\t"
      "v_mfma_f32_16x16x32_bf16 v[80:83], a[24:27], v[148:151], v[80:83]\n\t"
      "ds_read_b64_tr_b16 a[66:67], v214 offset:24704\n\t"
      "buffer_load_dwordx4 v213, s[16:19], s51 offen offset:2048 lds\n\t"
      "v_mfma_f32_16x16x32_bf16 v[84:87], a[28:31], v[148:151], v[84:87]\n\t"
      "ds_read_b64_tr_b16 a[70:71], v214 offset:24712\n\t"
      "v_mfma_f32_16x16x32_bf16 v[88:91], a[32:35], v[148:151], v[88:91]\n\t"
      "v_mfma_f32_16x16x32_bf16 v[92:95], a[36:39], v[148:151], v[92:95]\n\t"
      "s_add_u32 m0, s41, 0x12000\n\t"
      "s_mov_b32 s51, 0x1e000\n\t"
      "buffer_load_dwordx4 v211, s[16:19], s51 offen lds\n\t"
      "v_mfma_f32_16x16x32_bf16 v[96:99], a[40:43], v[148:151], v[96:99]\n\t"
      "ds_read_b128 v[160:163], v198 offset:12288\n\t"
      "v_mfma_f32_16x16x32_bf16 v[100:103], a[44:47], v[148:151], v[100:103]\n\t"
      "s_waitcnt lgkmcnt(8)\n\t"
      "v_mfma_f32_16x16x32_bf16 v[104:107], a[24:27], v[152:155], v[104:107]\n\t"
      "buffer_load_dwordx4 v212, s[16:19], s51 offen offset:1024 lds\n\t"
      "v_mfma_f32_16x16x32_bf16 v[108:111], a[28:31], v[152:155], v[108:111]\n\t"
      "v_mfma_f32_16x16x32_bf16 v[112:115], a[32:35], v[152:155], v[112:115]\n\t"
      "s_waitcnt vmcnt(11)\n\t"
      "ds_read_b64_tr_b16 a[72:73], v214 offset:36864\n\t"
      "v_mfma_f32_16x16x32_bf16 v[116:119], a[36:39], v[152:155], v[116:119]\n\t"
      "ds_read_b64_tr_b16 a[76:77], v214 offset:36872\n\t"
      "buffer_load_dwordx4 v213, s[16:19], s51 offen offset:2048 lds\n\t"
      "v_mfma_f32_16x16x32_bf16 v[120:123], a[40:43], v[152:155], v[120:123]\n\t"
      "ds_read_b64_tr_b16 a[80:81], v214 offset:36928\n\t"
      "ds_read_b128 v[128:131], v198 offset:24576\n\t"
      "v_mfma_f32_16x16x32_bf16 v[132:135], a[44:47], v[152:155], v[132:135]\n\t"
      "ds_read_b64_tr_b16 a[84:85], v214 offset:36936\n\t"
      "s_waitcnt lgkmcnt(6)\n\t"
      "v_mfma_f32_16x16x32_bf16 v[32:35], a[48:51], v[156:159], v[32:35]\n\t"
      "ds_read_b64_tr_b16 a[88:89], v214 offset:36992\n\t"
      "s_add_u32 m0, s41, 0x15000\n\t"
      "s_mov_b32 s51, 0x1ec00\n\t"
      "buffer_load_dwordx4 v211, s[16:19], s51 offen lds\n\t"
      "v_mfma_f32_16x16x32_bf16 v[36:39], a[52:55], v[156:159], v[36:39]\n\t"
      "ds_read_b64_tr_b16 a[92:93], v214 offset:37000\n\t"
      "v_mfma_f32_16x16x32_bf16 v[40:43], a[56:59], v[156:159], v[40:43]\n\t"
      "v_mfma_f32_16x16x32_bf16 v[44:47], a[60:63], v[156:159], v[44:47]\n\t"
      "buffer_load_dwordx4 v212, s[16:19], s51 offen offset:1024 lds\n\t"
      "v_mfma_f32_16x16x32_bf16 v[48:51], a[64:67], v[156:159], v[48:51]\n\t"
      "ds_read_b128 v[136:139], v198 offset:36864\n\t"
      "v_mfma_f32_16x16x32_bf16 v[52:55], a[68:71], v[156:159], v[52:55]\n\t"
      "s_waitcnt lgkmcnt(8)\n\t"
      "v_mfma_f32_16x16x32_bf16 v[56:59], a[48:51], v[160:163], v[56:59]\n\t"
      "buffer_load_dwordx4 v213, s[16:19], s51 offen offset:2048 lds\n\t"
      "v_mfma_f32_16x16x32_bf16 v[60:63], a[52:55], v[160:163], v[60:63]\n\t"
      "v_mfma_f32_16x16x32_bf16 v[64:67], a[56:59], v[160:163], v[64:67]\n\t"
      "s_waitcnt vmcnt(12)\n\t"
      "ds_read_b64_tr_b16 a[74:75], v214 offset:49152\n\t"
      "v_mfma_f32_16x16x32_bf16 v[68:71], a[60:63], v[160:163], v[68:71]\n\t"
      "ds_read_b64_tr_b16 a[78:79], v214 offset:49160\n\t"
      "s_add_u32 m0, s41, 0x18000\n\t"
      "s_mov_b32 s51, 0x24000\n\t"
      "buffer_load_dwordx4 v211, s[16:19], s51 offen lds\n\t"
      "v_mfma_f32_16x16x32_bf16 v[72:75], a[64:67], v[160:163], v[72:75]\n\t"
      "ds_read_b64_tr_b16 a[82:83], v214 offset:49216\n\t"
      "ds_read_b128 v[140:143], v199 offset:0\n\t"
      "v_mfma_f32_16x16x32_bf16 v[76:79], a[68:71], v[160:163], v[76:79]\n\t"
      "ds_read_b64_tr_b16 a[86:87], v214 offset:49224\n\t"
      "s_waitcnt lgkmcnt(9)\n\t"
      "v_mfma_f32_16x16x32_bf16 v[80:83], a[48:51], v[128:131], v[80:83]\n\t"
      "ds_read_b64_tr_b16 a[90:91], v214 offset:49280\n\t"
      "buffer_load_dwordx4 v212, s[16:19], s51 offen offset:1024 lds\n\t"
      "v_mfma_f32_16x16x32_bf16 v[84:87], a[52:55], v[128:131], v[84:87]\n\t"
      "ds_read_b64_tr_b16 a[94:95], v214 offset:49288\n\t"
      "v_mfma_f32_16x16x32_bf16 v[88:91], a[56:59], v[128:131], v[88:91]\n\t"
      "v_mfma_f32_16x16x32_bf16 v[92:95], a[60:63], v[128:131], v[92:95]\n\t"
      "buffer_load_dwordx4 v213, s[16:19], s51 offen offset:2048 lds\n\t"
      "v_mfma_f32_16x16x32_bf16 v[96:99], a[64:67], v[128:131], v[96:99]\n\t"
      "ds_read_b128 v[144:147], v199 offset:12288\n\t"
      "v_mfma_f32_16x16x32_bf16 v[100:103], a[68:71], v[128:131], v[100:103]\n\t"
      "s_waitcnt lgkmcnt(8)\n\t"
      "v_mfma_f32_16x16x32_bf16 v[104:107], a[48:51], v[136:139], v[104:107]\n\t"
      "s_add_u32 m0, s41, 0x1b000\n\t"
      "s_mov_b32 s51, 0x24c00\n\t"
      "buffer_load_dwordx4 v211, s[16:19], s51 offen lds\n\t"
      "v_mfma_f32_16x16x32_bf16 v[108:111], a[52:55], v[136:139], v[108:111]\n\t"
      "v_mfma_f32_16x16x32_bf16 v[112:115], a[56:59], v[136:139], v[112:115]\n\t"
      "s_waitcnt vmcnt(13)\n\t"
      "ds_read_b64_tr_b16 a[96:97], v221 offset:0\n\t"
      "v_mfma_f32_16x16x32_bf16 v[116:119], a[60:63], v[136:139], v[116:119]\n\t"
      "ds_read_b64_tr_b16 a[100:101], v221 offset:8\n\t"
      "buffer_load_dwordx4 v212, s[16:19], s51 offen offset:1024 lds\n\t"
      "v_mfma_f32_16x16x32_bf16 v[120:123], a[64:67], v[136:139], v[120:123]\n\t"
      "ds_read_b64_tr_b16 a[104:105], v221 offset:64\n\t"
      "ds_read_b128 v[148:151], v199 offset:24576\n\t"
      "v_mfma_f32_16x16x32_bf16 v[132:135], a[68:71], v[136:139], v[132:135]\n\t"
      "ds_read_b64_tr_b16 a[108:109], v221 offset:72\n\t"
      "s_waitcnt lgkmcnt(6)\n\t"
      "v_mfma_f32_16x16x32_bf16 v[32:35], a[72:75], v[140:143], v[32:35]\n\t"
      "ds_read_b64_tr_b16 a[112:113], v221 offset:128\n\t"
      "buffer_load_dwordx4 v213, s[16:19], s51 offen offset:2048 lds\n\t"
      "v_mfma_f32_16x16x32_bf16 v[36:39], a[76:79], v[140:143], v[36:39]\n\t"
      "ds_read_b64_tr_b16 a[116:117], v221 offset:136\n\t"
      "v_mfma_f32_16x16x32_bf16 v[40:43], a[80:83], v[140:143], v[40:43]\n\t"
      "v_mfma_f32_16x16x32_bf16 v[44:47], a[84:87], v[140:143], v[44:47]\n\t"
      "s_add_u32 m0, s41, 0x1e000\n\t"
      "s_mov_b32 s51, 0x2a000\n\t"
      "buffer_load_dwordx4 v211, s[16:19], s51 offen lds\n\t"
      "v_mfma_f32_16x16x32_bf16 v[48:51], a[88:91], v[140:143], v[48:51]\n\t"
      "ds_read_b128 v[152:155], v199 offset:36864\n\t"
      "v_mfma_f32_16x16x32_bf16 v[52:55], a[92:95], v[140:143], v[52:55]\n\t"
      "s_waitcnt lgkmcnt(8)\n\t"
      "v_mfma_f32_16x16x32_bf16 v[56:59], a[72:75], v[144:147], v[56:59]\n\t"
      "buffer_load_dwordx4 v212, s[16:19], s51 offen offset:1024 lds\n\t"
      "v_mfma_f32_16x16x32_bf16 v[60:63], a[76:79], v[144:147], v[60:63]\n\t"
      "v_mfma_f32_16x16x32_bf16 v[64:67], a[80:83], v[144:147], v[64:67]\n\t"
      "s_waitcnt vmcnt(14)\n\t"
      "ds_read_b64_tr_b16 a[98:99], v221 offset:12288\n\t"
      "v_mfma_f32_16x16x32_bf16 v[68:71], a[84:87], v[144:147], v[68:71]\n\t"
      "ds_read_b64_tr_b16 a[102:103], v221 offset:12296\n\t"
      "buffer_load_dwordx4 v213, s[16:19], s51 offen offset:2048 lds\n\t"
      "v_mfma_f32_16x16x32_bf16 v[72:75], a[88:91], v[144:147], v[72:75]\n\t"
      "ds_read_b64_tr_b16 a[106:107], v221 offset:12352\n\t"
      "ds_read_b128 v[156:159], v197 offset:256\n\t"
      "v_mfma_f32_16x16x32_bf16 v[76:79], a[92:95], v[144:147], v[76:79]\n\t"
      "ds_read_b64_tr_b16 a[110:111], v221 offset:12360\n\t"
      "s_waitcnt lgkmcnt(9)\n\t"
      "v_mfma_f32_16x16x32_bf16 v[80:83], a[72:75], v[148:151], v[80:83]\n\t"
      "ds_read_b64_tr_b16 a[114:115], v221 offset:12416\n\t"
      "s_waitcnt lgkmcnt(7)\n\t"
      "s_add_u32 m0, s41, 0x21000\n\t"
      "s_mov_b32 s51, 0x2ac00\n\t"
      "buffer_load_dwordx4 v211, s[16:19], s51 offen lds\n\t"
      "v_mfma_f32_16x16x32_bf16 v[84:87], a[76:79], v[148:151], v[84:87]\n\t"
      "ds_read_b64_tr_b16 a[118:119], v221 offset:12424\n\t"
      "v_mfma_f32_16x16x32_bf16 v[88:91], a[80:83], v[148:151], v[88:91]\n\t"
      "v_mfma_f32_16x16x32_bf16 v[92:95], a[84:87], v[148:151], v[92:95]\n\t"
      "buffer_load_dwordx4 v212, s[16:19], s51 offen offset:1024 lds\n\t"
      "v_mfma_f32_16x16x32_bf16 v[96:99], a[88:91], v[148:151], v[96:99]\n\t"
      "ds_read_b128 v[160:163], v197 offset:12544\n\t"
      "v_mfma_f32_16x16x32_bf16 v[100:103], a[92:95], v[148:151], v[100:103]\n\t"
      "s_waitcnt lgkmcnt(8)\n\t"
      "v_mfma_f32_16x16x32_bf16 v[104:107], a[72:75], v[152:155], v[104:107]\n\t"
      "buffer_load_dwordx4 v213, s[16:19], s51 offen offset:2048 lds\n\t"
      "v_mfma_f32_16x16x32_bf16 v[108:111], a[76:79], v[152:155], v[108:111]\n\t"
      "v_mfma_f32_16x16x32_bf16 v[112:115], a[80:83], v[152:155], v[112:115]\n\t"
      "s_waitcnt vmcnt(15)\n\t"
      "ds_read_b64_tr_b16 a[120:121], v214 offset:0\n\t"
      "v_mfma_f32_16x16x32_bf16 v[116:119], a[84:87], v[152:155], v[116:119]\n\t"
      "ds_read_b64_tr_b16 a[124:125], v214 offset:8\n\t"
      "s_waitcnt lgkmcnt(3)\n\t"
      "s_add_u32 m0, s41, 0x24000\n\t"
      "s_mov_b32 s51, 0x30000\n\t"
      "buffer_load_dwordx4 v211, s[16:19], s51 offen lds\n\t"
      "v_mfma_f32_16x16x32_bf16 v[120:123], a[88:91], v[152:155], v[120:123]\n\t"
      "ds_read_b64_tr_b16 a[128:129], v214 offset:64\n\t"
      "ds_read_b128 v[128:131], v197 offset:24832\n\t"
      "v_mfma_f32_16x16x32_bf16 v[132:135], a[92:95], v[152:155], v[132:135]\n\t"
      "ds_read_b64_tr_b16 a[132:133], v214 offset:72\n\t"
      "v_mfma_f32_16x16x32_bf16 v[32:35], a[96:99], v[156:159], v[32:35]\n\t"
      "ds_read_b64_tr_b16 a[136:137], v214 offset:128\n\t"
      "buffer_load_dwordx4 v212, s[16:19], s51 offen offset:1024 lds\n\t"
      "v_mfma_f32_16x16x32_bf16 v[36:39], a[100:103], v[156:159], v[36:39]\n\t"
      "ds_read_b64_tr_b16 a[140:141], v214 offset:136\n\t"
      "v_mfma_f32_16x16x32_bf16 v[40:43], a[104:107], v[156:159], v[40:43]\n\t"
      "v_mfma_f32_16x16x32_bf16 v[44:47], a[108:111], v[156:159], v[44:47]\n\t"
      "buffer_load_dwordx4 v213, s[16:19], s51 offen offset:2048 lds\n\t"
      "v_mfma_f32_16x16x32_bf16 v[48:51], a[112:115], v[156:159], v[48:51]\n\t"
      "ds_read_b128 v[136:139], v197 offset:37120\n\t"
      "v_mfma_f32_16x16x32_bf16 v[52:55], a[116:119], v[156:159], v[52:55]\n\t"
      "s_waitcnt lgkmcnt(8)\n\t"
      "v_mfma_f32_16x16x32_bf16 v[56:59], a[96:99], v[160:163], v[56:59]\n\t"
      "v_mfma_f32_16x16x32_bf16 v[60:63], a[100:103], v[160:163], v[60:63]\n\t"
      "v_mfma_f32_16x16x32_bf16 v[64:67], a[104:107], v[160:163], v[64:67]\n\t"
      "s_waitcnt vmcnt(15)\n\t"
      "ds_read_b64_tr_b16 a[122:123], v214 offset:12288\n\t"
      "s_waitcnt lgkmcnt(2)\n\t"
      "s_add_u32 m0, s41, 0x12000\n\t"
      "s_mov_b32 s51, 0x30c00\n\t"
      "buffer_load_dwordx4 v211, s[16:19], s51 offen lds\n\t"
      "v_mfma_f32_16x16x32_bf16 v[68:71], a[108:111], v[160:163], v[68:71]\n\t"
      "ds_read_b64_tr_b16 a[126:127], v214 offset:12296\n\t"
      "v_mfma_f32_16x16x32_bf16 v[72:75], a[112:115], v[160:163], v[72:75]\n\t"
      "ds_read_b64_tr_b16 a[130:131], v214 offset:12352\n\t"
      "ds_read_b128 v[140:143], v198 offset:256\n\t"
      "v_mfma_f32_16x16x32_bf16 v[76:79], a[116:119], v[160:163], v[76:79]\n\t"
      "ds_read_b64_tr_b16 a[134:135], v214 offset:12360\n\t"
      "buffer_load_dwordx4 v212, s[16:19], s51 offen offset:1024 lds\n\t"
      "v_mfma_f32_16x16x32_bf16 v[80:83], a[96:99], v[128:131], v[80:83]\n\t"
      "ds_read_b64_tr_b16 a[138:139], v214 offset:12416\n\t"
      "v_mfma_f32_16x16x32_bf16 v[84:87], a[100:103], v[128:131], v[84:87]\n\t"
      "ds_read_b64_tr_b16 a[142:143], v214 offset:12424\n\t"
      "v_mfma_f32_16x16x32_bf16 v[88:91], a[104:107], v[128:131], v[88:91]\n\t"
      "buffer_load_dwordx4 v213, s[16:19], s51 offen offset:2048 lds\n\t"
      "v_mfma_f32_16x16x32_bf16 v[92:95], a[108:111], v[128:131], v[92:95]\n\t"
      "v_mfma_f32_16x16x32_bf16 v[96:99], a[112:115], v[128:131], v[96:99]\n\t"
      "ds_read_b128 v[144:147], v198 offset:12544\n\t"
      "v_mfma_f32_16x16x32_bf16 v[100:103], a[116:119], v[128:131], v[100:103]\n\t"
      "s_waitcnt lgkmcnt(8)\n\t"
      "v_mfma_f32_16x16x32_bf16 v[104:107], a[96:99], v[136:139], v[104:107]\n\t"
      "v_mfma_f32_16x16x32_bf16 v[108:111], a[100:103], v[136:139], v[108:111]\n\t"
      "v_mfma_f32_16x16x32_bf16 v[112:115], a[104:107], v[136:139], v[112:115]\n\t"
      "s_waitcnt vmcnt(15)\n\t"
      "ds_read_b64_tr_b16 a[144:145], v214 offset:24576\n\t"
      "s_waitcnt lgkmcnt(2)\n\t"
      "s_add_u32 m0, s41, 0x15000\n\t"
      "s_mov_b32 s51, 0x36000\n\t"
      "buffer_load_dwordx4 v211, s[16:19], s51 offen lds\n\t"
      "v_mfma_f32_16x16x32_bf16 v[116:119], a[108:111], v[136:139], v[116:119]\n\t"
      "ds_read_b64_tr_b16 a[148:149], v214 offset:24584\n\t"
      "v_mfma_f32_16x16x32_bf16 v[120:123], a[112:115], v[136:139], v[120:123]\n\t"
      "ds_read_b64_tr_b16 a[152:153], v214 offset:24640\n\t"
      "ds_read_b128 v[148:151], v198 offset:24832\n\t"
      "v_mfma_f32_16x16x32_bf16 v[132:135], a[116:119], v[136:139], v[132:135]\n\t"
      "ds_read_b64_tr_b16 a[156:157], v214 offset:24648\n\t"
      "buffer_load_dwordx4 v212, s[16:19], s51 offen offset:1024 lds\n\t"
      "v_mfma_f32_16x16x32_bf16 v[32:35], a[120:123], v[140:143], v[32:35]\n\t"
      "ds_read_b64_tr_b16 a[160:161], v214 offset:24704\n\t"
      "v_mfma_f32_16x16x32_bf16 v[36:39], a[124:127], v[140:143], v[36:39]\n\t"
      "ds_read_b64_tr_b16 a[164:165], v214 offset:24712\n\t"
      "v_mfma_f32_16x16x32_bf16 v[40:43], a[128:131], v[140:143], v[40:43]\n\t"
      "buffer_load_dwordx4 v213, s[16:19], s51 offen offset:2048 lds\n\t"
      "v_mfma_f32_16x16x32_bf16 v[44:47], a[132:135], v[140:143], v[44:47]\n\t"
      "v_mfma_f32_16x16x32_bf16 v[48:51], a[136:139], v[140:143], v[48:51]\n\t"
      "ds_read_b128 v[152:155], v198 offset:37120\n\t"
      "v_mfma_f32_16x16x32_bf16 v[52:55], a[140:143], v[140:143], v[52:55]\n\t"
      "s_waitcnt lgkmcnt(8)\n\t"
      "v_mfma_f32_16x16x32_bf16 v[56:59], a[120:123], v[144:147], v[56:59]\n\t"
      "v_mfma_f32_16x16x32_bf16 v[60:63], a[124:127], v[144:147], v[60:63]\n\t"
      "v_mfma_f32_16x16x32_bf16 v[64:67], a[128:131], v[144:147], v[64:67]\n\t"
      "s_waitcnt vmcnt(15)\n\t"
      "ds_read_b64_tr_b16 a[146:147], v214 offset:36864\n\t"
      "s_waitcnt lgkmcnt(2)\n\t"
      "s_add_u32 m0, s41, 0x18000\n\t"
      "s_mov_b32 s51, 0x36c00\n\t"
      "buffer_load_dwordx4 v211, s[16:19], s51 offen lds\n\t"
      "v_mfma_f32_16x16x32_bf16 v[68:71], a[132:135], v[144:147], v[68:71]\n\t"
      "ds_read_b64_tr_b16 a[150:151], v214 offset:36872\n\t"
      "v_mfma_f32_16x16x32_bf16 v[72:75], a[136:139], v[144:147], v[72:75]\n\t"
      "ds_read_b64_tr_b16 a[154:155], v214 offset:36928\n\t"
      "ds_read_b128 v[156:159], v199 offset:256\n\t"
      "v_mfma_f32_16x16x32_bf16 v[76:79], a[140:143], v[144:147], v[76:79]\n\t"
      "ds_read_b64_tr_b16 a[158:159], v214 offset:36936\n\t"
      "buffer_load_dwordx4 v212, s[16:19], s51 offen offset:1024 lds\n\t"
      "v_mfma_f32_16x16x32_bf16 v[80:83], a[120:123], v[148:151], v[80:83]\n\t"
      "ds_read_b64_tr_b16 a[162:163], v214 offset:36992\n\t"
      "v_mfma_f32_16x16x32_bf16 v[84:87], a[124:127], v[148:151], v[84:87]\n\t"
      "ds_read_b64_tr_b16 a[166:167], v214 offset:37000\n\t"
      "v_mfma_f32_16x16x32_bf16 v[88:91], a[128:131], v[148:151], v[88:91]\n\t"
      "buffer_load_dwordx4 v213, s[16:19], s51 offen offset:2048 lds\n\t"
      "v_mfma_f32_16x16x32_bf16 v[92:95], a[132:135], v[148:151], v[92:95]\n\t"
      "v_mfma_f32_16x16x32_bf16 v[96:99], a[136:139], v[148:151], v[96:99]\n\t"
      "ds_read_b128 v[160:163], v199 offset:12544\n\t"
      "v_mfma_f32_16x16x32_bf16 v[100:103], a[140:143], v[148:151], v[100:103]\n\t"
      "s_waitcnt lgkmcnt(8)\n\t"
      "v_mfma_f32_16x16x32_bf16 v[104:107], a[120:123], v[152:155], v[104:107]\n\t"
      "v_mfma_f32_16x16x32_bf16 v[108:111], a[124:127], v[152:155], v[108:111]\n\t"
      "v_mfma_f32_16x16x32_bf16 v[112:115], a[128:131], v[152:155], v[112:115]\n\t"
      "s_waitcnt vmcnt(15)\n\t"
      "ds_read_b64_tr_b16 a[168:169], v214 offset:49152\n\t"
      "s_waitcnt lgkmcnt(2)\n\t"
      "s_add_u32 m0, s41, 0x1b000\n\t"
      "s_mov_b32 s51, 0x3c000\n\t"
      "buffer_load_dwordx4 v211, s[16:19], s51 offen lds\n\t"
      "v_mfma_f32_16x16x32_bf16 v[116:119], a[132:135], v[152:155], v[116:119]\n\t"
      "ds_read_b64_tr_b16 a[172:173], v214 offset:49160\n\t"
      "v_mfma_f32_16x16x32_bf16 v[120:123], a[136:139], v[152:155], v[120:123]\n\t"
      "ds_read_b64_tr_b16 a[176:177], v214 offset:49216\n\t"
      "ds_read_b128 v[128:131], v199 offset:24832\n\t"
      "v_mfma_f32_16x16x32_bf16 v[132:135], a[140:143], v[152:155], v[132:135]\n\t"
      "ds_read_b64_tr_b16 a[180:181], v214 offset:49224\n\t"
      "buffer_load_dwordx4 v212, s[16:19], s51 offen offset:1024 lds\n\t"
      "v_mfma_f32_16x16x32_bf16 v[32:35], a[144:147], v[156:159], v[32:35]\n\t"
      "ds_read_b64_tr_b16 a[184:185], v214 offset:49280\n\t"
      "v_mfma_f32_16x16x32_bf16 v[36:39], a[148:151], v[156:159], v[36:39]\n\t"
      "ds_read_b64_tr_b16 a[188:189], v214 offset:49288\n\t"
      "v_mfma_f32_16x16x32_bf16 v[40:43], a[152:155], v[156:159], v[40:43]\n\t"
      "buffer_load_dwordx4 v213, s[16:19], s51 offen offset:2048 lds\n\t"
      "v_mfma_f32_16x16x32_bf16 v[44:47], a[156:159], v[156:159], v[44:47]\n\t"
      "v_mfma_f32_16x16x32_bf16 v[48:51], a[160:163], v[156:159], v[48:51]\n\t"
      "ds_read_b128 v[136:139], v199 offset:37120\n\t"
      "v_mfma_f32_16x16x32_bf16 v[52:55], a[164:167], v[156:159], v[52:55]\n\t"
      "s_waitcnt lgkmcnt(8)\n\t"
      "v_mfma_f32_16x16x32_bf16 v[56:59], a[144:147], v[160:163], v[56:59]\n\t"
      "v_mfma_f32_16x16x32_bf16 v[60:63], a[148:151], v[160:163], v[60:63]\n\t"
      "v_mfma_f32_16x16x32_bf16 v[64:67], a[152:155], v[160:163], v[64:67]\n\t"
      "s_waitcnt vmcnt(15)\n\t"
      "ds_read_b64_tr_b16 a[170:171], v221 offset:0\n\t"
      "s_waitcnt lgkmcnt(2)\n\t"
      "s_add_u32 m0, s41, 0x1e000\n\t"
      "s_mov_b32 s51, 0x3cc00\n\t"
      "buffer_load_dwordx4 v211, s[16:19], s51 offen lds\n\t"
      "v_mfma_f32_16x16x32_bf16 v[68:71], a[156:159], v[160:163], v[68:71]\n\t"
      "ds_read_b64_tr_b16 a[174:175], v221 offset:8\n\t"
      "v_mfma_f32_16x16x32_bf16 v[72:75], a[160:163], v[160:163], v[72:75]\n\t"
      "ds_read_b64_tr_b16 a[178:179], v221 offset:64\n\t"
      "ds_read_b128 v[140:143], v196 offset:256\n\t"
      "v_mfma_f32_16x16x32_bf16 v[76:79], a[164:167], v[160:163], v[76:79]\n\t"
      "ds_read_b64_tr_b16 a[182:183], v221 offset:72\n\t"
      "buffer_load_dwordx4 v212, s[16:19], s51 offen offset:1024 lds\n\t"
      "v_mfma_f32_16x16x32_bf16 v[80:83], a[144:147], v[128:131], v[80:83]\n\t"
      "ds_read_b64_tr_b16 a[186:187], v221 offset:128\n\t"
      "v_mfma_f32_16x16x32_bf16 v[84:87], a[148:151], v[128:131], v[84:87]\n\t"
      "ds_read_b64_tr_b16 a[190:191], v221 offset:136\n\t"
      "v_mfma_f32_16x16x32_bf16 v[88:91], a[152:155], v[128:131], v[88:91]\n\t"
      "buffer_load_dwordx4 v213, s[16:19], s51 offen offset:2048 lds\n\t"
      "v_mfma_f32_16x16x32_bf16 v[92:95], a[156:159], v[128:131], v[92:95]\n\t"
      "v_mfma_f32_16x16x32_bf16 v[96:99], a[160:163], v[128:131], v[96:99]\n\t"
      "ds_read_b128 v[144:147], v196 offset:12544\n\t"
      "v_mfma_f32_16x16x32_bf16 v[100:103], a[164:167], v[128:131], v[100:103]\n\t"
      "s_waitcnt lgkmcnt(8)\n\t"
      "v_mfma_f32_16x16x32_bf16 v[104:107], a[144:147], v[136:139], v[104:107]\n\t"
      "v_mfma_f32_16x16x32_bf16 v[108:111], a[148:151], v[136:139], v[108:111]\n\t"
      "v_mfma_f32_16x16x32_bf16 v[112:115], a[152:155], v[136:139], v[112:115]\n\t"
      "s_waitcnt vmcnt(15)\n\t"
      "ds_read_b64_tr_b16 a[192:193], v221 offset:12288\n\t"
      "s_waitcnt lgkmcnt(2)\n\t"
      "s_add_u32 m0, s41, 0x21000\n\t"
      "s_mov_b32 s51, 0x42000\n\t"
      "buffer_load_dwordx4 v211, s[16:19], s51 offen lds\n\t"
      "v_mfma_f32_16x16x32_bf16 v[116:119], a[156:159], v[136:139], v[116:119]\n\t"
      "ds_read_b64_tr_b16 a[196:197], v221 offset:12296\n\t"
      "v_mfma_f32_16x16x32_bf16 v[120:123], a[160:163], v[136:139], v[120:123]\n\t"
      "ds_read_b64_tr_b16 a[200:201], v221 offset:12352\n\t"
      "ds_read_b128 v[148:151], v196 offset:24832\n\t"
      "v_mfma_f32_16x16x32_bf16 v[132:135], a[164:167], v[136:139], v[132:135]\n\t"
      "ds_read_b64_tr_b16 a[204:205], v221 offset:12360\n\t"
      "buffer_load_dwordx4 v212, s[16:19], s51 offen offset:1024 lds\n\t"
      "v_mfma_f32_16x16x32_bf16 v[32:35], a[168:171], v[140:143], v[32:35]\n\t"
      "ds_read_b64_tr_b16 a[208:209], v221 offset:12416\n\t"
      "v_mfma_f32_16x16x32_bf16 v[36:39], a[172:175], v[140:143], v[36:39]\n\t"
      "ds_read_b64_tr_b16 a[212:213], v221 offset:12424\n\t"
      "v_mfma_f32_16x16x32_bf16 v[40:43], a[176:179], v[140:143], v[40:43]\n\t"
      "buffer_load_dwordx4 v213, s[16:19], s51 offen offset:2048 lds\n\t"
      "v_mfma_f32_16x16x32_bf16 v[44:47], a[180:183], v[140:143], v[44:47]\n\t"
      "v_mfma_f32_16x16x32_bf16 v[48:51], a[184:187], v[140:143], v[48:51]\n\t"
      "ds_read_b128 v[152:155], v196 offset:37120\n\t"
      "v_mfma_f32_16x16x32_bf16 v[52:55], a[188:191], v[140:143], v[52:55]\n\t"
      "s_waitcnt lgkmcnt(8)\n\t"
      "v_mfma_f32_16x16x32_bf16 v[56:59], a[168:171], v[144:147], v[56:59]\n\t"
      "v_mfma_f32_16x16x32_bf16 v[60:63], a[172:175], v[144:147], v[60:63]\n\t"
      "v_mfma_f32_16x16x32_bf16 v[64:67], a[176:179], v[144:147], v[64:67]\n\t"
      "s_waitcnt vmcnt(15)\n\t"
      "ds_read_b64_tr_b16 a[194:195], v214 offset:0\n\t"
      "s_waitcnt lgkmcnt(2)\n\t"
      "s_add_u32 m0, s41, 0x24000\n\t"
      "s_mov_b32 s51, 0x42c00\n\t"
      "buffer_load_dwordx4 v211, s[16:19], s51 offen lds\n\t"
      "v_mfma_f32_16x16x32_bf16 v[68:71], a[180:183], v[144:147], v[68:71]\n\t"
      "ds_read_b64_tr_b16 a[198:199], v214 offset:8\n\t"
      "v_mfma_f32_16x16x32_bf16 v[72:75], a[184:187], v[144:147], v[72:75]\n\t"
      "ds_read_b64_tr_b16 a[202:203], v214 offset:64\n\t"
      "ds_read_b128 v[156:159], v198 offset:512\n\t"
      "v_mfma_f32_16x16x32_bf16 v[76:79], a[188:191], v[144:147], v[76:79]\n\t"
      "ds_read_b64_tr_b16 a[206:207], v214 offset:72\n\t"
      "buffer_load_dwordx4 v212, s[16:19], s51 offen offset:1024 lds\n\t"
      "v_mfma_f32_16x16x32_bf16 v[80:83], a[168:171], v[148:151], v[80:83]\n\t"
      "ds_read_b64_tr_b16 a[210:211], v214 offset:128\n\t"
      "v_mfma_f32_16x16x32_bf16 v[84:87], a[172:175], v[148:151], v[84:87]\n\t"
      "ds_read_b64_tr_b16 a[214:215], v214 offset:136\n\t"
      "v_mfma_f32_16x16x32_bf16 v[88:91], a[176:179], v[148:151], v[88:91]\n\t"
      "buffer_load_dwordx4 v213, s[16:19], s51 offen offset:2048 lds\n\t"
      "v_mfma_f32_16x16x32_bf16 v[92:95], a[180:183], v[148:151], v[92:95]\n\t"
      "v_mfma_f32_16x16x32_bf16 v[96:99], a[184:187], v[148:151], v[96:99]\n\t"
      "ds_read_b128 v[160:163], v198 offset:12800\n\t"
      "v_mfma_f32_16x16x32_bf16 v[100:103], a[188:191], v[148:151], v[100:103]\n\t"
      "s_waitcnt lgkmcnt(8)\n\t"
      "v_mfma_f32_16x16x32_bf16 v[104:107], a[168:171], v[152:155], v[104:107]\n\t"
      "s_add_u32 m0, s41, 0xc000\n\t"
      "s_add_u32 s51, s40, 0xc00000\n\t"
      "buffer_load_dwordx4 v208, s[20:23], s51 offen lds\n\t"
      "v_mfma_f32_16x16x32_bf16 v[108:111], a[172:175], v[152:155], v[108:111]\n\t"
      "v_mfma_f32_16x16x32_bf16 v[112:115], a[176:179], v[152:155], v[112:115]\n\t"
      "s_waitcnt vmcnt(16)\n\t"
      "ds_read_b64_tr_b16 a[216:217], v214 offset:12288\n\t"
      "buffer_load_dwordx4 v208, s[20:23], s51 offen offset:1024 lds\n\t"
      "v_mfma_f32_16x16x32_bf16 v[116:119], a[180:183], v[152:155], v[116:119]\n\t"
      "ds_read_b64_tr_b16 a[220:221], v214 offset:12296\n\t"
      "v_mfma_f32_16x16x32_bf16 v[120:123], a[184:187], v[152:155], v[120:123]\n\t"
      "ds_read_b64_tr_b16 a[224:225], v214 offset:12352\n\t"
      "ds_read_b128 v[128:131], v198 offset:25088\n\t"
      "buffer_load_dwordx4 v208, s[20:23], s51 offen offset:2048 lds\n\t"
      "v_mfma_f32_16x16x32_bf16 v[132:135], a[188:191], v[152:155], v[132:135]\n\t"
      "ds_read_b64_tr_b16 a[228:229], v214 offset:12360\n\t"
      "s_waitcnt lgkmcnt(6)\n\t"
      "v_mfma_f32_16x16x32_bf16 v[32:35], a[192:195], v[156:159], v[32:35]\n\t"
      "ds_read_b64_tr_b16 a[232:233], v214 offset:12416\n\t"
      "s_add_u32 m0, s41, 0xf000\n\t"
      "s_add_u32 s51, s40, 0xc00000\n\t"
      "buffer_load_dwordx4 v209, s[20:23], s51 offen lds\n\t"
      "v_mfma_f32_16x16x32_bf16 v[36:39], a[196:199], v[156:159], v[36:39]\n\t"
      "ds_read_b64_tr_b16 a[236:237], v214 offset:12424\n\t"
      "v_mfma_f32_16x16x32_bf16 v[40:43], a[200:203], v[156:159], v[40:43]\n\t"
      "buffer_load_dwordx4 v209, s[20:23], s51 offen offset:1024 lds\n\t"
      "v_mfma_f32_16x16x32_bf16 v[44:47], a[204:207], v[156:159], v[44:47]\n\t"
      "v_mfma_f32_16x16x32_bf16 v[48:51], a[208:211], v[156:159], v[48:51]\n\t"
      "ds_read_b128 v[136:139], v198 offset:37376\n\t"
      "buffer_load_dwordx4 v209, s[20:23], s51 offen offset:2048 lds\n\t"
      "v_mfma_f32_16x16x32_bf16 v[52:55], a[212:215], v[156:159], v[52:55]\n\t"
      "s_waitcnt lgkmcnt(8)\n\t"
      "v_mfma_f32_16x16x32_bf16 v[56:59], a[192:195], v[160:163], v[56:59]\n\t"
      "v_mfma_f32_16x16x32_bf16 v[60:63], a[196:199], v[160:163], v[60:63]\n\t"
      "v_mfma_f32_16x16x32_bf16 v[64:67], a[200:203], v[160:163], v[64:67]\n\t"
      "s_waitcnt vmcnt(18)\n\t"
      "ds_read_b64_tr_b16 a[218:219], v214 offset:24576\n\t"
      "v_mfma_f32_16x16x32_bf16 v[68:71], a[204:207], v[160:163], v[68:71]\n\t"
      "ds_read_b64_tr_b16 a[222:223], v214 offset:24584\n\t"
      "v_mfma_f32_16x16x32_bf16 v[72:75], a[208:211], v[160:163], v[72:75]\n\t"
      "ds_read_b64_tr_b16 a[226:227], v214 offset:24640\n\t"
      "ds_read_b128 v[140:143], v199 offset:512\n\t"
      "v_mfma_f32_16x16x32_bf16 v[76:79], a[212:215], v[160:163], v[76:79]\n\t"
      "ds_read_b64_tr_b16 a[230:231], v214 offset:24648\n\t"
      "s_waitcnt lgkmcnt(9)\n\t"
      "v_mfma_f32_16x16x32_bf16 v[80:83], a[192:195], v[128:131], v[80:83]\n\t"
      "ds_read_b64_tr_b16 a[234:235], v214 offset:24704\n\t"
      "v_mfma_f32_16x16x32_bf16 v[84:87], a[196:199], v[128:131], v[84:87]\n\t"
      "ds_read_b64_tr_b16 a[238:239], v214 offset:24712\n\t"
      "v_mfma_f32_16x16x32_bf16 v[88:91], a[200:203], v[128:131], v[88:91]\n\t"
      "v_mfma_f32_16x16x32_bf16 v[92:95], a[204:207], v[128:131], v[92:95]\n\t"
      "v_mfma_f32_16x16x32_bf16 v[96:99], a[208:211], v[128:131], v[96:99]\n\t"
      "ds_read_b128 v[144:147], v199 offset:12800\n\t"
      "v_mfma_f32_16x16x32_bf16 v[100:103], a[212:215], v[128:131], v[100:103]\n\t"
      "s_waitcnt lgkmcnt(8)\n\t"
      "v_mfma_f32_16x16x32_bf16 v[104:107], a[192:195], v[136:139], v[104:107]\n\t"
      "v_mfma_f32_16x16x32_bf16 v[108:111], a[196:199], v[136:139], v[108:111]\n\t"
      "v_mfma_f32_16x16x32_bf16 v[112:115], a[200:203], v[136:139], v[112:115]\n\t"
      "s_waitcnt vmcnt(15)\n\t"
      "ds_read_b64_tr_b16 a[240:241], v214 offset:36864\n\t"
      "v_mfma_f32_16x16x32_bf16 v[116:119], a[204:207], v[136:139], v[116:119]\n\t"
      "ds_read_b64_tr_b16 a[244:245], v214 offset:36872\n\t"
      "v_mfma_f32_16x16x32_bf16 v[120:123], a[208:211], v[136:139], v[120:123]\n\t"
      "ds_read_b64_tr_b16 a[248:249], v214 offset:36928\n\t"
      "ds_read_b128 v[148:151], v199 offset:25088\n\t"
      "v_mfma_f32_16x16x32_bf16 v[132:135], a[212:215], v[136:139], v[132:135]\n\t"
      "ds_read_b64_tr_b16 a[252:253], v214 offset:36936\n\t"
      "s_waitcnt lgkmcnt(6)\n\t"
      "v_mfma_f32_16x16x32_bf16 v[32:35], a[216:219], v[140:143], v[32:35]\n\t"
      "ds_read_b64_tr_b16 v[164:165], v214 offset:36992\n\t"
      "v_mfma_f32_16x16x32_bf16 v[36:39], a[220:223], v[140:143], v[36:39]\n\t"
      "ds_read_b64_tr_b16 v[168:169], v214 offset:37000\n\t"
      "v_mfma_f32_16x16x32_bf16 v[40:43], a[224:227], v[140:143], v[40:43]\n\t"
      "s_waitcnt vmcnt(12)\n\t"
      "ds_read_b64_tr_b16 a[242:243], v214 offset:49152\n\t"
      "v_mfma_f32_16x16x32_bf16 v[44:47], a[228:231], v[140:143], v[44:47]\n\t"
      "ds_read_b64_tr_b16 a[246:247], v214 offset:49160\n\t"
      "v_mfma_f32_16x16x32_bf16 v[48:51], a[232:235], v[140:143], v[48:51]\n\t"
      "ds_read_b64_tr_b16 a[250:251], v214 offset:49216\n\t"
      "ds_read_b128 v[152:155], v199 offset:37376\n\t"
      "v_mfma_f32_16x16x32_bf16 v[52:55], a[236:239], v[140:143], v[52:55]\n\t"
      "ds_read_b64_tr_b16 a[254:255], v214 offset:49224\n\t"
      "s_waitcnt lgkmcnt(12)\n\t"
      "v_mfma_f32_16x16x32_bf16 v[56:59], a[216:219], v[144:147], v[56:59]\n\t"
      "ds_read_b64_tr_b16 v[166:167], v214 offset:49280\n\t"
      "v_mfma_f32_16x16x32_bf16 v[60:63], a[220:223], v[144:147], v[60:63]\n\t"
      "ds_read_b64_tr_b16 v[170:171], v214 offset:49288\n\t"
      "v_mfma_f32_16x16x32_bf16 v[64:67], a[224:227], v[144:147], v[64:67]\n\t"
      "s_waitcnt vmcnt(9)\n\t"
      "ds_read_b64_tr_b16 v[172:173], v221 offset:0\n\t"
      "v_mfma_f32_16x16x32_bf16 v[68:71], a[228:231], v[144:147], v[68:71]\n\t"
      "ds_read_b64_tr_b16 v[176:177], v221 offset:8\n\t"
      "v_mfma_f32_16x16x32_bf16 v[72:75], a[232:235], v[144:147], v[72:75]\n\t"
      "ds_read_b64_tr_b16 v[180:181], v221 offset:64\n\t"
      "ds_read_b128 v[156:159], v196 offset:512\n\t"
      "v_mfma_f32_16x16x32_bf16 v[76:79], a[236:239], v[144:147], v[76:79]\n\t"
      "ds_read_b64_tr_b16 v[184:185], v221 offset:72\n\t"
      "s_waitcnt lgkmcnt(14)\n\t"
      "v_mfma_f32_16x16x32_bf16 v[80:83], a[216:219], v[148:151], v[80:83]\n\t"
      "ds_read_b64_tr_b16 v[188:189], v221 offset:128\n\t"
      "v_mfma_f32_16x16x32_bf16 v[84:87], a[220:223], v[148:151], v[84:87]\n\t"
      "ds_read_b64_tr_b16 v[192:193], v221 offset:136\n\t"
      "v_mfma_f32_16x16x32_bf16 v[88:91], a[224:227], v[148:151], v[88:91]\n\t"
      "v_mfma_f32_16x16x32_bf16 v[92:95], a[228:231], v[148:151], v[92:95]\n\t"
      "v_mfma_f32_16x16x32_bf16 v[96:99], a[232:235], v[148:151], v[96:99]\n\t"
      "ds_read_b128 v[160:163], v196 offset:12800\n\t"
      "v_mfma_f32_16x16x32_bf16 v[100:103], a[236:239], v[148:151], v[100:103]\n\t"
      "s_waitcnt lgkmcnt(11)\n\t"
      "v_mfma_f32_16x16x32_bf16 v[104:107], a[216:219], v[152:155], v[104:107]\n\t"
      "v_mfma_f32_16x16x32_bf16 v[108:111], a[220:223], v[152:155], v[108:111]\n\t"
      "v_mfma_f32_16x16x32_bf16 v[112:115], a[224:227], v[152:155], v[112:115]\n\t"
      "s_waitcnt vmcnt(6)\n\t"
      "ds_read_b64_tr_b16 v[174:175], v221 offset:12288\n\t"
      "buffer_load_dwordx2 v[240:241], v245, s[24:27], 0 offen sc1\n\t"
      "v_mfma_f32_16x16x32_bf16 v[116:119], a[228:231], v[152:155], v[116:119]\n\t"
      "ds_read_b64_tr_b16 v[178:179], v221 offset:12296\n\t"
      "s_cmp_eq_u32 s41, s43\n\t"
      "s_cbranch_scc0 .Lc4_%=\n\t"
      "buffer_load_dword v254, v253, s[24:27], 0 offen sc1\n\t"
      ".Lc4_%=:\n\t"
      "v_mfma_f32_16x16x32_bf16 v[120:123], a[232:235], v[152:155], v[120:123]\n\t"
      "ds_read_b64_tr_b16 v[182:183], v221 offset:12352\n\t"
      "ds_read_b128 v[140:143], v197 offset:512\n\t"
      "v_mfma_f32_16x16x32_bf16 v[132:135], a[236:239], v[152:155], v[132:135]\n\t"
      "ds_read_b64_tr_b16 v[186:187], v221 offset:12360\n\t"
      "s_waitcnt lgkmcnt(9)\n\t"
      "v_mfma_f32_16x16x32_bf16 v[32:35], a[240:243], v[156:159], v[32:35]\n\t"
      "ds_read_b64_tr_b16 v[190:191], v221 offset:12416\n\t"
      "v_mfma_f32_16x16x32_bf16 v[36:39], a[244:247], v[156:159], v[36:39]\n\t"
      "ds_read_b64_tr_b16 v[194:195], v221 offset:12424\n\t"
      "v_mfma_f32_16x16x32_bf16 v[40:43], a[248:251], v[156:159], v[40:43]\n\t"
      "v_mfma_f32_16x16x32_bf16 v[44:47], a[252:255], v[156:159], v[44:47]\n\t"
      "v_mfma_f32_16x16x32_bf16 v[48:51], v[164:167], v[156:159], v[48:51]\n\t"
      "ds_read_b128 v[144:147], v197 offset:12800\n\t"
      "v_mfma_f32_16x16x32_bf16 v[52:55], v[168:171], v[156:159], v[52:55]\n\t"
      "s_waitcnt lgkmcnt(8)\n\t"
      "v_mfma_f32_16x16x32_bf16 v[56:59], a[240:243], v[160:163], v[56:59]\n\t"
      "v_mfma_f32_16x16x32_bf16 v[60:63], a[244:247], v[160:163], v[60:63]\n\t"
      "v_mfma_f32_16x16x32_bf16 v[64:67], a[248:251], v[160:163], v[64:67]\n\t"
      "v_mfma_f32_16x16x32_bf16 v[68:71], a[252:255], v[160:163], v[68:71]\n\t"
      "v_mfma_f32_16x16x32_bf16 v[72:75], v[164:167], v[160:163], v[72:75]\n\t"
      "ds_read_b128 v[128:131], v196 offset:25088\n\t"
      "v_mfma_f32_16x16x32_bf16 v[76:79], v[168:171], v[160:163], v[76:79]\n\t"
      "s_waitcnt lgkmcnt(2)\n\t"
      "v_mfma_f32_16x16x32_bf16 v[32:35], v[172:175], v[140:143], v[32:35]\n\t"
      "v_mfma_f32_16x16x32_bf16 v[36:39], v[176:179], v[140:143], v[36:39]\n\t"
      "v_mfma_f32_16x16x32_bf16 v[40:43], v[180:183], v[140:143], v[40:43]\n\t"
      "v_mfma_f32_16x16x32_bf16 v[44:47], v[184:187], v[140:143], v[44:47]\n\t"
      "v_mfma_f32_16x16x32_bf16 v[48:51], v[188:191], v[140:143], v[48:51]\n\t"
      "ds_read_b128 v[136:139], v196 offset:37376\n\t"
      "v_mfma_f32_16x16x32_bf16 v[52:55], v[192:195], v[140:143], v[52:55]\n\t"
      "s_waitcnt lgkmcnt(2)\n\t"
      "v_mfma_f32_16x16x32_bf16 v[56:59], v[172:175], v[144:147], v[56:59]\n\t"
      "v_mfma_f32_16x16x32_bf16 v[60:63], v[176:179], v[144:147], v[60:63]\n\t"
      "s_waitcnt vmcnt(4) lgkmcnt(2)\n\t"
      "s_barrier\n\t"
      "v_mfma_f32_16x16x32_bf16 v[64:67], v[180:183], v[144:147], v[64:67]\n\t"
      "v_cvt_pk_bf16_f32 v32, v32, v33\n\t"
      "v_cvt_pk_bf16_f32 v33, v34, v35\n\t"
      "v_mfma_f32_16x16x32_bf16 v[68:71], v[184:187], v[144:147], v[68:71]\n\t"
      "v_cvt_pk_bf16_f32 v34, v36, v37\n\t"
      "v_cvt_pk_bf16_f32 v35, v38, v39\n\t"
      "v_mfma_f32_16x16x32_bf16 v[72:75], v[188:191], v[144:147], v[72:75]\n\t"
      "ds_read_b128 v[148:151], v197 offset:25088\n\t"
      "ds_write_b128 v217, v[32:35] offset:24576\n\t"
      "v_cvt_pk_bf16_f32 v40, v40, v41\n\t"
      "v_mfma_f32_16x16x32_bf16 v[76:79], v[192:195], v[144:147], v[76:79]\n\t"
      "v_cvt_pk_bf16_f32 v41, v42, v43\n\t"
      "v_cvt_pk_bf16_f32 v42, v44, v45\n\t"
      "s_mov_b32 m0, s41\n\t"
      "s_add_u32 s51, s40, 0x1200000\n\t"
      "buffer_load_dwordx4 v208, s[20:23], s51 offen lds\n\t"
      "s_waitcnt lgkmcnt(3)\n\t"
      "v_mfma_f32_16x16x32_bf16 v[80:83], a[240:243], v[128:131], v[80:83]\n\t"
      "v_cvt_pk_bf16_f32 v43, v46, v47\n\t"
      "ds_write_b128 v218, v[40:43] offset:24576\n\t"
      "ds_read_b128 v[32:35], v196 offset:49152\n\t"
      "v_mfma_f32_16x16x32_bf16 v[84:87], a[244:247], v[128:131], v[84:87]\n\t"
      "v_cvt_pk_bf16_f32 v48, v48, v49\n\t"
      "v_cvt_pk_bf16_f32 v49, v50, v51\n\t"
      "v_mfma_f32_16x16x32_bf16 v[88:91], a[248:251], v[128:131], v[88:91]\n\t"
      "v_cvt_pk_bf16_f32 v50, v52, v53\n\t"
      "v_cvt_pk_bf16_f32 v51, v54, v55\n\t"
      "buffer_load_dwordx4 v208, s[20:23], s51 offen offset:1024 lds\n\t"
      "v_mfma_f32_16x16x32_bf16 v[92:95], a[252:255], v[128:131], v[92:95]\n\t"
      "ds_write_b128 v219, v[48:51] offset:24576\n\t"
      "ds_read_b128 v[36:39], v197 offset:49152\n\t"
      "v_cvt_pk_bf16_f32 v56, v56, v57\n\t"
      "v_mfma_f32_16x16x32_bf16 v[96:99], v[164:167], v[128:131], v[96:99]\n\t"
      "ds_read_b128 v[152:155], v197 offset:37376\n\t"
      "v_cvt_pk_bf16_f32 v57, v58, v59\n\t"
      "v_cvt_pk_bf16_f32 v58, v60, v61\n\t"
      "v_mfma_f32_16x16x32_bf16 v[100:103], v[168:171], v[128:131], v[100:103]\n\t"
      "v_cvt_pk_bf16_f32 v59, v62, v63\n\t"
      "ds_write_b128 v217, v[56:59] offset:36864\n\t"
      "ds_read_b128 v[40:43], v198 offset:49152\n\t"
      "s_waitcnt lgkmcnt(9)\n\t"
      "v_mfma_f32_16x16x32_bf16 v[104:107], a[240:243], v[136:139], v[104:107]\n\t"
      "v_cvt_pk_bf16_f32 v64, v64, v65\n\t"
      "v_cvt_pk_bf16_f32 v65, v66, v67\n\t"
      "buffer_load_dwordx4 v208, s[20:23], s51 offen offset:2048 lds\n\t"
      "v_mfma_f32_16x16x32_bf16 v[108:111], a[244:247], v[136:139], v[108:111]\n\t"
      "v_cvt_pk_bf16_f32 v66, v68, v69\n\t"
      "v_cvt_pk_bf16_f32 v67, v70, v71\n\t"
      "v_mfma_f32_16x16x32_bf16 v[112:115], a[248:251], v[136:139], v[112:115]\n\t"
      "ds_write_b128 v218, v[64:67] offset:36864\n\t"
      "ds_read_b128 v[44:47], v199 offset:49152\n\t"
      "v_cvt_pk_bf16_f32 v72, v72, v73\n\t"
      "v_mfma_f32_16x16x32_bf16 v[116:119], a[252:255], v[136:139], v[116:119]\n\t"
      "v_cvt_pk_bf16_f32 v73, v74, v75\n\t"
      "v_cvt_pk_bf16_f32 v74, v76, v77\n\t"
      "v_mfma_f32_16x16x32_bf16 v[120:123], v[164:167], v[136:139], v[120:123]\n\t"
      "v_cvt_pk_bf16_f32 v75, v78, v79\n\t"
      "ds_write_b128 v219, v[72:75] offset:36864\n\t"
      "ds_read_b128 v[48:51], v197 offset:49408\n\t"
      "v_mfma_f32_16x16x32_bf16 v[132:135], v[168:171], v[136:139], v[132:135]\n\t"
      "s_add_u32 m0, s41, 0x3000\n\t"
      "s_add_u32 s51, s40, 0x1200000\n\t"
      "buffer_load_dwordx4 v209, s[20:23], s51 offen lds\n\t"
      "ds_read_b128 v[52:55], v198 offset:49408\n\t"
      "s_waitcnt lgkmcnt(13)\n\t"
      "v_mfma_f32_16x16x32_bf16 v[80:83], v[172:175], v[148:151], v[80:83]\n\t"
      "ds_read_b128 v[56:59], v199 offset:49408\n\t"
      "ds_read_b128 v[60:63], v196 offset:49408\n\t"
      "v_mfma_f32_16x16x32_bf16 v[84:87], v[176:179], v[148:151], v[84:87]\n\t"
      "ds_read_b128 v[64:67], v198 offset:49664\n\t"
      "s_memrealtime s[80:81]\n\t"
      "v_mfma_f32_16x16x32_bf16 v[88:91], v[180:183], v[148:151], v[88:91]\n\t"
      "buffer_load_dwordx4 v209, s[20:23], s51 offen offset:1024 lds\n\t"
      "ds_read_b128 v[68:71], v199 offset:49664\n\t"
      "v_mfma_f32_16x16x32_bf16 v[92:95], v[184:187], v[148:151], v[92:95]\n\t"
      "ds_read_b128 v[72:75], v196 offset:49664\n\t"
      "ds_read_b128 v[76:79], v197 offset:49664\n\t"
      "v_mfma_f32_16x16x32_bf16 v[96:99], v[188:191], v[148:151], v[96:99]\n\t"
      "v_mfma_f32_16x16x32_bf16 v[100:103], v[192:195], v[148:151], v[100:103]\n\t"
      "buffer_load_dwordx4 v209, s[20:23], s51 offen offset:2048 lds\n\t"
      "s_waitcnt lgkmcnt(13)\n\t"
      "v_mfma_f32_16x16x32_bf16 v[104:107], v[172:175], v[152:155], v[104:107]\n\t"
      "v_mfma_f32_16x16x32_bf16 v[108:111], v[176:179], v[152:155], v[108:111]\n\t"
      "v_mfma_f32_16x16x32_bf16 v[112:115], v[180:183], v[152:155], v[112:115]\n\t"
      "v_mfma_f32_16x16x32_bf16 v[116:119], v[184:187], v[152:155], v[116:119]\n\t"
      "v_mfma_f32_16x16x32_bf16 v[120:123], v[188:191], v[152:155], v[120:123]\n\t"
      "v_mfma_f32_16x16x32_bf16 v[132:135], v[192:195], v[152:155], v[132:135]\n\t"
      "v_add_u32_e32 v196, s46, v200\n\t"
      "v_add_u32_e32 v197, s46, v201\n\t"
      "v_add_u32_e32 v198, s46, v202\n\t"
      "v_add_u32_e32 v199, s46, v203\n\t"
      "s_mov_b32 s86, 77\n\t"
      "s_mov_b32 s84, 92\n\t"
      "s_branch .Lp2_%=\n\t"
      ".Lend_%=:\n\t"
      ".Lconv_end:\n\t"
      :
      : "{s[16:31]}"(d16), "{s[36:39]}"(db), "{s[52:53]}"(rs), "{s40}"(t0), "{s41}"(p0), "{s43}"(lds0), "{s94}"(ty), "{s95}"(tres), "{s[60:61]}"(pres),
      "{s69}"(pre1), "{s74}"(plm), "{s75}"(plb), "{s66}"(sst ? 24576u : 0u - 24576u), "{s89}"((blk >> 4) % 3u),
      "{s71}"(ty ? (wv == 0u ? 584459459u + (blk >> 4) % 3u * 1082401u : 18228419u + (blk >> 4) % 3u * 33825u) : (wv == 0u ? 18264259u + (blk >> 4) % 3u * 33825u : 17603u + (blk >> 4) % 3u * 33u)),
      "{s93}"((0x3fff0u << ty) & 0x3ffffu), "{s92}"((0x3ffffu << (5u + ((((blk >> 4) * 5u + (blk & 3u) * 4u) & 15u) * 11u >> 4))) & 0x3ffffu),
      "{v208}"(ga), "{v209}"(gb), "{v211}"(woff[0]), "{v212}"(woff[1]), "{v213}"(woff[2]), "{v248}"(ta), "{v249}"(ta), "{v250}"(tp), "{v251}"(t1),
      "{v252}"(mk), "{v245}"(vgv), "{v246}"(vxw), "{v247}"(vxr), "{v253}"(vcell), "{v200}"(cfa[0]), "{v201}"(cfa[1]), "{v202}"(cfa[2]),
      "{v203}"(cfa[3]), "{v204}"(csw[0]), "{v205}"(csw[1]), "{v206}"(csw[2]), "{v207}"(csr), "{v214}"(tbase + 73728u), "{v221}"(tbase + 135168u),
      "{v[226:229]}"(br0), "{v[230:233]}"(br1), "{v[234:237]}"(br2), "{v6}"(cpo)
      : "memory", "scc", "vcc",
      "s42", "s44", "s45", "s46", "s47", "s48", "s49", "s50", "s51", "s54", "s55", "s56", "s57", "s58", "s59", "s62", "s63", "s64", "s65", "s67",
      "s68", "s70", "s72", "s73", "s76", "s77", "s78", "s79", "s80", "s81", "s82", "s83", "s84", "s85", "s86", "s87", "s88", "s90", "s91", "s96",
      "s97", "s98", "s99",
      "v1", "v2", "v3", "v4", "v5", "v7", "v8", "v9", "v10", "v11", "v12", "v13", "v14", "v15", "v16", "v17", "v18", "v19", "v20", "v21", "v22", "v23",
      "v24", "v25", "v26", "v27", "v28", "v29", "v30", "v31", "v32", "v33", "v34", "v35", "v36", "v37", "v38", "v39", "v40", "v41", "v42", "v43",
      "v44", "v45", "v46", "v47", "v48", "v49", "v50", "v51", "v52", "v53", "v54", "v55", "v56", "v57", "v58", "v59", "v60", "v61", "v62", "v63",
      "v64", "v65", "v66", "v67", "v68", "v69", "v70", "v71", "v72", "v73", "v74", "v75", "v76", "v77", "v78", "v79", "v80", "v81", "v82", "v83",
      "v84", "v85", "v86", "v87", "v88", "v89", "v90", "v91", "v92", "v93", "v94", "v95", "v96", "v97", "v98", "v99", "v100", "v101", "v102", "v103",
      "v104", "v105", "v106", "v107", "v108", "v109", "v110", "v111", "v112", "v113", "v114", "v115", "v116", "v117", "v118", "v119", "v120", "v121",
      "v122", "v123", "v124", "v125", "v126", "v127", "v128", "v129", "v130", "v131", "v132", "v133", "v134", "v135", "v136", "v137", "v138", "v139",
      "v140", "v141", "v142", "v143", "v144", "v145", "v146", "v147", "v148", "v149", "v150", "v151", "v152", "v153", "v154", "v155", "v156", "v157",
      "v158", "v159", "v160", "v161", "v162", "v163", "v164", "v165", "v166", "v167", "v168", "v169", "v170", "v171", "v172", "v173", "v174", "v175",
      "v176", "v177", "v178", "v179", "v180", "v181", "v182", "v183", "v184", "v185", "v186", "v187", "v188", "v189", "v190", "v191", "v192", "v193",
      "v194", "v195", "v196", "v197", "v198", "v199", "v210", "v215", "v216", "v217", "v218", "v219", "v220", "v222", "v223", "v224", "v225", "v238",
      "v239", "v240", "v241", "v242", "v243", "v244", "v254", "v255",
      "a0", "a1", "a2", "a3", "a4", "a5", "a6", "a7", "a8", "a9", "a10", "a11", "a12", "a13", "a14", "a15", "a16", "a17", "a18", "a19", "a20", "a21",
      "a22", "a23", "a24", "a25", "a26", "a27", "a28", "a29", "a30", "a31", "a32", "a33", "a34", "a35", "a36", "a37", "a38", "a39", "a40", "a41",
      "a42", "a43", "a44", "a45", "a46", "a47", "a48", "a49", "a50", "a51", "a52", "a53", "a54", "a55", "a56", "a57", "a58", "a59", "a60", "a61",
      "a62", "a63", "a64", "a65", "a66", "a67", "a68", "a69", "a70", "a71", "a72", "a73", "a74", "a75", "a76", "a77", "a78", "a79", "a80", "a81",
      "a82", "a83", "a84", "a85", "a86", "a87", "a88", "a89", "a90", "a91", "a92", "a93", "a94", "a95", "a96", "a97", "a98", "a99", "a100", "a101",
      "a102", "a103", "a104", "a105", "a106", "a107", "a108", "a109", "a110", "a111", "a112", "a113", "a114", "a115", "a116", "a117", "a118", "a119",
      "a120", "a121", "a122", "a123", "a124", "a125", "a126", "a127", "a128", "a129", "a130", "a131", "a132", "a133", "a134", "a135", "a136", "a137",
      "a138", "a139", "a140", "a141", "a142", "a143", "a144", "a145", "a146", "a147", "a148", "a149", "a150", "a151", "a152", "a153", "a154", "a155",
      "a156", "a157", "a158", "a159", "a160", "a161", "a162", "a163", "a164", "a165", "a166", "a167", "a168", "a169", "a170", "a171", "a172", "a173",
      "a174", "a175", "a176", "a177", "a178", "a179", "a180", "a181", "a182", "a183", "a184", "a185", "a186", "a187", "a188", "a189", "a190", "a191",
      "a192", "a193", "a194", "a195", "a196", "a197", "a198", "a199", "a200", "a201", "a202", "a203", "a204", "a205", "a206", "a207", "a208", "a209",
      "a210", "a211", "a212", "a213", "a214", "a215", "a216", "a217", "a218", "a219", "a220", "a221", "a222", "a223", "a224", "a225", "a226", "a227",
      "a228", "a229", "a230", "a231", "a232", "a233", "a234", "a235", "a236", "a237", "a238", "a239", "a240", "a241", "a242", "a243", "a244", "a245",
      "a246", "a247", "a248", "a249", "a250", "a251", "a252", "a253", "a254", "a255");
}

extern "C" void kernel_launch(void* const* d_in, const int* in_sizes, int n_in,
                              void* d_out, int out_size, void* d_ws, size_t ws_size,
                              hipStream_t stream) {
  conv1x1_kernel<<<256, 256, 0, stream>>>(d_in[1], 0x0002000000048000ull, d_in[0], 0x0002000006c00000ull, d_ws, 0x0002000000000c00ull, d_out, d_in[2]);
}
```
